```python
import torch

def _softmax(x, axis=-1):
    return torch.softmax(x, dim=axis)
def _gelu(x):
    return torch.nn.functional.gelu(x, approximate="tanh")
def _silu(x):
    return torch.nn.functional.silu(x)
def _sigmoid(x):
    return torch.sigmoid(x)
def _layernorm(x, axis=-1, eps=1e-5):
    m = x.mean(dim=axis, keepdim=True)
    v = x.var(dim=axis, keepdim=True, unbiased=False)
    return (x - m) / torch.sqrt(v + eps)
def _scatter_add(tgt, idx, src, axis=0):
    return tgt.clone().index_add_(axis, idx, src)
def _segment_sum(src, seg, n):
    out = torch.zeros((n,) + src.shape[1:], dtype=src.dtype)
    return out.index_add_(0, seg, src)
def _pad(x, pw):
    flat = tuple(v for pair in reversed(pw) for v in pair)
    return torch.nn.functional.pad(x, flat)

kernel_name = 'gated_mix__linear_kernel_hedgehog__grouped_kv_mqa__prompt'

def setup_inputs():

    _w = (lambda a: (lambda t: t.to(torch.bfloat16) if t.is_floating_point() else t)(torch.as_tensor(a)))
    def _A_setup():
        B, S, D, H = 1, 2048, 640, 32
        Dh = D // H
        return {
            "x": _w((lambda *s: torch.randn(*s, dtype=torch.bfloat16) * 0.05)(B, S, D)),
            "wqkv": _w((lambda *s: torch.randn(*s, dtype=torch.bfloat16) * 1.6)(D, 3 * D)),
            "w_phi": _w((lambda *s: torch.randn(*s, dtype=torch.bfloat16) * 1.6)(H, Dh, Dh) * (Dh ** -0.5)),
            "wo": _w((lambda *s: torch.randn(*s, dtype=torch.bfloat16) * 0.05)(D, D)),
        }
    _A_in = _A_setup()

    def _B_setup():
        B, S, D, H = 1, 2048, 640, 32
        Dh = D // H
        return {
            "x": _w((lambda *s: torch.randn(*s, dtype=torch.bfloat16) * 0.05)(B, S, D)),
            "wq": _w((lambda *s: torch.randn(*s, dtype=torch.bfloat16) * 1.6)(D, H * Dh)),
            "wk": _w((lambda *s: torch.randn(*s, dtype=torch.bfloat16) * 1.6)(D, Dh)),
            "wv": _w((lambda *s: torch.randn(*s, dtype=torch.bfloat16) * 0.05)(D, Dh)),
            "wo": _w((lambda *s: torch.randn(*s, dtype=torch.bfloat16) * 0.05)(H * Dh, D)),
        }
    _B_in = _B_setup()

    return {
        "A_x": _A_in["x"],
        "A_wqkv": _A_in["wqkv"],
        "A_w_phi": _A_in["w_phi"],
        "A_wo": _A_in["wo"],
        "B_x": _B_in["x"],
        "B_wq": _B_in["wq"],
        "B_wk": _B_in["wk"],
        "B_wv": _B_in["wv"],
        "B_wo": _B_in["wo"],
        "gate_w": _w((lambda *s: torch.randn(*s, dtype=torch.bfloat16) * 0.05)(640) * 16.0),
    }

def reference(A_x, A_wqkv, A_w_phi, A_wo, B_x, B_wq, B_wk, B_wv, B_wo, gate_w):
    def _A_ref(x, wqkv, w_phi, wo):
        B, S, D = x.shape
        H = 32; Dh = D // H
        qkv = (lambda x, *d: x.permute(*d))((x @ wqkv).reshape(B, S, 3, H, Dh), 2, 0, 3, 1, 4)
        q, k, v = qkv[0], qkv[1], qkv[2]
        pq = torch.einsum("bhsd,hdf->bhsf", q, w_phi)
        pk = torch.einsum("bhsd,hdf->bhsf", k, w_phi)
        phi_q = torch.concatenate([_softmax(pq, axis=-1), _softmax(-pq, axis=-1)], axis=-1)
        phi_k = torch.concatenate([_softmax(pk, axis=-1), _softmax(-pk, axis=-1)], axis=-1)
        kv = torch.einsum("bhsf,bhsd->bhfd", phi_k, v)
        z  = torch.einsum("bhsf->bhf", phi_k)
        num = torch.einsum("bhqf,bhfd->bhqd", phi_q, kv)
        den = torch.einsum("bhqf,bhf->bhq", phi_q, z)[..., None] + 1e-6
        o = num / den
        return (lambda x, *d: x.permute(*d))(o, 0, 2, 1, 3).reshape(B, S, D) @ wo

    def _B_ref(x, wq, wk, wv, wo):
        B, S, D = x.shape
        H = 32
        Dh = D // H
        q = (lambda x, *d: x.permute(*d))((x @ wq).reshape(B, S, H, Dh), 0, 2, 1, 3)
        k = x @ wk
        v = x @ wv
        scores = torch.einsum("bhqd,bkd->bhqk", q, k) * (Dh ** -0.5)
        p = _softmax(scores, axis=-1)
        o = torch.einsum("bhqk,bkd->bhqd", p, v)
        return (lambda x, *d: x.permute(*d))(o, 0, 2, 1, 3).reshape(B, S, H * Dh) @ wo

    x = A_x
    g = _sigmoid((x * gate_w).sum(-1, keepdims=True))
    a = _A_ref(x, A_wqkv, A_w_phi, A_wo)
    b = _B_ref(x, B_wq, B_wk, B_wv, B_wo)
    return g * a + (1 - g) * b
```

```cpp
#include <hip/hip_runtime.h>
#include <hip/hip_bf16.h>
typedef __hip_bfloat16 bf16;
constexpr int S_=2048, D_=640, H_=32, DH=20;
__device__ inline float b2f(bf16 v){ return __bfloat162float(v); }

__global__ void gemm_naive(const bf16* A, const bf16* B, float* C, int M, int N, int K, int ldb, int ldc) {
  int n = blockIdx.x * 64 + (threadIdx.x & 63);
  int m = blockIdx.y * 4 + (threadIdx.x >> 6);
  if (n >= N || m >= M) return;
  float acc = 0.f;
  for (int k = 0; k < K; ++k) acc += b2f(A[(size_t)m * K + k]) * b2f(B[(size_t)k * ldb + n]);
  C[(size_t)m * ldc + n] = acc;
}
__global__ void phi_k(const float* qkv, const bf16* wphi, float* phi) {
  int idx = blockIdx.x * 256 + threadIdx.x;
  if (idx >= 2 * S_ * H_) return;
  int which = idx / (S_ * H_), s = (idx / H_) % S_, h = idx % H_;
  const float* v = qkv + (size_t)s * 1920 + which * 640 + h * DH;
  float* o = phi + (size_t)idx * 40;
  float mx = -1e30f, mn = 1e30f;
  for (int f = 0; f < DH; ++f) { float a = 0; for (int d = 0; d < DH; ++d) a += v[d] * b2f(wphi[h * 400 + d * DH + f]); o[f] = a; mx = fmaxf(mx, a); mn = fminf(mn, a); }
  float sp = 0, sn = 0;
  for (int f = 0; f < DH; ++f) { float a = o[f]; float ep = expf(a - mx), en = expf(mn - a); sp += ep; sn += en; o[f] = ep; o[20 + f] = en; }
  for (int f = 0; f < DH; ++f) { o[f] /= sp; o[20 + f] /= sn; }
}
__global__ void kv_k(const float* qkv, const float* phi, float* kv) {
  int idx = blockIdx.x * 256 + threadIdx.x;
  if (idx >= H_ * 40 * 21) return;
  int h = idx / 840, f = (idx / 21) % 40, d = idx % 21;
  const float* pk = phi + (size_t)S_ * H_ * 40;
  float a = 0;
  for (int s = 0; s < S_; ++s) a += pk[((size_t)s * H_ + h) * 40 + f] * (d < 20 ? qkv[(size_t)s * 1920 + 1280 + h * DH + d] : 1.f);
  kv[idx] = a;
}
__global__ void oa_k(const float* phi, const float* kv, bf16* ocat) {
  int idx = blockIdx.x * 256 + threadIdx.x;
  if (idx >= S_ * H_) return;
  int s = idx / H_, h = idx % H_;
  const float* pq = phi + (size_t)idx * 40;
  float den = 1e-6f; for (int f = 0; f < 40; ++f) den += pq[f] * kv[h * 840 + f * 21 + 20];
  for (int d = 0; d < DH; ++d) { float a = 0; for (int f = 0; f < 40; ++f) a += pq[f] * kv[h * 840 + f * 21 + d]; ocat[(size_t)s * 640 + h * DH + d] = __float2bfloat16(a / den); }
}
__global__ void attn_k(const float* qb, const float* kb, const float* vb, bf16* ob) {
  int idx = blockIdx.x * 64 + threadIdx.x;
  int s = idx / H_, h = idx % H_;
  float q[DH];
#pragma unroll
  for (int d = 0; d < DH; ++d) q[d] = qb[(size_t)s * 640 + h * DH + d] * 0.22360679775f;
  float m = -1e30f, l = 0, o[DH];
#pragma unroll
  for (int d = 0; d < DH; ++d) o[d] = 0;
  for (int k = 0; k < S_; ++k) {
    float sc = 0;
#pragma unroll
    for (int d = 0; d < DH; ++d) sc += q[d] * kb[k * DH + d];
    float mn = fmaxf(m, sc), al = __expf(m - mn), p = __expf(sc - mn);
    l = l * al + p; m = mn;
#pragma unroll
    for (int d = 0; d < DH; ++d) o[d] = o[d] * al + p * vb[k * DH + d];
  }
#pragma unroll
  for (int d = 0; d < DH; ++d) ob[(size_t)s * 640 + h * DH + d] = __float2bfloat16(o[d] / l);
}
__global__ void fin_k(const bf16* x, const bf16* gw, const float* a, const float* b, bf16* out) {
  int s = blockIdx.x; __shared__ float red[64];
  float acc = 0; for (int k = threadIdx.x; k < 640; k += 64) acc += b2f(x[(size_t)s * 640 + k]) * b2f(gw[k]);
  red[threadIdx.x] = acc; __syncthreads();
  float t = 0; for (int i = 0; i < 64; ++i) t += red[i];
  float g = 1.f / (1.f + expf(-t));
  for (int n = threadIdx.x; n < 640; n += 64) out[(size_t)s * 640 + n] = __float2bfloat16(g * a[(size_t)s * 640 + n] + (1.f - g) * b[(size_t)s * 640 + n]);
}
extern "C" void kernel_launch(void* const* d_in, const int* in_sizes, int n_in, void* d_out, int out_size, void* d_ws, size_t ws_size, hipStream_t stream) {
  const bf16* x = (const bf16*)d_in[0]; const bf16* wqkv = (const bf16*)d_in[1]; const bf16* wphi = (const bf16*)d_in[2]; const bf16* awo = (const bf16*)d_in[3];
  const bf16* wq = (const bf16*)d_in[5]; const bf16* wk = (const bf16*)d_in[6]; const bf16* wv = (const bf16*)d_in[7]; const bf16* bwo = (const bf16*)d_in[8]; const bf16* gw = (const bf16*)d_in[9];
  char* ws = (char*)d_ws; size_t off = 0;
  auto alloc = [&](size_t b) { char* p = ws + off; off += (b + 255) & ~(size_t)255; return p; };
  float* qkv = (float*)alloc((size_t)S_ * 1920 * 4);
  float* qb = (float*)alloc((size_t)S_ * 640 * 4);
  float* kb = (float*)alloc((size_t)S_ * 20 * 4);
  float* vb = (float*)alloc((size_t)S_ * 20 * 4);
  float* phi = (float*)alloc((size_t)2 * S_ * H_ * 40 * 4);
  float* kv = (float*)alloc(H_ * 840 * 4);
  bf16* oa = (bf16*)alloc((size_t)S_ * 640 * 2);
  bf16* ob = (bf16*)alloc((size_t)S_ * 640 * 2);
  float* ya = (float*)alloc((size_t)S_ * 640 * 4);
  float* yb = (float*)alloc((size_t)S_ * 640 * 4);
  gemm_naive<<<dim3(30, S_ / 4), 256, 0, stream>>>(x, wqkv, qkv, S_, 1920, 640, 1920, 1920);
  gemm_naive<<<dim3(10, S_ / 4), 256, 0, stream>>>(x, wq, qb, S_, 640, 640, 640, 640);
  gemm_naive<<<dim3(1, S_ / 4), 256, 0, stream>>>(x, wk, kb, S_, 20, 640, 20, 20);
  gemm_naive<<<dim3(1, S_ / 4), 256, 0, stream>>>(x, wv, vb, S_, 20, 640, 20, 20);
  phi_k<<<2 * S_ * H_ / 256, 256, 0, stream>>>(qkv, wphi, phi);
  kv_k<<<(H_ * 840 + 255) / 256, 256, 0, stream>>>(qkv, phi, kv);
  oa_k<<<S_ * H_ / 256, 256, 0, stream>>>(phi, kv, oa);
  attn_k<<<S_ * H_ / 64, 64, 0, stream>>>(qb, kb, vb, ob);
  gemm_naive<<<dim3(10, S_ / 4), 256, 0, stream>>>(oa, awo, ya, S_, 640, 640, 640, 640);
  gemm_naive<<<dim3(10, S_ / 4), 256, 0, stream>>>(ob, bwo, yb, S_, 640, 640, 640, 640);
  fin_k<<<S_, 64, 0, stream>>>(x, gw, ya, yb, (bf16*)d_out);
}
```

```cpp
#include <hip/hip_runtime.h>
#include <hip/hip_bf16.h>
#include <stdint.h>

typedef unsigned int u32;
typedef u32 u32x2 __attribute__((ext_vector_type(2)));
typedef u32 u32x4 __attribute__((ext_vector_type(4)));
typedef float f32x2 __attribute__((ext_vector_type(2)));
typedef float f32x4 __attribute__((ext_vector_type(4)));
typedef float f32x8 __attribute__((ext_vector_type(8)));
typedef float f32x16 __attribute__((ext_vector_type(16)));
typedef __bf16 bf16x2 __attribute__((ext_vector_type(2)));
typedef __bf16 bf16x8 __attribute__((ext_vector_type(8)));
typedef __bf16 bf16x16 __attribute__((ext_vector_type(16)));
typedef short s16x4 __attribute__((ext_vector_type(4)));
#define LDSAS __attribute__((address_space(3)))
typedef LDSAS char* ldsp;

constexpr int SEQ = 2048, DM = 640, NH = 32, DH = 20;
constexpr float LOG2E = 1.4426950408889634f;
constexpr float QSCALE = 1.4426950408889634f * 0.22360679774997896f;

constexpr u32 REC = 2624;
constexpr size_t WS_QB   = 0;
constexpr size_t WS_PHIQ = WS_QB + 2621440 + 256;
constexpr size_t WS_KVIM = WS_PHIQ + 5242880 + 256;
constexpr size_t WS_KVP  = WS_KVIM + 64 * REC + 256;
constexpr size_t WS_GATE = WS_KVP + 32 * 16 * 840 * 4;
constexpr size_t WS_OFR  = WS_GATE + 8192;
constexpr u32 OOB = 0x40000000u;

#define SBAR() __builtin_amdgcn_sched_barrier(0)
#define PIN(x) asm volatile("" : "+v"(x))
#define WAIT_BAR(N) asm volatile("s_waitcnt vmcnt(" #N ") lgkmcnt(0)\n\ts_barrier" ::: "memory")

__device__ __forceinline__ u32 pk2(float a, float b) {
  f32x2 v = {a, b};
  return __builtin_bit_cast(u32, __builtin_convertvector(v, bf16x2));
}
__device__ __forceinline__ f32x4 mfma16(u32x4 a, u32x4 b, f32x4 c) {
  return __builtin_amdgcn_mfma_f32_16x16x32_bf16(__builtin_bit_cast(bf16x8, a), __builtin_bit_cast(bf16x8, b), c, 0, 0, 0);
}
__device__ __forceinline__ f32x16 mfma32(u32x4 a, u32x4 b, f32x16 c) {
  return __builtin_amdgcn_mfma_f32_32x32x16_bf16(__builtin_bit_cast(bf16x8, a), __builtin_bit_cast(bf16x8, b), c, 0, 0, 0);
}
__device__ __forceinline__ f32x16 smf(u32x4 a, f32x8 b, f32x16 c, int idx) {
  return __builtin_amdgcn_smfmac_f32_32x32x32_bf16(__builtin_bit_cast(bf16x8, a), __builtin_bit_cast(bf16x16, b), c, idx, 0, 0);
}
__device__ __forceinline__ u32x2 trd(ldsp p) {
  return __builtin_bit_cast(u32x2, __builtin_amdgcn_ds_read_tr16_b64_v4i16((LDSAS s16x4*)p));
}
__device__ __forceinline__ u32x4 trd2(ldsp p0, ldsp p1) {
  u32x2 a = trd(p0), b = trd(p1);
  return (u32x4){a[0], a[1], b[0], b[1]};
}
__device__ __forceinline__ u32x4 ld128(ldsp p) { return *(LDSAS u32x4*)p; }
__device__ __forceinline__ u32x2 ld64(ldsp p) { return *(LDSAS u32x2*)p; }

__device__ __forceinline__ u32x4 mkrsrc(const void* p, u32 bytes) {
  uint64_t a = (uint64_t)p;
  return (u32x4){(u32)a, (u32)(a >> 32) & 0xffffu, bytes, 0x00020000u};
}
__device__ __forceinline__ void dma_buf(u32x4 rs, u32 voff, u32 soff, u32 ldsaddr) {
  u32 keep;
  asm volatile("s_mov_b32 %0, m0\n\ts_mov_b32 m0, %4\n\ts_nop 0\n\tbuffer_load_dwordx4 %1, %2, %3 offen lds\n\ts_mov_b32 m0, %0"
               : "=&s"(keep) : "v"(voff), "s"(rs), "s"(soff), "s"(ldsaddr) : "memory");
}
__device__ __forceinline__ void dma_glb(const void* g, u32 ldsaddr) {
  u32 keep;
  asm volatile("s_mov_b32 %0, m0\n\ts_mov_b32 m0, %2\n\ts_nop 0\n\tglobal_load_lds_dwordx4 %1, off\n\ts_mov_b32 m0, %0"
               : "=&s"(keep) : "v"(g), "s"(ldsaddr) : "memory");
}

constexpr u32 K1_SLOT = 40960, K1_W = 16384, K1_EX = 36864, K1_GT = 39936;
constexpr u32 K1_WPHI = 2 * K1_SLOT;
constexpr u32 K1_PHK = 0, K1_PHK_P = 336, K1_VI = 45056, K1_VI_P = 208;

__global__ void __launch_bounds__(512, 2)
k1_proj(const void* __restrict__ x, const void* __restrict__ wqkv, const void* __restrict__ wphi, const void* __restrict__ wq,
        const void* __restrict__ wk, const void* __restrict__ wv, const void* __restrict__ gw, char* __restrict__ ws) {
  extern __shared__ __attribute__((aligned(16))) char smem[];
  ldsp lds = (ldsp)smem;
  const u32 lds0 = (u32)(uintptr_t)lds;
  const int tid = threadIdx.x, lane = tid & 63;
  const int w = __builtin_amdgcn_readfirstlane(tid >> 6);
  const int bid = blockIdx.x, xcd = bid & 7, jb = bid >> 3;
  const int tt = (xcd & 1) * 8 + (jb & 7), combo = (xcd >> 1) * 4 + (jb >> 3), type = combo & 1, hg = combo >> 1;

  const u32x4 rsX = mkrsrc(x, SEQ * DM * 2);
  const u32x4 rsQKV = mkrsrc(wqkv, DM * 3 * DM * 2);
  const u32x4 rsG1 = type ? rsQKV : mkrsrc(wq, DM * DM * 2);
  const u32 pitch1 = type ? 3840u : 1280u;
  const u32 col0 = (type ? 640u : 0u) + 80u * hg, col1 = (type ? 1280u : 0u) + 80u * hg;
  u32 vx[2], vw[3], st[3];
  u32x4 rw[3];
#pragma unroll
  for (int i = 0; i < 2; ++i) {
    const int row = 8 * (2 * w + i) + (lane >> 3);
    vx[i] = (u32)(128 * tt + row) * 1280u + (u32)(((lane & 7) ^ ((row >> 1) & 7)) * 16);
  }
#pragma unroll
  for (int jj = 0; jj < 3; ++jj) {
    const int p = w + 8 * jj;
    if (p < 20) {
      const bool g1 = p >= 10;
      const int c = (g1 ? p - 10 : p) * 64 + lane, rho = c / 10, cc = c - rho * 10;
      const int k = (rho & 32) + 8 * ((rho >> 2) & 3) + 4 * ((rho >> 4) & 1) + (rho & 3);
      const u32 pitch = g1 ? pitch1 : 3840u;
      vw[jj] = (u32)k * pitch + (g1 ? col1 : col0) * 2u + (u32)cc * 16u;
      rw[jj] = g1 ? rsG1 : rsQKV;
      st[jj] = 64u * pitch;
    } else if (p < 23) {
      const u32 byte = (u32)((p - 20) * 64 + lane) * 16u;
      vw[jj] = byte < 2560u ? byte : OOB;
      rw[jj] = mkrsrc(type ? wv : wk, DM * DH * 2);
      st[jj] = 2560u;
    } else {
      vw[jj] = lane < 8 ? (u32)lane * 16u : OOB;
      rw[jj] = mkrsrc(gw, DM * 2);
      st[jj] = 128u;
    }
  }
  asm volatile("s_nop 4" ::: "memory");
  auto issue = [&](int s) {
    const u32 base = lds0 + (u32)(s & 3) * K1_SLOT;
    dma_buf(rsX, vx[0], (u32)s * 128u, base + (u32)(2 * w) * 1024u);
    dma_buf(rsX, vx[1], (u32)s * 128u, base + (u32)(2 * w + 1) * 1024u);
#pragma unroll
    for (int jj = 0; jj < 3; ++jj) dma_buf(rw[jj], vw[jj], (u32)s * st[jj], base + K1_W + (u32)(w + 8 * jj) * 1024u);
  };
  issue(0); issue(1); issue(2);

  const int cg = w >> 2, tq = w & 3, g = lane >> 4, i16 = lane & 15;
  const u32 aW = K1_W + (u32)cg * 10240u + (u32)(4 * g + (i16 >> 2)) * 160u + (u32)(i16 & 3) * 40u;
  const u32 aX = (u32)(32 * tq + i16) * 128u + (u32)((g ^ (i16 >> 1)) * 16);
  const u32 aXe = (u32)(16 * hg + i16) * 128u + (u32)((g ^ (i16 >> 1)) * 16);
  const u32 aE = (w == 6) ? K1_GT + (u32)g * 16u : K1_EX + (u32)(8 * g + (i16 >> 2)) * 40u + (u32)(i16 & 3) * 8u + (u32)(w & 1) * 32u;

  f32x4 acc[5][2];
#pragma unroll
  for (int T = 0; T < 5; ++T) { acc[T][0] = (f32x4){0.f, 0.f, 0.f, 0.f}; acc[T][1] = (f32x4){0.f, 0.f, 0.f, 0.f}; }
  f32x4 acce = {0.f, 0.f, 0.f, 0.f};

  for (int s = 0; s < 10; ++s) {
    if (s < 8) WAIT_BAR(10); else if (s == 8) WAIT_BAR(6); else WAIT_BAR(1);
    if (s < 7) issue(s + 3);
    else if (s == 7) {
      const u32 byte = (u32)(w * 64 + lane) * 16u;
      dma_buf(mkrsrc(wphi, NH * 400 * 2), byte < 3200u ? byte : OOB, (u32)hg * 3200u, lds0 + K1_WPHI + (u32)w * 1024u);
    }
    ldsp sb = lds + (u32)(s & 3) * K1_SLOT;
#pragma unroll
    for (int kk = 0; kk < 2; ++kk) {
      u32x4 xf0 = ld128(sb + (aX ^ (kk * 64)));
      u32x4 xf1 = ld128(sb + (aX ^ (kk * 64)) + 2048);
#pragma unroll
      for (int T = 0; T < 5; ++T) {
        u32x4 wf = trd2(sb + aW + kk * 5120 + T * 8, sb + aW + kk * 5120 + 2560 + T * 8);
        acc[T][0] = mfma16(wf, xf0, acc[T][0]);
        acc[T][1] = mfma16(wf, xf1, acc[T][1]);
      }
      if (w >= 4 && w < 7) {
        u32x4 xe = ld128(sb + (aXe ^ (kk * 64)));
        u32x4 we;
        if (w == 6) we = ld128(sb + aE + kk * 64);
        else we = trd2(sb + aE + kk * 1280, sb + aE + kk * 1280 + 160);
        if (type && w != 6) acce = mfma16(xe, we, acce); else acce = mfma16(we, xe, acce);
      }
    }
  }
  WAIT_BAR(0);

  const int head = 4 * hg + g;
  if (cg == 0) {
    const u32 aP = K1_WPHI + (((i16 & 3) == g) ? (u32)g * 800u + (u32)(i16 >> 2) * 40u : 3200u);
    f32x4 pq[5][2];
    u32x4 bq[3][2];
#pragma unroll
    for (int n = 0; n < 2; ++n) {
      bq[0][n] = (u32x4){pk2(acc[0][n][0], acc[0][n][1]), pk2(acc[0][n][2], acc[0][n][3]), pk2(acc[1][n][0], acc[1][n][1]), pk2(acc[1][n][2], acc[1][n][3])};
      bq[1][n] = (u32x4){pk2(acc[2][n][0], acc[2][n][1]), pk2(acc[2][n][2], acc[2][n][3]), pk2(acc[3][n][0], acc[3][n][1]), pk2(acc[3][n][2], acc[3][n][3])};
      bq[2][n] = (u32x4){pk2(acc[4][n][0], acc[4][n][1]), pk2(acc[4][n][2], acc[4][n][3]), 0u, 0u};
    }
#pragma unroll
    for (int T = 0; T < 5; ++T) {
      u32x4 a0 = trd2(lds + aP + T * 8, lds + aP + T * 8 + 160);
      u32x4 a1 = trd2(lds + aP + T * 8 + 320, lds + aP + T * 8 + 480);
      u32x2 a2h = trd(lds + aP + T * 8 + 640);
      u32x4 a2 = {a2h[0], a2h[1], 0u, 0u};
#pragma unroll
      for (int n = 0; n < 2; ++n) {
        f32x4 c = {0.f, 0.f, 0.f, 0.f};
        c = mfma16(a0, bq[0][n], c);
        c = mfma16(a1, bq[1][n], c);
        c = mfma16(a2, bq[2][n], c);
        pq[T][n] = c;
      }
    }
#pragma unroll
    for (int n = 0; n < 2; ++n) {
      float mx = pq[0][n][0], mn = pq[0][n][0];
#pragma unroll
      for (int T = 0; T < 5; ++T)
#pragma unroll
        for (int i = 0; i < 4; ++i) { mx = fmaxf(mx, pq[T][n][i]); mn = fminf(mn, pq[T][n][i]); }
      const float cp = -mx * LOG2E, cn = mn * LOG2E;
      f32x4 ep[5], en[5];
      float sp = 0.f, sn = 0.f;
#pragma unroll
      for (int T = 0; T < 5; ++T)
#pragma unroll
        for (int i = 0; i < 4; ++i) {
          ep[T][i] = __builtin_amdgcn_exp2f(__builtin_fmaf(pq[T][n][i], LOG2E, cp));
          en[T][i] = __builtin_amdgcn_exp2f(__builtin_fmaf(pq[T][n][i], -LOG2E, cn));
          sp += ep[T][i]; sn += en[T][i];
        }
      const float rp = __builtin_amdgcn_rcpf(sp), rn = __builtin_amdgcn_rcpf(sn);
      u32 ph[20];
#pragma unroll
      for (int T = 0; T < 5; ++T) {
        ph[2 * T] = pk2(ep[T][0] * rp, ep[T][1] * rp); ph[2 * T + 1] = pk2(ep[T][2] * rp, ep[T][3] * rp);
        ph[10 + 2 * T] = pk2(en[T][0] * rn, en[T][1] * rn); ph[11 + 2 * T] = pk2(en[T][2] * rn, en[T][3] * rn);
      }
      const int tl = 32 * tq + 16 * n + i16;
      if (type == 0) {
        u32x4* dst = (u32x4*)(ws + WS_PHIQ + ((size_t)head * SEQ + 128 * tt + tl) * 80);
#pragma unroll
        for (int c = 0; c < 5; ++c) dst[c] = (u32x4){ph[4 * c], ph[4 * c + 1], ph[4 * c + 2], ph[4 * c + 3]};
      } else {
        ldsp dst = lds + K1_PHK + (u32)tl * K1_PHK_P + (u32)g * 80u;
#pragma unroll
        for (int c = 0; c < 5; ++c) *(LDSAS u32x4*)(dst + 16 * c) = (u32x4){ph[4 * c], ph[4 * c + 1], ph[4 * c + 2], ph[4 * c + 3]};
      }
    }
  } else {
#pragma unroll
    for (int n = 0; n < 2; ++n) {
      const int tl = 32 * tq + 16 * n + i16;
      if (type == 0) {
        u32x2* dst = (u32x2*)(ws + WS_QB + ((size_t)head * SEQ + 128 * tt + tl) * 40);
#pragma unroll
        for (int T = 0; T < 5; ++T)
          dst[T] = (u32x2){pk2(acc[T][n][0] * QSCALE, acc[T][n][1] * QSCALE), pk2(acc[T][n][2] * QSCALE, acc[T][n][3] * QSCALE)};
      } else {
        ldsp dst = lds + K1_VI + (u32)tl * K1_VI_P + (u32)g * 48u;
        *(LDSAS u32x4*)(dst) = (u32x4){pk2(acc[0][n][0], acc[0][n][1]), pk2(acc[0][n][2], acc[0][n][3]), pk2(acc[1][n][0], acc[1][n][1]), pk2(acc[1][n][2], acc[1][n][3])};
        *(LDSAS u32x4*)(dst + 16) = (u32x4){pk2(acc[2][n][0], acc[2][n][1]), pk2(acc[2][n][2], acc[2][n][3]), pk2(acc[3][n][0], acc[3][n][1]), pk2(acc[3][n][2], acc[3][n][3])};
        *(LDSAS u32x4*)(dst + 32) = (u32x4){pk2(acc[4][n][0], acc[4][n][1]), pk2(acc[4][n][2], acc[4][n][3]), 0x00003F80u, 0u};
      }
    }
    const int key0 = 128 * tt + 16 * hg;
    char* kvim = ws + WS_KVIM;
    const u32x2 ev = {pk2(acce[0], acce[1]), pk2(acce[2], acce[3])};
    if (type == 0) {
      const int key = key0 + i16, tile = key >> 5, rec = (tile + 62) & 63;
      if (w == 4) *(u32x2*)(kvim + rec * REC + (g >> 1) * 512 + (key & 31) * 16 + (g & 1) * 8) = ev;
      else if (w == 5 && g == 0) *(u32x2*)(kvim + rec * REC + 1024 + (key & 31) * 8) = ev;
    } else {
      if (w == 4 || w == 5) {
        const int d = 16 * (w & 1) + i16, key = key0 + 4 * g, rec = key >> 5;
        const u32x2 val = (d == 20) ? (u32x2){0x3F803F80u, 0x3F803F80u} : ev;
        if (d <= 20) *(u32x2*)(kvim + rec * REC + 1280 + ((key >> 4) & 1) * 672 + ((key >> 3) & 1) * 336 + d * 16 + (key & 7) * 2) = val;
      } else if (w == 6 && g == 0) {
        ((float*)(ws + WS_GATE))[key0 + i16] = __builtin_amdgcn_rcpf(1.f + __builtin_amdgcn_exp2f(-acce[0] * LOG2E));
      }
    }
  }
  if (type) {
    WAIT_BAR(0);
    const int hl = w >> 1, dt = w & 1;
    const u32 trow = (u32)(8 * g + (i16 >> 2));
    ldsp pa = lds + K1_PHK + trow * K1_PHK_P + (u32)hl * 80u + (u32)(i16 & 3) * 8u;
    ldsp pb = lds + K1_VI + trow * K1_VI_P + (u32)hl * 48u + (u32)dt * 32u + (u32)(i16 & 3) * 8u;
    f32x4 kv[3];
#pragma unroll
    for (int ft = 0; ft < 3; ++ft) kv[ft] = (f32x4){0.f, 0.f, 0.f, 0.f};
#pragma unroll
    for (int ks = 0; ks < 4; ++ks) {
      u32x4 bv = trd2(pb + ks * 32 * K1_VI_P, pb + (ks * 32 + 4) * K1_VI_P);
#pragma unroll
      for (int ft = 0; ft < 3; ++ft) {
        u32x4 av = trd2(pa + ks * 32 * K1_PHK_P + ft * 32, pa + (ks * 32 + 4) * K1_PHK_P + ft * 32);
        kv[ft] = mfma16(av, bv, kv[ft]);
      }
    }
    const int d = 16 * dt + i16;
    float* dst = (float*)(ws + WS_KVP) + ((size_t)(4 * hg + hl) * 16 + tt) * 840 + d * 40 + 4 * g;
    if (d <= 20) {
      *(f32x4*)(dst) = kv[0];
      *(f32x4*)(dst + 16) = kv[1];
      if (g < 2) *(f32x4*)(dst + 32) = kv[2];
    }
  }
}

constexpr u32 K2_RING = 48 * REC;
constexpr u32 K2_DUMP = K2_RING;
constexpr u32 K2_KVT = K2_RING + 1024;
constexpr u32 K2_LDS = K2_KVT + 2048;

__device__ __forceinline__ f32x8 lo8(const f32x16& s) { return __builtin_shufflevector(s, s, 0, 1, 2, 3, 4, 5, 6, 7); }
__device__ __forceinline__ f32x8 hi8(const f32x16& s) { return __builtin_shufflevector(s, s, 8, 9, 10, 11, 12, 13, 14, 15); }
__device__ __forceinline__ float max16(const f32x16& s) {
  float a = fmaxf(fmaxf(s[0], s[1]), s[2]), b = fmaxf(fmaxf(s[3], s[4]), s[5]);
  a = fmaxf(fmaxf(a, s[6]), s[7]); b = fmaxf(fmaxf(b, s[8]), s[9]);
  a = fmaxf(fmaxf(a, s[10]), s[11]); b = fmaxf(fmaxf(b, s[12]), s[13]);
  a = fmaxf(fmaxf(a, s[14]), s[15]);
  return fmaxf(a, b);
}
__device__ __forceinline__ float xhalf_max(float m) {
  auto r = __builtin_amdgcn_permlane32_swap(__float_as_uint(m), __float_as_uint(m), false, false);
  return fmaxf(__uint_as_float(r[0]), __uint_as_float(r[1]));
}

__global__ void __launch_bounds__(512, 2) k2_attn(char* __restrict__ ws) {
  extern __shared__ __attribute__((aligned(16))) char smem[];
  ldsp lds = (ldsp)smem;
  const u32 lds0 = (u32)(uintptr_t)lds;
  const int tid = threadIdx.x, lane = tid & 63, r32 = lane & 31, hh = lane >> 5;
  const int w = __builtin_amdgcn_readfirstlane(tid >> 6);
  const int head = blockIdx.x >> 3, tb = blockIdx.x & 7;
  const int tok = 256 * tb + 32 * w + r32;
  const char* kvim = ws + WS_KVIM;

  const char* qrow = ws + WS_QB + ((size_t)head * SEQ + tok) * 40;
  const u32x2 qa = *(const u32x2*)(qrow + hh * 16), qb = *(const u32x2*)(qrow + hh * 16 + 8), qc = *(const u32x2*)(qrow + 32);
  const u32x4 k1a = *(const u32x4*)(kvim + 62 * REC + lane * 16);
  const u32x2 k2a = *(const u32x2*)(kvim + 62 * REC + 1024 + r32 * 8);
  u32x4 k1 = *(const u32x4*)(kvim + 63 * REC + lane * 16);
  const u32x2 k2b = *(const u32x2*)(kvim + 63 * REC + 1024 + r32 * 8);
  PIN(k1);
  const u32x4 rsKV = mkrsrc(kvim, 64 * REC);
  const u32 vl = (u32)lane * 16u;
  asm volatile("s_nop 4" ::: "memory");
#pragma unroll
  for (int j = 0; j < 16; ++j) {
    const u32 p = (u32)(w + 8 * j);
    dma_buf(rsKV, vl, p < 123u ? p * 1024u : OOB, lds0 + (p < 123u ? p * 1024u : K2_DUMP));
  }
  const f32x16 zero16 = {0.f, 0.f, 0.f, 0.f, 0.f, 0.f, 0.f, 0.f, 0.f, 0.f, 0.f, 0.f, 0.f, 0.f, 0.f, 0.f};
  const u32x4 q1 = {qa[0], qa[1], qb[0], qb[1]};
  u32x4 q2 = {hh ? 0u : qc[0], hh ? 0u : qc[1], 0u, 0u};
  u32x4 k2 = {k2b[0], k2b[1], 0x00003F80u, 0u};
  f32x16 S0, S1, S2, S3 = zero16, O = zero16;
  S0 = mfma32(k1a, q1, zero16);
  S0 = mfma32((u32x4){k2a[0], k2a[1], 0u, 0u}, q2, S0);
  {
    const float m = xhalf_max(max16(S0));
    const u32 nmb = pk2(-m, 0.f) & 0xffffu;
    const float nm = __uint_as_float(nmb << 16);
    q2[2] = hh ? 0u : nmb;
#pragma unroll
    for (int r = 0; r < 16; ++r) S0[r] += nm;
  }
  u32x4 vlo = {0u, 0u, 0u, 0u}, vhi = {0u, 0u, 0u, 0u};
  const int idx = 0xDDDD;
  u32 aK1 = (u32)lane * 16u, aK2 = 1024u + (u32)r32 * 8u, aV = 1280u + (u32)hh * 336u + (u32)(r32 < 20 ? r32 : 20) * 16u;

#define EX4(X, i) do { X[i] = __builtin_amdgcn_exp2f(X[i]); X[i + 1] = __builtin_amdgcn_exp2f(X[i + 1]); X[i + 2] = __builtin_amdgcn_exp2f(X[i + 2]); X[i + 3] = __builtin_amdgcn_exp2f(X[i + 3]); PIN(X); SBAR(); } while (0)
#define STEP(SN, SC, SP, t) do { \
    SN = mfma32(k1, q1, zero16); k1 = ld128(lds + aK1 + (t) * REC); EX4(SC, 0); \
    SN = mfma32(k2, q2, SN); { u32x2 t2 = ld64(lds + aK2 + (t) * REC); k2[0] = t2[0]; k2[1] = t2[1]; } EX4(SC, 4); \
    O = smf(vlo, lo8(SP), O, idx); vlo = ld128(lds + aV + (t) * REC); EX4(SC, 8); \
    O = smf(vhi, hi8(SP), O, idx); vhi = ld128(lds + aV + (t) * REC + 672); EX4(SC, 12); \
  } while (0)

  WAIT_BAR(13);
  for (int gi = 0; gi < 8; ++gi) {
    if (gi == 1) WAIT_BAR(5);
    else if (gi == 2) {
      WAIT_BAR(0);
#pragma unroll
      for (int j = 0; j < 6; ++j) {
        const u32 p = (u32)(w + 8 * j);
        dma_buf(rsKV, vl, p < 41u ? K2_RING + p * 1024u : OOB, lds0 + (p < 41u ? p * 1024u : K2_DUMP));
      }
    } else if (gi == 6) {
      WAIT_BAR(0);
      aK1 -= K2_RING; aK2 -= K2_RING; aV -= K2_RING;
    }
    SBAR();
    STEP(S1, S0, S3, 0); STEP(S2, S1, S0, 1); STEP(S3, S2, S1, 2); STEP(S0, S3, S2, 3);
    STEP(S1, S0, S3, 4); STEP(S2, S1, S0, 5); STEP(S3, S2, S1, 6); STEP(S0, S3, S2, 7);
    aK1 += 8 * REC; aK2 += 8 * REC; aV += 8 * REC;
  }
  O = smf(vlo, lo8(S3), O, idx);
  O = smf(vhi, hi8(S3), O, idx);

  {
    float t = 0.f;
#pragma unroll
    for (int r = 0; r < 12; ++r) t = __builtin_fmaf(O[r], 0.f, t);
    const bool bad = !(t == 0.f) || !(O[12] < 1.0e30f);
    if (__builtin_expect(__any(bad), 0)) {
      u32x4 qz = q2; qz[2] = 0u;
      float m = -INFINITY;
      for (int rec = 0; rec < 64; ++rec) {
        const u32x4 a1 = *(const u32x4*)(kvim + rec * REC + lane * 16);
        const u32x2 a2 = *(const u32x2*)(kvim + rec * REC + 1024 + r32 * 8);
        f32x16 s = mfma32(a1, q1, zero16);
        s = mfma32((u32x4){a2[0], a2[1], 0u, 0u}, qz, s);
        m = fmaxf(m, max16(s));
      }
      m = xhalf_max(m);
      O = zero16;
      const u32 av = 1280u + (u32)hh * 336u + (u32)(r32 < 20 ? r32 : 20) * 16u;
      for (int t2 = 0; t2 < 64; ++t2) {
        const int rec = (t2 + 62) & 63;
        const u32x4 a1 = *(const u32x4*)(kvim + rec * REC + lane * 16);
        const u32x2 a2 = *(const u32x2*)(kvim + rec * REC + 1024 + r32 * 8);
        const u32x4 b1 = *(const u32x4*)(kvim + t2 * REC + av);
        const u32x4 b2 = *(const u32x4*)(kvim + t2 * REC + av + 672);
        f32x16 s = mfma32(a1, q1, zero16);
        s = mfma32((u32x4){a2[0], a2[1], 0u, 0u}, qz, s);
#pragma unroll
        for (int r = 0; r < 16; ++r) s[r] = __builtin_amdgcn_exp2f(s[r] - m);
        O = smf(b1, lo8(s), O, idx);
        O = smf(b2, hi8(s), O, idx);
      }
    }
  }

  if (tid < 210) {
    const f32x4* src = (const f32x4*)(ws + WS_KVP) + (size_t)head * 16 * 210 + tid;
    f32x4 a = src[0];
#pragma unroll
    for (int p = 1; p < 16; ++p) a += src[p * 210];
    const int d = tid / 10, f4 = tid - d * 10;
    *(LDSAS u32x2*)(lds + K2_KVT + d * 96 + f4 * 8) = (u32x2){pk2(a[0], a[1]), pk2(a[2], a[3])};
  } else if (tid >= 256 && tid < 256 + 21) {
    *(LDSAS u32x4*)(lds + K2_KVT + (tid - 256) * 96 + 80) = (u32x4){0u, 0u, 0u, 0u};
  }
  const char* prow = ws + WS_PHIQ + ((size_t)head * SEQ + tok) * 80;
  const u32x4 p0 = *(const u32x4*)(prow + hh * 16), p1 = *(const u32x4*)(prow + 32 + hh * 16);
  u32x4 p2 = *(const u32x4*)(prow + 64);
  if (hh) p2 = (u32x4){0u, 0u, 0u, 0u};
  const float gt = ((const float*)(ws + WS_GATE))[tok];
  __syncthreads();
  ldsp ak = lds + K2_KVT + (u32)(r32 < 20 ? r32 : 20) * 96u + (u32)hh * 16u;
  f32x16 N = mfma32(ld128(ak), p0, zero16);
  N = mfma32(ld128(ak + 32), p1, N);
  N = mfma32(ld128(ak + 64), p2, N);
  const float sa = gt * __builtin_amdgcn_rcpf(N[12] + 1e-6f), sb = (1.f - gt) * __builtin_amdgcn_rcpf(O[12]);
  const u32x4 ua = {pk2(N[0] * sa, N[1] * sa), pk2(N[2] * sa, N[3] * sa), pk2(N[4] * sa, N[5] * sa), pk2(N[6] * sa, N[7] * sa)};
  const u32x4 ub = {pk2(O[0] * sb, O[1] * sb), pk2(O[2] * sb, O[3] * sb), pk2(O[4] * sb, O[5] * sb), pk2(O[6] * sb, O[7] * sb)};
  const u32x4 um = {pk2(N[8] * sa, N[9] * sa), pk2(N[10] * sa, N[11] * sa), pk2(O[8] * sb, O[9] * sb), pk2(O[10] * sb, O[11] * sb)};
  char* obase = ws + WS_OFR + (size_t)(tok >> 4) * 40960 + (tok & 15) * 16 + head * 5 * 256;
  *(u32x4*)(obase + (hh ? 3 : 0) * 256) = ua;
  *(u32x4*)(obase + (hh ? 4 : 2) * 256) = ub;
  if (!hh) *(u32x4*)(obase + 256) = um;
}

constexpr u32 K3_SLOT = 49152, K3_W = 20480;

__global__ void __launch_bounds__(512, 2)
k3_out(const char* __restrict__ awo, const char* __restrict__ bwo, const char* __restrict__ ws, char* __restrict__ out) {
  extern __shared__ __attribute__((aligned(16))) char smem[];
  ldsp lds = (ldsp)smem;
  const u32 lds0 = (u32)(uintptr_t)lds;
  const int tid = threadIdx.x, lane = tid & 63;
  const int w = __builtin_amdgcn_readfirstlane(tid >> 6);
  const int xcd = blockIdx.x & 7, jb = blockIdx.x >> 3;
  const int t64 = xcd * 4 + (jb & 3), cgp = jb >> 2;

  if (w >= 4) {
    const int lw = w - 4;
    const char* src[12];
#pragma unroll
    for (int j = 0; j < 12; ++j) {
      const int p = lw + 4 * j;
      if (p < 20) {
        const int n = p / 5, kk = p - n * 5;
        src[j] = ws + WS_OFR + ((size_t)(4 * t64 + n) * 40 + kk) * 1024 + lane * 16;
      } else {
        const int pp = p < 45 ? p - 20 : 0;
        const int c = pp * 64 + lane, rho = c / 10, cc = c - rho * 10;
        const int U = 4 * (rho >> 5) + ((rho >> 2) & 3), u = (rho >> 4) & 1, q = rho & 3;
        const int hl = U / 5, un = U - hl * 5;
        const bool isB = (un == 2) || (un == 4) || (un == 1 && u == 1);
        const int d = (un == 1) ? 16 + q : (un >= 3 ? 4 : 0) + 8 * u + q;
        src[j] = (isB ? bwo : awo) + (size_t)(hl * 20 + d) * 1280 + cgp * 160 + cc * 16;
      }
    }
    auto issue = [&](int slot) {
#pragma unroll
      for (int j = 0; j < 12; ++j) {
        dma_glb(src[j], lds0 + (u32)slot * K3_SLOT + (u32)(lw + 4 * j) * 1024u);
        src[j] += (j < 5) ? 5120 : 102400;
      }
    };
    issue(0); issue(1);
    int slot = 2;
    for (int s = 0; s < 8; ++s) {
      if (s < 7) WAIT_BAR(12); else WAIT_BAR(0);
      if (s < 6) { issue(slot); slot = slot == 2 ? 0 : slot + 1; }
    }
  } else {
    const int g = lane >> 4, i16 = lane & 15;
    const u32 aW = K3_W + (u32)(4 * g + (i16 >> 2)) * 160u + (u32)(i16 & 3) * 40u;
    const u32 aO = (u32)w * 5120u + (u32)lane * 16u;
    f32x4 acc[5];
#pragma unroll
    for (int T = 0; T < 5; ++T) acc[T] = (f32x4){0.f, 0.f, 0.f, 0.f};
    int slot = 0;
    for (int s = 0; s < 8; ++s) {
      WAIT_BAR(0);
      ldsp sb = lds + (u32)slot * K3_SLOT;
      slot = slot == 2 ? 0 : slot + 1;
#pragma unroll
      for (int kk = 0; kk < 5; ++kk) {
        u32x4 of = ld128(sb + aO + kk * 1024);
#pragma unroll
        for (int T = 0; T < 5; ++T) {
          u32x4 wf = trd2(sb + aW + kk * 5120 + T * 8, sb + aW + kk * 5120 + 2560 + T * 8);
          acc[T] = mfma16(wf, of, acc[T]);
        }
      }
    }
    u32x2* dst = (u32x2*)(out + (size_t)(64 * t64 + 16 * w + i16) * 1280 + cgp * 160 + g * 40);
#pragma unroll
    for (int T = 0; T < 5; ++T) dst[T] = (u32x2){pk2(acc[T][0], acc[T][1]), pk2(acc[T][2], acc[T][3])};
  }
}

extern "C" void kernel_launch(void* const* d_in, const int* in_sizes, int n_in, void* d_out, int out_size, void* d_ws,
                              size_t ws_size, hipStream_t stream) {
  (void)in_sizes; (void)n_in; (void)out_size; (void)ws_size;
  (void)hipFuncSetAttribute((const void*)k1_proj, hipFuncAttributeMaxDynamicSharedMemorySize, 4 * K1_SLOT);
  (void)hipFuncSetAttribute((const void*)k2_attn, hipFuncAttributeMaxDynamicSharedMemorySize, K2_LDS);
  (void)hipFuncSetAttribute((const void*)k3_out, hipFuncAttributeMaxDynamicSharedMemorySize, 3 * K3_SLOT);
  char* ws = (char*)d_ws;
  k1_proj<<<256, 512, 4 * K1_SLOT, stream>>>(d_in[0], d_in[1], d_in[2], d_in[5], d_in[6], d_in[7], d_in[9], ws);
  k2_attn<<<256, 512, K2_LDS, stream>>>(ws);
  k3_out<<<256, 512, 3 * K3_SLOT, stream>>>((const char*)d_in[3], (const char*)d_in[8], ws, (char*)d_out);
}
```

```cpp
#include <hip/hip_runtime.h>
#include <hip/hip_bf16.h>
#include <stdint.h>

typedef unsigned int u32;
typedef u32 u32x2 __attribute__((ext_vector_type(2)));
typedef u32 u32x4 __attribute__((ext_vector_type(4)));
typedef float f32x2 __attribute__((ext_vector_type(2)));
typedef float f32x4 __attribute__((ext_vector_type(4)));
typedef float f32x8 __attribute__((ext_vector_type(8)));
typedef float f32x16 __attribute__((ext_vector_type(16)));
typedef __bf16 bf16x2 __attribute__((ext_vector_type(2)));
typedef __bf16 bf16x8 __attribute__((ext_vector_type(8)));
typedef __bf16 bf16x16 __attribute__((ext_vector_type(16)));
typedef short s16x4 __attribute__((ext_vector_type(4)));
#define LDSAS __attribute__((address_space(3)))
typedef LDSAS char* ldsp;

constexpr int SEQ = 2048, DM = 640, NH = 32, DH = 20;
constexpr float LOG2E = 1.4426950408889634f;
constexpr float QSCALE = 1.4426950408889634f * 0.22360679774997896f;

constexpr u32 REC = 2688;
constexpr size_t WS_QB   = 0;
constexpr size_t WS_PHIQ = WS_QB + 2621440 + 256;
constexpr size_t WS_KVIM = WS_PHIQ + 5242880 + 256;
constexpr size_t WS_KVP  = WS_KVIM + 64 * REC + 256;
constexpr size_t WS_GATE = WS_KVP + 32 * 16 * 840 * 4;
constexpr size_t WS_OFR  = WS_GATE + 8192;
constexpr size_t WS_WOF  = WS_OFR + 5242880;
constexpr u32 OOB = 0x40000000u;
#define STAMP_DECL
#define STAMP(i)
#define RSTAMP(i)
#define STAMP_FLUSH(kern)

#define SBAR() __builtin_amdgcn_sched_barrier(0)
#define PIN(x) asm volatile("" : "+v"(x))
#define LBAR() asm volatile("s_waitcnt lgkmcnt(0)\n\ts_barrier" ::: "memory")
#define WAIT_BAR(N) asm volatile("s_waitcnt vmcnt(" #N ") lgkmcnt(0)\n\ts_barrier" ::: "memory")

__device__ __forceinline__ u32 pk2(float a, float b) {
  f32x2 v = {a, b};
  return __builtin_bit_cast(u32, __builtin_convertvector(v, bf16x2));
}
__device__ __forceinline__ f32x4 mfma16(u32x4 a, u32x4 b, f32x4 c) {
  return __builtin_amdgcn_mfma_f32_16x16x32_bf16(__builtin_bit_cast(bf16x8, a), __builtin_bit_cast(bf16x8, b), c, 0, 0, 0);
}
__device__ __forceinline__ f32x16 mfma32(u32x4 a, u32x4 b, f32x16 c) {
  return __builtin_amdgcn_mfma_f32_32x32x16_bf16(__builtin_bit_cast(bf16x8, a), __builtin_bit_cast(bf16x8, b), c, 0, 0, 0);
}
typedef short s16x4b __attribute__((ext_vector_type(4)));
__device__ __forceinline__ f32x16 mfma32k8(u32x2 a, u32x2 b, f32x16 c) {
  return __builtin_amdgcn_mfma_f32_32x32x8bf16_1k(__builtin_bit_cast(s16x4b, a), __builtin_bit_cast(s16x4b, b), c, 0, 0, 0);
}
__device__ __forceinline__ f32x16 smf(u32x4 a, f32x8 b, f32x16 c, int idx) {
  return __builtin_amdgcn_smfmac_f32_32x32x32_bf16(__builtin_bit_cast(bf16x8, a), __builtin_bit_cast(bf16x16, b), c, idx, 0, 0);
}
__device__ __forceinline__ u32x2 trd(ldsp p) {
  return __builtin_bit_cast(u32x2, __builtin_amdgcn_ds_read_tr16_b64_v4i16((LDSAS s16x4*)p));
}
__device__ __forceinline__ u32x4 trd2(ldsp p0, ldsp p1) {
  u32x2 a = trd(p0), b = trd(p1);
  return (u32x4){a[0], a[1], b[0], b[1]};
}
__device__ __forceinline__ u32x4 ld128(ldsp p) { return *(LDSAS u32x4*)p; }
__device__ __forceinline__ u32x2 ld64(ldsp p) { return *(LDSAS u32x2*)p; }

__device__ __forceinline__ u32x4 mkrsrc(const void* p, u32 bytes) {
  uint64_t a = (uint64_t)p;
  return (u32x4){(u32)a, (u32)(a >> 32) & 0xffffu, bytes, 0x00020000u};
}
__device__ __forceinline__ void dma_buf(u32x4 rs, u32 voff, u32 soff, u32 ldsaddr) {
  u32 keep;
  asm volatile("s_mov_b32 %0, m0\n\ts_mov_b32 m0, %4\n\ts_nop 0\n\tbuffer_load_dwordx4 %1, %2, %3 offen lds\n\ts_mov_b32 m0, %0"
               : "=&s"(keep) : "v"(voff), "s"(rs), "s"(soff), "s"(ldsaddr) : "memory");
}
__device__ __forceinline__ void dma_glb(const void* g, u32 ldsaddr) {
  u32 keep;
  asm volatile("s_mov_b32 %0, m0\n\ts_mov_b32 m0, %2\n\ts_nop 0\n\tglobal_load_lds_dwordx4 %1, off\n\ts_mov_b32 m0, %0"
               : "=&s"(keep) : "v"(g), "s"(ldsaddr) : "memory");
}

#define ATTN_LOOP_ASM \
  "s_nop 7\n\t" \
  "s_mov_b32 %[cnt], 0\n\t" \
  "1:\n\t" \
  "s_bitcmp1_b32 %[mask], %[cnt]\n\t" \
  "s_cbranch_scc0 2f\n\t" \
  "s_barrier\n\t" \
  "2:\n\t" \
  "v_mfma_f32_32x32x16_bf16 v[48:63], v[108:111], v[100:103], 0\n\t" \
  "ds_read_b128 v[108:111], v124 offset:0\n\t" \
  "v_exp_f32 v32, v32\n\t" \
  "v_exp_f32 v33, v33\n\t" \
  "v_exp_f32 v34, v34\n\t" \
  "v_exp_f32 v35, v35\n\t" \
  "v_mfma_f32_32x32x8_bf16 v[48:63], v[112:113], v[106:107], v[48:63]\n\t" \
  "ds_read_b32 v112, v125 offset:0\n\t" \
  "v_exp_f32 v36, v36\n\t" \
  "v_exp_f32 v37, v37\n\t" \
  "v_exp_f32 v38, v38\n\t" \
  "v_exp_f32 v39, v39\n\t" \
  "v_smfmac_f32_32x32x32_bf16 v[16:31], v[116:119], v[80:87], v114\n\t" \
  "ds_read_b128 v[116:119], v126 offset:0\n\t" \
  "v_exp_f32 v40, v40\n\t" \
  "v_exp_f32 v41, v41\n\t" \
  "v_exp_f32 v42, v42\n\t" \
  "v_exp_f32 v43, v43\n\t" \
  "v_smfmac_f32_32x32x32_bf16 v[16:31], v[120:123], v[88:95], v114\n\t" \
  "ds_read_b128 v[120:123], v126 offset:704\n\t" \
  "v_exp_f32 v44, v44\n\t" \
  "v_exp_f32 v45, v45\n\t" \
  "v_exp_f32 v46, v46\n\t" \
  "v_exp_f32 v47, v47\n\t" \
  "s_waitcnt lgkmcnt(2)\n\t" \
  "v_mfma_f32_32x32x16_bf16 v[64:79], v[108:111], v[96:99], 0\n\t" \
  "v_exp_f32 v48, v48\n\t" \
  "v_exp_f32 v49, v49\n\t" \
  "v_exp_f32 v50, v50\n\t" \
  "v_exp_f32 v51, v51\n\t" \
  "v_mfma_f32_32x32x8_bf16 v[64:79], v[112:113], v[104:105], v[64:79]\n\t" \
  "v_exp_f32 v52, v52\n\t" \
  "v_exp_f32 v53, v53\n\t" \
  "v_exp_f32 v54, v54\n\t" \
  "v_exp_f32 v55, v55\n\t" \
  "s_waitcnt lgkmcnt(0)\n\t" \
  "v_smfmac_f32_32x32x32_bf16 v[0:15], v[116:119], v[32:39], v114\n\t" \
  "v_exp_f32 v56, v56\n\t" \
  "v_exp_f32 v57, v57\n\t" \
  "v_exp_f32 v58, v58\n\t" \
  "v_exp_f32 v59, v59\n\t" \
  "v_smfmac_f32_32x32x32_bf16 v[0:15], v[120:123], v[40:47], v114\n\t" \
  "v_exp_f32 v60, v60\n\t" \
  "v_exp_f32 v61, v61\n\t" \
  "v_exp_f32 v62, v62\n\t" \
  "v_exp_f32 v63, v63\n\t" \
  "v_mfma_f32_32x32x16_bf16 v[80:95], v[108:111], v[100:103], 0\n\t" \
  "ds_read_b128 v[108:111], v124 offset:5376\n\t" \
  "v_exp_f32 v64, v64\n\t" \
  "v_exp_f32 v65, v65\n\t" \
  "v_exp_f32 v66, v66\n\t" \
  "v_exp_f32 v67, v67\n\t" \
  "v_mfma_f32_32x32x8_bf16 v[80:95], v[112:113], v[106:107], v[80:95]\n\t" \
  "ds_read_b32 v112, v125 offset:5376\n\t" \
  "v_exp_f32 v68, v68\n\t" \
  "v_exp_f32 v69, v69\n\t" \
  "v_exp_f32 v70, v70\n\t" \
  "v_exp_f32 v71, v71\n\t" \
  "v_smfmac_f32_32x32x32_bf16 v[16:31], v[116:119], v[48:55], v114\n\t" \
  "ds_read_b128 v[116:119], v126 offset:5376\n\t" \
  "v_exp_f32 v72, v72\n\t" \
  "v_exp_f32 v73, v73\n\t" \
  "v_exp_f32 v74, v74\n\t" \
  "v_exp_f32 v75, v75\n\t" \
  "v_smfmac_f32_32x32x32_bf16 v[16:31], v[120:123], v[56:63], v114\n\t" \
  "ds_read_b128 v[120:123], v126 offset:6080\n\t" \
  "v_exp_f32 v76, v76\n\t" \
  "v_exp_f32 v77, v77\n\t" \
  "v_exp_f32 v78, v78\n\t" \
  "v_exp_f32 v79, v79\n\t" \
  "s_waitcnt lgkmcnt(2)\n\t" \
  "v_mfma_f32_32x32x16_bf16 v[32:47], v[108:111], v[96:99], 0\n\t" \
  "v_exp_f32 v80, v80\n\t" \
  "v_exp_f32 v81, v81\n\t" \
  "v_exp_f32 v82, v82\n\t" \
  "v_exp_f32 v83, v83\n\t" \
  "v_mfma_f32_32x32x8_bf16 v[32:47], v[112:113], v[104:105], v[32:47]\n\t" \
  "v_exp_f32 v84, v84\n\t" \
  "v_exp_f32 v85, v85\n\t" \
  "v_exp_f32 v86, v86\n\t" \
  "v_exp_f32 v87, v87\n\t" \
  "s_waitcnt lgkmcnt(0)\n\t" \
  "v_smfmac_f32_32x32x32_bf16 v[0:15], v[116:119], v[64:71], v114\n\t" \
  "v_exp_f32 v88, v88\n\t" \
  "v_exp_f32 v89, v89\n\t" \
  "v_exp_f32 v90, v90\n\t" \
  "v_exp_f32 v91, v91\n\t" \
  "v_smfmac_f32_32x32x32_bf16 v[0:15], v[120:123], v[72:79], v114\n\t" \
  "v_exp_f32 v92, v92\n\t" \
  "v_exp_f32 v93, v93\n\t" \
  "v_exp_f32 v94, v94\n\t" \
  "v_exp_f32 v95, v95\n\t" \
  "v_mfma_f32_32x32x16_bf16 v[48:63], v[108:111], v[100:103], 0\n\t" \
  "ds_read_b128 v[108:111], v124 offset:10752\n\t" \
  "v_exp_f32 v32, v32\n\t" \
  "v_exp_f32 v33, v33\n\t" \
  "v_exp_f32 v34, v34\n\t" \
  "v_exp_f32 v35, v35\n\t" \
  "v_mfma_f32_32x32x8_bf16 v[48:63], v[112:113], v[106:107], v[48:63]\n\t" \
  "ds_read_b32 v112, v125 offset:10752\n\t" \
  "v_exp_f32 v36, v36\n\t" \
  "v_exp_f32 v37, v37\n\t" \
  "v_exp_f32 v38, v38\n\t" \
  "v_exp_f32 v39, v39\n\t" \
  "v_smfmac_f32_32x32x32_bf16 v[16:31], v[116:119], v[80:87], v114\n\t" \
  "ds_read_b128 v[116:119], v126 offset:10752\n\t" \
  "v_exp_f32 v40, v40\n\t" \
  "v_exp_f32 v41, v41\n\t" \
  "v_exp_f32 v42, v42\n\t" \
  "v_exp_f32 v43, v43\n\t" \
  "v_smfmac_f32_32x32x32_bf16 v[16:31], v[120:123], v[88:95], v114\n\t" \
  "ds_read_b128 v[120:123], v126 offset:11456\n\t" \
  "v_exp_f32 v44, v44\n\t" \
  "v_exp_f32 v45, v45\n\t" \
  "v_exp_f32 v46, v46\n\t" \
  "v_exp_f32 v47, v47\n\t" \
  "s_waitcnt lgkmcnt(2)\n\t" \
  "v_mfma_f32_32x32x16_bf16 v[64:79], v[108:111], v[96:99], 0\n\t" \
  "v_exp_f32 v48, v48\n\t" \
  "v_exp_f32 v49, v49\n\t" \
  "v_exp_f32 v50, v50\n\t" \
  "v_exp_f32 v51, v51\n\t" \
  "v_mfma_f32_32x32x8_bf16 v[64:79], v[112:113], v[104:105], v[64:79]\n\t" \
  "v_exp_f32 v52, v52\n\t" \
  "v_exp_f32 v53, v53\n\t" \
  "v_exp_f32 v54, v54\n\t" \
  "v_exp_f32 v55, v55\n\t" \
  "s_waitcnt lgkmcnt(0)\n\t" \
  "v_smfmac_f32_32x32x32_bf16 v[0:15], v[116:119], v[32:39], v114\n\t" \
  "v_exp_f32 v56, v56\n\t" \
  "v_exp_f32 v57, v57\n\t" \
  "v_exp_f32 v58, v58\n\t" \
  "v_exp_f32 v59, v59\n\t" \
  "v_smfmac_f32_32x32x32_bf16 v[0:15], v[120:123], v[40:47], v114\n\t" \
  "v_exp_f32 v60, v60\n\t" \
  "v_exp_f32 v61, v61\n\t" \
  "v_exp_f32 v62, v62\n\t" \
  "v_exp_f32 v63, v63\n\t" \
  "v_mfma_f32_32x32x16_bf16 v[80:95], v[108:111], v[100:103], 0\n\t" \
  "ds_read_b128 v[108:111], v124 offset:16128\n\t" \
  "v_exp_f32 v64, v64\n\t" \
  "v_exp_f32 v65, v65\n\t" \
  "v_exp_f32 v66, v66\n\t" \
  "v_exp_f32 v67, v67\n\t" \
  "v_mfma_f32_32x32x8_bf16 v[80:95], v[112:113], v[106:107], v[80:95]\n\t" \
  "ds_read_b32 v112, v125 offset:16128\n\t" \
  "v_exp_f32 v68, v68\n\t" \
  "v_exp_f32 v69, v69\n\t" \
  "v_exp_f32 v70, v70\n\t" \
  "v_exp_f32 v71, v71\n\t" \
  "v_smfmac_f32_32x32x32_bf16 v[16:31], v[116:119], v[48:55], v114\n\t" \
  "ds_read_b128 v[116:119], v126 offset:16128\n\t" \
  "v_exp_f32 v72, v72\n\t" \
  "v_exp_f32 v73, v73\n\t" \
  "v_exp_f32 v74, v74\n\t" \
  "v_exp_f32 v75, v75\n\t" \
  "v_smfmac_f32_32x32x32_bf16 v[16:31], v[120:123], v[56:63], v114\n\t" \
  "ds_read_b128 v[120:123], v126 offset:16832\n\t" \
  "v_exp_f32 v76, v76\n\t" \
  "v_exp_f32 v77, v77\n\t" \
  "v_exp_f32 v78, v78\n\t" \
  "v_exp_f32 v79, v79\n\t" \
  "s_waitcnt lgkmcnt(2)\n\t" \
  "v_mfma_f32_32x32x16_bf16 v[32:47], v[108:111], v[96:99], 0\n\t" \
  "s_cmp_eq_u32 %[cnt], 5\n\t" \
  "s_cselect_b32 %[inc], %[wrap], %[stg]\n\t" \
  "v_exp_f32 v80, v80\n\t" \
  "v_exp_f32 v81, v81\n\t" \
  "v_exp_f32 v82, v82\n\t" \
  "v_exp_f32 v83, v83\n\t" \
  "v_mfma_f32_32x32x8_bf16 v[32:47], v[112:113], v[104:105], v[32:47]\n\t" \
  "v_add_u32 v124, %[inc], v124\n\t" \
  "v_add_u32 v125, %[inc], v125\n\t" \
  "v_add_u32 v126, %[inc], v126\n\t" \
  "v_exp_f32 v84, v84\n\t" \
  "v_exp_f32 v85, v85\n\t" \
  "v_exp_f32 v86, v86\n\t" \
  "v_exp_f32 v87, v87\n\t" \
  "s_waitcnt lgkmcnt(0)\n\t" \
  "v_smfmac_f32_32x32x32_bf16 v[0:15], v[116:119], v[64:71], v114\n\t" \
  "s_add_u32 %[cnt], %[cnt], 1\n\t" \
  "s_cmp_lt_u32 %[cnt], 8\n\t" \
  "v_exp_f32 v88, v88\n\t" \
  "v_exp_f32 v89, v89\n\t" \
  "v_exp_f32 v90, v90\n\t" \
  "v_exp_f32 v91, v91\n\t" \
  "v_smfmac_f32_32x32x32_bf16 v[0:15], v[120:123], v[72:79], v114\n\t" \
  "v_exp_f32 v92, v92\n\t" \
  "v_exp_f32 v93, v93\n\t" \
  "v_exp_f32 v94, v94\n\t" \
  "v_exp_f32 v95, v95\n\t" \
  "s_cbranch_scc1 1b\n\t" \
  "s_nop 1\n\t" \
  "v_smfmac_f32_32x32x32_bf16 v[16:31], v[116:119], v[80:87], v114\n\t" \
  "v_smfmac_f32_32x32x32_bf16 v[16:31], v[120:123], v[88:95], v114\n\t" \
  "s_nop 15\n\t"
#define K1_ROT ((tt + 3 * (jb >> 3)) % 10)
constexpr u32 K1_SLOT = 40960, K1_W = 16384, K1_EX = 36864;
constexpr u32 K1_WPHI = 2 * K1_SLOT;
constexpr u32 K1_PHK = 0, K1_PHK_P = 336, K1_VI = 45056, K1_VI_P = 208;

__device__ __forceinline__ float bflo(u32 v) { return __uint_as_float(v << 16); }
__device__ __forceinline__ float bfhi(u32 v) { return __uint_as_float(v & 0xffff0000u); }

__global__ void __launch_bounds__(768, 3)
k1_proj(const char* __restrict__ x, const char* __restrict__ wqkv, const char* __restrict__ wphi, const char* __restrict__ wq,
        const char* __restrict__ wk, const char* __restrict__ wv, const char* __restrict__ gw, const char* __restrict__ awo,
        const char* __restrict__ bwo, char* __restrict__ ws) {
  extern __shared__ __attribute__((aligned(16))) char smem[];
  ldsp lds = (ldsp)smem;
  const u32 lds0 = (u32)(uintptr_t)lds;
  const int tid = threadIdx.x, lane = tid & 63;
  const int w = __builtin_amdgcn_readfirstlane(tid >> 6);
  STAMP_DECL; RSTAMP(14); STAMP(0);
  const int bid = blockIdx.x, xcd = bid & 7, jb = bid >> 3;
  const int tt = (xcd & 1) * 8 + (jb & 7), combo = (xcd >> 1) * 4 + (jb >> 3), type = combo & 1, hg = combo >> 1;

  if (w >= 8) {
    __builtin_amdgcn_s_setprio(3);
    const int lw = w - 8, g1 = lw >> 1, hb = lw & 1;
    const u32x4 rsX = mkrsrc(x - 4096, SEQ * DM * 2 + 4096);
    const bool useq = g1 && !type;
    const u32x4 rsW = useq ? mkrsrc(wq - 4096, DM * DM * 2 + 4096) : mkrsrc(wqkv - 4096, DM * 3 * DM * 2 + 4096);
    const u32 pitch = useq ? 1280u : 3840u;
    const u32 col = (type ? (g1 ? 1280u : 640u) : 0u) + 80u * hg;
    const u32x4 rsE = mkrsrc(type ? wv : wk, DM * DH * 2);
    u32 vx[4], vw[5];
    const u32 vx0 = 4096u + (u32)(lane >> 3) * 1280u + (u32)(((lane & 7) ^ (lane >> 4)) * 16);
#pragma unroll
    for (int i = 0; i < 4; ++i) vx[i] = (vx0 ^ (u32)((i & 1) * 64)) + (u32)i * 9216u;
#pragma unroll
    for (int j = 0; j < 5; ++j) {
      const u32 c = 64u * j + lane, rho = (c * 205u) >> 11, cc = c - rho * 10u;
      const u32 k = 8u * ((rho >> 2) & 3u) + 4u * (rho >> 4) + (rho & 3u);
      vw[j] = 4096u + k * pitch + cc * 16u - (u32)(j & 3) * 1024u;
    }
    const u32 be = (u32)(lw * 64 + lane) * 16u;
    const u32 ve = (hg < 2 && be < 2560u) ? be : OOB;
    const u32 sx0 = (u32)(128 * tt + 32 * lw) * 1280u, sw0 = 32u * hb * pitch + col * 2u;
    int ks = K1_ROT;
    auto issue = [&](int s) {
      const u32 base = lds0 + (u32)(s & 3) * K1_SLOT;
      u32 keep;
      asm volatile(
          "s_mov_b32 %0, m0\n\ts_mov_b32 m0, %[mx]\n\ts_nop 0\n\t"
          "buffer_load_dwordx4 %[x0], %[rx], %[sx] offen lds\n\t"
          "buffer_load_dwordx4 %[x1], %[rx], %[sx] offen offset:1024 lds\n\t"
          "buffer_load_dwordx4 %[x2], %[rx], %[sx] offen offset:2048 lds\n\t"
          "buffer_load_dwordx4 %[x3], %[rx], %[sx] offen offset:3072 lds\n\t"
          "s_mov_b32 m0, %[mw]\n\ts_nop 0\n\t"
          "buffer_load_dwordx4 %[w0], %[rw], %[sw] offen lds\n\t"
          "buffer_load_dwordx4 %[w1], %[rw], %[sw] offen offset:1024 lds\n\t"
          "buffer_load_dwordx4 %[w2], %[rw], %[sw] offen offset:2048 lds\n\t"
          "buffer_load_dwordx4 %[w3], %[rw], %[sw] offen offset:3072 lds\n\t"
          "s_add_u32 m0, %[mw], 4096\n\ts_nop 0\n\t"
          "buffer_load_dwordx4 %[w4], %[rw], %[sw] offen lds\n\t"
          "s_mov_b32 m0, %[me]\n\ts_nop 0\n\t"
          "buffer_load_dwordx4 %[e0], %[re], %[se] offen lds\n\t"
          "s_mov_b32 m0, %0"
          : "=&s"(keep)
          : [mx] "s"(base + (u32)lw * 4096u), [mw] "s"(base + K1_W + (u32)lw * 5120u), [me] "s"(base + K1_EX + (u32)lw * 1024u),
            [rx] "s"(rsX), [rw] "s"(rsW), [re] "s"(rsE), [sx] "s"(sx0 + (u32)ks * 128u), [sw] "s"(sw0 + (u32)ks * 64u * pitch), [se] "s"((u32)ks * 2560u),
            [x0] "v"(vx[0]), [x1] "v"(vx[1]), [x2] "v"(vx[2]), [x3] "v"(vx[3]), [w0] "v"(vw[0]), [w1] "v"(vw[1]), [w2] "v"(vw[2]), [w3] "v"(vw[3]),
            [w4] "v"(vw[4]), [e0] "v"(ve)
          : "memory", "scc");
      ks = ks == 9 ? 0 : ks + 1;
    };
    asm volatile("s_nop 4" ::: "memory");
    issue(0); issue(1);
    STAMP(1);
    WAIT_BAR(10);
    issue(2);
#pragma unroll 1
    for (int s = 1; s < 8; ++s) {
      issue(s + 2);
      WAIT_BAR(20);
    }
    u32x4 r0, r1;
    u32 xg[2][5], gg[5];
    dma_buf(mkrsrc(wphi, NH * 400 * 2), be < 3200u ? be : OOB, (u32)hg * 3200u, lds0 + K1_WPHI + (u32)lw * 1024u);
    WAIT_BAR(11);
    WAIT_BAR(0);
    {
      const char* src[2];
#pragma unroll
      for (int i = 0; i < 2; ++i) {
        const int t = min(tid - 512 + 256 * i, 399);
        const int c = (bid & 3) * 400 + t, rho = c / 10, cc = c - rho * 10;
        const int U = 4 * (rho >> 5) + ((rho >> 2) & 3), u = (rho >> 4) & 1, q = rho & 3;
        const int hl = U / 5, un = U - hl * 5;
        const bool isB = (un == 2) || (un == 4) || (un == 1 && u == 1);
        const int d = (un == 1) ? 16 + q : (un >= 3 ? 4 : 0) + 8 * u + q;
        src[i] = (isB ? bwo : awo) + (size_t)(((bid >> 2) & 7) * 80 + hl * 20 + d) * 1280 + (bid >> 5) * 160 + cc * 16;
      }
      r0 = *(const u32x4*)src[0]; r1 = *(const u32x4*)src[1];
    }
    const int gtok = 128 * tt + 8 * combo + 2 * lw;
#pragma unroll
    for (int i = 0; i < 5; ++i) {
      gg[i] = *(const u32*)(gw + (i * 64 + lane) * 4);
      xg[0][i] = *(const u32*)(x + (size_t)gtok * 1280 + (i * 64 + lane) * 4);
      xg[1][i] = *(const u32*)(x + (size_t)gtok * 1280 + 1280 + (i * 64 + lane) * 4);
    }
    asm volatile("s_barrier" ::: "memory");
    char* wdst = ws + WS_WOF + (size_t)bid * 6400 + (tid - 512) * 16;
    *(u32x4*)wdst = r0;
    if (tid - 512 < 144) *(u32x4*)(wdst + 4096) = r1;
    float a0 = 0.f, a1 = 0.f;
#pragma unroll
    for (int i = 0; i < 5; ++i) {
      a0 = __builtin_fmaf(bflo(xg[0][i]), bflo(gg[i]), a0); a0 = __builtin_fmaf(bfhi(xg[0][i]), bfhi(gg[i]), a0);
      a1 = __builtin_fmaf(bflo(xg[1][i]), bflo(gg[i]), a1); a1 = __builtin_fmaf(bfhi(xg[1][i]), bfhi(gg[i]), a1);
    }
#pragma unroll
    for (int m = 32; m >= 1; m >>= 1) { a0 += __shfl_xor(a0, m); a1 += __shfl_xor(a1, m); }
    if (lane < 2) ((float*)(ws + WS_GATE))[gtok + lane] = __builtin_amdgcn_rcpf(1.f + __builtin_amdgcn_exp2f(-(lane ? a1 : a0) * LOG2E));
    STAMP(2); RSTAMP(15); STAMP_FLUSH(0);
    return;
  }

  const int cg = w >> 2, tq = w & 3, g = lane >> 4, i16 = lane & 15, nsel = hg & 1;
  const u32 aW = K1_W + (u32)cg * 10240u + (u32)(4 * g + (i16 >> 2)) * 160u + (u32)(i16 & 3) * 40u;
  const u32 aX = (u32)(32 * tq + 16 * nsel + i16) * 128u + (u32)((g ^ (i16 >> 1)) * 16);
  const u32 aE = K1_EX + (u32)(8 * g + (i16 >> 2)) * 40u + (u32)(i16 & 3) * 8u + (u32)cg * 32u;

  f32x4 acc[5][2];
#pragma unroll
  for (int T = 0; T < 5; ++T) { acc[T][0] = (f32x4){0.f, 0.f, 0.f, 0.f}; acc[T][1] = (f32x4){0.f, 0.f, 0.f, 0.f}; }
  f32x4 acce = {0.f, 0.f, 0.f, 0.f};
  const u32x4 z4 = {0u, 0u, 0u, 0u};
  u32x4 xa0, xa1, wa[5], ea, xb0 = z4, xb1 = z4, wb[5] = {z4, z4, z4, z4, z4}, eb = z4;
#define K1_READ(X0, X1, WF, EF, kk) do { \
    X0 = ld128(sb + (aX ^ ((kk) * 64))); X1 = ld128(sb + (aX ^ ((kk) * 64 + 2048))); \
    _Pragma("unroll") for (int T = 0; T < 5; ++T) WF[T] = trd2(sb + aW + (kk) * 5120 + T * 8, sb + aW + (kk) * 5120 + 2560 + T * 8); \
    EF = trd2(sb + aE + (kk) * 1280, sb + aE + (kk) * 1280 + 160); } while (0)
#define K1_MMA(X0, X1, WF, EF) do { \
    _Pragma("unroll") for (int T = 0; T < 5; ++T) { acc[T][0] = mfma16(WF[T], X0, acc[T][0]); acc[T][1] = mfma16(WF[T], X1, acc[T][1]); } \
    acce = mfma16(EF, X0, acce); } while (0)

#define K1_SCHED() do { \
    _Pragma("unroll") for (int i_ = 0; i_ < 7; ++i_) { __builtin_amdgcn_sched_group_barrier(0x8, 1, 0); __builtin_amdgcn_sched_group_barrier(0x100, 2, 0); } \
    __builtin_amdgcn_sched_group_barrier(0x8, 4, 0); SBAR(); } while (0)
#pragma unroll 1
  for (int s = 0; s < 10; ++s) {
    LBAR();
    if (s == 0) STAMP(2); if (s == 1) STAMP(3); if (s == 5) STAMP(4); if (s == 9) STAMP(5);
    ldsp sb = lds + (u32)(s & 3) * K1_SLOT;
    SBAR();
    K1_READ(xa0, xa1, wa, ea, 0);
    K1_MMA(xb0, xb1, wb, eb);
    K1_SCHED();
    K1_READ(xb0, xb1, wb, eb, 1);
    K1_MMA(xa0, xa1, wa, ea);
    K1_SCHED();
  }
  K1_MMA(xb0, xb1, wb, eb);
  STAMP(6);
  u32x4 bq0, bq1; u32x2 bq2;
  {
    ldsp xw = lds + 3 * K1_SLOT + (u32)tq * 2560u;
    if (cg == 0) {
      bq0 = (u32x4){pk2(acc[0][0][0], acc[0][0][1]), pk2(acc[0][0][2], acc[0][0][3]), pk2(acc[1][0][0], acc[1][0][1]), pk2(acc[1][0][2], acc[1][0][3])};
      bq1 = (u32x4){pk2(acc[2][0][0], acc[2][0][1]), pk2(acc[2][0][2], acc[2][0][3]), pk2(acc[3][0][0], acc[3][0][1]), pk2(acc[3][0][2], acc[3][0][3])};
      bq2 = (u32x2){pk2(acc[4][0][0], acc[4][0][1]), pk2(acc[4][0][2], acc[4][0][3])};
      *(LDSAS u32x4*)(xw + lane * 16) = (u32x4){pk2(acc[0][1][0], acc[0][1][1]), pk2(acc[0][1][2], acc[0][1][3]), pk2(acc[1][1][0], acc[1][1][1]), pk2(acc[1][1][2], acc[1][1][3])};
      *(LDSAS u32x4*)(xw + 1024 + lane * 16) = (u32x4){pk2(acc[2][1][0], acc[2][1][1]), pk2(acc[2][1][2], acc[2][1][3]), pk2(acc[3][1][0], acc[3][1][1]), pk2(acc[3][1][2], acc[3][1][3])};
      *(LDSAS u32x2*)(xw + 2048 + lane * 8) = (u32x2){pk2(acc[4][1][0], acc[4][1][1]), pk2(acc[4][1][2], acc[4][1][3])};
    }
    LBAR();
    if (cg == 1) { bq0 = ld128(xw + lane * 16); bq1 = ld128(xw + 1024 + lane * 16); bq2 = ld64(xw + 2048 + lane * 8); }
  }
  STAMP(7);
  const int head = 4 * hg + g;
  const u32 tile32 = (u32)(head * 64 + 4 * tt + tq);
  if (cg == 1) {
    const float sc = type ? 1.f : QSCALE;
#pragma unroll
    for (int n = 0; n < 2; ++n) {
      const int t32 = 16 * (n ^ nsel) + i16;
      u32 v[10];
#pragma unroll
      for (int T = 0; T < 5; ++T) { const f32x4 a = acc[T][n] * sc; v[2 * T] = pk2(a[0], a[1]); v[2 * T + 1] = pk2(a[2], a[3]); }
      if (type == 0) {
        char* dst = ws + WS_QB + (size_t)tile32 * 1280;
        *(u32x4*)(dst + t32 * 16) = (u32x4){v[0], v[1], v[2], v[3]};
        *(u32x4*)(dst + 512 + t32 * 16) = (u32x4){v[4], v[5], v[6], v[7]};
        *(u32x2*)(dst + 1024 + t32 * 8) = (u32x2){v[8], v[9]};
      } else {
        ldsp dst = lds + K1_VI + (u32)(32 * tq + t32) * K1_VI_P + (u32)g * 48u;
        *(LDSAS u32x4*)(dst) = (u32x4){v[0], v[1], v[2], v[3]};
        *(LDSAS u32x4*)(dst + 16) = (u32x4){v[4], v[5], v[6], v[7]};
        *(LDSAS u32x4*)(dst + 32) = (u32x4){v[8], v[9], 0x00003F80u, 0u};
      }
    }
  }
  {
    const u32 aP = K1_WPHI + (((i16 & 3) == g) ? (u32)g * 800u + (u32)(i16 >> 2) * 40u : 3200u);
    f32x4 pq[5];
#pragma unroll
    for (int T = 0; T < 5; ++T) {
      u32x4 a0 = trd2(lds + aP + T * 8, lds + aP + T * 8 + 160);
      u32x4 a1 = trd2(lds + aP + T * 8 + 320, lds + aP + T * 8 + 480);
      u32x2 a2h = trd(lds + aP + T * 8 + 640);
      f32x4 c = {0.f, 0.f, 0.f, 0.f};
      c = mfma16(a0, bq0, c);
      c = mfma16(a1, bq1, c);
      c = mfma16((u32x4){a2h[0], a2h[1], 0u, 0u}, (u32x4){bq2[0], bq2[1], 0u, 0u}, c);
      pq[T] = c;
    }
    float mx = fmaxf(pq[0][0], pq[0][1]), mn = fminf(pq[0][0], pq[0][1]);
    mx = fmaxf(fmaxf(mx, pq[0][2]), pq[0][3]); mn = fminf(fminf(mn, pq[0][2]), pq[0][3]);
#pragma unroll
    for (int T = 1; T < 5; ++T) {
      mx = fmaxf(fmaxf(mx, pq[T][0]), pq[T][1]); mx = fmaxf(fmaxf(mx, pq[T][2]), pq[T][3]);
      mn = fminf(fminf(mn, pq[T][0]), pq[T][1]); mn = fminf(fminf(mn, pq[T][2]), pq[T][3]);
    }
    const float cp = -mx * LOG2E, cn = mn * LOG2E;
    f32x4 ep[5], en[5];
    f32x4 sp = {0.f, 0.f, 0.f, 0.f}, sn = {0.f, 0.f, 0.f, 0.f};
#pragma unroll
    for (int T = 0; T < 5; ++T) {
      const f32x4 ap = pq[T] * LOG2E + cp, an = pq[T] * (-LOG2E) + cn;
#pragma unroll
      for (int i = 0; i < 4; ++i) { ep[T][i] = __builtin_amdgcn_exp2f(ap[i]); en[T][i] = __builtin_amdgcn_exp2f(an[i]); }
      sp += ep[T]; sn += en[T];
    }
    const float rp = __builtin_amdgcn_rcpf((sp[0] + sp[1]) + (sp[2] + sp[3])), rn = __builtin_amdgcn_rcpf((sn[0] + sn[1]) + (sn[2] + sn[3]));
    u32 ph[20];
#pragma unroll
    for (int T = 0; T < 5; ++T) {
      const f32x4 a = ep[T] * rp, b = en[T] * rn;
      ph[2 * T] = pk2(a[0], a[1]); ph[2 * T + 1] = pk2(a[2], a[3]);
      ph[10 + 2 * T] = pk2(b[0], b[1]); ph[11 + 2 * T] = pk2(b[2], b[3]);
    }
    const int t32 = 16 * (cg ^ nsel) + i16;
    if (type == 0) {
      char* dst = ws + WS_PHIQ + (size_t)tile32 * 2560 + t32 * 16;
#pragma unroll
      for (int c = 0; c < 5; ++c) *(u32x4*)(dst + c * 512) = (u32x4){ph[4 * c], ph[4 * c + 1], ph[4 * c + 2], ph[4 * c + 3]};
    } else {
      ldsp dst = lds + K1_PHK + (u32)(32 * tq + t32) * K1_PHK_P + (u32)g * 80u;
#pragma unroll
      for (int c = 0; c < 5; ++c) *(LDSAS u32x4*)(dst + 16 * c) = (u32x4){ph[4 * c], ph[4 * c + 1], ph[4 * c + 2], ph[4 * c + 3]};
    }
  }
  if (hg < 2) {
    const int k32 = 16 * nsel + i16, tile = 4 * tt + tq;
    const u32x2 ev = {pk2(acce[0], acce[1]), pk2(acce[2], acce[3])};
    if (type == 0) {
      char* rec = ws + WS_KVIM + ((tile + 62) & 63) * REC;
      if (cg == 0) *(u32x2*)(rec + (g >> 1) * 512 + k32 * 16 + (g & 1) * 8) = ev;
      else if (g == 0) { *(u32*)(rec + 1024 + k32 * 4) = ev[0]; *(u32*)(rec + 1152 + k32 * 4) = ev[1]; }
    } else {
      unsigned short* vp = (unsigned short*)(ws + WS_KVIM + tile * REC + 1280 + (k32 >> 3) * 352 + (16 * cg + 4 * g) * 16 + (k32 & 7) * 2);
      if (cg == 0 || g == 0) {
        vp[0] = (unsigned short)ev[0]; vp[8] = (unsigned short)(ev[0] >> 16); vp[16] = (unsigned short)ev[1]; vp[24] = (unsigned short)(ev[1] >> 16);
      } else if (g == 1) { vp[0] = 0x3F80; vp[8] = 0; }
    }
  }
  STAMP(8);
  if (type) {
    LBAR();
    STAMP(9);
    const int hl = w >> 1, dt = w & 1;
    const u32 trow = (u32)(8 * g + (i16 >> 2));
    ldsp pa = lds + K1_PHK + trow * K1_PHK_P + (u32)hl * 80u + (u32)(i16 & 3) * 8u;
    ldsp pb = lds + K1_VI + trow * K1_VI_P + (u32)hl * 48u + (u32)dt * 32u + (u32)(i16 & 3) * 8u;
    f32x4 kv[3];
#pragma unroll
    for (int ft = 0; ft < 3; ++ft) kv[ft] = (f32x4){0.f, 0.f, 0.f, 0.f};
#pragma unroll
    for (int ks = 0; ks < 4; ++ks) {
      u32x4 bv = trd2(pb + ks * 32 * K1_VI_P, pb + (ks * 32 + 4) * K1_VI_P);
#pragma unroll
      for (int ft = 0; ft < 3; ++ft) {
        u32x4 av = trd2(pa + ks * 32 * K1_PHK_P + ft * 32, pa + (ks * 32 + 4) * K1_PHK_P + ft * 32);
        kv[ft] = mfma16(av, bv, kv[ft]);
      }
    }
    const int d = 16 * dt + i16;
    float* dst = (float*)(ws + WS_KVP) + ((size_t)(4 * hg + hl) * 16 + tt) * 840 + d * 40 + 4 * g;
    if (d <= 20) {
      *(f32x4*)(dst) = kv[0];
      *(f32x4*)(dst + 16) = kv[1];
      if (g < 2) *(f32x4*)(dst + 32) = kv[2];
    }
  }
  STAMP(10); RSTAMP(15); STAMP_FLUSH(0);
}

constexpr u32 K2_STG = 8 * REC;
constexpr u32 K2_RING = 6 * K2_STG;
constexpr u32 K2_DUMP = K2_RING;
constexpr u32 K2_KVT = K2_RING + 1024;
constexpr u32 K2_LDS = K2_KVT + 2048;
constexpr u32 K2_MRG = 2 * K2_STG;

__device__ __forceinline__ f32x8 lo8(const f32x16& s) { return __builtin_shufflevector(s, s, 0, 1, 2, 3, 4, 5, 6, 7); }
__device__ __forceinline__ f32x8 hi8(const f32x16& s) { return __builtin_shufflevector(s, s, 8, 9, 10, 11, 12, 13, 14, 15); }
__device__ __forceinline__ float max16(const f32x16& s) {
  float a = fmaxf(fmaxf(s[0], s[1]), s[2]), b = fmaxf(fmaxf(s[3], s[4]), s[5]);
  a = fmaxf(fmaxf(a, s[6]), s[7]); b = fmaxf(fmaxf(b, s[8]), s[9]);
  a = fmaxf(fmaxf(a, s[10]), s[11]); b = fmaxf(fmaxf(b, s[12]), s[13]);
  a = fmaxf(fmaxf(a, s[14]), s[15]);
  return fmaxf(a, b);
}
__device__ __forceinline__ float xhalf_max(float m) {
  auto r = __builtin_amdgcn_permlane32_swap(__float_as_uint(m), __float_as_uint(m), false, false);
  return fmaxf(__uint_as_float(r[0]), __uint_as_float(r[1]));
}

__global__ void __launch_bounds__(768, 3) k2_attn(char* __restrict__ ws) {
  extern __shared__ __attribute__((aligned(16))) char smem[];
  ldsp lds = (ldsp)smem;
  const u32 lds0 = (u32)(uintptr_t)lds;
  const int tid = threadIdx.x, lane = tid & 63, r32 = lane & 31, hh = lane >> 5;
  const int w = __builtin_amdgcn_readfirstlane(tid >> 6);
  STAMP_DECL; RSTAMP(14); STAMP(0);
  const int head = blockIdx.x >> 3, tb = blockIdx.x & 7;
  const char* kvim = ws + WS_KVIM;
  const f32x16 zero16 = {0.f, 0.f, 0.f, 0.f, 0.f, 0.f, 0.f, 0.f, 0.f, 0.f, 0.f, 0.f, 0.f, 0.f, 0.f, 0.f};

  if (w >= 8) {
    __builtin_amdgcn_s_setprio(3);
    const int hw = w - 8, ht = tid - 512;
    const u32x4 rsKV = mkrsrc(kvim, 64 * REC);
    const u32 vl = (u32)lane * 16u;
    asm volatile("s_nop 4" ::: "memory");
    auto issue = [&](int st) {
#pragma unroll
      for (int j = 0; j < 6; ++j) {
        const u32 p = (u32)(hw + 4 * j);
        const bool ok = p < 21u;
        dma_buf(rsKV, vl, ok ? (u32)st * K2_STG + p * 1024u : OOB, lds0 + (ok ? (u32)(st % 6) * K2_STG + p * 1024u : K2_DUMP));
      }
    };
    issue(0); issue(1);
    STAMP(2);
    WAIT_BAR(6);
    STAMP(3);
    issue(2); issue(3);
    STAMP(4);
    WAIT_BAR(12);
    STAMP(5);
    issue(4); issue(5);
    STAMP(6);
    WAIT_BAR(12);
    STAMP(7);
    issue(6); issue(7);
    STAMP(8);
    if (ht < 210) {
      const f32x4* src = (const f32x4*)(ws + WS_KVP) + (size_t)head * 16 * 210 + ht;
      f32x4 a = src[0];
#pragma unroll
      for (int p = 1; p < 16; ++p) a += src[p * 210];
      const int d = ht / 10, f4 = ht - d * 10;
      *(LDSAS u32x2*)(lds + K2_KVT + d * 96 + f4 * 8) = (u32x2){pk2(a[0], a[1]), pk2(a[2], a[3])};
    } else if (ht >= 224 && ht < 224 + 21) {
      *(LDSAS u32x4*)(lds + K2_KVT + (ht - 224) * 96 + 80) = (u32x4){0u, 0u, 0u, 0u};
    }
    u32x4 p0[2], p1[2], p2[2]; float gt[2];
#pragma unroll
    for (int j = 0; j < 2; ++j) {
      const int tok = 256 * tb + 64 * hw + 32 * j + r32;
      const char* prow = ws + WS_PHIQ + (size_t)(head * 64 + 8 * tb + 2 * hw + j) * 2560;
      p0[j] = *(const u32x4*)(prow + lane * 16); p1[j] = *(const u32x4*)(prow + 1024 + lane * 16); p2[j] = *(const u32x4*)(prow + 2048 + r32 * 16);
      gt[j] = ((const float*)(ws + WS_GATE))[tok];
    }
    STAMP(9);
    WAIT_BAR(0);
    STAMP(10);
    ldsp ak = lds + K2_KVT + (u32)(r32 < 20 ? r32 : 20) * 96u + (u32)hh * 16u;
    const u32x4 a0 = ld128(ak), a1 = ld128(ak + 32), a2 = ld128(ak + 64);
#pragma unroll
    for (int j = 0; j < 2; ++j) {
      const int tok = 256 * tb + 64 * hw + 32 * j + r32;
      if (hh) p2[j] = (u32x4){0u, 0u, 0u, 0u};
      f32x16 N = mfma32(a0, p0[j], zero16);
      N = mfma32(a1, p1[j], N);
      N = mfma32(a2, p2[j], N);
      const float sa = gt[j] * __builtin_amdgcn_rcpf(N[12] + 1e-6f);
      char* obase = ws + WS_OFR + (size_t)(tok >> 4) * 40960 + (tok & 15) * 16 + head * 5 * 256;
      *(u32x4*)(obase + (hh ? 3 : 0) * 256) = (u32x4){pk2(N[0] * sa, N[1] * sa), pk2(N[2] * sa, N[3] * sa), pk2(N[4] * sa, N[5] * sa), pk2(N[6] * sa, N[7] * sa)};
      if (!hh) *(u32x2*)(obase + 256) = (u32x2){pk2(N[8] * sa, N[9] * sa), pk2(N[10] * sa, N[11] * sa)};
    }
    const u32x4 rsWO = mkrsrc(ws + WS_WOF, 1638400);
#pragma unroll
    for (int j = 0; j < 13; ++j) {
      const u32 p = (u32)(hw + 4 * j);
      dma_buf(rsWO, vl, p < 50u ? (u32)head * 51200u + p * 1024u : OOB, lds0 + K2_DUMP);
    }
    asm volatile("s_waitcnt vmcnt(0)" ::: "memory");
    STAMP(11); RSTAMP(15); STAMP_FLUSH(1);
    return;
  }

  const int pr = w & 3, par = w >> 2;
  const int tok0 = 256 * tb + 64 * pr + r32;
  u32x4 q1[2]; u32x2 q2[2];
#pragma unroll
  for (int j = 0; j < 2; ++j) {
    const char* qrow = ws + WS_QB + (size_t)(head * 64 + 8 * tb + 2 * pr + j) * 1280;
    q1[j] = *(const u32x4*)(qrow + lane * 16);
    q2[j] = (u32x2){*(const u32*)(qrow + 1024 + r32 * 8 + hh * 4), 0u};
  }
  const u32x4 k1a = *(const u32x4*)(kvim + 62 * REC + lane * 16);
  const u32 k2a = *(const u32*)(kvim + 62 * REC + 1024 + lane * 4);
  u32x4 k1 = *(const u32x4*)(kvim + (62 + par) * REC + lane * 16);
  u32x2 k2 = {*(const u32*)(kvim + (62 + par) * REC + 1024 + lane * 4), hh ? 0u : 0x00003F80u};
  const float gt = ((const float*)(ws + WS_GATE))[tok0 + 32 * par];
  f32x16 S0, S1, S2, S3 = zero16, O0 = zero16, O1 = zero16;
#pragma unroll
  for (int j = 0; j < 2; ++j) {
    f32x16 t = mfma32(k1a, q1[j], zero16);
    t = mfma32k8((u32x2){k2a, 0u}, q2[j], t);
    const float m = xhalf_max(max16(t));
    q2[j][1] = hh ? 0u : (pk2(-m, 0.f) & 0xffffu);
  }
  S0 = mfma32(k1, q1[0], zero16);
  S0 = mfma32k8(k2, q2[0], S0);
  u32x4 vlo = {0u, 0u, 0u, 0u}, vhi = {0u, 0u, 0u, 0u};
  const int idx = 0xDDDD;
  const u32 vrow = r32 <= 20 ? (u32)r32 : (r32 == 24 ? 20u : 21u);
  u32 aK1 = lds0 + (u32)par * REC + (u32)lane * 16u, aK2 = lds0 + (u32)par * REC + 1024u + (u32)lane * 4u, aV = lds0 + (u32)par * REC + 1280u + (u32)hh * 352u + vrow * 16u;

  STAMP(1);
  {
    u32 cnt, inc;
    asm volatile(ATTN_LOOP_ASM
                 : "+{v[0:15]}"(O0), "+{v[16:31]}"(O1), "+{v[32:47]}"(S0), "=&{v[48:63]}"(S1), "=&{v[64:79]}"(S2), "+{v[80:95]}"(S3),
                   "+{v[108:111]}"(k1), "+{v[112:113]}"(k2), "+{v[116:119]}"(vlo), "+{v[120:123]}"(vhi),
                   "+{v124}"(aK1), "+{v125}"(aK2), "+{v126}"(aV), [cnt] "=&s"(cnt), [inc] "=&s"(inc)
                 : "{v[96:99]}"(q1[0]), "{v[100:103]}"(q1[1]), "{v[104:105]}"(q2[0]), "{v[106:107]}"(q2[1]), "{v114}"(idx),
                   [mask] "s"(0x17u), [wrap] "s"(K2_STG - K2_RING), [stg] "s"(K2_STG)
                 : "memory", "scc");
  }
  STAMP(7);
  {
    const f32x16 snd = par ? O0 : O1;
    ldsp mw = lds + K2_MRG + (u32)w * 4096u + (u32)lane * 16u;
#pragma unroll
    for (int c = 0; c < 4; ++c) *(LDSAS f32x4*)(mw + c * 1024) = (f32x4){snd[4 * c], snd[4 * c + 1], snd[4 * c + 2], snd[4 * c + 3]};
  }
  LBAR();
  STAMP(8);
  f32x16 O = par ? O1 : O0;
  {
    ldsp mr = lds + K2_MRG + (u32)(w ^ 4) * 4096u + (u32)lane * 16u;
#pragma unroll
    for (int c = 0; c < 4; ++c) { const f32x4 v = *(LDSAS f32x4*)(mr + c * 1024); O[4 * c] += v[0]; O[4 * c + 1] += v[1]; O[4 * c + 2] += v[2]; O[4 * c + 3] += v[3]; }
  }
  float l = hh ? O[8] : O[12];
  {
    float t = 0.f;
#pragma unroll
    for (int r = 0; r < 12; ++r) t = __builtin_fmaf(O[r], 0.f, t);
    const bool bad = !(t == 0.f) || !(l < 1.0e30f);
    if (__builtin_expect(__any(bad), 0)) {
      const u32x4 qx = par ? q1[1] : q1[0];
      const u32x2 qz = {par ? q2[1][0] : q2[0][0], 0u};
      float m = -INFINITY;
      for (int rec = 0; rec < 64; ++rec) {
        const u32x4 a1 = *(const u32x4*)(kvim + rec * REC + lane * 16);
        const u32 a2 = *(const u32*)(kvim + rec * REC + 1024 + lane * 4);
        f32x16 s = mfma32(a1, qx, zero16);
        s = mfma32k8((u32x2){a2, 0u}, qz, s);
        m = fmaxf(m, max16(s));
      }
      m = xhalf_max(m);
      O = zero16;
      const u32 av = 1280u + (u32)hh * 352u + vrow * 16u;
      for (int t2 = 0; t2 < 64; ++t2) {
        const int rec = (t2 + 62) & 63;
        const u32x4 a1 = *(const u32x4*)(kvim + rec * REC + lane * 16);
        const u32 a2 = *(const u32*)(kvim + rec * REC + 1024 + lane * 4);
        const u32x4 b1 = *(const u32x4*)(kvim + t2 * REC + av);
        const u32x4 b2 = *(const u32x4*)(kvim + t2 * REC + av + 704);
        f32x16 s = mfma32(a1, qx, zero16);
        s = mfma32k8((u32x2){a2, 0u}, qz, s);
#pragma unroll
        for (int r = 0; r < 16; ++r) s[r] = __builtin_amdgcn_exp2f(s[r] - m);
        O = smf(b1, lo8(s), O, idx);
        O = smf(b2, hi8(s), O, idx);
      }
      l = hh ? O[8] : O[12];
    }
  }
  const float sb = (1.f - gt) * __builtin_amdgcn_rcpf(l);
  const int tok = tok0 + 32 * par;
  char* obase = ws + WS_OFR + (size_t)(tok >> 4) * 40960 + (tok & 15) * 16 + head * 5 * 256;
  *(u32x4*)(obase + (hh ? 4 : 2) * 256) = (u32x4){pk2(O[0] * sb, O[1] * sb), pk2(O[2] * sb, O[3] * sb), pk2(O[4] * sb, O[5] * sb), pk2(O[6] * sb, O[7] * sb)};
  if (!hh) *(u32x2*)(obase + 256 + 8) = (u32x2){pk2(O[8] * sb, O[9] * sb), pk2(O[10] * sb, O[11] * sb)};
  STAMP(9); RSTAMP(15); STAMP_FLUSH(1);
}

constexpr u32 K3_SLOT = 49152, K3_W = 20480;

__global__ void __launch_bounds__(512, 2) k3_out(char* __restrict__ ws, char* __restrict__ out) {
  extern __shared__ __attribute__((aligned(16))) char smem[];
  ldsp lds = (ldsp)smem;
  const u32 lds0 = (u32)(uintptr_t)lds;
  const int tid = threadIdx.x, lane = tid & 63;
  const int w = __builtin_amdgcn_readfirstlane(tid >> 6);
  STAMP_DECL; RSTAMP(14); STAMP(0);
  const int xcd = blockIdx.x & 7, jb = blockIdx.x >> 3;
  const int t64 = xcd * 4 + (jb & 3), cgp = jb >> 2;

  if (w >= 4) {
    const int lw = w - 4;
    const u32x4 rsO = mkrsrc(ws + WS_OFR, 5242880), rsW = mkrsrc(ws + WS_WOF, 1638400);
    const u32 vl = (u32)lane * 16u;
    int ks = (cgp + 2 * (jb & 3)) & 7;
    asm volatile("s_nop 4" ::: "memory");
    auto issue = [&](int slot) {
      const u32 base = lds0 + (u32)slot * K3_SLOT;
#pragma unroll
      for (int j = 0; j < 5; ++j) dma_buf(rsO, vl, (u32)((4 * t64 + lw) * 40 + 5 * ks + j) * 1024u, base + (u32)(5 * lw + j) * 1024u);
#pragma unroll
      for (int j = 0; j < 7; ++j) {
        const u32 p = (u32)(lw + 4 * j);
        dma_buf(rsW, vl, p < 25u ? (u32)(cgp * 8 + ks) * 25600u + p * 1024u : OOB, base + K3_W + p * 1024u);
      }
      ks = (ks + 1) & 7;
    };
    STAMP(1);
    issue(0); issue(1); issue(2);
    STAMP(2);
    WAIT_BAR(24);
    int slot = 0;
    for (int s = 1; s < 8; ++s) {
      if (s < 7) WAIT_BAR(12); else WAIT_BAR(0);
      if (s < 6) { issue(slot); slot = slot == 2 ? 0 : slot + 1; }
    }
  } else {
    const int g = lane >> 4, i16 = lane & 15;
    const u32 aW = K3_W + (u32)(4 * g + (i16 >> 2)) * 160u + (u32)(i16 & 3) * 40u;
    const u32 aO = (u32)w * 5120u + (u32)lane * 16u;
    f32x4 acc[5];
#pragma unroll
    for (int T = 0; T < 5; ++T) acc[T] = (f32x4){0.f, 0.f, 0.f, 0.f};
    int slot = 0;
    for (int s = 0; s < 8; ++s) {
      LBAR();
      if (s == 0) STAMP(1); if (s == 1) STAMP(2); if (s == 4) STAMP(3); if (s == 7) STAMP(4);
      ldsp sb = lds + (u32)slot * K3_SLOT;
      slot = slot == 2 ? 0 : slot + 1;
#pragma unroll
      for (int kk = 0; kk < 5; ++kk) {
        u32x4 of = ld128(sb + aO + kk * 1024);
#pragma unroll
        for (int T = 0; T < 5; ++T) {
          u32x4 wf = trd2(sb + aW + kk * 5120 + T * 8, sb + aW + kk * 5120 + 2560 + T * 8);
          acc[T] = mfma16(wf, of, acc[T]);
        }
      }
    }
    u32x2* dst = (u32x2*)(out + (size_t)(64 * t64 + 16 * w + i16) * 1280 + cgp * 160 + g * 40);
#pragma unroll
    for (int T = 0; T < 5; ++T) dst[T] = (u32x2){pk2(acc[T][0], acc[T][1]), pk2(acc[T][2], acc[T][3])};
  }
  STAMP(5);
  STAMP(6); RSTAMP(15); STAMP_FLUSH(2);
}

extern "C" void kernel_launch(void* const* d_in, const int* in_sizes, int n_in, void* d_out, int out_size, void* d_ws,
                              size_t ws_size, hipStream_t stream) {
  (void)in_sizes; (void)n_in; (void)out_size; (void)ws_size;
  (void)hipFuncSetAttribute((const void*)k1_proj, hipFuncAttributeMaxDynamicSharedMemorySize, 4 * K1_SLOT);
  (void)hipFuncSetAttribute((const void*)k2_attn, hipFuncAttributeMaxDynamicSharedMemorySize, K2_LDS);
  (void)hipFuncSetAttribute((const void*)k3_out, hipFuncAttributeMaxDynamicSharedMemorySize, 3 * K3_SLOT);
  char* ws = (char*)d_ws;
  k1_proj<<<256, 768, 4 * K1_SLOT, stream>>>((const char*)d_in[0], (const char*)d_in[1], (const char*)d_in[2], (const char*)d_in[5], (const char*)d_in[6],
      (const char*)d_in[7], (const char*)d_in[9], (const char*)d_in[3], (const char*)d_in[8], ws);
  k2_attn<<<256, 768, K2_LDS, stream>>>(ws);
  k3_out<<<256, 512, 3 * K3_SLOT, stream>>>(ws, (char*)d_out);
}
```

```cpp
#include <hip/hip_runtime.h>
#include <hip/hip_bf16.h>
#include <stdint.h>

typedef unsigned int u32;
typedef u32 u32x2 __attribute__((ext_vector_type(2)));
typedef u32 u32x4 __attribute__((ext_vector_type(4)));
typedef float f32x2 __attribute__((ext_vector_type(2)));
typedef float f32x4 __attribute__((ext_vector_type(4)));
typedef float f32x8 __attribute__((ext_vector_type(8)));
typedef float f32x16 __attribute__((ext_vector_type(16)));
typedef __bf16 bf16x2 __attribute__((ext_vector_type(2)));
typedef __bf16 bf16x8 __attribute__((ext_vector_type(8)));
typedef __bf16 bf16x16 __attribute__((ext_vector_type(16)));
typedef short s16x4 __attribute__((ext_vector_type(4)));
#define LDSAS __attribute__((address_space(3)))
typedef LDSAS char* ldsp;

constexpr int SEQ = 2048, DM = 640, NH = 32, DH = 20;
constexpr float LOG2E = 1.4426950408889634f;
constexpr float QSCALE = 1.4426950408889634f * 0.22360679774997896f;

constexpr u32 REC = 2688;
constexpr size_t WS_QB   = 0;
constexpr size_t WS_PHIQ = WS_QB + 2621440 + 256;
constexpr size_t WS_KVIM = WS_PHIQ + 5242880 + 256;
constexpr size_t WS_KVP  = WS_KVIM + 64 * REC + 256;
constexpr size_t WS_GATE = WS_KVP + 32 * 16 * 840 * 4;
constexpr size_t WS_OFR  = WS_GATE + 8192;
constexpr size_t WS_WOF  = WS_OFR + 5242880;
constexpr size_t WS_FLGA = WS_WOF + 1638400;
constexpr size_t WS_FLGB = WS_FLGA + 2048;
constexpr size_t WS_EPOCH = WS_FLGB + 2048;
constexpr u32 OOB = 0x40000000u;
#define ISTAMP_INIT()
#define ISTAMP_FLUSH(kern)
#define ISTAMP(kern, i)
#define IRSTAMP(kern, i)
#define STAMP_DECL
#define STAMP(i)
#define RSTAMP(i)
#define STAMP_FLUSH(kern)

#define SBAR() __builtin_amdgcn_sched_barrier(0)
#define PIN(x) asm volatile("" : "+v"(x))
#define LBAR() asm volatile("s_waitcnt lgkmcnt(0)\n\ts_barrier" ::: "memory")
#define WAIT_BAR(N) asm volatile("s_waitcnt vmcnt(" #N ") lgkmcnt(0)\n\ts_barrier" ::: "memory")

__device__ __forceinline__ int lane_id() {
  int l;
  asm volatile("v_mbcnt_lo_u32_b32 %0, -1, 0\n\tv_mbcnt_hi_u32_b32 %0, -1, %0" : "=v"(l));
  return l;
}
__device__ __forceinline__ u32 pk2(float a, float b) {
  f32x2 v = {a, b};
  return __builtin_bit_cast(u32, __builtin_convertvector(v, bf16x2));
}
__device__ __forceinline__ f32x4 mfma16(u32x4 a, u32x4 b, f32x4 c) {
  return __builtin_amdgcn_mfma_f32_16x16x32_bf16(__builtin_bit_cast(bf16x8, a), __builtin_bit_cast(bf16x8, b), c, 0, 0, 0);
}
__device__ __forceinline__ f32x16 mfma32(u32x4 a, u32x4 b, f32x16 c) {
  return __builtin_amdgcn_mfma_f32_32x32x16_bf16(__builtin_bit_cast(bf16x8, a), __builtin_bit_cast(bf16x8, b), c, 0, 0, 0);
}
typedef short s16x4b __attribute__((ext_vector_type(4)));
__device__ __forceinline__ f32x16 mfma32k8(u32x2 a, u32x2 b, f32x16 c) {
  return __builtin_amdgcn_mfma_f32_32x32x8bf16_1k(__builtin_bit_cast(s16x4b, a), __builtin_bit_cast(s16x4b, b), c, 0, 0, 0);
}
__device__ __forceinline__ f32x16 smf(u32x4 a, f32x8 b, f32x16 c, int idx) {
  return __builtin_amdgcn_smfmac_f32_32x32x32_bf16(__builtin_bit_cast(bf16x8, a), __builtin_bit_cast(bf16x16, b), c, idx, 0, 0);
}
__device__ __forceinline__ u32x2 trd(ldsp p) {
  return __builtin_bit_cast(u32x2, __builtin_amdgcn_ds_read_tr16_b64_v4i16((LDSAS s16x4*)p));
}
__device__ __forceinline__ u32x4 trd2(ldsp p0, ldsp p1) {
  u32x2 a = trd(p0), b = trd(p1);
  return (u32x4){a[0], a[1], b[0], b[1]};
}
__device__ __forceinline__ u32x4 ld128(ldsp p) { return *(LDSAS u32x4*)p; }
__device__ __forceinline__ u32x2 ld64(ldsp p) { return *(LDSAS u32x2*)p; }

__device__ __forceinline__ u32x4 mkrsrc(const void* p, u32 bytes) {
  uint64_t a = (uint64_t)p;
  return (u32x4){(u32)__builtin_amdgcn_readfirstlane((u32)a), (u32)__builtin_amdgcn_readfirstlane((u32)(a >> 32) & 0xffffu), bytes, 0x00020000u};
}
__device__ __forceinline__ void dma_buf(u32x4 rs, u32 voff, u32 soff, u32 ldsaddr) {
  u32 keep;
  asm volatile("s_mov_b32 %0, m0\n\ts_mov_b32 m0, %4\n\ts_nop 2\n\tbuffer_load_dwordx4 %1, %2, %3 offen lds\n\ts_mov_b32 m0, %0"
               : "=&s"(keep) : "v"(voff), "s"(rs), "s"(__builtin_amdgcn_readfirstlane(soff)), "s"(__builtin_amdgcn_readfirstlane(ldsaddr)) : "memory");
}
__device__ __forceinline__ void dma_glb(const void* g, u32 ldsaddr) {
  u32 keep;
  asm volatile("s_mov_b32 %0, m0\n\ts_mov_b32 m0, %2\n\ts_nop 0\n\tglobal_load_lds_dwordx4 %1, off\n\ts_mov_b32 m0, %0"
               : "=&s"(keep) : "v"(g), "s"(ldsaddr) : "memory");
}

#define ATTN_LOOP_ASM \
  "s_nop 11\n\t" \
  "v_mov_b64 v[0:1], 0\n\t" \
  "v_mov_b64 v[2:3], 0\n\t" \
  "v_mov_b64 v[4:5], 0\n\t" \
  "v_mov_b64 v[6:7], 0\n\t" \
  "v_mov_b64 v[8:9], 0\n\t" \
  "v_mov_b64 v[10:11], 0\n\t" \
  "v_mov_b64 v[12:13], 0\n\t" \
  "v_mov_b64 v[14:15], 0\n\t" \
  "v_mov_b64 v[16:17], 0\n\t" \
  "v_mov_b64 v[18:19], 0\n\t" \
  "v_mov_b64 v[20:21], 0\n\t" \
  "v_mov_b64 v[22:23], 0\n\t" \
  "v_mov_b64 v[24:25], 0\n\t" \
  "v_mov_b64 v[26:27], 0\n\t" \
  "v_mov_b64 v[28:29], 0\n\t" \
  "v_mov_b64 v[30:31], 0\n\t" \
  "v_mov_b64 v[80:81], 0\n\t" \
  "v_mov_b64 v[82:83], 0\n\t" \
  "v_mov_b64 v[84:85], 0\n\t" \
  "v_mov_b64 v[86:87], 0\n\t" \
  "v_mov_b64 v[88:89], 0\n\t" \
  "v_mov_b64 v[90:91], 0\n\t" \
  "v_mov_b64 v[92:93], 0\n\t" \
  "v_mov_b64 v[94:95], 0\n\t" \
  "v_mov_b64 v[116:117], 0\n\t" \
  "v_mov_b64 v[118:119], 0\n\t" \
  "v_mov_b64 v[120:121], 0\n\t" \
  "v_mov_b64 v[122:123], 0\n\t" \
  "s_waitcnt lgkmcnt(0)\n\t" \
  "s_barrier\n\t" \
  "s_mov_b32 %[cnt], 0\n\t" \
  "s_mov_b32 %[slot], 0\n\t" \
  "s_cmp_lg_u32 %[par], 0\n\t" \
  "s_cbranch_scc1 4f\n\t" \
  "1:\n\t" \
  "v_mfma_f32_32x32x16_bf16 v[48:63], v[108:111], v[100:103], 0\n\t" \
  "ds_read_b128 v[108:111], v124 offset:0\n\t" \
  "v_exp_f32 v32, v32\n\t" \
  "v_exp_f32 v33, v33\n\t" \
  "v_exp_f32 v34, v34\n\t" \
  "v_exp_f32 v35, v35\n\t" \
  "v_mfma_f32_32x32x8_bf16 v[48:63], v[112:113], v[106:107], v[48:63]\n\t" \
  "ds_read_b32 v112, v125 offset:0\n\t" \
  "v_exp_f32 v36, v36\n\t" \
  "v_exp_f32 v37, v37\n\t" \
  "v_exp_f32 v38, v38\n\t" \
  "v_exp_f32 v39, v39\n\t" \
  "v_smfmac_f32_32x32x32_bf16 v[16:31], v[116:119], v[80:87], v114\n\t" \
  "ds_read_b128 v[116:119], v126 offset:0\n\t" \
  "v_exp_f32 v40, v40\n\t" \
  "v_exp_f32 v41, v41\n\t" \
  "v_exp_f32 v42, v42\n\t" \
  "v_exp_f32 v43, v43\n\t" \
  "v_smfmac_f32_32x32x32_bf16 v[16:31], v[120:123], v[88:95], v114\n\t" \
  "ds_read_b128 v[120:123], v126 offset:704\n\t" \
  "v_exp_f32 v44, v44\n\t" \
  "v_exp_f32 v45, v45\n\t" \
  "v_exp_f32 v46, v46\n\t" \
  "v_exp_f32 v47, v47\n\t" \
  "s_waitcnt lgkmcnt(2)\n\t" \
  "v_mfma_f32_32x32x16_bf16 v[64:79], v[108:111], v[96:99], 0\n\t" \
  "v_exp_f32 v48, v48\n\t" \
  "v_exp_f32 v49, v49\n\t" \
  "v_exp_f32 v50, v50\n\t" \
  "v_exp_f32 v51, v51\n\t" \
  "v_mfma_f32_32x32x8_bf16 v[64:79], v[112:113], v[104:105], v[64:79]\n\t" \
  "v_exp_f32 v52, v52\n\t" \
  "v_exp_f32 v53, v53\n\t" \
  "v_exp_f32 v54, v54\n\t" \
  "v_exp_f32 v55, v55\n\t" \
  "s_waitcnt lgkmcnt(0)\n\t" \
  "v_smfmac_f32_32x32x32_bf16 v[0:15], v[116:119], v[32:39], v114\n\t" \
  "v_exp_f32 v56, v56\n\t" \
  "v_exp_f32 v57, v57\n\t" \
  "v_exp_f32 v58, v58\n\t" \
  "v_exp_f32 v59, v59\n\t" \
  "v_smfmac_f32_32x32x32_bf16 v[0:15], v[120:123], v[40:47], v114\n\t" \
  "v_exp_f32 v60, v60\n\t" \
  "v_exp_f32 v61, v61\n\t" \
  "v_exp_f32 v62, v62\n\t" \
  "v_exp_f32 v63, v63\n\t" \
  "v_mfma_f32_32x32x16_bf16 v[80:95], v[108:111], v[100:103], 0\n\t" \
  "ds_read_b128 v[108:111], v124 offset:5376\n\t" \
  "v_exp_f32 v64, v64\n\t" \
  "v_exp_f32 v65, v65\n\t" \
  "v_exp_f32 v66, v66\n\t" \
  "v_exp_f32 v67, v67\n\t" \
  "v_mfma_f32_32x32x8_bf16 v[80:95], v[112:113], v[106:107], v[80:95]\n\t" \
  "ds_read_b32 v112, v125 offset:5376\n\t" \
  "v_exp_f32 v68, v68\n\t" \
  "v_exp_f32 v69, v69\n\t" \
  "v_exp_f32 v70, v70\n\t" \
  "v_exp_f32 v71, v71\n\t" \
  "v_smfmac_f32_32x32x32_bf16 v[16:31], v[116:119], v[48:55], v114\n\t" \
  "ds_read_b128 v[116:119], v126 offset:5376\n\t" \
  "v_exp_f32 v72, v72\n\t" \
  "v_exp_f32 v73, v73\n\t" \
  "v_exp_f32 v74, v74\n\t" \
  "v_exp_f32 v75, v75\n\t" \
  "v_smfmac_f32_32x32x32_bf16 v[16:31], v[120:123], v[56:63], v114\n\t" \
  "ds_read_b128 v[120:123], v126 offset:6080\n\t" \
  "s_add_u32 %[cnt], %[cnt], 1\n\t" \
  "s_cmp_eq_u32 %[slot], 4\n\t" \
  "s_cselect_b32 %[inc], %[wrap], %[stg]\n\t" \
  "v_exp_f32 v76, v76\n\t" \
  "v_exp_f32 v77, v77\n\t" \
  "v_exp_f32 v78, v78\n\t" \
  "v_exp_f32 v79, v79\n\t" \
  "s_waitcnt lgkmcnt(2)\n\t" \
  "v_mfma_f32_32x32x16_bf16 v[32:47], v[108:111], v[96:99], 0\n\t" \
  "ds_read_b32 v115, v127 offset:32\n\t" \
  "s_cselect_b32 %[slot], -1, %[slot]\n\t" \
  "s_add_u32 %[slot], %[slot], 1\n\t" \
  "v_exp_f32 v80, v80\n\t" \
  "v_exp_f32 v81, v81\n\t" \
  "v_exp_f32 v82, v82\n\t" \
  "v_exp_f32 v83, v83\n\t" \
  "v_mfma_f32_32x32x8_bf16 v[32:47], v[112:113], v[104:105], v[32:47]\n\t" \
  "v_add_u32 v124, %[inc], v124\n\t" \
  "v_add_u32 v125, %[inc], v125\n\t" \
  "v_add_u32 v126, %[inc], v126\n\t" \
  "v_exp_f32 v84, v84\n\t" \
  "v_exp_f32 v85, v85\n\t" \
  "v_exp_f32 v86, v86\n\t" \
  "v_exp_f32 v87, v87\n\t" \
  "s_waitcnt lgkmcnt(1)\n\t" \
  "v_smfmac_f32_32x32x32_bf16 v[0:15], v[116:119], v[64:71], v114\n\t" \
  "v_exp_f32 v88, v88\n\t" \
  "v_exp_f32 v89, v89\n\t" \
  "v_exp_f32 v90, v90\n\t" \
  "v_exp_f32 v91, v91\n\t" \
  "s_waitcnt lgkmcnt(0)\n\t" \
  "v_readfirstlane_b32 %[rdy], v115\n\t" \
  "v_mov_b32 v115, %[cnt]\n\t" \
  "v_smfmac_f32_32x32x32_bf16 v[0:15], v[120:123], v[72:79], v114\n\t" \
  "ds_write_b32 v127, v115\n\t" \
  "s_cmp_gt_u32 %[rdy], %[cnt]\n\t" \
  "v_exp_f32 v92, v92\n\t" \
  "v_exp_f32 v93, v93\n\t" \
  "v_exp_f32 v94, v94\n\t" \
  "v_exp_f32 v95, v95\n\t" \
  "s_cbranch_scc1 1b\n\t" \
  "s_cmp_ge_u32 %[cnt], 16\n\t" \
  "s_cbranch_scc1 3f\n\t" \
  "s_mov_b32 %[inc], 0\n\t" \
  "2:\n\t" \
  "s_sleep 1\n\t" \
  "ds_read_b32 v115, v127 offset:32\n\t" \
  "s_add_u32 %[inc], %[inc], 1\n\t" \
  "s_waitcnt lgkmcnt(0)\n\t" \
  "v_readfirstlane_b32 %[rdy], v115\n\t" \
  "s_nop 3\n\t" \
  "s_cmp_gt_u32 %[rdy], %[cnt]\n\t" \
  "s_cbranch_scc1 1b\n\t" \
  "s_cmp_lt_u32 %[inc], 0x100000\n\t" \
  "s_cbranch_scc1 2b\n\t" \
  "s_branch 3f\n\t" \
  "4:\n\t" \
  "v_mfma_f32_32x32x16_bf16 v[48:63], v[108:111], v[100:103], 0\n\t" \
  "ds_read_b128 v[108:111], v124 offset:0\n\t" \
  "s_setprio 1\n\t" \
  "v_exp_f32 v32, v32\n\t" \
  "v_exp_f32 v33, v33\n\t" \
  "v_exp_f32 v34, v34\n\t" \
  "v_exp_f32 v35, v35\n\t" \
  "v_mfma_f32_32x32x8_bf16 v[48:63], v[112:113], v[106:107], v[48:63]\n\t" \
  "ds_read_b32 v112, v125 offset:0\n\t" \
  "v_exp_f32 v36, v36\n\t" \
  "v_exp_f32 v37, v37\n\t" \
  "v_exp_f32 v38, v38\n\t" \
  "v_exp_f32 v39, v39\n\t" \
  "v_smfmac_f32_32x32x32_bf16 v[16:31], v[116:119], v[80:87], v114\n\t" \
  "ds_read_b128 v[116:119], v126 offset:0\n\t" \
  "v_exp_f32 v40, v40\n\t" \
  "v_exp_f32 v41, v41\n\t" \
  "v_exp_f32 v42, v42\n\t" \
  "v_exp_f32 v43, v43\n\t" \
  "v_smfmac_f32_32x32x32_bf16 v[16:31], v[120:123], v[88:95], v114\n\t" \
  "ds_read_b128 v[120:123], v126 offset:704\n\t" \
  "v_exp_f32 v44, v44\n\t" \
  "v_exp_f32 v45, v45\n\t" \
  "v_exp_f32 v46, v46\n\t" \
  "v_exp_f32 v47, v47\n\t" \
  "s_waitcnt lgkmcnt(2)\n\t" \
  "v_mfma_f32_32x32x16_bf16 v[64:79], v[108:111], v[96:99], 0\n\t" \
  "v_exp_f32 v48, v48\n\t" \
  "v_exp_f32 v49, v49\n\t" \
  "v_exp_f32 v50, v50\n\t" \
  "v_exp_f32 v51, v51\n\t" \
  "v_mfma_f32_32x32x8_bf16 v[64:79], v[112:113], v[104:105], v[64:79]\n\t" \
  "v_exp_f32 v52, v52\n\t" \
  "v_exp_f32 v53, v53\n\t" \
  "v_exp_f32 v54, v54\n\t" \
  "v_exp_f32 v55, v55\n\t" \
  "s_waitcnt lgkmcnt(0)\n\t" \
  "v_smfmac_f32_32x32x32_bf16 v[0:15], v[116:119], v[32:39], v114\n\t" \
  "v_exp_f32 v56, v56\n\t" \
  "v_exp_f32 v57, v57\n\t" \
  "v_exp_f32 v58, v58\n\t" \
  "v_exp_f32 v59, v59\n\t" \
  "v_smfmac_f32_32x32x32_bf16 v[0:15], v[120:123], v[40:47], v114\n\t" \
  "v_exp_f32 v60, v60\n\t" \
  "v_exp_f32 v61, v61\n\t" \
  "v_exp_f32 v62, v62\n\t" \
  "v_exp_f32 v63, v63\n\t" \
  "v_mfma_f32_32x32x16_bf16 v[80:95], v[108:111], v[100:103], 0\n\t" \
  "ds_read_b128 v[108:111], v124 offset:5376\n\t" \
  "v_exp_f32 v64, v64\n\t" \
  "v_exp_f32 v65, v65\n\t" \
  "v_exp_f32 v66, v66\n\t" \
  "v_exp_f32 v67, v67\n\t" \
  "v_mfma_f32_32x32x8_bf16 v[80:95], v[112:113], v[106:107], v[80:95]\n\t" \
  "ds_read_b32 v112, v125 offset:5376\n\t" \
  "v_exp_f32 v68, v68\n\t" \
  "v_exp_f32 v69, v69\n\t" \
  "v_exp_f32 v70, v70\n\t" \
  "v_exp_f32 v71, v71\n\t" \
  "v_smfmac_f32_32x32x32_bf16 v[16:31], v[116:119], v[48:55], v114\n\t" \
  "ds_read_b128 v[116:119], v126 offset:5376\n\t" \
  "v_exp_f32 v72, v72\n\t" \
  "v_exp_f32 v73, v73\n\t" \
  "v_exp_f32 v74, v74\n\t" \
  "v_exp_f32 v75, v75\n\t" \
  "v_smfmac_f32_32x32x32_bf16 v[16:31], v[120:123], v[56:63], v114\n\t" \
  "ds_read_b128 v[120:123], v126 offset:6080\n\t" \
  "s_add_u32 %[cnt], %[cnt], 1\n\t" \
  "s_cmp_eq_u32 %[slot], 4\n\t" \
  "s_cselect_b32 %[inc], %[wrap], %[stg]\n\t" \
  "v_exp_f32 v76, v76\n\t" \
  "v_exp_f32 v77, v77\n\t" \
  "v_exp_f32 v78, v78\n\t" \
  "v_exp_f32 v79, v79\n\t" \
  "s_waitcnt lgkmcnt(2)\n\t" \
  "v_mfma_f32_32x32x16_bf16 v[32:47], v[108:111], v[96:99], 0\n\t" \
  "ds_read_b32 v115, v127 offset:32\n\t" \
  "s_cselect_b32 %[slot], -1, %[slot]\n\t" \
  "s_add_u32 %[slot], %[slot], 1\n\t" \
  "v_exp_f32 v80, v80\n\t" \
  "v_exp_f32 v81, v81\n\t" \
  "v_exp_f32 v82, v82\n\t" \
  "v_exp_f32 v83, v83\n\t" \
  "v_mfma_f32_32x32x8_bf16 v[32:47], v[112:113], v[104:105], v[32:47]\n\t" \
  "v_add_u32 v124, %[inc], v124\n\t" \
  "v_add_u32 v125, %[inc], v125\n\t" \
  "v_add_u32 v126, %[inc], v126\n\t" \
  "s_setprio 0\n\t" \
  "v_exp_f32 v84, v84\n\t" \
  "v_exp_f32 v85, v85\n\t" \
  "v_exp_f32 v86, v86\n\t" \
  "v_exp_f32 v87, v87\n\t" \
  "s_waitcnt lgkmcnt(1)\n\t" \
  "v_smfmac_f32_32x32x32_bf16 v[0:15], v[116:119], v[64:71], v114\n\t" \
  "v_exp_f32 v88, v88\n\t" \
  "v_exp_f32 v89, v89\n\t" \
  "v_exp_f32 v90, v90\n\t" \
  "v_exp_f32 v91, v91\n\t" \
  "s_waitcnt lgkmcnt(0)\n\t" \
  "v_readfirstlane_b32 %[rdy], v115\n\t" \
  "v_mov_b32 v115, %[cnt]\n\t" \
  "v_smfmac_f32_32x32x32_bf16 v[0:15], v[120:123], v[72:79], v114\n\t" \
  "ds_write_b32 v127, v115\n\t" \
  "s_cmp_gt_u32 %[rdy], %[cnt]\n\t" \
  "v_exp_f32 v92, v92\n\t" \
  "v_exp_f32 v93, v93\n\t" \
  "v_exp_f32 v94, v94\n\t" \
  "v_exp_f32 v95, v95\n\t" \
  "s_cbranch_scc1 4b\n\t" \
  "s_cmp_ge_u32 %[cnt], 16\n\t" \
  "s_cbranch_scc1 3f\n\t" \
  "s_mov_b32 %[inc], 0\n\t" \
  "5:\n\t" \
  "s_sleep 1\n\t" \
  "ds_read_b32 v115, v127 offset:32\n\t" \
  "s_add_u32 %[inc], %[inc], 1\n\t" \
  "s_waitcnt lgkmcnt(0)\n\t" \
  "v_readfirstlane_b32 %[rdy], v115\n\t" \
  "s_nop 3\n\t" \
  "s_cmp_gt_u32 %[rdy], %[cnt]\n\t" \
  "s_cbranch_scc1 4b\n\t" \
  "s_cmp_lt_u32 %[inc], 0x100000\n\t" \
  "s_cbranch_scc1 5b\n\t" \
  "3:\n\t" \
  "s_nop 1\n\t" \
  "v_smfmac_f32_32x32x32_bf16 v[16:31], v[116:119], v[80:87], v114\n\t" \
  "v_smfmac_f32_32x32x32_bf16 v[16:31], v[120:123], v[88:95], v114\n\t" \
  "s_waitcnt lgkmcnt(0)\n\t" \
  "s_nop 15\n\t"
#define TILE_GEMM_ASM \
  "s_nop 4\n\t" \
  "buffer_load_dwordx4 v[60:63], v116, %[rs], %[s0] offen offset:0 sc1\n\t" \
  "buffer_load_dwordx4 v[64:67], v116, %[rs], %[s1] offen offset:0 sc1\n\t" \
  "buffer_load_dwordx4 v[68:71], v116, %[rs], %[s0] offen offset:2048 sc1\n\t" \
  "buffer_load_dwordx4 v[72:75], v116, %[rs], %[s1] offen offset:2048 sc1\n\t" \
  "s_add_u32 %[s0], %[s0], 4096\n\t" \
  "buffer_load_dwordx4 v[76:79], v116, %[rs], %[s0] offen offset:0 sc1\n\t" \
  "s_add_u32 %[s1], %[s1], 4096\n\t" \
  "buffer_load_dwordx4 v[80:83], v116, %[rs], %[s1] offen offset:0 sc1\n\t" \
  "buffer_load_dwordx4 v[84:87], v116, %[rs], %[s0] offen offset:2048 sc1\n\t" \
  "buffer_load_dwordx4 v[88:91], v116, %[rs], %[s1] offen offset:2048 sc1\n\t" \
  "s_add_u32 %[s0], %[s0], 4096\n\t" \
  "buffer_load_dwordx4 v[92:95], v116, %[rs], %[s0] offen offset:0 sc1\n\t" \
  "s_add_u32 %[s1], %[s1], 4096\n\t" \
  "buffer_load_dwordx4 v[96:99], v116, %[rs], %[s1] offen offset:0 sc1\n\t" \
  "buffer_load_dwordx4 v[100:103], v116, %[rs], %[s0] offen offset:2048 sc1\n\t" \
  "buffer_load_dwordx4 v[104:107], v116, %[rs], %[s1] offen offset:2048 sc1\n\t" \
  "s_add_u32 %[s0], %[s0], 4096\n\t" \
  "buffer_load_dwordx4 v[108:111], v116, %[rs], %[s0] offen offset:0 sc1\n\t" \
  "s_add_u32 %[s1], %[s1], 4096\n\t" \
  "buffer_load_dwordx4 v[112:115], v116, %[rs], %[s1] offen offset:0 sc1\n\t" \
  "ds_read_b128 v[40:43], v117 offset:0\n\t" \
  "ds_read_b128 v[44:47], v117 offset:1024\n\t" \
  "ds_read_b128 v[48:51], v117 offset:2048\n\t" \
  "ds_read_b128 v[52:55], v117 offset:3072\n\t" \
  "ds_read_b128 v[56:59], v117 offset:4096\n\t" \
  "s_mov_b32 %[it], 0\n\t" \
  "1:\n\t" \
  "ds_read_b32 v119, v118\n\t" \
  "s_add_u32 %[it], %[it], 1\n\t" \
  "s_waitcnt lgkmcnt(0)\n\t" \
  "v_cmp_gt_u32 vcc, %[want], v119\n\t" \
  "s_cbranch_vccz 2f\n\t" \
  "s_sleep 6\n\t" \
  "s_cmp_lt_u32 %[it], 0x100000\n\t" \
  "s_cbranch_scc1 1b\n\t" \
  "2:\n\t" \
  "v_mov_b32 v119, 1\n\t" \
  "s_waitcnt vmcnt(12)\n\t" \
  "v_mfma_f32_16x16x32_bf16 v[0:3], v[40:43], v[60:63], v[0:3]\n\t" \
  "v_mfma_f32_16x16x32_bf16 v[4:7], v[40:43], v[64:67], v[4:7]\n\t" \
  "ds_read_b128 v[40:43], v117 offset:10240\n\t" \
  "v_mfma_f32_16x16x32_bf16 v[8:11], v[44:47], v[60:63], v[8:11]\n\t" \
  "v_mfma_f32_16x16x32_bf16 v[12:15], v[44:47], v[64:67], v[12:15]\n\t" \
  "ds_read_b128 v[44:47], v117 offset:11264\n\t" \
  "v_mfma_f32_16x16x32_bf16 v[16:19], v[48:51], v[60:63], v[16:19]\n\t" \
  "v_mfma_f32_16x16x32_bf16 v[20:23], v[48:51], v[64:67], v[20:23]\n\t" \
  "ds_read_b128 v[48:51], v117 offset:12288\n\t" \
  "v_mfma_f32_16x16x32_bf16 v[24:27], v[52:55], v[60:63], v[24:27]\n\t" \
  "v_mfma_f32_16x16x32_bf16 v[28:31], v[52:55], v[64:67], v[28:31]\n\t" \
  "ds_read_b128 v[52:55], v117 offset:13312\n\t" \
  "v_mfma_f32_16x16x32_bf16 v[32:35], v[56:59], v[60:63], v[32:35]\n\t" \
  "v_mfma_f32_16x16x32_bf16 v[36:39], v[56:59], v[64:67], v[36:39]\n\t" \
  "ds_read_b128 v[56:59], v117 offset:14336\n\t" \
  "buffer_load_dwordx4 v[60:63], v116, %[rs], %[s0] offen offset:2048 sc1\n\t" \
  "buffer_load_dwordx4 v[64:67], v116, %[rs], %[s1] offen offset:2048 sc1\n\t" \
  "s_waitcnt vmcnt(12)\n\t" \
  "s_waitcnt lgkmcnt(4)\n\t" \
  "v_mfma_f32_16x16x32_bf16 v[0:3], v[40:43], v[68:71], v[0:3]\n\t" \
  "v_mfma_f32_16x16x32_bf16 v[4:7], v[40:43], v[72:75], v[4:7]\n\t" \
  "ds_read_b128 v[40:43], v117 offset:20480\n\t" \
  "s_waitcnt lgkmcnt(4)\n\t" \
  "v_mfma_f32_16x16x32_bf16 v[8:11], v[44:47], v[68:71], v[8:11]\n\t" \
  "v_mfma_f32_16x16x32_bf16 v[12:15], v[44:47], v[72:75], v[12:15]\n\t" \
  "ds_read_b128 v[44:47], v117 offset:21504\n\t" \
  "s_waitcnt lgkmcnt(4)\n\t" \
  "v_mfma_f32_16x16x32_bf16 v[16:19], v[48:51], v[68:71], v[16:19]\n\t" \
  "v_mfma_f32_16x16x32_bf16 v[20:23], v[48:51], v[72:75], v[20:23]\n\t" \
  "ds_read_b128 v[48:51], v117 offset:22528\n\t" \
  "s_waitcnt lgkmcnt(4)\n\t" \
  "v_mfma_f32_16x16x32_bf16 v[24:27], v[52:55], v[68:71], v[24:27]\n\t" \
  "v_mfma_f32_16x16x32_bf16 v[28:31], v[52:55], v[72:75], v[28:31]\n\t" \
  "ds_read_b128 v[52:55], v117 offset:23552\n\t" \
  "s_waitcnt lgkmcnt(4)\n\t" \
  "v_mfma_f32_16x16x32_bf16 v[32:35], v[56:59], v[68:71], v[32:35]\n\t" \
  "v_mfma_f32_16x16x32_bf16 v[36:39], v[56:59], v[72:75], v[36:39]\n\t" \
  "ds_read_b128 v[56:59], v117 offset:24576\n\t" \
  "s_add_u32 %[s0], %[s0], 4096\n\t" \
  "buffer_load_dwordx4 v[68:71], v116, %[rs], %[s0] offen offset:0 sc1\n\t" \
  "s_add_u32 %[s1], %[s1], 4096\n\t" \
  "buffer_load_dwordx4 v[72:75], v116, %[rs], %[s1] offen offset:0 sc1\n\t" \
  "s_waitcnt vmcnt(12)\n\t" \
  "s_waitcnt lgkmcnt(4)\n\t" \
  "v_mfma_f32_16x16x32_bf16 v[0:3], v[40:43], v[76:79], v[0:3]\n\t" \
  "v_mfma_f32_16x16x32_bf16 v[4:7], v[40:43], v[80:83], v[4:7]\n\t" \
  "ds_read_b128 v[40:43], v117 offset:30720\n\t" \
  "s_waitcnt lgkmcnt(4)\n\t" \
  "v_mfma_f32_16x16x32_bf16 v[8:11], v[44:47], v[76:79], v[8:11]\n\t" \
  "v_mfma_f32_16x16x32_bf16 v[12:15], v[44:47], v[80:83], v[12:15]\n\t" \
  "ds_read_b128 v[44:47], v117 offset:31744\n\t" \
  "s_waitcnt lgkmcnt(4)\n\t" \
  "v_mfma_f32_16x16x32_bf16 v[16:19], v[48:51], v[76:79], v[16:19]\n\t" \
  "v_mfma_f32_16x16x32_bf16 v[20:23], v[48:51], v[80:83], v[20:23]\n\t" \
  "ds_read_b128 v[48:51], v117 offset:32768\n\t" \
  "s_waitcnt lgkmcnt(4)\n\t" \
  "v_mfma_f32_16x16x32_bf16 v[24:27], v[52:55], v[76:79], v[24:27]\n\t" \
  "v_mfma_f32_16x16x32_bf16 v[28:31], v[52:55], v[80:83], v[28:31]\n\t" \
  "ds_read_b128 v[52:55], v117 offset:33792\n\t" \
  "s_waitcnt lgkmcnt(4)\n\t" \
  "v_mfma_f32_16x16x32_bf16 v[32:35], v[56:59], v[76:79], v[32:35]\n\t" \
  "v_mfma_f32_16x16x32_bf16 v[36:39], v[56:59], v[80:83], v[36:39]\n\t" \
  "ds_read_b128 v[56:59], v117 offset:34816\n\t" \
  "buffer_load_dwordx4 v[76:79], v116, %[rs], %[s0] offen offset:2048 sc1\n\t" \
  "buffer_load_dwordx4 v[80:83], v116, %[rs], %[s1] offen offset:2048 sc1\n\t" \
  "s_waitcnt vmcnt(12)\n\t" \
  "s_waitcnt lgkmcnt(4)\n\t" \
  "v_mfma_f32_16x16x32_bf16 v[0:3], v[40:43], v[84:87], v[0:3]\n\t" \
  "v_mfma_f32_16x16x32_bf16 v[4:7], v[40:43], v[88:91], v[4:7]\n\t" \
  "ds_read_b128 v[40:43], v117 offset:40960\n\t" \
  "s_waitcnt lgkmcnt(4)\n\t" \
  "v_mfma_f32_16x16x32_bf16 v[8:11], v[44:47], v[84:87], v[8:11]\n\t" \
  "v_mfma_f32_16x16x32_bf16 v[12:15], v[44:47], v[88:91], v[12:15]\n\t" \
  "ds_read_b128 v[44:47], v117 offset:41984\n\t" \
  "s_waitcnt lgkmcnt(4)\n\t" \
  "v_mfma_f32_16x16x32_bf16 v[16:19], v[48:51], v[84:87], v[16:19]\n\t" \
  "v_mfma_f32_16x16x32_bf16 v[20:23], v[48:51], v[88:91], v[20:23]\n\t" \
  "ds_read_b128 v[48:51], v117 offset:43008\n\t" \
  "s_waitcnt lgkmcnt(4)\n\t" \
  "v_mfma_f32_16x16x32_bf16 v[24:27], v[52:55], v[84:87], v[24:27]\n\t" \
  "v_mfma_f32_16x16x32_bf16 v[28:31], v[52:55], v[88:91], v[28:31]\n\t" \
  "ds_read_b128 v[52:55], v117 offset:44032\n\t" \
  "s_waitcnt lgkmcnt(4)\n\t" \
  "v_mfma_f32_16x16x32_bf16 v[32:35], v[56:59], v[84:87], v[32:35]\n\t" \
  "v_mfma_f32_16x16x32_bf16 v[36:39], v[56:59], v[88:91], v[36:39]\n\t" \
  "ds_read_b128 v[56:59], v117 offset:45056\n\t" \
  "s_waitcnt vmcnt(10)\n\t" \
  "v_add_u32 v117, 51200, v117\n\t" \
  "s_waitcnt lgkmcnt(4)\n\t" \
  "v_mfma_f32_16x16x32_bf16 v[0:3], v[40:43], v[92:95], v[0:3]\n\t" \
  "v_mfma_f32_16x16x32_bf16 v[4:7], v[40:43], v[96:99], v[4:7]\n\t" \
  "ds_read_b128 v[40:43], v117 offset:0\n\t" \
  "s_waitcnt lgkmcnt(4)\n\t" \
  "v_mfma_f32_16x16x32_bf16 v[8:11], v[44:47], v[92:95], v[8:11]\n\t" \
  "v_mfma_f32_16x16x32_bf16 v[12:15], v[44:47], v[96:99], v[12:15]\n\t" \
  "ds_read_b128 v[44:47], v117 offset:1024\n\t" \
  "s_waitcnt lgkmcnt(4)\n\t" \
  "v_mfma_f32_16x16x32_bf16 v[16:19], v[48:51], v[92:95], v[16:19]\n\t" \
  "v_mfma_f32_16x16x32_bf16 v[20:23], v[48:51], v[96:99], v[20:23]\n\t" \
  "ds_read_b128 v[48:51], v117 offset:2048\n\t" \
  "s_waitcnt lgkmcnt(4)\n\t" \
  "v_mfma_f32_16x16x32_bf16 v[24:27], v[52:55], v[92:95], v[24:27]\n\t" \
  "v_mfma_f32_16x16x32_bf16 v[28:31], v[52:55], v[96:99], v[28:31]\n\t" \
  "ds_read_b128 v[52:55], v117 offset:3072\n\t" \
  "s_waitcnt lgkmcnt(4)\n\t" \
  "v_mfma_f32_16x16x32_bf16 v[32:35], v[56:59], v[92:95], v[32:35]\n\t" \
  "v_mfma_f32_16x16x32_bf16 v[36:39], v[56:59], v[96:99], v[36:39]\n\t" \
  "ds_read_b128 v[56:59], v117 offset:4096\n\t" \
  "s_mov_b64 exec, 1\n\t" \
  "ds_add_u32 v118, v119 offset:104\n\t" \
  "s_mov_b64 exec, -1\n\t" \
  "s_waitcnt vmcnt(8)\n\t" \
  "s_waitcnt lgkmcnt(4)\n\t" \
  "v_mfma_f32_16x16x32_bf16 v[0:3], v[40:43], v[100:103], v[0:3]\n\t" \
  "v_mfma_f32_16x16x32_bf16 v[4:7], v[40:43], v[104:107], v[4:7]\n\t" \
  "ds_read_b128 v[40:43], v117 offset:10240\n\t" \
  "s_waitcnt lgkmcnt(4)\n\t" \
  "v_mfma_f32_16x16x32_bf16 v[8:11], v[44:47], v[100:103], v[8:11]\n\t" \
  "v_mfma_f32_16x16x32_bf16 v[12:15], v[44:47], v[104:107], v[12:15]\n\t" \
  "ds_read_b128 v[44:47], v117 offset:11264\n\t" \
  "s_waitcnt lgkmcnt(4)\n\t" \
  "v_mfma_f32_16x16x32_bf16 v[16:19], v[48:51], v[100:103], v[16:19]\n\t" \
  "v_mfma_f32_16x16x32_bf16 v[20:23], v[48:51], v[104:107], v[20:23]\n\t" \
  "ds_read_b128 v[48:51], v117 offset:12288\n\t" \
  "s_waitcnt lgkmcnt(4)\n\t" \
  "v_mfma_f32_16x16x32_bf16 v[24:27], v[52:55], v[100:103], v[24:27]\n\t" \
  "v_mfma_f32_16x16x32_bf16 v[28:31], v[52:55], v[104:107], v[28:31]\n\t" \
  "ds_read_b128 v[52:55], v117 offset:13312\n\t" \
  "s_waitcnt lgkmcnt(4)\n\t" \
  "v_mfma_f32_16x16x32_bf16 v[32:35], v[56:59], v[100:103], v[32:35]\n\t" \
  "v_mfma_f32_16x16x32_bf16 v[36:39], v[56:59], v[104:107], v[36:39]\n\t" \
  "ds_read_b128 v[56:59], v117 offset:14336\n\t" \
  "s_waitcnt vmcnt(6)\n\t" \
  "s_waitcnt lgkmcnt(4)\n\t" \
  "v_mfma_f32_16x16x32_bf16 v[0:3], v[40:43], v[108:111], v[0:3]\n\t" \
  "v_mfma_f32_16x16x32_bf16 v[4:7], v[40:43], v[112:115], v[4:7]\n\t" \
  "ds_read_b128 v[40:43], v117 offset:20480\n\t" \
  "s_waitcnt lgkmcnt(4)\n\t" \
  "v_mfma_f32_16x16x32_bf16 v[8:11], v[44:47], v[108:111], v[8:11]\n\t" \
  "v_mfma_f32_16x16x32_bf16 v[12:15], v[44:47], v[112:115], v[12:15]\n\t" \
  "ds_read_b128 v[44:47], v117 offset:21504\n\t" \
  "s_waitcnt lgkmcnt(4)\n\t" \
  "v_mfma_f32_16x16x32_bf16 v[16:19], v[48:51], v[108:111], v[16:19]\n\t" \
  "v_mfma_f32_16x16x32_bf16 v[20:23], v[48:51], v[112:115], v[20:23]\n\t" \
  "ds_read_b128 v[48:51], v117 offset:22528\n\t" \
  "s_waitcnt lgkmcnt(4)\n\t" \
  "v_mfma_f32_16x16x32_bf16 v[24:27], v[52:55], v[108:111], v[24:27]\n\t" \
  "v_mfma_f32_16x16x32_bf16 v[28:31], v[52:55], v[112:115], v[28:31]\n\t" \
  "ds_read_b128 v[52:55], v117 offset:23552\n\t" \
  "s_waitcnt lgkmcnt(4)\n\t" \
  "v_mfma_f32_16x16x32_bf16 v[32:35], v[56:59], v[108:111], v[32:35]\n\t" \
  "v_mfma_f32_16x16x32_bf16 v[36:39], v[56:59], v[112:115], v[36:39]\n\t" \
  "ds_read_b128 v[56:59], v117 offset:24576\n\t" \
  "s_waitcnt vmcnt(4)\n\t" \
  "s_waitcnt lgkmcnt(4)\n\t" \
  "v_mfma_f32_16x16x32_bf16 v[0:3], v[40:43], v[60:63], v[0:3]\n\t" \
  "v_mfma_f32_16x16x32_bf16 v[4:7], v[40:43], v[64:67], v[4:7]\n\t" \
  "ds_read_b128 v[40:43], v117 offset:30720\n\t" \
  "s_waitcnt lgkmcnt(4)\n\t" \
  "v_mfma_f32_16x16x32_bf16 v[8:11], v[44:47], v[60:63], v[8:11]\n\t" \
  "v_mfma_f32_16x16x32_bf16 v[12:15], v[44:47], v[64:67], v[12:15]\n\t" \
  "ds_read_b128 v[44:47], v117 offset:31744\n\t" \
  "s_waitcnt lgkmcnt(4)\n\t" \
  "v_mfma_f32_16x16x32_bf16 v[16:19], v[48:51], v[60:63], v[16:19]\n\t" \
  "v_mfma_f32_16x16x32_bf16 v[20:23], v[48:51], v[64:67], v[20:23]\n\t" \
  "ds_read_b128 v[48:51], v117 offset:32768\n\t" \
  "s_waitcnt lgkmcnt(4)\n\t" \
  "v_mfma_f32_16x16x32_bf16 v[24:27], v[52:55], v[60:63], v[24:27]\n\t" \
  "v_mfma_f32_16x16x32_bf16 v[28:31], v[52:55], v[64:67], v[28:31]\n\t" \
  "ds_read_b128 v[52:55], v117 offset:33792\n\t" \
  "s_waitcnt lgkmcnt(4)\n\t" \
  "v_mfma_f32_16x16x32_bf16 v[32:35], v[56:59], v[60:63], v[32:35]\n\t" \
  "v_mfma_f32_16x16x32_bf16 v[36:39], v[56:59], v[64:67], v[36:39]\n\t" \
  "ds_read_b128 v[56:59], v117 offset:34816\n\t" \
  "s_waitcnt vmcnt(2)\n\t" \
  "s_waitcnt lgkmcnt(4)\n\t" \
  "v_mfma_f32_16x16x32_bf16 v[0:3], v[40:43], v[68:71], v[0:3]\n\t" \
  "v_mfma_f32_16x16x32_bf16 v[4:7], v[40:43], v[72:75], v[4:7]\n\t" \
  "ds_read_b128 v[40:43], v117 offset:40960\n\t" \
  "s_waitcnt lgkmcnt(4)\n\t" \
  "v_mfma_f32_16x16x32_bf16 v[8:11], v[44:47], v[68:71], v[8:11]\n\t" \
  "v_mfma_f32_16x16x32_bf16 v[12:15], v[44:47], v[72:75], v[12:15]\n\t" \
  "ds_read_b128 v[44:47], v117 offset:41984\n\t" \
  "s_waitcnt lgkmcnt(4)\n\t" \
  "v_mfma_f32_16x16x32_bf16 v[16:19], v[48:51], v[68:71], v[16:19]\n\t" \
  "v_mfma_f32_16x16x32_bf16 v[20:23], v[48:51], v[72:75], v[20:23]\n\t" \
  "ds_read_b128 v[48:51], v117 offset:43008\n\t" \
  "s_waitcnt lgkmcnt(4)\n\t" \
  "v_mfma_f32_16x16x32_bf16 v[24:27], v[52:55], v[68:71], v[24:27]\n\t" \
  "v_mfma_f32_16x16x32_bf16 v[28:31], v[52:55], v[72:75], v[28:31]\n\t" \
  "ds_read_b128 v[52:55], v117 offset:44032\n\t" \
  "s_waitcnt lgkmcnt(4)\n\t" \
  "v_mfma_f32_16x16x32_bf16 v[32:35], v[56:59], v[68:71], v[32:35]\n\t" \
  "v_mfma_f32_16x16x32_bf16 v[36:39], v[56:59], v[72:75], v[36:39]\n\t" \
  "ds_read_b128 v[56:59], v117 offset:45056\n\t" \
  "s_waitcnt vmcnt(0)\n\t" \
  "s_waitcnt lgkmcnt(4)\n\t" \
  "v_mfma_f32_16x16x32_bf16 v[0:3], v[40:43], v[76:79], v[0:3]\n\t" \
  "v_mfma_f32_16x16x32_bf16 v[4:7], v[40:43], v[80:83], v[4:7]\n\t" \
  "s_waitcnt lgkmcnt(3)\n\t" \
  "v_mfma_f32_16x16x32_bf16 v[8:11], v[44:47], v[76:79], v[8:11]\n\t" \
  "v_mfma_f32_16x16x32_bf16 v[12:15], v[44:47], v[80:83], v[12:15]\n\t" \
  "s_waitcnt lgkmcnt(2)\n\t" \
  "v_mfma_f32_16x16x32_bf16 v[16:19], v[48:51], v[76:79], v[16:19]\n\t" \
  "v_mfma_f32_16x16x32_bf16 v[20:23], v[48:51], v[80:83], v[20:23]\n\t" \
  "s_waitcnt lgkmcnt(1)\n\t" \
  "v_mfma_f32_16x16x32_bf16 v[24:27], v[52:55], v[76:79], v[24:27]\n\t" \
  "v_mfma_f32_16x16x32_bf16 v[28:31], v[52:55], v[80:83], v[28:31]\n\t" \
  "s_waitcnt lgkmcnt(0)\n\t" \
  "v_mfma_f32_16x16x32_bf16 v[32:35], v[56:59], v[76:79], v[32:35]\n\t" \
  "v_mfma_f32_16x16x32_bf16 v[36:39], v[56:59], v[80:83], v[36:39]\n\t" \
  "s_nop 7\n\t"
#define K1_LOOP_ASM \
  "v_mov_b64 v[0:1], 0\n\t" \
  "v_mov_b64 v[2:3], 0\n\t" \
  "v_mov_b64 v[4:5], 0\n\t" \
  "v_mov_b64 v[6:7], 0\n\t" \
  "v_mov_b64 v[8:9], 0\n\t" \
  "v_mov_b64 v[10:11], 0\n\t" \
  "v_mov_b64 v[12:13], 0\n\t" \
  "v_mov_b64 v[14:15], 0\n\t" \
  "v_mov_b64 v[16:17], 0\n\t" \
  "v_mov_b64 v[18:19], 0\n\t" \
  "v_mov_b64 v[20:21], 0\n\t" \
  "v_mov_b64 v[22:23], 0\n\t" \
  "v_mov_b64 v[24:25], 0\n\t" \
  "v_mov_b64 v[26:27], 0\n\t" \
  "v_mov_b64 v[28:29], 0\n\t" \
  "v_mov_b64 v[30:31], 0\n\t" \
  "v_mov_b64 v[32:33], 0\n\t" \
  "v_mov_b64 v[34:35], 0\n\t" \
  "v_mov_b64 v[36:37], 0\n\t" \
  "v_mov_b64 v[38:39], 0\n\t" \
  "v_mov_b64 v[40:41], 0\n\t" \
  "v_mov_b64 v[42:43], 0\n\t" \
  "v_mov_b64 v[44:45], 0\n\t" \
  "v_mov_b64 v[46:47], 0\n\t" \
  "v_mov_b64 v[48:49], 0\n\t" \
  "v_mov_b64 v[50:51], 0\n\t" \
  "v_mov_b64 v[52:53], 0\n\t" \
  "v_mov_b64 v[54:55], 0\n\t" \
  "v_mov_b64 v[56:57], 0\n\t" \
  "v_mov_b64 v[58:59], 0\n\t" \
  "v_mov_b64 v[60:61], 0\n\t" \
  "v_mov_b64 v[62:63], 0\n\t" \
  "v_mov_b64 v[64:65], 0\n\t" \
  "v_mov_b64 v[66:67], 0\n\t" \
  "v_mov_b64 v[68:69], 0\n\t" \
  "v_mov_b64 v[70:71], 0\n\t" \
  "v_mov_b64 v[72:73], 0\n\t" \
  "v_mov_b64 v[74:75], 0\n\t" \
  "v_mov_b64 v[76:77], 0\n\t" \
  "v_mov_b64 v[78:79], 0\n\t" \
  "v_mov_b64 v[80:81], 0\n\t" \
  "v_mov_b64 v[82:83], 0\n\t" \
  "v_mov_b64 v[124:125], 0\n\t" \
  "v_mov_b64 v[126:127], 0\n\t" \
  "v_mov_b64 v[128:129], 0\n\t" \
  "v_mov_b64 v[130:131], 0\n\t" \
  "v_mov_b64 v[132:133], 0\n\t" \
  "v_mov_b64 v[134:135], 0\n\t" \
  "v_mov_b64 v[136:137], 0\n\t" \
  "v_mov_b64 v[138:139], 0\n\t" \
  "v_mov_b64 v[140:141], 0\n\t" \
  "v_mov_b64 v[142:143], 0\n\t" \
  "v_mov_b64 v[144:145], 0\n\t" \
  "v_mov_b64 v[146:147], 0\n\t" \
  "v_mov_b64 v[148:149], 0\n\t" \
  "v_mov_b64 v[150:151], 0\n\t" \
  "v_mov_b64 v[152:153], 0\n\t" \
  "v_mov_b64 v[154:155], 0\n\t" \
  "v_mov_b64 v[156:157], 0\n\t" \
  "v_mov_b64 v[158:159], 0\n\t" \
  "v_mov_b64 v[160:161], 0\n\t" \
  "v_mov_b64 v[162:163], 0\n\t" \
  "s_mov_b32 %[cnt], 0\n\t" \
  "1:\n\t" \
  "s_barrier\n\t" \
  "v_mfma_f32_16x16x32_bf16 v[0:3], v[140:143], v[124:127], v[0:3]\n\t" \
  "ds_read_b128 v[84:87], v164\n\t" \
  "v_mfma_f32_16x16x32_bf16 v[4:7], v[140:143], v[128:131], v[4:7]\n\t" \
  "ds_read_b64_tr_b16 v[100:101], v166 offset:0\n\t" \
  "v_mfma_f32_16x16x32_bf16 v[8:11], v[140:143], v[132:135], v[8:11]\n\t" \
  "ds_read_b64_tr_b16 v[102:103], v166 offset:2560\n\t" \
  "v_mfma_f32_16x16x32_bf16 v[12:15], v[140:143], v[136:139], v[12:15]\n\t" \
  "ds_read_b128 v[88:91], v164 offset:2048\n\t" \
  "v_mfma_f32_16x16x32_bf16 v[16:19], v[144:147], v[124:127], v[16:19]\n\t" \
  "ds_read_b128 v[92:95], v164 offset:4096\n\t" \
  "v_mfma_f32_16x16x32_bf16 v[20:23], v[144:147], v[128:131], v[20:23]\n\t" \
  "ds_read_b128 v[96:99], v164 offset:6144\n\t" \
  "v_mfma_f32_16x16x32_bf16 v[24:27], v[144:147], v[132:135], v[24:27]\n\t" \
  "ds_read_b64_tr_b16 v[104:105], v166 offset:8\n\t" \
  "v_mfma_f32_16x16x32_bf16 v[28:31], v[144:147], v[136:139], v[28:31]\n\t" \
  "ds_read_b64_tr_b16 v[106:107], v166 offset:2568\n\t" \
  "v_mfma_f32_16x16x32_bf16 v[32:35], v[148:151], v[124:127], v[32:35]\n\t" \
  "ds_read_b64_tr_b16 v[108:109], v166 offset:16\n\t" \
  "v_mfma_f32_16x16x32_bf16 v[36:39], v[148:151], v[128:131], v[36:39]\n\t" \
  "ds_read_b64_tr_b16 v[110:111], v166 offset:2576\n\t" \
  "v_mfma_f32_16x16x32_bf16 v[40:43], v[148:151], v[132:135], v[40:43]\n\t" \
  "ds_read_b64_tr_b16 v[112:113], v166 offset:24\n\t" \
  "v_mfma_f32_16x16x32_bf16 v[44:47], v[148:151], v[136:139], v[44:47]\n\t" \
  "ds_read_b64_tr_b16 v[114:115], v166 offset:2584\n\t" \
  "v_mfma_f32_16x16x32_bf16 v[48:51], v[152:155], v[124:127], v[48:51]\n\t" \
  "ds_read_b64_tr_b16 v[116:117], v166 offset:32\n\t" \
  "v_mfma_f32_16x16x32_bf16 v[52:55], v[152:155], v[128:131], v[52:55]\n\t" \
  "ds_read_b64_tr_b16 v[118:119], v166 offset:2592\n\t" \
  "v_mfma_f32_16x16x32_bf16 v[56:59], v[152:155], v[132:135], v[56:59]\n\t" \
  "ds_read_b64_tr_b16 v[120:121], v167 offset:0\n\t" \
  "v_mfma_f32_16x16x32_bf16 v[60:63], v[152:155], v[136:139], v[60:63]\n\t" \
  "ds_read_b64_tr_b16 v[122:123], v167 offset:160\n\t" \
  "v_mfma_f32_16x16x32_bf16 v[64:67], v[156:159], v[124:127], v[64:67]\n\t" \
  "s_and_b32 %[inc], %[cnt], 3\n\t" \
  "v_mfma_f32_16x16x32_bf16 v[68:71], v[156:159], v[128:131], v[68:71]\n\t" \
  "s_cmp_eq_u32 %[inc], 3\n\t" \
  "v_mfma_f32_16x16x32_bf16 v[72:75], v[156:159], v[132:135], v[72:75]\n\t" \
  "s_cselect_b32 %[inc], %[wrap], %[stp]\n\t" \
  "v_mfma_f32_16x16x32_bf16 v[76:79], v[156:159], v[136:139], v[76:79]\n\t" \
  "v_add_u32 v164, %[inc], v164\n\t" \
  "v_mfma_f32_16x16x32_bf16 v[80:83], v[160:163], v[124:127], v[80:83]\n\t" \
  "s_waitcnt lgkmcnt(0)\n\t" \
  "v_mfma_f32_16x16x32_bf16 v[0:3], v[100:103], v[84:87], v[0:3]\n\t" \
  "ds_read_b128 v[124:127], v165\n\t" \
  "v_mfma_f32_16x16x32_bf16 v[4:7], v[100:103], v[88:91], v[4:7]\n\t" \
  "ds_read_b64_tr_b16 v[140:141], v166 offset:5120\n\t" \
  "v_mfma_f32_16x16x32_bf16 v[8:11], v[100:103], v[92:95], v[8:11]\n\t" \
  "ds_read_b64_tr_b16 v[142:143], v166 offset:7680\n\t" \
  "v_mfma_f32_16x16x32_bf16 v[12:15], v[100:103], v[96:99], v[12:15]\n\t" \
  "ds_read_b128 v[128:131], v165 offset:2048\n\t" \
  "v_mfma_f32_16x16x32_bf16 v[16:19], v[104:107], v[84:87], v[16:19]\n\t" \
  "ds_read_b128 v[132:135], v165 offset:4096\n\t" \
  "v_mfma_f32_16x16x32_bf16 v[20:23], v[104:107], v[88:91], v[20:23]\n\t" \
  "ds_read_b128 v[136:139], v165 offset:6144\n\t" \
  "v_mfma_f32_16x16x32_bf16 v[24:27], v[104:107], v[92:95], v[24:27]\n\t" \
  "ds_read_b64_tr_b16 v[144:145], v166 offset:5128\n\t" \
  "v_mfma_f32_16x16x32_bf16 v[28:31], v[104:107], v[96:99], v[28:31]\n\t" \
  "ds_read_b64_tr_b16 v[146:147], v166 offset:7688\n\t" \
  "v_mfma_f32_16x16x32_bf16 v[32:35], v[108:111], v[84:87], v[32:35]\n\t" \
  "ds_read_b64_tr_b16 v[148:149], v166 offset:5136\n\t" \
  "v_mfma_f32_16x16x32_bf16 v[36:39], v[108:111], v[88:91], v[36:39]\n\t" \
  "ds_read_b64_tr_b16 v[150:151], v166 offset:7696\n\t" \
  "v_mfma_f32_16x16x32_bf16 v[40:43], v[108:111], v[92:95], v[40:43]\n\t" \
  "ds_read_b64_tr_b16 v[152:153], v166 offset:5144\n\t" \
  "v_mfma_f32_16x16x32_bf16 v[44:47], v[108:111], v[96:99], v[44:47]\n\t" \
  "ds_read_b64_tr_b16 v[154:155], v166 offset:7704\n\t" \
  "v_mfma_f32_16x16x32_bf16 v[48:51], v[112:115], v[84:87], v[48:51]\n\t" \
  "ds_read_b64_tr_b16 v[156:157], v166 offset:5152\n\t" \
  "v_mfma_f32_16x16x32_bf16 v[52:55], v[112:115], v[88:91], v[52:55]\n\t" \
  "ds_read_b64_tr_b16 v[158:159], v166 offset:7712\n\t" \
  "v_mfma_f32_16x16x32_bf16 v[56:59], v[112:115], v[92:95], v[56:59]\n\t" \
  "ds_read_b64_tr_b16 v[160:161], v167 offset:1280\n\t" \
  "v_mfma_f32_16x16x32_bf16 v[60:63], v[112:115], v[96:99], v[60:63]\n\t" \
  "ds_read_b64_tr_b16 v[162:163], v167 offset:1440\n\t" \
  "v_mfma_f32_16x16x32_bf16 v[64:67], v[116:119], v[84:87], v[64:67]\n\t" \
  "v_add_u32 v165, %[inc], v165\n\t" \
  "v_mfma_f32_16x16x32_bf16 v[68:71], v[116:119], v[88:91], v[68:71]\n\t" \
  "v_add_u32 v166, %[inc], v166\n\t" \
  "v_mfma_f32_16x16x32_bf16 v[72:75], v[116:119], v[92:95], v[72:75]\n\t" \
  "v_add_u32 v167, %[inc], v167\n\t" \
  "v_mfma_f32_16x16x32_bf16 v[76:79], v[116:119], v[96:99], v[76:79]\n\t" \
  "s_add_u32 %[cnt], %[cnt], 1\n\t" \
  "v_mfma_f32_16x16x32_bf16 v[80:83], v[120:123], v[84:87], v[80:83]\n\t" \
  "s_cmp_lt_u32 %[cnt], 10\n\t" \
  "s_waitcnt lgkmcnt(0)\n\t" \
  "s_cbranch_scc1 1b\n\t" \
  "v_mfma_f32_16x16x32_bf16 v[0:3], v[140:143], v[124:127], v[0:3]\n\t" \
  "v_mfma_f32_16x16x32_bf16 v[4:7], v[140:143], v[128:131], v[4:7]\n\t" \
  "v_mfma_f32_16x16x32_bf16 v[8:11], v[140:143], v[132:135], v[8:11]\n\t" \
  "v_mfma_f32_16x16x32_bf16 v[12:15], v[140:143], v[136:139], v[12:15]\n\t" \
  "v_mfma_f32_16x16x32_bf16 v[16:19], v[144:147], v[124:127], v[16:19]\n\t" \
  "v_mfma_f32_16x16x32_bf16 v[20:23], v[144:147], v[128:131], v[20:23]\n\t" \
  "v_mfma_f32_16x16x32_bf16 v[24:27], v[144:147], v[132:135], v[24:27]\n\t" \
  "v_mfma_f32_16x16x32_bf16 v[28:31], v[144:147], v[136:139], v[28:31]\n\t" \
  "v_mfma_f32_16x16x32_bf16 v[32:35], v[148:151], v[124:127], v[32:35]\n\t" \
  "v_mfma_f32_16x16x32_bf16 v[36:39], v[148:151], v[128:131], v[36:39]\n\t" \
  "v_mfma_f32_16x16x32_bf16 v[40:43], v[148:151], v[132:135], v[40:43]\n\t" \
  "v_mfma_f32_16x16x32_bf16 v[44:47], v[148:151], v[136:139], v[44:47]\n\t" \
  "v_mfma_f32_16x16x32_bf16 v[48:51], v[152:155], v[124:127], v[48:51]\n\t" \
  "v_mfma_f32_16x16x32_bf16 v[52:55], v[152:155], v[128:131], v[52:55]\n\t" \
  "v_mfma_f32_16x16x32_bf16 v[56:59], v[152:155], v[132:135], v[56:59]\n\t" \
  "v_mfma_f32_16x16x32_bf16 v[60:63], v[152:155], v[136:139], v[60:63]\n\t" \
  "v_mfma_f32_16x16x32_bf16 v[64:67], v[156:159], v[124:127], v[64:67]\n\t" \
  "v_mfma_f32_16x16x32_bf16 v[68:71], v[156:159], v[128:131], v[68:71]\n\t" \
  "v_mfma_f32_16x16x32_bf16 v[72:75], v[156:159], v[132:135], v[72:75]\n\t" \
  "v_mfma_f32_16x16x32_bf16 v[76:79], v[156:159], v[136:139], v[76:79]\n\t" \
  "v_mfma_f32_16x16x32_bf16 v[80:83], v[160:163], v[124:127], v[80:83]\n\t" \
  "s_nop 7\n\t"
#define K1_LOOP_CLOB "v84", "v85", "v86", "v87", "v88", "v89", "v90", "v91", "v92", "v93", "v94", "v95", "v96", "v97", "v98", "v99", "v100", "v101", "v102", "v103", "v104", "v105", "v106", "v107", "v108", "v109", "v110", "v111", "v112", "v113", "v114", "v115", "v116", "v117", "v118", "v119", "v120", "v121", "v122", "v123", "v124", "v125", "v126", "v127", "v128", "v129", "v130", "v131", "v132", "v133", "v134", "v135", "v136", "v137", "v138", "v139", "v140", "v141", "v142", "v143", "v144", "v145", "v146", "v147", "v148", "v149", "v150", "v151", "v152", "v153", "v154", "v155", "v156", "v157", "v158", "v159", "v160", "v161", "v162", "v163"
#define K1_ROT ((tt + 3 * (jb >> 3)) % 10)
constexpr int K1_THREADS = 512;
constexpr u32 K1_SLOT = 40960, K1_W = 16384, K1_EX = 36864;
constexpr u32 K1_WPHI = 2 * K1_SLOT;
constexpr u32 K1_XCH = 3 * K1_SLOT;
constexpr u32 K1_SCR = K1_XCH + 20480;
constexpr u32 K1_VI = 0, K1_VI_P = 208, K1_PHK = 27648, K1_PHK_P = 336;

__device__ __forceinline__ float bflo(u32 v) { return __uint_as_float(v << 16); }
__device__ __forceinline__ float bfhi(u32 v) { return __uint_as_float(v & 0xffff0000u); }

extern "C" __global__ void __launch_bounds__(512, 2)
k1_proj(const char* __restrict__ x, const char* __restrict__ wqkv, const char* __restrict__ wphi, const char* __restrict__ wq,
        const char* __restrict__ wk, const char* __restrict__ wv, const char* __restrict__ gw, const char* __restrict__ awo,
        const char* __restrict__ bwo, char* __restrict__ ws, const char* __restrict__ outp) {
  extern __shared__ __attribute__((aligned(16))) char smem[];
  ldsp lds = (ldsp)smem;
  const u32 lds0 = (u32)(uintptr_t)lds;
  const int lane = threadIdx.x & 63;
  const int w = __builtin_amdgcn_readfirstlane(threadIdx.x >> 6);
  STAMP_DECL; RSTAMP(14); STAMP(0);
  const int bid = blockIdx.x, xcd = bid & 7, jb = bid >> 3;
  const int tt = (xcd & 1) * 8 + (jb & 7), combo = (xcd >> 1) * 4 + (jb >> 3), type = combo & 1, hg = combo >> 1;
  const int g = lane >> 4, i16 = lane & 15, nsel = hg & 3, head = 4 * hg + g;
  const int blk = (tt * 8 + hg) * 4 + (w - 4);
  const bool relay = !type && w >= 4 && blk < 320;

  if (w >= 4) {
    __builtin_amdgcn_s_setprio(3);
    const int lw = w - 4, g1 = lw >> 1, hb = lw & 1;
    const u32x4 rsX = mkrsrc(x - 4096, SEQ * DM * 2 + 4096);
    const bool useq = g1 && !type;
    const u32x4 rsW = useq ? mkrsrc(wq - 4096, DM * DM * 2 + 4096) : mkrsrc(wqkv - 4096, DM * 3 * DM * 2 + 4096);
    const u32 pitch = useq ? 1280u : 3840u;
    const u32 col = (type ? (g1 ? 1280u : 640u) : 0u) + 80u * hg;
    const u32x4 rsE = mkrsrc(type ? wv : wk, DM * DH * 2);
    u32 vx[4], vw[5];
    const u32 vx0 = 4096u + (u32)(lane >> 3) * 1280u + (u32)(((lane & 7) ^ (lane >> 4)) * 16);
#pragma unroll
    for (int i = 0; i < 4; ++i) {
      const int p = 2 * lw + (i >> 1), st = (p & 4) | ((p & 3) ^ nsel);
      vx[i] = (vx0 ^ (u32)((i & 1) * 64)) + (u32)(st * 16 + (i & 1) * 8) * 1280u - (u32)i * 1024u;
    }
#pragma unroll
    for (int j = 0; j < 5; ++j) {
      const u32 c = 64u * j + lane, rho = (c * 205u) >> 11, cc = c - rho * 10u;
      const u32 k = 8u * ((rho >> 2) & 3u) + 4u * (rho >> 4) + (rho & 3u);
      vw[j] = 4096u + k * pitch + cc * 16u - (u32)(j & 3) * 1024u;
    }
    const u32 be = (u32)(lw * 64 + lane) * 16u;
    const u32 ve = (hg < 4 && be < 2560u) ? be : OOB;
    const u32 sx0 = (u32)(128 * tt) * 1280u, sw0 = 32u * hb * pitch + col * 2u;
    int ks = K1_ROT;
    auto issue = [&](int s) {
      const u32 base = lds0 + (u32)(s & 3) * K1_SLOT;
      u32 keep;
      asm volatile(
          "s_mov_b32 %0, m0\n\ts_mov_b32 m0, %[mx]\n\ts_nop 2\n\t"
          "buffer_load_dwordx4 %[x0], %[rx], %[sx] offen lds\n\t"
          "buffer_load_dwordx4 %[x1], %[rx], %[sx] offen offset:1024 lds\n\t"
          "buffer_load_dwordx4 %[x2], %[rx], %[sx] offen offset:2048 lds\n\t"
          "buffer_load_dwordx4 %[x3], %[rx], %[sx] offen offset:3072 lds\n\t"
          "s_mov_b32 m0, %[mw]\n\ts_nop 0\n\t"
          "buffer_load_dwordx4 %[w0], %[rw], %[sw] offen lds\n\t"
          "buffer_load_dwordx4 %[w1], %[rw], %[sw] offen offset:1024 lds\n\t"
          "buffer_load_dwordx4 %[w2], %[rw], %[sw] offen offset:2048 lds\n\t"
          "buffer_load_dwordx4 %[w3], %[rw], %[sw] offen offset:3072 lds\n\t"
          "s_add_u32 m0, %[mw], 4096\n\ts_nop 0\n\t"
          "buffer_load_dwordx4 %[w4], %[rw], %[sw] offen lds\n\t"
          "s_mov_b32 m0, %[me]\n\ts_nop 0\n\t"
          "buffer_load_dwordx4 %[e0], %[re], %[se] offen lds\n\t"
          "s_mov_b32 m0, %0"
          : "=&s"(keep)
          : [mx] "s"(base + (u32)lw * 4096u), [mw] "s"(base + K1_W + (u32)lw * 5120u), [me] "s"(base + K1_EX + (u32)lw * 1024u),
            [rx] "s"(rsX), [rw] "s"(rsW), [re] "s"(rsE), [sx] "s"(sx0 + (u32)ks * 128u), [sw] "s"(sw0 + (u32)ks * 64u * pitch), [se] "s"((u32)ks * 2560u),
            [x0] "v"(vx[0]), [x1] "v"(vx[1]), [x2] "v"(vx[2]), [x3] "v"(vx[3]), [w0] "v"(vw[0]), [w1] "v"(vw[1]), [w2] "v"(vw[2]), [w3] "v"(vw[3]),
            [w4] "v"(vw[4]), [e0] "v"(ve)
          : "memory", "scc");
      ks = ks == 9 ? 0 : ks + 1;
    };
    issue(0); issue(1);
    STAMP(1);
    WAIT_BAR(10);
    STAMP(2);
    issue(2);
#pragma unroll 1
    for (int s = 1; s < 8; ++s) {
      issue(s + 2);
      WAIT_BAR(20);
      if (s == 1) STAMP(3); if (s == 3) STAMP(4); if (s == 5) STAMP(5);
    }
    STAMP(6);
    dma_buf(mkrsrc(wphi, NH * 400 * 2), be < 3200u ? be : OOB, (u32)hg * 3200u, lds0 + K1_WPHI + (u32)lw * 1024u);
    __builtin_amdgcn_s_setprio(0);
    u32 vo[5];
    if (relay) {
      const u32 kk = (u32)blk % 20u;
#pragma unroll
      for (int j = 0; j < 5; ++j) {
        const u32 c = 64u * j + lane, rho = (c * 205u) >> 11, cc = c - rho * 10u;
        const u32 U = 4u * kk + ((rho >> 2) & 3u), u = rho >> 4, q = rho & 3u;
        const u32 hp = (U * 205u) >> 10, un = U - hp * 5u;
        const u32 row = (un < 4u) ? (2u * hp + (un >> 1)) * 20u + 4u * (un & 1u) + 8u * u + q : (2u * hp + u) * 20u + 16u + q;
        vo[j] = row * 1280u + cc * 16u;
      }
    }
    WAIT_BAR(11);
    if (relay) {
      const u32x4 rs = mkrsrc(((blk / 20) & 1) ? bwo : awo, DM * DM * 2);
#pragma unroll
      for (int j = 0; j < 5; ++j) dma_buf(rs, vo[j], (u32)(blk / 40) * 160u, lds0 + K1_SCR + (u32)lw * 5120u + (u32)j * 1024u);
      WAIT_BAR(5);
    } else {
      WAIT_BAR(0);
    }
    STAMP(7);
  } else {
    {
      const int gtok = 128 * tt + 8 * combo + 2 * w;
      float a0 = 0.f, a1 = 0.f;
      u32 xg[2][5], gg[5];
#pragma unroll
      for (int i = 0; i < 5; ++i) {
        gg[i] = *(const u32*)(gw + (i * 64 + lane) * 4);
        xg[0][i] = *(const u32*)(x + (size_t)gtok * 1280 + (i * 64 + lane) * 4);
        xg[1][i] = *(const u32*)(x + (size_t)gtok * 1280 + 1280 + (i * 64 + lane) * 4);
      }
#pragma unroll
      for (int i = 0; i < 5; ++i) {
        a0 = __builtin_fmaf(bflo(xg[0][i]), bflo(gg[i]), a0); a0 = __builtin_fmaf(bfhi(xg[0][i]), bfhi(gg[i]), a0);
        a1 = __builtin_fmaf(bflo(xg[1][i]), bflo(gg[i]), a1); a1 = __builtin_fmaf(bfhi(xg[1][i]), bfhi(gg[i]), a1);
      }
#pragma unroll
      for (int m = 32; m >= 1; m >>= 1) { a0 += __shfl_xor(a0, m); a1 += __shfl_xor(a1, m); }
      if (lane < 2) ((float*)(ws + WS_GATE))[gtok + lane] = __builtin_amdgcn_rcpf(1.f + __builtin_amdgcn_exp2f(-(lane ? a1 : a0) * LOG2E));
      if (bid == 0 && threadIdx.x == 0) *(unsigned long long*)(ws + WS_EPOCH) += 0x9E3779B97F4A7C15ull;
    }
    const int cg = w >> 1, th = w & 1;
    u32 aW = lds0 + K1_W + (u32)cg * 10240u + (u32)(4 * g + (i16 >> 2)) * 160u + (u32)(i16 & 3) * 40u;
    u32 aX0 = lds0 + (u32)(64 * th + i16) * 128u + (u32)((g ^ (i16 >> 1)) * 16), aX1 = aX0 ^ 64u;
    u32 aE = lds0 + K1_EX + (u32)(8 * g + (i16 >> 2)) * 40u + (u32)(i16 & 3) * 8u + (u32)cg * 32u;
    f32x16 ac[5];
    f32x4 acce;
    STAMP(1);
    {
      u32 cnt, inc;
      asm volatile(K1_LOOP_ASM
                   : "=&{v[0:15]}"(ac[0]), "=&{v[16:31]}"(ac[1]), "=&{v[32:47]}"(ac[2]), "=&{v[48:63]}"(ac[3]), "=&{v[64:79]}"(ac[4]), "=&{v[80:83]}"(acce),
                     "+{v164}"(aX0), "+{v165}"(aX1), "+{v166}"(aW), "+{v167}"(aE), [cnt] "=&s"(cnt), [inc] "=&s"(inc)
                   : [wrap] "s"(0u - 3u * K1_SLOT), [stp] "s"(K1_SLOT)
                   : "memory", "scc", K1_LOOP_CLOB);
    }
    STAMP(6);
    const float sc = (cg && !type) ? QSCALE : 1.f;
#pragma unroll
    for (int n = 0; n < 4; ++n) {
      u32 v[10];
#pragma unroll
      for (int T = 0; T < 5; ++T) { v[2 * T] = pk2(ac[T][4 * n] * sc, ac[T][4 * n + 1] * sc); v[2 * T + 1] = pk2(ac[T][4 * n + 2] * sc, ac[T][4 * n + 3] * sc); }
      const int tl = 64 * th + 16 * (n ^ nsel) + i16;
      if (cg == 0) {
        ldsp xw = lds + K1_XCH + (u32)(2 * n + th) * 2560u;
        *(LDSAS u32x4*)(xw + lane * 16) = (u32x4){v[0], v[1], v[2], v[3]};
        *(LDSAS u32x4*)(xw + 1024 + lane * 16) = (u32x4){v[4], v[5], v[6], v[7]};
        *(LDSAS u32x2*)(xw + 2048 + lane * 8) = (u32x2){v[8], v[9]};
      } else if (type == 0) {
        char* dst = ws + WS_QB + (size_t)(head * 64 + 4 * tt + (tl >> 5)) * 1280;
        *(u32x4*)(dst + (tl & 31) * 16) = (u32x4){v[0], v[1], v[2], v[3]};
        *(u32x4*)(dst + 512 + (tl & 31) * 16) = (u32x4){v[4], v[5], v[6], v[7]};
        *(u32x2*)(dst + 1024 + (tl & 31) * 8) = (u32x2){v[8], v[9]};
      } else {
        ldsp dst = lds + K1_VI + (u32)tl * K1_VI_P + (u32)g * 48u;
        *(LDSAS u32x4*)(dst) = (u32x4){v[0], v[1], v[2], v[3]};
        *(LDSAS u32x4*)(dst + 16) = (u32x4){v[4], v[5], v[6], v[7]};
        *(LDSAS u32x4*)(dst + 32) = (u32x4){v[8], v[9], 0x00003F80u, 0u};
      }
    }
    if (hg < 4) {
      const int tl = 64 * th + 16 * nsel + i16, k32 = tl & 31, tile = 4 * tt + (tl >> 5);
      const u32x2 ev = {pk2(acce[0], acce[1]), pk2(acce[2], acce[3])};
      if (type == 0) {
        char* rec = ws + WS_KVIM + ((tile + 62) & 63) * REC;
        if (cg == 0) *(u32x2*)(rec + (g >> 1) * 512 + k32 * 16 + (g & 1) * 8) = ev;
        else if (g == 0) { *(u32*)(rec + 1024 + k32 * 4) = ev[0]; *(u32*)(rec + 1152 + k32 * 4) = ev[1]; }
      } else {
        unsigned short* vp = (unsigned short*)(ws + WS_KVIM + tile * REC + 1280 + (k32 >> 3) * 352 + (16 * cg + 4 * g) * 16 + (k32 & 7) * 2);
        if (cg == 0 || g == 0) {
          vp[0] = (unsigned short)ev[0]; vp[8] = (unsigned short)(ev[0] >> 16); vp[16] = (unsigned short)ev[1]; vp[24] = (unsigned short)(ev[1] >> 16);
        } else if (g == 1) { vp[0] = 0x3F80; vp[8] = 0; }
      }
    }
    STAMP(7);
  }

  {
    const u32 aP = K1_WPHI + (((i16 & 3) == g) ? (u32)g * 800u + (u32)(i16 >> 2) * 40u : 3200u);
    u32x4 a0[5], a1[5]; u32x2 a2[5];
#pragma unroll
    for (int T = 0; T < 5; ++T) {
      a0[T] = trd2(lds + aP + T * 8, lds + aP + T * 8 + 160);
      a1[T] = trd2(lds + aP + T * 8 + 320, lds + aP + T * 8 + 480);
      a2[T] = trd(lds + aP + T * 8 + 640);
    }
    LBAR();
    STAMP(8);
    ldsp xr = lds + K1_XCH + (u32)w * 2560u;
    const u32x4 bq0 = ld128(xr + lane * 16), bq1 = ld128(xr + 1024 + lane * 16);
    const u32x2 bq2 = ld64(xr + 2048 + lane * 8);
    f32x4 pq[5];
#pragma unroll
    for (int T = 0; T < 5; ++T) pq[T] = mfma16(a0[T], bq0, (f32x4){0.f, 0.f, 0.f, 0.f});
#pragma unroll
    for (int T = 0; T < 5; ++T) pq[T] = mfma16(a1[T], bq1, pq[T]);
#pragma unroll
    for (int T = 0; T < 5; ++T) pq[T] = mfma16((u32x4){a2[T][0], a2[T][1], 0u, 0u}, (u32x4){bq2[0], bq2[1], 0u, 0u}, pq[T]);
    float mx = fmaxf(pq[0][0], pq[0][1]), mn = fminf(pq[0][0], pq[0][1]);
    mx = fmaxf(fmaxf(mx, pq[0][2]), pq[0][3]); mn = fminf(fminf(mn, pq[0][2]), pq[0][3]);
#pragma unroll
    for (int T = 1; T < 5; ++T) {
      mx = fmaxf(fmaxf(mx, pq[T][0]), pq[T][1]); mx = fmaxf(fmaxf(mx, pq[T][2]), pq[T][3]);
      mn = fminf(fminf(mn, pq[T][0]), pq[T][1]); mn = fminf(fminf(mn, pq[T][2]), pq[T][3]);
    }
    const float cp = -mx * LOG2E, cn = mn * LOG2E;
    f32x4 ep[5], en[5];
#pragma unroll
    for (int T = 0; T < 5; ++T) { ep[T] = pq[T] * LOG2E + cp; en[T] = pq[T] * (-LOG2E) + cn; PIN(ep[T]); PIN(en[T]); }
#pragma unroll
    for (int T = 0; T < 5; ++T)
#pragma unroll
      for (int i = 0; i < 4; ++i) { ep[T][i] = __builtin_amdgcn_exp2f(ep[T][i]); en[T][i] = __builtin_amdgcn_exp2f(en[T][i]); }
    const f32x4 sp = (ep[0] + ep[1]) + (ep[2] + ep[3]) + ep[4], sn = (en[0] + en[1]) + (en[2] + en[3]) + en[4];
    const float rp = __builtin_amdgcn_rcpf((sp[0] + sp[1]) + (sp[2] + sp[3])), rn = __builtin_amdgcn_rcpf((sn[0] + sn[1]) + (sn[2] + sn[3]));
    u32 ph[20];
#pragma unroll
    for (int T = 0; T < 5; ++T) {
      const f32x4 a = ep[T] * rp, b = en[T] * rn;
      ph[2 * T] = pk2(a[0], a[1]); ph[2 * T + 1] = pk2(a[2], a[3]);
      ph[10 + 2 * T] = pk2(b[0], b[1]); ph[11 + 2 * T] = pk2(b[2], b[3]);
    }
    const int tl = 64 * (w & 1) + 16 * ((w >> 1) ^ nsel) + i16;
    if (type == 0) {
      char* dst = ws + WS_PHIQ + (size_t)(head * 64 + 4 * tt + (tl >> 5)) * 2560 + (tl & 31) * 16;
#pragma unroll
      for (int c = 0; c < 5; ++c) *(u32x4*)(dst + c * 512) = (u32x4){ph[4 * c], ph[4 * c + 1], ph[4 * c + 2], ph[4 * c + 3]};
    } else {
      ldsp dst = lds + K1_PHK + (u32)tl * K1_PHK_P + (u32)g * 80u;
#pragma unroll
      for (int c = 0; c < 5; ++c) *(LDSAS u32x4*)(dst + 16 * c) = (u32x4){ph[4 * c], ph[4 * c + 1], ph[4 * c + 2], ph[4 * c + 3]};
    }
  }
  STAMP(9);
  if (type) {
    LBAR();
    const int hl = w >> 1, dt = w & 1;
    const u32 trow = (u32)(8 * g + (i16 >> 2));
    ldsp pa = lds + K1_PHK + trow * K1_PHK_P + (u32)hl * 80u + (u32)(i16 & 3) * 8u;
    ldsp pb = lds + K1_VI + trow * K1_VI_P + (u32)hl * 48u + (u32)dt * 32u + (u32)(i16 & 3) * 8u;
    f32x4 kv[3];
#pragma unroll
    for (int ft = 0; ft < 3; ++ft) kv[ft] = (f32x4){0.f, 0.f, 0.f, 0.f};
#pragma unroll
    for (int ks = 0; ks < 4; ++ks) {
      u32x4 bv = trd2(pb + ks * 32 * K1_VI_P, pb + (ks * 32 + 4) * K1_VI_P);
#pragma unroll
      for (int ft = 0; ft < 3; ++ft) {
        u32x4 av = trd2(pa + ks * 32 * K1_PHK_P + ft * 32, pa + (ks * 32 + 4) * K1_PHK_P + ft * 32);
        kv[ft] = mfma16(av, bv, kv[ft]);
      }
    }
    const int d = 16 * dt + i16;
    float* dst = (float*)(ws + WS_KVP) + ((size_t)(4 * hg + hl) * 16 + tt) * 840 + d * 40 + 4 * g;
    if (d <= 20) {
      *(f32x4*)(dst) = kv[0];
      *(f32x4*)(dst + 16) = kv[1];
      if (g < 2) *(f32x4*)(dst + 32) = kv[2];
    }
  } else if (relay) {
    asm volatile("s_waitcnt vmcnt(5)" ::: "memory");
    ldsp p = lds + K1_SCR + (u32)(w - 4) * 5120u + (u32)(4 * g + (i16 >> 2)) * 160u + (u32)(i16 & 3) * 40u;
    char* wdst = ws + WS_WOF + (size_t)blk * 5120 + lane * 16;
#pragma unroll
    for (int T = 0; T < 5; ++T) *(u32x4*)(wdst + T * 1024) = trd2(p + T * 8, p + 2560 + T * 8);
  }
  STAMP(11); RSTAMP(15); STAMP_FLUSH(0);
}

constexpr u32 SL = 4 * REC;
constexpr u32 L_IMG = 5 * SL;
constexpr u32 L_KVT = L_IMG + 102400;
constexpr u32 L_GT = L_KVT + 2048;
constexpr u32 L_FLG = L_GT + 2048;
constexpr u32 K2_LDS = 163840;
constexpr u32 L_MRG = SL;
constexpr u32 F_GBAR = L_FLG + 96, F_APUB = L_FLG + 100, F_AFREE = L_FLG + 104, F_IMG = L_FLG + 108, F_BPUB = L_FLG + 112;
constexpr u32 SPIN_CAP = 1u << 20;
#define LSLEEP 2
#define NOF 12
#define AWAIT 7
#define GPRIO 3
typedef __attribute__((address_space(1))) unsigned long long gu64;

__device__ __forceinline__ f32x8 lo8(const f32x16& s) { return __builtin_shufflevector(s, s, 0, 1, 2, 3, 4, 5, 6, 7); }
__device__ __forceinline__ f32x8 hi8(const f32x16& s) { return __builtin_shufflevector(s, s, 8, 9, 10, 11, 12, 13, 14, 15); }
__device__ __forceinline__ float max16(const f32x16& s) {
  float a = fmaxf(fmaxf(s[0], s[1]), s[2]), b = fmaxf(fmaxf(s[3], s[4]), s[5]);
  a = fmaxf(fmaxf(a, s[6]), s[7]); b = fmaxf(fmaxf(b, s[8]), s[9]);
  a = fmaxf(fmaxf(a, s[10]), s[11]); b = fmaxf(fmaxf(b, s[12]), s[13]);
  a = fmaxf(fmaxf(a, s[14]), s[15]);
  return fmaxf(a, b);
}
__device__ __forceinline__ float xhalf_max(float m) {
  auto r = __builtin_amdgcn_permlane32_swap(__float_as_uint(m), __float_as_uint(m), false, false);
  return fmaxf(__uint_as_float(r[0]), __uint_as_float(r[1]));
}
__device__ __forceinline__ float from_hi(float v) {
  auto r = __builtin_amdgcn_permlane32_swap(__float_as_uint(v), __float_as_uint(v), false, false);
  return __uint_as_float(r[1]);
}
__device__ __forceinline__ u32 lds_ld(ldsp p) { return __hip_atomic_load((LDSAS u32*)p, __ATOMIC_RELAXED, __HIP_MEMORY_SCOPE_WORKGROUP); }
__device__ __forceinline__ void lds_st(ldsp p, u32 v) { __hip_atomic_store((LDSAS u32*)p, v, __ATOMIC_RELAXED, __HIP_MEMORY_SCOPE_WORKGROUP); }
__device__ __forceinline__ void lds_wait_ge(ldsp p, u32 want) {
#pragma unroll 1
  for (u32 it = 0; it < SPIN_CAP; ++it) {
    if ((u32)__builtin_amdgcn_readfirstlane(lds_ld(p)) >= want) break;
    __builtin_amdgcn_s_sleep(LSLEEP);
  }
  asm volatile("" ::: "memory");
}
__device__ __forceinline__ u32 lds_arrive(ldsp p, int) {
  u32 old; unsigned long long keep;
  asm volatile("v_mov_b32 %0, 1\n\ts_mov_b64 %1, exec\n\ts_mov_b64 exec, 1\n\ts_waitcnt lgkmcnt(0)\n\tds_add_rtn_u32 %0, %2, %0\n\t"
               "s_waitcnt lgkmcnt(0)\n\ts_mov_b64 exec, %1" : "=&v"(old), "=&s"(keep) : "v"((u32)(uintptr_t)p) : "memory");
  return (u32)__builtin_amdgcn_readfirstlane(old);
}
__device__ __forceinline__ void flags_wait(const char* f, int n, unsigned long long tag, int lane) {
  const gu64* p = (const gu64*)f + (lane < n ? lane : 0);
#pragma unroll 1
  for (u32 it = 0; it < SPIN_CAP; ++it) {
    const unsigned long long v = __hip_atomic_load(p, __ATOMIC_RELAXED, __HIP_MEMORY_SCOPE_AGENT);
    if (__all(v == tag)) break;
    __builtin_amdgcn_s_sleep(2);
  }
  asm volatile("" ::: "memory");
}
__device__ __forceinline__ void dma4(u32x4 rs, u32 voff, u32 soff, u32 ldsaddr) {
  u32 keep;
  asm volatile("s_mov_b32 %0, m0\n\ts_mov_b32 m0, %4\n\ts_nop 2\n\t"
               "buffer_load_dwordx4 %1, %2, %3 offen lds\n\tbuffer_load_dwordx4 %1, %2, %3 offen offset:1024 lds\n\t"
               "buffer_load_dwordx4 %1, %2, %3 offen offset:2048 lds\n\tbuffer_load_dwordx4 %1, %2, %3 offen offset:3072 lds\n\t"
               "s_mov_b32 m0, %0" : "=&s"(keep) : "v"(voff), "s"(rs), "s"(__builtin_amdgcn_readfirstlane(soff)), "s"(__builtin_amdgcn_readfirstlane(ldsaddr)) : "memory");
}
__device__ __forceinline__ void dma2h(u32x4 rs, u32 voff, u32 soff, u32 ldsaddr) {
  u32 keep;
  asm volatile("s_mov_b32 %0, m0\n\ts_mov_b32 m0, %4\n\ts_nop 2\n\t"
               "buffer_load_dwordx4 %1, %2, %3 offen lds\n\tbuffer_load_dwordx4 %1, %2, %3 offen offset:1024 lds\n\t"
               "s_mov_b32 exec_hi, 0\n\tbuffer_load_dwordx4 %1, %2, %3 offen offset:2048 lds\n\ts_mov_b32 exec_hi, -1\n\t"
               "s_mov_b32 m0, %0" : "=&s"(keep) : "v"(voff), "s"(rs), "s"(__builtin_amdgcn_readfirstlane(soff)), "s"(__builtin_amdgcn_readfirstlane(ldsaddr)) : "memory");
}

__device__ __forceinline__ f32x16 attn_exact(const char* kvim, u32x4 qx, u32 q2lo, int lane) {
  const f32x16 zero16 = {0.f, 0.f, 0.f, 0.f, 0.f, 0.f, 0.f, 0.f, 0.f, 0.f, 0.f, 0.f, 0.f, 0.f, 0.f, 0.f};
  const int r32 = lane & 31, hh = lane >> 5;
  const u32x2 qz = {q2lo, 0u};
  float m = -INFINITY;
#pragma unroll 1
  for (int rec = 0; rec < 64; ++rec) {
    const u32x4 a1 = *(const u32x4*)(kvim + rec * REC + lane * 16);
    const u32 a2 = *(const u32*)(kvim + rec * REC + 1024 + lane * 4);
    f32x16 s = mfma32(a1, qx, zero16);
    s = mfma32k8((u32x2){a2, 0u}, qz, s);
    m = fmaxf(m, max16(s));
  }
  m = xhalf_max(m);
  f32x16 O = zero16;
  const u32 av = 1280u + (u32)hh * 352u + (u32)(r32 <= 20 ? r32 : 21) * 16u;
#pragma unroll 1
  for (int t2 = 0; t2 < 64; ++t2) {
    const int rec = (t2 + 62) & 63;
    const u32x4 a1 = *(const u32x4*)(kvim + rec * REC + lane * 16);
    const u32 a2 = *(const u32*)(kvim + rec * REC + 1024 + lane * 4);
    const u32x4 b1 = *(const u32x4*)(kvim + t2 * REC + av);
    const u32x4 b2 = *(const u32x4*)(kvim + t2 * REC + av + 704);
    f32x16 s = mfma32(a1, qx, zero16);
    s = mfma32k8((u32x2){a2, 0u}, qz, s);
#pragma unroll
    for (int r = 0; r < 16; ++r) s[r] = __builtin_amdgcn_exp2f(s[r] - m);
    O = smf(b1, lo8(s), O, 0xDDDD);
    O = smf(b2, hi8(s), O, 0xDDDD);
  }
  return O;
}

__global__ void __launch_bounds__(832, 4) k2_attn(char* __restrict__ ws, char* __restrict__ out) {
  extern __shared__ __attribute__((aligned(16))) char smem[];
  ldsp lds = (ldsp)smem;
  const u32 lds0 = (u32)(uintptr_t)lds;
  const int w = __builtin_amdgcn_readfirstlane(threadIdx.x >> 6);
  ISTAMP_INIT(); IRSTAMP(1, 14); ISTAMP(1, 0);
  const int xcd = blockIdx.x & 7, jb = blockIdx.x >> 3;
  const int t64 = xcd * 4 + (jb & 3), hg = jb >> 2;
  const char* kvim = ws + WS_KVIM;
  const f32x16 zero16 = {0.f, 0.f, 0.f, 0.f, 0.f, 0.f, 0.f, 0.f, 0.f, 0.f, 0.f, 0.f, 0.f, 0.f, 0.f, 0.f};

  if (w >= 8) {
    __builtin_amdgcn_s_setprio(3);
    const int lane = lane_id(), r32 = lane & 31, hh = lane >> 5;
    const u32x4 rsKV = mkrsrc(kvim, 64 * REC);
    const u32 vl = (u32)lane * 16u;
    asm volatile("s_nop 4" ::: "memory");
    dma4(rsKV, vl, (u32)(w - 8) * 4096u, lds0 + (u32)(w - 8) * 4096u);
    if (w == 12) {
      dma_buf(rsKV, vl, 20480u, lds0 + 20480u);
      lds_st(lds + L_FLG + lane * 4, (lane >= 8 && lane < 16) ? 2u : 0u);
      WAIT_BAR(0);
      u32 s = 2, slot = 2, ps = 2;
#pragma unroll 1
      for (u32 it = 0; it < SPIN_CAP && ps < 16; ++it) {
        const u32 fl = lds_ld(lds + L_FLG + (lane & 7) * 4);
        const u32 ns = s;
        if (s < 16 && __all(fl + 4 >= s)) {
          const u32 so = s * SL, la = lds0 + slot * SL;
          if (s >= 5) ISTAMP(1, s - 2);
          dma4(rsKV, vl, so, la); dma4(rsKV, vl, so + 4096u, la + 4096u); dma2h(rsKV, vl, so + 8192u, la + 8192u);
          asm volatile("s_waitcnt vmcnt(11)" ::: "memory");
          ++s; slot = slot == 4 ? 0 : slot + 1;
        } else {
          asm volatile("s_waitcnt vmcnt(0)" ::: "memory");
          if (ps == ns) __builtin_amdgcn_s_sleep(6);
        }
        if (ps != ns) { ps = ns; if (lane < 8) lds_st(lds + L_FLG + 32 + lane * 4, ps); }
      }
      ISTAMP(1, 1);
      const u32x4 rsWO = mkrsrc(ws + WS_WOF + (size_t)hg * 204800 + 102400, 102400);
#pragma unroll 1
      for (u32 h = 0; h < 2; ++h) {
        lds_wait_ge(lds + F_AFREE, 4u + 4u * h);
        const u32 o = h * 51200u;
#pragma unroll 1
        for (u32 c = 0; c < 12; ++c) dma4(rsWO, vl, o + c * 4096u, lds0 + L_IMG + o + c * 4096u);
        dma_buf(rsWO, vl, o + 49152u, lds0 + L_IMG + o + 49152u);
        dma_buf(rsWO, vl, o + 50176u, lds0 + L_IMG + o + 50176u);
      }
      asm volatile("s_waitcnt vmcnt(0)\ngm_warm_end:" ::: "memory");
      lds_arrive(lds + F_GBAR, lane);
      ISTAMP(1, 2); IRSTAMP(1, 15); ISTAMP_FLUSH(1);
      return;
    }
    __builtin_amdgcn_s_setprio(GPRIO);
    const int hw = w - 8, ht = hw * 64 + lane, head = jb;
    const unsigned long long tag = *(const __attribute__((address_space(4))) unsigned long long*)(ws + WS_EPOCH);
    WAIT_BAR(0);
    {
      const u32 ln = (u32)(jb * 42 + ht);
      u32 dummy; if (ht < 42) { asm volatile("buffer_load_dword %0, %1, %2, 0 offen" : "=v"(dummy) : "v"(ln * 128u), "s"(rsKV) : "memory"); }
      asm volatile("s_waitcnt vmcnt(0)" ::: "memory");
    }
    {
      const __amdgpu_buffer_rsrc_t rp = __builtin_amdgcn_make_buffer_rsrc(ws + WS_KVP + (size_t)head * 16 * 3360, 0, 16 * 3360, 0x00020000);
      const int vo = ht < 210 ? ht * 16 : (int)OOB;
      f32x4 a = __builtin_bit_cast(f32x4, __builtin_amdgcn_raw_buffer_load_b128(rp, vo, 0, 0));
#pragma unroll
      for (int p = 1; p < 16; ++p) a += __builtin_bit_cast(f32x4, __builtin_amdgcn_raw_buffer_load_b128(rp, vo, p * 3360, 0));
      const int d = (ht * 205) >> 11, f4 = ht - d * 10;
      if (ht < 210) *(LDSAS u32x2*)(lds + L_KVT + d * 96 + f4 * 8) = (u32x2){pk2(a[0], a[1]), pk2(a[2], a[3])};
      else if (ht >= 224 && ht < 224 + 21) *(LDSAS u32x4*)(lds + L_KVT + (ht - 224) * 96 + 80) = (u32x4){0u, 0u, 0u, 0u};
    }
    u32x4 p0[2], p1[2], p2[2]; float gt[2];
    {
      const char* prow = ws + WS_PHIQ + (size_t)(head * 64 + 8 * xcd + 2 * hw) * 2560;
#pragma unroll
      for (int j = 0; j < 2; ++j) {
        p0[j] = *(const u32x4*)(prow + j * 2560 + lane * 16); p1[j] = *(const u32x4*)(prow + j * 2560 + 1024 + lane * 16);
        p2[j] = *(const u32x4*)(prow + j * 2560 + 2048 + r32 * 16);
        gt[j] = ((const float*)(ws + WS_GATE))[256 * xcd + 64 * hw + 32 * j + r32];
      }
    }
    ISTAMP(1, 9);
    lds_arrive(lds + F_GBAR, lane);
    lds_wait_ge(lds + F_GBAR, 4);
    ISTAMP(1, 10);
    const __amdgpu_buffer_rsrc_t rO = __builtin_amdgcn_make_buffer_rsrc(ws + WS_OFR, 0, 5242880, 0x00020000);
    {
      ldsp ak = lds + L_KVT + (u32)(r32 < 20 ? r32 : 20) * 96u + (u32)hh * 16u;
      const u32x4 a0 = ld128(ak), a1 = ld128(ak + 32), a2 = ld128(ak + 64);
      const int vo = (r32 >> 4) * 20480 + (r32 & 15) * 16;
#pragma unroll
      for (int j = 0; j < 2; ++j) {
        if (hh) p2[j] = (u32x4){0u, 0u, 0u, 0u};
        f32x16 N = mfma32(a0, p0[j], zero16);
        N = mfma32(a1, p1[j], N);
        N = mfma32(a2, p2[j], N);
        const float sa = gt[j] * __builtin_amdgcn_rcpf(N[12] + 1e-6f);
        const int so = (16 * xcd + 4 * hw + 2 * j) * 20480 + (head >> 1) * 1280;
        __builtin_amdgcn_raw_buffer_store_b128((u32x4){pk2(N[0] * sa, N[1] * sa), pk2(N[2] * sa, N[3] * sa), pk2(N[4] * sa, N[5] * sa), pk2(N[6] * sa, N[7] * sa)},
                                               rO, vo + hh * 256 + so + (head & 1) * 512, 0, 16);
        __builtin_amdgcn_raw_buffer_store_b64((u32x2){pk2(N[8] * sa, N[9] * sa), pk2(N[10] * sa, N[11] * sa)}, rO, hh ? (int)OOB : vo + so + 1024 + (head & 1) * 8, 0, 16);
      }
    }
    ISTAMP(1, 11);
    asm volatile("s_waitcnt vmcnt(0)" ::: "memory");
    if (lds_arrive(lds + F_APUB, lane) == 3 && lane == 0)
      __hip_atomic_store((gu64*)(ws + WS_FLGA) + xcd * 32 + head, tag, __ATOMIC_RELAXED, __HIP_MEMORY_SCOPE_AGENT);
    ISTAMP(1, 1);
    const u32x4 rsWO = mkrsrc(ws + WS_WOF + (size_t)hg * 204800, 204800);
    {
      u32 dummy; if (ht < 200) { asm volatile("buffer_load_dword %0, %1, %2, 0 offen" : "=v"(dummy) : "v"(102400u + (u32)((jb & 3) * 200 + ht) * 128u), "s"(rsWO) : "memory"); }
      asm volatile("s_waitcnt vmcnt(0)" ::: "memory");
    }
    f32x16 ac0 = zero16, ac1 = zero16; f32x8 ac2 = {0.f, 0.f, 0.f, 0.f, 0.f, 0.f, 0.f, 0.f};
    const u32x4 rsO = mkrsrc(ws + WS_OFR, 5242880);
    {
      const u32 so = (u32)hw * 25600u, la = lds0 + L_IMG + so;
#pragma unroll 1
      for (u32 c = 0; c < 6; ++c) dma4(rsWO, vl, so + c * 4096u, la + c * 4096u);
      dma_buf(rsWO, vl, so + 24576u, la + 24576u);
      asm volatile("s_waitcnt vmcnt(0)" ::: "memory");
      lds_arrive(lds + F_GBAR, lane);
    }
    ISTAMP(1, 2);
#pragma unroll 1
    for (int part = 0; part < 2; ++part) {
      if (hw == 0) {
        if (part == 0) flags_wait(ws + WS_FLGA + xcd * 256, 32, tag, lane_id());
        else { lds_wait_ge(lds + F_BPUB, 4); ISTAMP(1, 12); flags_wait(ws + WS_FLGB + t64 * 64, 8, tag, lane_id()); }
        lds_arrive(lds + F_GBAR, lane);
      }
      lds_wait_ge(lds + F_GBAR, part ? 11u : 9u);
      if (part == 0) ISTAMP(1, 3); else ISTAMP(1, 6);
      {
        u32 s0 = (u32)(part * 2621440 + (4 * t64 + 2 * (hw & 1)) * 20480 + (hw >> 1) * 1024), s1 = s0 + 20480u, it;
        const int ln = lane_id();
        u32 bw = lds0 + L_IMG + (u32)ln * 16u + (u32)(hw >> 1) * 5120u;
        asm volatile(TILE_GEMM_ASM
                     : "+{v[0:15]}"(ac0), "+{v[16:31]}"(ac1), "+{v[32:39]}"(ac2), "+{v117}"(bw), [s0] "+s"(s0), [s1] "+s"(s1), [it] "=&s"(it)
                     : "{v116}"((u32)ln * 16u), "{v118}"(lds0 + L_FLG + (u32)(ln & AWAIT) * 4u), [rs] "s"(rsO), [want] "s"(part ? 0u : 16u)
                     : "memory", "scc", "vcc", "v40", "v41", "v42", "v43", "v44", "v45", "v46", "v47", "v48", "v49", "v50", "v51", "v52", "v53", "v54", "v55", "v56", "v57", "v58", "v59",
                       "v60", "v61", "v62", "v63", "v64", "v65", "v66", "v67", "v68", "v69", "v70", "v71", "v72", "v73", "v74", "v75", "v76", "v77", "v78", "v79",
                       "v80", "v81", "v82", "v83", "v84", "v85", "v86", "v87", "v88", "v89", "v90", "v91", "v92", "v93", "v94", "v95", "v96", "v97", "v98", "v99",
                       "v100", "v101", "v102", "v103", "v104", "v105", "v106", "v107", "v108", "v109", "v110", "v111", "v112", "v113", "v114", "v115", "v119");
      }
      if (part == 0) { lds_wait_ge(lds + F_AFREE, 4u); lds_arrive(lds + F_AFREE, lane); ISTAMP(1, 5); }
    }
    ISTAMP(1, 7);
    {
      const int ln = lane_id(), kh = hw >> 1;
      f32x4 acc[5][2];
#pragma unroll
      for (int n = 0; n < 2; ++n) {
        acc[0][n] = (f32x4){ac0[4 * n], ac0[4 * n + 1], ac0[4 * n + 2], ac0[4 * n + 3]}; acc[1][n] = (f32x4){ac0[8 + 4 * n], ac0[9 + 4 * n], ac0[10 + 4 * n], ac0[11 + 4 * n]};
        acc[2][n] = (f32x4){ac1[4 * n], ac1[4 * n + 1], ac1[4 * n + 2], ac1[4 * n + 3]}; acc[3][n] = (f32x4){ac1[8 + 4 * n], ac1[9 + 4 * n], ac1[10 + 4 * n], ac1[11 + 4 * n]};
        acc[4][n] = (f32x4){ac2[4 * n], ac2[4 * n + 1], ac2[4 * n + 2], ac2[4 * n + 3]};
      }
      ldsp xw = lds + (u32)hw * 5120u + ln * 16, xr = lds + (u32)(hw ^ 2) * 5120u + ln * 16;
#pragma unroll
      for (int T = 0; T < 5; ++T) *(LDSAS f32x4*)(xw + T * 1024) = kh ? acc[T][0] : acc[T][1];
      lds_arrive(lds + F_GBAR, ln);
      lds_wait_ge(lds + F_GBAR, 15u);
      u32x2* dst = (u32x2*)(out + (size_t)(64 * t64 + 32 * (hw & 1) + 16 * kh + (ln & 15)) * 1280 + hg * 160 + (ln >> 4) * 40);
#pragma unroll
      for (int T = 0; T < 5; ++T) {
        const f32x4 v = (kh ? acc[T][1] : acc[T][0]) + *(LDSAS f32x4*)(xr + T * 1024);
        dst[T] = (u32x2){pk2(v[0], v[1]), pk2(v[2], v[3])};
      }
    }
    ISTAMP(1, 8); IRSTAMP(1, 15); ISTAMP_FLUSH(1);
    return;
  }

  const int pr = w & 3, par = w >> 2, head = 4 * hg + pr;
  const int idx = 0xDDDD;
  u32x4 q1[2]; u32x2 q2[2];
  f32x16 O0, O1;
  {
    const int lane = lane_id(), r32 = lane & 31, hh = lane >> 5;
#pragma unroll
    for (int j = 0; j < 2; ++j) {
      const char* qrow = ws + WS_QB + (size_t)(head * 64 + 2 * t64 + j) * 1280;
      q1[j] = *(const u32x4*)(qrow + lane * 16);
      q2[j] = (u32x2){*(const u32*)(qrow + 1024 + r32 * 8 + hh * 4), 0u};
    }
    const u32x4 k1a = *(const u32x4*)(kvim + 62 * REC + lane * 16);
    const u32 k2a = *(const u32*)(kvim + 62 * REC + 1024 + lane * 4);
    u32x4 k1 = *(const u32x4*)(kvim + (62 + par) * REC + lane * 16);
    u32x2 k2 = {*(const u32*)(kvim + (62 + par) * REC + 1024 + lane * 4), hh ? 0u : 0x00003F80u};
    if (w == 0) *(LDSAS float*)(lds + L_GT + lane * 4) = ((const float*)(ws + WS_GATE))[64 * t64 + lane];
    f32x16 S0, S1, S2, S3;
#pragma unroll
    for (int j = 0; j < 2; ++j) {
      f32x16 t = mfma32(k1a, q1[j], zero16);
      t = mfma32k8((u32x2){k2a, 0u}, q2[j], t);
      const float m = xhalf_max(max16(t));
      q2[j][1] = hh ? 0u : (pk2(-m, 0.f) & 0xffffu);
    }
    S0 = mfma32(k1, q1[0], zero16);
    S0 = mfma32k8(k2, q2[0], S0);
    u32x4 vlo, vhi;
    u32 aK1 = lds0 + (u32)par * REC + (u32)lane * 16u, aK2 = lds0 + (u32)par * REC + 1024u + (u32)lane * 4u;
    u32 aV = lds0 + (u32)par * REC + 1280u + (u32)hh * 352u + (u32)(r32 <= 20 ? r32 : 21) * 16u;
    u32 aF = lds0 + L_FLG + (u32)w * 4u, tmp;
    ISTAMP(1, 1);
    u32 cnt, inc, slot, rdy;
#define ATTN_LOOP(TXT) asm volatile(TXT \
                 : "=&{v[0:15]}"(O0), "=&{v[16:31]}"(O1), "+{v[32:47]}"(S0), "=&{v[48:63]}"(S1), "=&{v[64:79]}"(S2), "=&{v[80:95]}"(S3), \
                   "+{v[108:111]}"(k1), "+{v[112:113]}"(k2), "=&{v[116:119]}"(vlo), "=&{v[120:123]}"(vhi), \
                   "+{v124}"(aK1), "+{v125}"(aK2), "+{v126}"(aV), "=&{v115}"(tmp), [cnt] "=&s"(cnt), [inc] "=&s"(inc), [slot] "=&s"(slot), [rdy] "=&s"(rdy) \
                 : "{v[96:99]}"(q1[0]), "{v[100:103]}"(q1[1]), "{v[104:105]}"(q2[0]), "{v[106:107]}"(q2[1]), "{v114}"(idx), "{v127}"(aF), \
                   [wrap] "s"(0u - 4u * SL), [stg] "s"(SL), [par] "s"(par) \
                 : "memory", "scc")
    ATTN_LOOP(ATTN_LOOP_ASM);
  }
  ISTAMP(1, 3);
  const int lane = lane_id();
  const int r32 = lane & 31, hh = lane >> 5;
  ldsp mg = lds + L_MRG + (u32)pr * (2 * REC);
  if (lds_arrive(lds + L_FLG + 64 + pr * 4, lane) == 0) {
#pragma unroll 1
    for (u32 it = 0; it < SPIN_CAP; ++it) {
      if (__all(lds_ld(lds + L_FLG + (lane & 7) * 4) >= 13u)) break;
      __builtin_amdgcn_s_sleep(2);
    }
    asm volatile("" ::: "memory");
#pragma unroll
    for (int j = 0; j < 2; ++j) {
      const f32x16 snd = j ? O1 : O0;
      ldsp mw = mg + j * REC;
      *(LDSAS f32x4*)(mw + lane * 16) = (f32x4){snd[0], snd[1], snd[2], snd[3]};
      *(LDSAS f32x4*)(mw + 1024 + lane * 16) = (f32x4){snd[4], snd[5], snd[6], snd[7]};
      if (!hh) *(LDSAS f32x4*)(mw + 2048 + r32 * 16) = (f32x4){snd[8], snd[9], snd[10], snd[11]};
      else *(LDSAS float*)(mw + 2560 + r32 * 4) = snd[8];
    }
    asm volatile("s_waitcnt lgkmcnt(0)" ::: "memory");
    lds_st(lds + L_FLG + 80 + pr * 4, 1u);
    ISTAMP(1, 4); IRSTAMP(1, 15); ISTAMP_FLUSH(1);
    return;
  }
  __builtin_amdgcn_s_setprio(3);
  lds_wait_ge(lds + L_FLG + 80 + pr * 4, 1u);
  ISTAMP(1, 4);
  const __amdgpu_buffer_rsrc_t rO = __builtin_amdgcn_make_buffer_rsrc(ws + WS_OFR + 2621440, 0, 2621440, 0x00020000);
#pragma unroll
  for (int j = 0; j < 2; ++j) {
    f32x16 O = j ? O1 : O0;
    ldsp mr = mg + j * REC;
    const f32x4 a = *(LDSAS f32x4*)(mr + lane * 16), b = *(LDSAS f32x4*)(mr + 1024 + lane * 16), c = *(LDSAS f32x4*)(mr + 2048 + r32 * 16);
    float l = from_hi(O[8]) + *(LDSAS float*)(mr + 2560 + r32 * 4);
    const float gt = *(LDSAS float*)(lds + L_GT + (32 * j + r32) * 4);
#pragma unroll
    for (int i = 0; i < 4; ++i) { O[i] += a[i]; O[4 + i] += b[i]; O[8 + i] += c[i]; }
    float t = 0.f;
#pragma unroll
    for (int r = 0; r < 12; ++r) t = __builtin_fmaf(O[r], 0.f, t);
    const bool bad = !(t == 0.f) || !(l < 1.0e30f);
    if (__builtin_expect(__any(bad), 0)) {
      O = attn_exact(kvim, q1[j], q2[j][0], lane);
      l = from_hi(O[8]);
    }
    const float sb = (1.f - gt) * __builtin_amdgcn_rcpf(l);
    const int vo = (4 * t64 + 2 * j + (r32 >> 4)) * 20480 + (r32 & 15) * 16 + (head >> 1) * 1280;
    __builtin_amdgcn_raw_buffer_store_b128((u32x4){pk2(O[0] * sb, O[1] * sb), pk2(O[2] * sb, O[3] * sb), pk2(O[4] * sb, O[5] * sb), pk2(O[6] * sb, O[7] * sb)},
                                           rO, vo + hh * 256 + (head & 1) * 512, 0, 16);
    __builtin_amdgcn_raw_buffer_store_b64((u32x2){pk2(O[8] * sb, O[9] * sb), pk2(O[10] * sb, O[11] * sb)}, rO, hh ? (int)OOB : vo + 1024 + (head & 1) * 8, 0, 16);
  }
  const unsigned long long tag = *(const __attribute__((address_space(4))) unsigned long long*)(ws + WS_EPOCH);
  ISTAMP(1, 5);
  asm volatile("s_waitcnt vmcnt(0)" ::: "memory");
  ISTAMP(1, 6);
  if (lds_arrive(lds + F_BPUB, lane) == 3 && lane == 0)
    __hip_atomic_store((gu64*)(ws + WS_FLGB) + t64 * 8 + hg, tag, __ATOMIC_RELAXED, __HIP_MEMORY_SCOPE_AGENT);
  ISTAMP(1, 7); IRSTAMP(1, 15); ISTAMP_FLUSH(1);
}

extern "C" void kernel_launch(void* const* d_in, const int* in_sizes, int n_in, void* d_out, int out_size, void* d_ws,
                              size_t ws_size, hipStream_t stream) {
  (void)in_sizes; (void)n_in; (void)out_size; (void)ws_size;
  (void)hipFuncSetAttribute((const void*)k1_proj, hipFuncAttributeMaxDynamicSharedMemorySize, 4 * K1_SLOT);
  (void)hipFuncSetAttribute((const void*)k2_attn, hipFuncAttributeMaxDynamicSharedMemorySize, K2_LDS);
  char* ws = (char*)d_ws;
  k1_proj<<<256, K1_THREADS, 4 * K1_SLOT, stream>>>((const char*)d_in[0], (const char*)d_in[1], (const char*)d_in[2], (const char*)d_in[5], (const char*)d_in[6],
      (const char*)d_in[7], (const char*)d_in[9], (const char*)d_in[3], (const char*)d_in[8], ws, (const char*)d_out);
  k2_attn<<<256, 832, K2_LDS, stream>>>(ws, (char*)d_out);
}
```

```cpp
#include <hip/hip_runtime.h>
#include <hip/hip_bf16.h>
#include <stdint.h>

typedef unsigned int u32;
typedef unsigned long long u64;
typedef u32 u32x2 __attribute__((ext_vector_type(2)));
typedef u32 u32x4 __attribute__((ext_vector_type(4)));
typedef float f32x2 __attribute__((ext_vector_type(2)));
typedef float f32x4 __attribute__((ext_vector_type(4)));
typedef float f32x8 __attribute__((ext_vector_type(8)));
typedef float f32x16 __attribute__((ext_vector_type(16)));
typedef __bf16 bf16x2 __attribute__((ext_vector_type(2)));
typedef __bf16 bf16x8 __attribute__((ext_vector_type(8)));
typedef __bf16 bf16x16 __attribute__((ext_vector_type(16)));
typedef short s16x4 __attribute__((ext_vector_type(4)));
#define LDSAS __attribute__((address_space(3)))
typedef LDSAS char* ldsp;
typedef __attribute__((address_space(1))) u64 gu64;

constexpr int SEQ = 2048, DM = 640, NH = 32, DH = 20;
constexpr float LOG2E = 1.4426950408889634f;
constexpr float QSCALE = 1.4426950408889634f * 0.22360679774997896f;

constexpr u32 REC = 2688;
constexpr u32 WS_QB   = 0;
constexpr u32 WS_KVIM = WS_QB + 1310720;
constexpr u32 WS_KVP  = WS_KVIM + 64 * REC;
constexpr u32 WS_KVT  = WS_KVP + 32 * 16 * 3360;
constexpr u32 WS_GATE = WS_KVT + 32 * 1680;
constexpr u32 WS_OFR  = WS_GATE + 8192;
constexpr u32 WS_F1   = WS_OFR + 5242880;
constexpr u32 WS_F2   = WS_F1 + 16384;
constexpr u32 WS_F3   = WS_F2 + 1024;
constexpr u32 WS_FLGA = WS_F3 + 1024;
constexpr u32 WS_FLGB = WS_FLGA + 2048;
constexpr u32 WS_FQ   = WS_FLGB + 8192;
constexpr u32 WS_EPOCH = WS_FQ + 16384;
constexpr u32 WS_END  = WS_EPOCH + 256;
constexpr u32 OOB = 0x40000000u;
constexpr u32 SPIN_CAP = 1u << 20;

#define SBAR() __builtin_amdgcn_sched_barrier(0)
#define PIN(x) asm volatile("" : "+v"(x))
#define LBAR() asm volatile("s_waitcnt lgkmcnt(0)\n\ts_barrier" ::: "memory")
#define WAIT_BAR(N) asm volatile("s_waitcnt vmcnt(" #N ") lgkmcnt(0)\n\ts_barrier" ::: "memory")
#define DRAIN() asm volatile("s_waitcnt vmcnt(0)" ::: "memory")

__device__ __forceinline__ int lane_id() {
  int l;
  asm volatile("v_mbcnt_lo_u32_b32 %0, -1, 0\n\tv_mbcnt_hi_u32_b32 %0, -1, %0" : "=v"(l));
  return l;
}
__device__ __forceinline__ u32 pk2(float a, float b) {
  f32x2 v = {a, b};
  return __builtin_bit_cast(u32, __builtin_convertvector(v, bf16x2));
}
__device__ __forceinline__ f32x4 mfma16(u32x4 a, u32x4 b, f32x4 c) {
  return __builtin_amdgcn_mfma_f32_16x16x32_bf16(__builtin_bit_cast(bf16x8, a), __builtin_bit_cast(bf16x8, b), c, 0, 0, 0);
}
__device__ __forceinline__ f32x16 mfma32(u32x4 a, u32x4 b, f32x16 c) {
  return __builtin_amdgcn_mfma_f32_32x32x16_bf16(__builtin_bit_cast(bf16x8, a), __builtin_bit_cast(bf16x8, b), c, 0, 0, 0);
}
__device__ __forceinline__ f32x16 mfma32k8(u32x2 a, u32x2 b, f32x16 c) {
  return __builtin_amdgcn_mfma_f32_32x32x8bf16_1k(__builtin_bit_cast(s16x4, a), __builtin_bit_cast(s16x4, b), c, 0, 0, 0);
}
__device__ __forceinline__ f32x16 smf(u32x4 a, f32x8 b, f32x16 c, int idx) {
  return __builtin_amdgcn_smfmac_f32_32x32x32_bf16(__builtin_bit_cast(bf16x8, a), __builtin_bit_cast(bf16x16, b), c, idx, 0, 0);
}
__device__ __forceinline__ u32x2 trd(ldsp p) {
  return __builtin_bit_cast(u32x2, __builtin_amdgcn_ds_read_tr16_b64_v4i16((LDSAS s16x4*)p));
}
__device__ __forceinline__ u32x4 trd2(ldsp p0, ldsp p1) {
  u32x2 a = trd(p0), b = trd(p1);
  return (u32x4){a[0], a[1], b[0], b[1]};
}
__device__ __forceinline__ u32x4 ld128(ldsp p) { return *(LDSAS u32x4*)p; }
__device__ __forceinline__ u32x2 ld64(ldsp p) { return *(LDSAS u32x2*)p; }
__device__ __forceinline__ float bflo(u32 v) { return __uint_as_float(v << 16); }
__device__ __forceinline__ float bfhi(u32 v) { return __uint_as_float(v & 0xffff0000u); }

__device__ __forceinline__ u32x4 mkrsrc(const void* p, u32 bytes) {
  uint64_t a = (uint64_t)p;
  return (u32x4){(u32)__builtin_amdgcn_readfirstlane((u32)a), (u32)__builtin_amdgcn_readfirstlane((u32)(a >> 32) & 0xffffu), bytes, 0x00020000u};
}
__device__ __forceinline__ void dma1(u32x4 rs, u32 voff, u32 soff, u32 ldsaddr) {
  u32 keep;
  asm volatile("s_mov_b32 %0, m0\n\ts_mov_b32 m0, %4\n\ts_nop 2\n\tbuffer_load_dwordx4 %1, %2, %3 offen lds\n\ts_mov_b32 m0, %0"
               : "=&s"(keep) : "v"(voff), "s"(rs), "s"(__builtin_amdgcn_readfirstlane(soff)), "s"(__builtin_amdgcn_readfirstlane(ldsaddr)) : "memory");
}
__device__ __forceinline__ void dma1s(u32x4 rs, u32 voff, u32 soff, u32 ldsaddr) {
  u32 keep;
  asm volatile("s_mov_b32 %0, m0\n\ts_mov_b32 m0, %4\n\ts_nop 2\n\tbuffer_load_dwordx4 %1, %2, %3 offen sc1 lds\n\ts_mov_b32 m0, %0"
               : "=&s"(keep) : "v"(voff), "s"(rs), "s"(__builtin_amdgcn_readfirstlane(soff)), "s"(__builtin_amdgcn_readfirstlane(ldsaddr)) : "memory");
}
__device__ __forceinline__ void dma4(u32x4 rs, u32 voff, u32 soff, u32 ldsaddr) {
  u32 keep;
  asm volatile("s_mov_b32 %0, m0\n\ts_mov_b32 m0, %4\n\ts_nop 2\n\t"
               "buffer_load_dwordx4 %1, %2, %3 offen sc1 lds\n\tbuffer_load_dwordx4 %1, %2, %3 offen offset:1024 sc1 lds\n\t"
               "buffer_load_dwordx4 %1, %2, %3 offen offset:2048 sc1 lds\n\tbuffer_load_dwordx4 %1, %2, %3 offen offset:3072 sc1 lds\n\t"
               "s_mov_b32 m0, %0"
               : "=&s"(keep) : "v"(voff), "s"(rs), "s"(__builtin_amdgcn_readfirstlane(soff)), "s"(__builtin_amdgcn_readfirstlane(ldsaddr)) : "memory");
}
__device__ __forceinline__ void dma2h(u32x4 rs, u32 voff, u32 soff, u32 ldsaddr) {
  u32 keep;
  asm volatile("s_mov_b32 %0, m0\n\ts_mov_b32 m0, %4\n\ts_nop 2\n\t"
               "buffer_load_dwordx4 %1, %2, %3 offen sc1 lds\n\tbuffer_load_dwordx4 %1, %2, %3 offen offset:1024 sc1 lds\n\t"
               "s_mov_b32 exec_hi, 0\n\tbuffer_load_dwordx4 %1, %2, %3 offen offset:2048 sc1 lds\n\ts_mov_b32 exec_hi, -1\n\t"
               "s_mov_b32 m0, %0"
               : "=&s"(keep) : "v"(voff), "s"(rs), "s"(__builtin_amdgcn_readfirstlane(soff)), "s"(__builtin_amdgcn_readfirstlane(ldsaddr)) : "memory");
}
typedef __amdgpu_buffer_rsrc_t rsrc_t;
__device__ __forceinline__ void st128(rsrc_t r, u32 off, u32x4 v) { __builtin_amdgcn_raw_buffer_store_b128(v, r, (int)off, 0, 16); }
__device__ __forceinline__ void st64(rsrc_t r, u32 off, u32x2 v) { __builtin_amdgcn_raw_buffer_store_b64(v, r, (int)off, 0, 16); }
__device__ __forceinline__ void st32(rsrc_t r, u32 off, u32 v) { __builtin_amdgcn_raw_buffer_store_b32(v, r, (int)off, 0, 16); }
__device__ __forceinline__ void st16(rsrc_t r, u32 off, u32 v) { __builtin_amdgcn_raw_buffer_store_b16((unsigned short)v, r, (int)off, 0, 16); }
__device__ __forceinline__ u32x4 gl128(rsrc_t r, u32 voff, u32 soff) { return __builtin_amdgcn_raw_buffer_load_b128(r, (int)voff, (int)soff, 16); }
__device__ __forceinline__ u32x2 gl64(rsrc_t r, u32 voff, u32 soff) { return __builtin_amdgcn_raw_buffer_load_b64(r, (int)voff, (int)soff, 16); }
__device__ __forceinline__ u32 gl32(rsrc_t r, u32 voff, u32 soff) { return __builtin_amdgcn_raw_buffer_load_b32(r, (int)voff, (int)soff, 16); }

__device__ __forceinline__ f32x8 lo8(const f32x16& s) { return __builtin_shufflevector(s, s, 0, 1, 2, 3, 4, 5, 6, 7); }
__device__ __forceinline__ f32x8 hi8(const f32x16& s) { return __builtin_shufflevector(s, s, 8, 9, 10, 11, 12, 13, 14, 15); }
__device__ __forceinline__ float max16(const f32x16& s) {
  float a = fmaxf(fmaxf(s[0], s[1]), s[2]), b = fmaxf(fmaxf(s[3], s[4]), s[5]);
  a = fmaxf(fmaxf(a, s[6]), s[7]); b = fmaxf(fmaxf(b, s[8]), s[9]);
  a = fmaxf(fmaxf(a, s[10]), s[11]); b = fmaxf(fmaxf(b, s[12]), s[13]);
  a = fmaxf(fmaxf(a, s[14]), s[15]);
  return fmaxf(a, b);
}
__device__ __forceinline__ float xhalf_max(float m) {
  auto r = __builtin_amdgcn_permlane32_swap(__float_as_uint(m), __float_as_uint(m), false, false);
  return fmaxf(__uint_as_float(r[0]), __uint_as_float(r[1]));
}
__device__ __forceinline__ float from_hi(float v) {
  auto r = __builtin_amdgcn_permlane32_swap(__float_as_uint(v), __float_as_uint(v), false, false);
  return __uint_as_float(r[1]);
}
__device__ __forceinline__ u32 lds_ld(ldsp p) { return __hip_atomic_load((LDSAS u32*)p, __ATOMIC_RELAXED, __HIP_MEMORY_SCOPE_WORKGROUP); }
__device__ __forceinline__ void lds_st(ldsp p, u32 v) { __hip_atomic_store((LDSAS u32*)p, v, __ATOMIC_RELAXED, __HIP_MEMORY_SCOPE_WORKGROUP); }
template <int SLEEP>
__device__ __forceinline__ void lds_wait_ge(ldsp p, u32 want) {
#pragma unroll 1
  for (u32 it = 0; it < SPIN_CAP; ++it) {
    if ((u32)__builtin_amdgcn_readfirstlane(lds_ld(p)) >= want) break;
    __builtin_amdgcn_s_sleep(SLEEP);
  }
  asm volatile("" ::: "memory");
}
__device__ __forceinline__ u32 lds_arrive(ldsp p) {
  u32 old; u64 keep;
  asm volatile("v_mov_b32 %0, 1\n\ts_mov_b64 %1, exec\n\ts_mov_b64 exec, 1\n\ts_waitcnt lgkmcnt(0)\n\tds_add_rtn_u32 %0, %2, %0\n\t"
               "s_waitcnt lgkmcnt(0)\n\ts_mov_b64 exec, %1" : "=&v"(old), "=&s"(keep) : "v"((u32)(uintptr_t)p) : "memory");
  return (u32)__builtin_amdgcn_readfirstlane(old);
}
template <int SLEEP>
__device__ __forceinline__ void flags_wait(rsrc_t r, u32 off, int n, u32x2 tag) {
  const int lane = lane_id();
  const u32 vo = (u32)(lane < n ? lane : 0) * 8u;
#pragma unroll 1
  for (u32 it = 0; it < SPIN_CAP; ++it) {
    asm volatile("" ::: "memory");
    const u32x2 v = gl64(r, vo, off);
    if (__all(v[0] == tag[0] && v[1] == tag[1])) break;
    __builtin_amdgcn_s_sleep(SLEEP);
  }
  asm volatile("" ::: "memory");
}
template <int SLEEP>
__device__ __forceinline__ void flags_wait256(rsrc_t r, u32 off, u32x2 tag) {
  const u32 vo = (u32)lane_id() * 16u;
#pragma unroll 1
  for (u32 it = 0; it < SPIN_CAP; ++it) {
    asm volatile("" ::: "memory");
    const u32x4 a = gl128(r, vo, off), b = gl128(r, vo, off + 1024);
    const u32 d = (a[0] ^ tag[0]) | (a[1] ^ tag[1]) | (a[2] ^ tag[0]) | (a[3] ^ tag[1]) | (b[0] ^ tag[0]) | (b[1] ^ tag[1]) | (b[2] ^ tag[0]) | (b[3] ^ tag[1]);
    if (__all(d == 0u)) break;
    __builtin_amdgcn_s_sleep(SLEEP);
  }
  asm volatile("" ::: "memory");
}

#define STAMP(i) do {} while (0)
#define RSTAMP(i) do {} while (0)
#define STAMP_INIT()
#define STAMP_FLUSH()

#define ATTN_LOOP_ASM \
  "s_nop 11\n\t" \
  "v_mov_b64 v[0:1], 0\n\t" \
  "v_mov_b64 v[2:3], 0\n\t" \
  "v_mov_b64 v[4:5], 0\n\t" \
  "v_mov_b64 v[6:7], 0\n\t" \
  "v_mov_b64 v[8:9], 0\n\t" \
  "v_mov_b64 v[10:11], 0\n\t" \
  "v_mov_b64 v[12:13], 0\n\t" \
  "v_mov_b64 v[14:15], 0\n\t" \
  "v_mov_b64 v[16:17], 0\n\t" \
  "v_mov_b64 v[18:19], 0\n\t" \
  "v_mov_b64 v[20:21], 0\n\t" \
  "v_mov_b64 v[22:23], 0\n\t" \
  "v_mov_b64 v[24:25], 0\n\t" \
  "v_mov_b64 v[26:27], 0\n\t" \
  "v_mov_b64 v[28:29], 0\n\t" \
  "v_mov_b64 v[30:31], 0\n\t" \
  "v_mov_b64 v[80:81], 0\n\t" \
  "v_mov_b64 v[82:83], 0\n\t" \
  "v_mov_b64 v[84:85], 0\n\t" \
  "v_mov_b64 v[86:87], 0\n\t" \
  "v_mov_b64 v[88:89], 0\n\t" \
  "v_mov_b64 v[90:91], 0\n\t" \
  "v_mov_b64 v[92:93], 0\n\t" \
  "v_mov_b64 v[94:95], 0\n\t" \
  "v_mov_b64 v[116:117], 0\n\t" \
  "v_mov_b64 v[118:119], 0\n\t" \
  "v_mov_b64 v[120:121], 0\n\t" \
  "v_mov_b64 v[122:123], 0\n\t" \
  "s_mov_b32 %[cnt], 0\n\t" \
  "s_mov_b32 %[slot], 0\n\t" \
  "2:\n\t" \
  "s_mov_b32 %[inc], 0\n\t" \
  "3:\n\t" \
  "ds_read_b32 v115, v127 offset:32\n\t" \
  "s_add_u32 %[inc], %[inc], 1\n\t" \
  "s_waitcnt lgkmcnt(0)\n\t" \
  "v_readfirstlane_b32 %[rdy], v115\n\t" \
  "s_nop 3\n\t" \
  "s_cmp_gt_u32 %[rdy], %[cnt]\n\t" \
  "s_cbranch_scc1 6f\n\t" \
  "s_sleep 1\n\t" \
  "s_cmp_lt_u32 %[inc], 0x100000\n\t" \
  "s_cbranch_scc1 3b\n\t" \
  "s_branch 9f\n\t" \
  "6:\n\t" \
  "s_cmp_lg_u32 %[par], 0\n\t" \
  "s_cbranch_scc1 5f\n\t" \
  "4:\n\t" \
  "v_mfma_f32_32x32x16_bf16 v[48:63], v[108:111], v[100:103], 0\n\t" \
  "ds_read_b128 v[108:111], v124 offset:0\n\t" \
  "v_exp_f32 v32, v32\n\t" \
  "v_exp_f32 v33, v33\n\t" \
  "v_exp_f32 v34, v34\n\t" \
  "v_exp_f32 v35, v35\n\t" \
  "v_mfma_f32_32x32x8_bf16 v[48:63], v[112:113], v[106:107], v[48:63]\n\t" \
  "ds_read_b32 v112, v125 offset:0\n\t" \
  "v_exp_f32 v36, v36\n\t" \
  "v_exp_f32 v37, v37\n\t" \
  "v_exp_f32 v38, v38\n\t" \
  "v_exp_f32 v39, v39\n\t" \
  "v_smfmac_f32_32x32x32_bf16 v[16:31], v[116:119], v[80:87], v114\n\t" \
  "ds_read_b128 v[116:119], v126 offset:0\n\t" \
  "v_exp_f32 v40, v40\n\t" \
  "v_exp_f32 v41, v41\n\t" \
  "v_exp_f32 v42, v42\n\t" \
  "v_exp_f32 v43, v43\n\t" \
  "v_smfmac_f32_32x32x32_bf16 v[16:31], v[120:123], v[88:95], v114\n\t" \
  "ds_read_b128 v[120:123], v126 offset:704\n\t" \
  "v_exp_f32 v44, v44\n\t" \
  "v_exp_f32 v45, v45\n\t" \
  "v_exp_f32 v46, v46\n\t" \
  "v_exp_f32 v47, v47\n\t" \
  "s_waitcnt lgkmcnt(2)\n\t" \
  "v_mfma_f32_32x32x16_bf16 v[64:79], v[108:111], v[96:99], 0\n\t" \
  "v_exp_f32 v48, v48\n\t" \
  "v_exp_f32 v49, v49\n\t" \
  "v_exp_f32 v50, v50\n\t" \
  "v_exp_f32 v51, v51\n\t" \
  "v_mfma_f32_32x32x8_bf16 v[64:79], v[112:113], v[104:105], v[64:79]\n\t" \
  "v_exp_f32 v52, v52\n\t" \
  "v_exp_f32 v53, v53\n\t" \
  "v_exp_f32 v54, v54\n\t" \
  "v_exp_f32 v55, v55\n\t" \
  "s_waitcnt lgkmcnt(0)\n\t" \
  "v_smfmac_f32_32x32x32_bf16 v[0:15], v[116:119], v[32:39], v114\n\t" \
  "v_exp_f32 v56, v56\n\t" \
  "v_exp_f32 v57, v57\n\t" \
  "v_exp_f32 v58, v58\n\t" \
  "v_exp_f32 v59, v59\n\t" \
  "v_smfmac_f32_32x32x32_bf16 v[0:15], v[120:123], v[40:47], v114\n\t" \
  "v_exp_f32 v60, v60\n\t" \
  "v_exp_f32 v61, v61\n\t" \
  "v_exp_f32 v62, v62\n\t" \
  "v_exp_f32 v63, v63\n\t" \
  "v_mfma_f32_32x32x16_bf16 v[80:95], v[108:111], v[100:103], 0\n\t" \
  "ds_read_b128 v[108:111], v124 offset:5376\n\t" \
  "v_exp_f32 v64, v64\n\t" \
  "v_exp_f32 v65, v65\n\t" \
  "v_exp_f32 v66, v66\n\t" \
  "v_exp_f32 v67, v67\n\t" \
  "v_mfma_f32_32x32x8_bf16 v[80:95], v[112:113], v[106:107], v[80:95]\n\t" \
  "ds_read_b32 v112, v125 offset:5376\n\t" \
  "v_exp_f32 v68, v68\n\t" \
  "v_exp_f32 v69, v69\n\t" \
  "v_exp_f32 v70, v70\n\t" \
  "v_exp_f32 v71, v71\n\t" \
  "v_smfmac_f32_32x32x32_bf16 v[16:31], v[116:119], v[48:55], v114\n\t" \
  "ds_read_b128 v[116:119], v126 offset:5376\n\t" \
  "v_exp_f32 v72, v72\n\t" \
  "v_exp_f32 v73, v73\n\t" \
  "v_exp_f32 v74, v74\n\t" \
  "v_exp_f32 v75, v75\n\t" \
  "v_smfmac_f32_32x32x32_bf16 v[16:31], v[120:123], v[56:63], v114\n\t" \
  "ds_read_b128 v[120:123], v126 offset:6080\n\t" \
  "s_add_u32 %[cnt], %[cnt], 1\n\t" \
  "s_cmp_eq_u32 %[slot], 4\n\t" \
  "s_cselect_b32 %[inc], %[wrap], %[stg]\n\t" \
  "v_exp_f32 v76, v76\n\t" \
  "v_exp_f32 v77, v77\n\t" \
  "v_exp_f32 v78, v78\n\t" \
  "v_exp_f32 v79, v79\n\t" \
  "s_waitcnt lgkmcnt(2)\n\t" \
  "v_mfma_f32_32x32x16_bf16 v[32:47], v[108:111], v[96:99], 0\n\t" \
  "ds_read_b32 v115, v127 offset:32\n\t" \
  "s_cselect_b32 %[slot], -1, %[slot]\n\t" \
  "s_add_u32 %[slot], %[slot], 1\n\t" \
  "v_exp_f32 v80, v80\n\t" \
  "v_exp_f32 v81, v81\n\t" \
  "v_exp_f32 v82, v82\n\t" \
  "v_exp_f32 v83, v83\n\t" \
  "v_mfma_f32_32x32x8_bf16 v[32:47], v[112:113], v[104:105], v[32:47]\n\t" \
  "v_add_u32 v124, %[inc], v124\n\t" \
  "v_add_u32 v125, %[inc], v125\n\t" \
  "v_add_u32 v126, %[inc], v126\n\t" \
  "v_exp_f32 v84, v84\n\t" \
  "v_exp_f32 v85, v85\n\t" \
  "v_exp_f32 v86, v86\n\t" \
  "v_exp_f32 v87, v87\n\t" \
  "s_waitcnt lgkmcnt(1)\n\t" \
  "v_smfmac_f32_32x32x32_bf16 v[0:15], v[116:119], v[64:71], v114\n\t" \
  "v_exp_f32 v88, v88\n\t" \
  "v_exp_f32 v89, v89\n\t" \
  "v_exp_f32 v90, v90\n\t" \
  "v_exp_f32 v91, v91\n\t" \
  "s_waitcnt lgkmcnt(0)\n\t" \
  "v_readfirstlane_b32 %[rdy], v115\n\t" \
  "v_mov_b32 v115, %[cnt]\n\t" \
  "v_smfmac_f32_32x32x32_bf16 v[0:15], v[120:123], v[72:79], v114\n\t" \
  "ds_write_b32 v127, v115\n\t" \
  "s_cmp_gt_u32 %[rdy], %[cnt]\n\t" \
  "v_exp_f32 v92, v92\n\t" \
  "v_exp_f32 v93, v93\n\t" \
  "v_exp_f32 v94, v94\n\t" \
  "v_exp_f32 v95, v95\n\t" \
  "s_cbranch_scc1 4b\n\t" \
  "s_cmp_ge_u32 %[cnt], 16\n\t" \
  "s_cbranch_scc1 9f\n\t" \
  "s_branch 2b\n\t" \
  "5:\n\t" \
  "v_mfma_f32_32x32x16_bf16 v[48:63], v[108:111], v[100:103], 0\n\t" \
  "ds_read_b128 v[108:111], v124 offset:0\n\t" \
  "s_setprio 1\n\t" \
  "v_exp_f32 v32, v32\n\t" \
  "v_exp_f32 v33, v33\n\t" \
  "v_exp_f32 v34, v34\n\t" \
  "v_exp_f32 v35, v35\n\t" \
  "v_mfma_f32_32x32x8_bf16 v[48:63], v[112:113], v[106:107], v[48:63]\n\t" \
  "ds_read_b32 v112, v125 offset:0\n\t" \
  "v_exp_f32 v36, v36\n\t" \
  "v_exp_f32 v37, v37\n\t" \
  "v_exp_f32 v38, v38\n\t" \
  "v_exp_f32 v39, v39\n\t" \
  "v_smfmac_f32_32x32x32_bf16 v[16:31], v[116:119], v[80:87], v114\n\t" \
  "ds_read_b128 v[116:119], v126 offset:0\n\t" \
  "v_exp_f32 v40, v40\n\t" \
  "v_exp_f32 v41, v41\n\t" \
  "v_exp_f32 v42, v42\n\t" \
  "v_exp_f32 v43, v43\n\t" \
  "v_smfmac_f32_32x32x32_bf16 v[16:31], v[120:123], v[88:95], v114\n\t" \
  "ds_read_b128 v[120:123], v126 offset:704\n\t" \
  "v_exp_f32 v44, v44\n\t" \
  "v_exp_f32 v45, v45\n\t" \
  "v_exp_f32 v46, v46\n\t" \
  "v_exp_f32 v47, v47\n\t" \
  "s_waitcnt lgkmcnt(2)\n\t" \
  "v_mfma_f32_32x32x16_bf16 v[64:79], v[108:111], v[96:99], 0\n\t" \
  "v_exp_f32 v48, v48\n\t" \
  "v_exp_f32 v49, v49\n\t" \
  "v_exp_f32 v50, v50\n\t" \
  "v_exp_f32 v51, v51\n\t" \
  "v_mfma_f32_32x32x8_bf16 v[64:79], v[112:113], v[104:105], v[64:79]\n\t" \
  "v_exp_f32 v52, v52\n\t" \
  "v_exp_f32 v53, v53\n\t" \
  "v_exp_f32 v54, v54\n\t" \
  "v_exp_f32 v55, v55\n\t" \
  "s_waitcnt lgkmcnt(0)\n\t" \
  "v_smfmac_f32_32x32x32_bf16 v[0:15], v[116:119], v[32:39], v114\n\t" \
  "v_exp_f32 v56, v56\n\t" \
  "v_exp_f32 v57, v57\n\t" \
  "v_exp_f32 v58, v58\n\t" \
  "v_exp_f32 v59, v59\n\t" \
  "v_smfmac_f32_32x32x32_bf16 v[0:15], v[120:123], v[40:47], v114\n\t" \
  "v_exp_f32 v60, v60\n\t" \
  "v_exp_f32 v61, v61\n\t" \
  "v_exp_f32 v62, v62\n\t" \
  "v_exp_f32 v63, v63\n\t" \
  "v_mfma_f32_32x32x16_bf16 v[80:95], v[108:111], v[100:103], 0\n\t" \
  "ds_read_b128 v[108:111], v124 offset:5376\n\t" \
  "v_exp_f32 v64, v64\n\t" \
  "v_exp_f32 v65, v65\n\t" \
  "v_exp_f32 v66, v66\n\t" \
  "v_exp_f32 v67, v67\n\t" \
  "v_mfma_f32_32x32x8_bf16 v[80:95], v[112:113], v[106:107], v[80:95]\n\t" \
  "ds_read_b32 v112, v125 offset:5376\n\t" \
  "v_exp_f32 v68, v68\n\t" \
  "v_exp_f32 v69, v69\n\t" \
  "v_exp_f32 v70, v70\n\t" \
  "v_exp_f32 v71, v71\n\t" \
  "v_smfmac_f32_32x32x32_bf16 v[16:31], v[116:119], v[48:55], v114\n\t" \
  "ds_read_b128 v[116:119], v126 offset:5376\n\t" \
  "v_exp_f32 v72, v72\n\t" \
  "v_exp_f32 v73, v73\n\t" \
  "v_exp_f32 v74, v74\n\t" \
  "v_exp_f32 v75, v75\n\t" \
  "v_smfmac_f32_32x32x32_bf16 v[16:31], v[120:123], v[56:63], v114\n\t" \
  "ds_read_b128 v[120:123], v126 offset:6080\n\t" \
  "s_add_u32 %[cnt], %[cnt], 1\n\t" \
  "s_cmp_eq_u32 %[slot], 4\n\t" \
  "s_cselect_b32 %[inc], %[wrap], %[stg]\n\t" \
  "v_exp_f32 v76, v76\n\t" \
  "v_exp_f32 v77, v77\n\t" \
  "v_exp_f32 v78, v78\n\t" \
  "v_exp_f32 v79, v79\n\t" \
  "s_waitcnt lgkmcnt(2)\n\t" \
  "v_mfma_f32_32x32x16_bf16 v[32:47], v[108:111], v[96:99], 0\n\t" \
  "ds_read_b32 v115, v127 offset:32\n\t" \
  "s_cselect_b32 %[slot], -1, %[slot]\n\t" \
  "s_add_u32 %[slot], %[slot], 1\n\t" \
  "v_exp_f32 v80, v80\n\t" \
  "v_exp_f32 v81, v81\n\t" \
  "v_exp_f32 v82, v82\n\t" \
  "v_exp_f32 v83, v83\n\t" \
  "v_mfma_f32_32x32x8_bf16 v[32:47], v[112:113], v[104:105], v[32:47]\n\t" \
  "v_add_u32 v124, %[inc], v124\n\t" \
  "v_add_u32 v125, %[inc], v125\n\t" \
  "v_add_u32 v126, %[inc], v126\n\t" \
  "s_setprio 0\n\t" \
  "v_exp_f32 v84, v84\n\t" \
  "v_exp_f32 v85, v85\n\t" \
  "v_exp_f32 v86, v86\n\t" \
  "v_exp_f32 v87, v87\n\t" \
  "s_waitcnt lgkmcnt(1)\n\t" \
  "v_smfmac_f32_32x32x32_bf16 v[0:15], v[116:119], v[64:71], v114\n\t" \
  "v_exp_f32 v88, v88\n\t" \
  "v_exp_f32 v89, v89\n\t" \
  "v_exp_f32 v90, v90\n\t" \
  "v_exp_f32 v91, v91\n\t" \
  "s_waitcnt lgkmcnt(0)\n\t" \
  "v_readfirstlane_b32 %[rdy], v115\n\t" \
  "v_mov_b32 v115, %[cnt]\n\t" \
  "v_smfmac_f32_32x32x32_bf16 v[0:15], v[120:123], v[72:79], v114\n\t" \
  "ds_write_b32 v127, v115\n\t" \
  "s_cmp_gt_u32 %[rdy], %[cnt]\n\t" \
  "v_exp_f32 v92, v92\n\t" \
  "v_exp_f32 v93, v93\n\t" \
  "v_exp_f32 v94, v94\n\t" \
  "v_exp_f32 v95, v95\n\t" \
  "s_cbranch_scc1 5b\n\t" \
  "s_cmp_ge_u32 %[cnt], 16\n\t" \
  "s_cbranch_scc1 9f\n\t" \
  "s_branch 2b\n\t" \
  "9:\n\t" \
  "s_nop 1\n\t" \
  "v_smfmac_f32_32x32x32_bf16 v[16:31], v[116:119], v[80:87], v114\n\t" \
  "v_smfmac_f32_32x32x32_bf16 v[16:31], v[120:123], v[88:95], v114\n\t" \
  "s_waitcnt lgkmcnt(0)\n\t" \
  "s_nop 15\n\t"
#define TILE_GEMM_ASM \
  "s_nop 4\n\t" \
  "buffer_load_dwordx4 v[60:63], v140, %[rs], %[s0] offen offset:0 sc1\n\t" \
  "buffer_load_dwordx4 v[64:67], v140, %[rs], %[s1] offen offset:0 sc1\n\t" \
  "buffer_load_dwordx4 v[68:71], v140, %[rs], %[s0] offen offset:1024 sc1\n\t" \
  "buffer_load_dwordx4 v[72:75], v140, %[rs], %[s1] offen offset:1024 sc1\n\t" \
  "buffer_load_dwordx4 v[76:79], v140, %[rs], %[s0] offen offset:2048 sc1\n\t" \
  "buffer_load_dwordx4 v[80:83], v140, %[rs], %[s1] offen offset:2048 sc1\n\t" \
  "buffer_load_dwordx4 v[84:87], v140, %[rs], %[s0] offen offset:3072 sc1\n\t" \
  "buffer_load_dwordx4 v[88:91], v140, %[rs], %[s1] offen offset:3072 sc1\n\t" \
  "s_add_u32 %[s0], %[s0], 4096\n\t" \
  "buffer_load_dwordx4 v[92:95], v140, %[rs], %[s0] offen offset:0 sc1\n\t" \
  "s_add_u32 %[s1], %[s1], 4096\n\t" \
  "buffer_load_dwordx4 v[96:99], v140, %[rs], %[s1] offen offset:0 sc1\n\t" \
  "buffer_load_dwordx4 v[100:103], v140, %[rs], %[s0] offen offset:1024 sc1\n\t" \
  "buffer_load_dwordx4 v[104:107], v140, %[rs], %[s1] offen offset:1024 sc1\n\t" \
  "buffer_load_dwordx4 v[108:111], v140, %[rs], %[s0] offen offset:2048 sc1\n\t" \
  "buffer_load_dwordx4 v[112:115], v140, %[rs], %[s1] offen offset:2048 sc1\n\t" \
  "buffer_load_dwordx4 v[116:119], v140, %[rs], %[s0] offen offset:3072 sc1\n\t" \
  "buffer_load_dwordx4 v[120:123], v140, %[rs], %[s1] offen offset:3072 sc1\n\t" \
  "s_add_u32 %[s0], %[s0], 4096\n\t" \
  "buffer_load_dwordx4 v[124:127], v140, %[rs], %[s0] offen offset:0 sc1\n\t" \
  "s_add_u32 %[s1], %[s1], 4096\n\t" \
  "buffer_load_dwordx4 v[128:131], v140, %[rs], %[s1] offen offset:0 sc1\n\t" \
  "buffer_load_dwordx4 v[132:135], v140, %[rs], %[s0] offen offset:1024 sc1\n\t" \
  "buffer_load_dwordx4 v[136:139], v140, %[rs], %[s1] offen offset:1024 sc1\n\t" \
  "s_mov_b32 %[it], 0\n\t" \
  "1:\n\t" \
  "ds_read_b32 v143, v142\n\t" \
  "s_add_u32 %[it], %[it], 1\n\t" \
  "s_waitcnt lgkmcnt(0)\n\t" \
  "v_cmp_gt_u32 vcc, %[want], v143\n\t" \
  "s_cbranch_vccz 2f\n\t" \
  "s_sleep 4\n\t" \
  "s_cmp_lt_u32 %[it], 0x100000\n\t" \
  "s_cbranch_scc1 1b\n\t" \
  "2:\n\t" \
  "ds_read_b64_tr_b16 v[40:41], v141 offset:0\n\t" \
  "ds_read_b64_tr_b16 v[42:43], v141 offset:2560\n\t" \
  "ds_read_b64_tr_b16 v[44:45], v141 offset:8\n\t" \
  "ds_read_b64_tr_b16 v[46:47], v141 offset:2568\n\t" \
  "ds_read_b64_tr_b16 v[48:49], v141 offset:16\n\t" \
  "ds_read_b64_tr_b16 v[50:51], v141 offset:2576\n\t" \
  "ds_read_b64_tr_b16 v[52:53], v141 offset:24\n\t" \
  "ds_read_b64_tr_b16 v[54:55], v141 offset:2584\n\t" \
  "ds_read_b64_tr_b16 v[56:57], v141 offset:32\n\t" \
  "ds_read_b64_tr_b16 v[58:59], v141 offset:2592\n\t" \
  "v_mov_b32 v143, 1\n\t" \
  "v_mov_b32 v142, %[afree]\n\t" \
  "s_waitcnt vmcnt(18)\n\t" \
  "s_waitcnt lgkmcnt(8)\n\t" \
  "v_mfma_f32_16x16x32_bf16 v[0:3], v[40:43], v[60:63], v[0:3]\n\t" \
  "v_mfma_f32_16x16x32_bf16 v[4:7], v[40:43], v[64:67], v[4:7]\n\t" \
  "ds_read_b64_tr_b16 v[40:41], v141 offset:5120\n\t" \
  "ds_read_b64_tr_b16 v[42:43], v141 offset:7680\n\t" \
  "s_waitcnt lgkmcnt(8)\n\t" \
  "v_mfma_f32_16x16x32_bf16 v[8:11], v[44:47], v[60:63], v[8:11]\n\t" \
  "v_mfma_f32_16x16x32_bf16 v[12:15], v[44:47], v[64:67], v[12:15]\n\t" \
  "ds_read_b64_tr_b16 v[44:45], v141 offset:5128\n\t" \
  "ds_read_b64_tr_b16 v[46:47], v141 offset:7688\n\t" \
  "s_waitcnt lgkmcnt(8)\n\t" \
  "v_mfma_f32_16x16x32_bf16 v[16:19], v[48:51], v[60:63], v[16:19]\n\t" \
  "v_mfma_f32_16x16x32_bf16 v[20:23], v[48:51], v[64:67], v[20:23]\n\t" \
  "ds_read_b64_tr_b16 v[48:49], v141 offset:5136\n\t" \
  "ds_read_b64_tr_b16 v[50:51], v141 offset:7696\n\t" \
  "s_waitcnt lgkmcnt(8)\n\t" \
  "v_mfma_f32_16x16x32_bf16 v[24:27], v[52:55], v[60:63], v[24:27]\n\t" \
  "v_mfma_f32_16x16x32_bf16 v[28:31], v[52:55], v[64:67], v[28:31]\n\t" \
  "ds_read_b64_tr_b16 v[52:53], v141 offset:5144\n\t" \
  "ds_read_b64_tr_b16 v[54:55], v141 offset:7704\n\t" \
  "s_waitcnt lgkmcnt(8)\n\t" \
  "v_mfma_f32_16x16x32_bf16 v[32:35], v[56:59], v[60:63], v[32:35]\n\t" \
  "v_mfma_f32_16x16x32_bf16 v[36:39], v[56:59], v[64:67], v[36:39]\n\t" \
  "ds_read_b64_tr_b16 v[56:57], v141 offset:5152\n\t" \
  "ds_read_b64_tr_b16 v[58:59], v141 offset:7712\n\t" \
  "s_waitcnt vmcnt(16)\n\t" \
  "s_waitcnt lgkmcnt(8)\n\t" \
  "v_mfma_f32_16x16x32_bf16 v[0:3], v[40:43], v[68:71], v[0:3]\n\t" \
  "v_mfma_f32_16x16x32_bf16 v[4:7], v[40:43], v[72:75], v[4:7]\n\t" \
  "ds_read_b64_tr_b16 v[40:41], v141 offset:10240\n\t" \
  "ds_read_b64_tr_b16 v[42:43], v141 offset:12800\n\t" \
  "s_waitcnt lgkmcnt(8)\n\t" \
  "v_mfma_f32_16x16x32_bf16 v[8:11], v[44:47], v[68:71], v[8:11]\n\t" \
  "v_mfma_f32_16x16x32_bf16 v[12:15], v[44:47], v[72:75], v[12:15]\n\t" \
  "ds_read_b64_tr_b16 v[44:45], v141 offset:10248\n\t" \
  "ds_read_b64_tr_b16 v[46:47], v141 offset:12808\n\t" \
  "s_waitcnt lgkmcnt(8)\n\t" \
  "v_mfma_f32_16x16x32_bf16 v[16:19], v[48:51], v[68:71], v[16:19]\n\t" \
  "v_mfma_f32_16x16x32_bf16 v[20:23], v[48:51], v[72:75], v[20:23]\n\t" \
  "ds_read_b64_tr_b16 v[48:49], v141 offset:10256\n\t" \
  "ds_read_b64_tr_b16 v[50:51], v141 offset:12816\n\t" \
  "s_waitcnt lgkmcnt(8)\n\t" \
  "v_mfma_f32_16x16x32_bf16 v[24:27], v[52:55], v[68:71], v[24:27]\n\t" \
  "v_mfma_f32_16x16x32_bf16 v[28:31], v[52:55], v[72:75], v[28:31]\n\t" \
  "ds_read_b64_tr_b16 v[52:53], v141 offset:10264\n\t" \
  "ds_read_b64_tr_b16 v[54:55], v141 offset:12824\n\t" \
  "s_waitcnt lgkmcnt(8)\n\t" \
  "v_mfma_f32_16x16x32_bf16 v[32:35], v[56:59], v[68:71], v[32:35]\n\t" \
  "v_mfma_f32_16x16x32_bf16 v[36:39], v[56:59], v[72:75], v[36:39]\n\t" \
  "ds_read_b64_tr_b16 v[56:57], v141 offset:10272\n\t" \
  "ds_read_b64_tr_b16 v[58:59], v141 offset:12832\n\t" \
  "s_waitcnt vmcnt(14)\n\t" \
  "s_waitcnt lgkmcnt(8)\n\t" \
  "v_mfma_f32_16x16x32_bf16 v[0:3], v[40:43], v[76:79], v[0:3]\n\t" \
  "v_mfma_f32_16x16x32_bf16 v[4:7], v[40:43], v[80:83], v[4:7]\n\t" \
  "ds_read_b64_tr_b16 v[40:41], v141 offset:15360\n\t" \
  "ds_read_b64_tr_b16 v[42:43], v141 offset:17920\n\t" \
  "s_waitcnt lgkmcnt(8)\n\t" \
  "v_mfma_f32_16x16x32_bf16 v[8:11], v[44:47], v[76:79], v[8:11]\n\t" \
  "v_mfma_f32_16x16x32_bf16 v[12:15], v[44:47], v[80:83], v[12:15]\n\t" \
  "ds_read_b64_tr_b16 v[44:45], v141 offset:15368\n\t" \
  "ds_read_b64_tr_b16 v[46:47], v141 offset:17928\n\t" \
  "s_waitcnt lgkmcnt(8)\n\t" \
  "v_mfma_f32_16x16x32_bf16 v[16:19], v[48:51], v[76:79], v[16:19]\n\t" \
  "v_mfma_f32_16x16x32_bf16 v[20:23], v[48:51], v[80:83], v[20:23]\n\t" \
  "ds_read_b64_tr_b16 v[48:49], v141 offset:15376\n\t" \
  "ds_read_b64_tr_b16 v[50:51], v141 offset:17936\n\t" \
  "s_waitcnt lgkmcnt(8)\n\t" \
  "v_mfma_f32_16x16x32_bf16 v[24:27], v[52:55], v[76:79], v[24:27]\n\t" \
  "v_mfma_f32_16x16x32_bf16 v[28:31], v[52:55], v[80:83], v[28:31]\n\t" \
  "ds_read_b64_tr_b16 v[52:53], v141 offset:15384\n\t" \
  "ds_read_b64_tr_b16 v[54:55], v141 offset:17944\n\t" \
  "s_waitcnt lgkmcnt(8)\n\t" \
  "v_mfma_f32_16x16x32_bf16 v[32:35], v[56:59], v[76:79], v[32:35]\n\t" \
  "v_mfma_f32_16x16x32_bf16 v[36:39], v[56:59], v[80:83], v[36:39]\n\t" \
  "ds_read_b64_tr_b16 v[56:57], v141 offset:15392\n\t" \
  "ds_read_b64_tr_b16 v[58:59], v141 offset:17952\n\t" \
  "s_waitcnt vmcnt(12)\n\t" \
  "s_waitcnt lgkmcnt(8)\n\t" \
  "v_mfma_f32_16x16x32_bf16 v[0:3], v[40:43], v[84:87], v[0:3]\n\t" \
  "v_mfma_f32_16x16x32_bf16 v[4:7], v[40:43], v[88:91], v[4:7]\n\t" \
  "ds_read_b64_tr_b16 v[40:41], v141 offset:20480\n\t" \
  "ds_read_b64_tr_b16 v[42:43], v141 offset:23040\n\t" \
  "s_waitcnt lgkmcnt(8)\n\t" \
  "v_mfma_f32_16x16x32_bf16 v[8:11], v[44:47], v[84:87], v[8:11]\n\t" \
  "v_mfma_f32_16x16x32_bf16 v[12:15], v[44:47], v[88:91], v[12:15]\n\t" \
  "ds_read_b64_tr_b16 v[44:45], v141 offset:20488\n\t" \
  "ds_read_b64_tr_b16 v[46:47], v141 offset:23048\n\t" \
  "s_waitcnt lgkmcnt(8)\n\t" \
  "v_mfma_f32_16x16x32_bf16 v[16:19], v[48:51], v[84:87], v[16:19]\n\t" \
  "v_mfma_f32_16x16x32_bf16 v[20:23], v[48:51], v[88:91], v[20:23]\n\t" \
  "ds_read_b64_tr_b16 v[48:49], v141 offset:20496\n\t" \
  "ds_read_b64_tr_b16 v[50:51], v141 offset:23056\n\t" \
  "s_waitcnt lgkmcnt(8)\n\t" \
  "v_mfma_f32_16x16x32_bf16 v[24:27], v[52:55], v[84:87], v[24:27]\n\t" \
  "v_mfma_f32_16x16x32_bf16 v[28:31], v[52:55], v[88:91], v[28:31]\n\t" \
  "ds_read_b64_tr_b16 v[52:53], v141 offset:20504\n\t" \
  "ds_read_b64_tr_b16 v[54:55], v141 offset:23064\n\t" \
  "s_waitcnt lgkmcnt(8)\n\t" \
  "v_mfma_f32_16x16x32_bf16 v[32:35], v[56:59], v[84:87], v[32:35]\n\t" \
  "v_mfma_f32_16x16x32_bf16 v[36:39], v[56:59], v[88:91], v[36:39]\n\t" \
  "ds_read_b64_tr_b16 v[56:57], v141 offset:20512\n\t" \
  "ds_read_b64_tr_b16 v[58:59], v141 offset:23072\n\t" \
  "s_waitcnt vmcnt(10)\n\t" \
  "s_waitcnt lgkmcnt(8)\n\t" \
  "v_mfma_f32_16x16x32_bf16 v[0:3], v[40:43], v[92:95], v[0:3]\n\t" \
  "v_mfma_f32_16x16x32_bf16 v[4:7], v[40:43], v[96:99], v[4:7]\n\t" \
  "ds_read_b64_tr_b16 v[40:41], v141 offset:25600\n\t" \
  "ds_read_b64_tr_b16 v[42:43], v141 offset:28160\n\t" \
  "s_waitcnt lgkmcnt(8)\n\t" \
  "v_mfma_f32_16x16x32_bf16 v[8:11], v[44:47], v[92:95], v[8:11]\n\t" \
  "v_mfma_f32_16x16x32_bf16 v[12:15], v[44:47], v[96:99], v[12:15]\n\t" \
  "ds_read_b64_tr_b16 v[44:45], v141 offset:25608\n\t" \
  "ds_read_b64_tr_b16 v[46:47], v141 offset:28168\n\t" \
  "s_waitcnt lgkmcnt(8)\n\t" \
  "v_mfma_f32_16x16x32_bf16 v[16:19], v[48:51], v[92:95], v[16:19]\n\t" \
  "v_mfma_f32_16x16x32_bf16 v[20:23], v[48:51], v[96:99], v[20:23]\n\t" \
  "ds_read_b64_tr_b16 v[48:49], v141 offset:25616\n\t" \
  "ds_read_b64_tr_b16 v[50:51], v141 offset:28176\n\t" \
  "s_waitcnt lgkmcnt(8)\n\t" \
  "v_mfma_f32_16x16x32_bf16 v[24:27], v[52:55], v[92:95], v[24:27]\n\t" \
  "v_mfma_f32_16x16x32_bf16 v[28:31], v[52:55], v[96:99], v[28:31]\n\t" \
  "ds_read_b64_tr_b16 v[52:53], v141 offset:25624\n\t" \
  "ds_read_b64_tr_b16 v[54:55], v141 offset:28184\n\t" \
  "s_waitcnt lgkmcnt(8)\n\t" \
  "v_mfma_f32_16x16x32_bf16 v[32:35], v[56:59], v[92:95], v[32:35]\n\t" \
  "v_mfma_f32_16x16x32_bf16 v[36:39], v[56:59], v[96:99], v[36:39]\n\t" \
  "ds_read_b64_tr_b16 v[56:57], v141 offset:25632\n\t" \
  "ds_read_b64_tr_b16 v[58:59], v141 offset:28192\n\t" \
  "s_waitcnt vmcnt(8)\n\t" \
  "s_waitcnt lgkmcnt(8)\n\t" \
  "v_mfma_f32_16x16x32_bf16 v[0:3], v[40:43], v[100:103], v[0:3]\n\t" \
  "v_mfma_f32_16x16x32_bf16 v[4:7], v[40:43], v[104:107], v[4:7]\n\t" \
  "ds_read_b64_tr_b16 v[40:41], v141 offset:30720\n\t" \
  "ds_read_b64_tr_b16 v[42:43], v141 offset:33280\n\t" \
  "s_waitcnt lgkmcnt(8)\n\t" \
  "v_mfma_f32_16x16x32_bf16 v[8:11], v[44:47], v[100:103], v[8:11]\n\t" \
  "v_mfma_f32_16x16x32_bf16 v[12:15], v[44:47], v[104:107], v[12:15]\n\t" \
  "ds_read_b64_tr_b16 v[44:45], v141 offset:30728\n\t" \
  "ds_read_b64_tr_b16 v[46:47], v141 offset:33288\n\t" \
  "s_waitcnt lgkmcnt(8)\n\t" \
  "v_mfma_f32_16x16x32_bf16 v[16:19], v[48:51], v[100:103], v[16:19]\n\t" \
  "v_mfma_f32_16x16x32_bf16 v[20:23], v[48:51], v[104:107], v[20:23]\n\t" \
  "ds_read_b64_tr_b16 v[48:49], v141 offset:30736\n\t" \
  "ds_read_b64_tr_b16 v[50:51], v141 offset:33296\n\t" \
  "s_waitcnt lgkmcnt(8)\n\t" \
  "v_mfma_f32_16x16x32_bf16 v[24:27], v[52:55], v[100:103], v[24:27]\n\t" \
  "v_mfma_f32_16x16x32_bf16 v[28:31], v[52:55], v[104:107], v[28:31]\n\t" \
  "ds_read_b64_tr_b16 v[52:53], v141 offset:30744\n\t" \
  "ds_read_b64_tr_b16 v[54:55], v141 offset:33304\n\t" \
  "s_waitcnt lgkmcnt(8)\n\t" \
  "v_mfma_f32_16x16x32_bf16 v[32:35], v[56:59], v[100:103], v[32:35]\n\t" \
  "v_mfma_f32_16x16x32_bf16 v[36:39], v[56:59], v[104:107], v[36:39]\n\t" \
  "ds_read_b64_tr_b16 v[56:57], v141 offset:30752\n\t" \
  "ds_read_b64_tr_b16 v[58:59], v141 offset:33312\n\t" \
  "s_waitcnt vmcnt(6)\n\t" \
  "s_waitcnt lgkmcnt(8)\n\t" \
  "v_mfma_f32_16x16x32_bf16 v[0:3], v[40:43], v[108:111], v[0:3]\n\t" \
  "v_mfma_f32_16x16x32_bf16 v[4:7], v[40:43], v[112:115], v[4:7]\n\t" \
  "ds_read_b64_tr_b16 v[40:41], v141 offset:35840\n\t" \
  "ds_read_b64_tr_b16 v[42:43], v141 offset:38400\n\t" \
  "s_waitcnt lgkmcnt(8)\n\t" \
  "v_mfma_f32_16x16x32_bf16 v[8:11], v[44:47], v[108:111], v[8:11]\n\t" \
  "v_mfma_f32_16x16x32_bf16 v[12:15], v[44:47], v[112:115], v[12:15]\n\t" \
  "ds_read_b64_tr_b16 v[44:45], v141 offset:35848\n\t" \
  "ds_read_b64_tr_b16 v[46:47], v141 offset:38408\n\t" \
  "s_waitcnt lgkmcnt(8)\n\t" \
  "v_mfma_f32_16x16x32_bf16 v[16:19], v[48:51], v[108:111], v[16:19]\n\t" \
  "v_mfma_f32_16x16x32_bf16 v[20:23], v[48:51], v[112:115], v[20:23]\n\t" \
  "ds_read_b64_tr_b16 v[48:49], v141 offset:35856\n\t" \
  "ds_read_b64_tr_b16 v[50:51], v141 offset:38416\n\t" \
  "s_waitcnt lgkmcnt(8)\n\t" \
  "v_mfma_f32_16x16x32_bf16 v[24:27], v[52:55], v[108:111], v[24:27]\n\t" \
  "v_mfma_f32_16x16x32_bf16 v[28:31], v[52:55], v[112:115], v[28:31]\n\t" \
  "ds_read_b64_tr_b16 v[52:53], v141 offset:35864\n\t" \
  "ds_read_b64_tr_b16 v[54:55], v141 offset:38424\n\t" \
  "s_waitcnt lgkmcnt(8)\n\t" \
  "v_mfma_f32_16x16x32_bf16 v[32:35], v[56:59], v[108:111], v[32:35]\n\t" \
  "v_mfma_f32_16x16x32_bf16 v[36:39], v[56:59], v[112:115], v[36:39]\n\t" \
  "ds_read_b64_tr_b16 v[56:57], v141 offset:35872\n\t" \
  "ds_read_b64_tr_b16 v[58:59], v141 offset:38432\n\t" \
  "s_waitcnt vmcnt(4)\n\t" \
  "s_waitcnt lgkmcnt(8)\n\t" \
  "v_mfma_f32_16x16x32_bf16 v[0:3], v[40:43], v[116:119], v[0:3]\n\t" \
  "v_mfma_f32_16x16x32_bf16 v[4:7], v[40:43], v[120:123], v[4:7]\n\t" \
  "ds_read_b64_tr_b16 v[40:41], v141 offset:40960\n\t" \
  "ds_read_b64_tr_b16 v[42:43], v141 offset:43520\n\t" \
  "s_waitcnt lgkmcnt(8)\n\t" \
  "v_mfma_f32_16x16x32_bf16 v[8:11], v[44:47], v[116:119], v[8:11]\n\t" \
  "v_mfma_f32_16x16x32_bf16 v[12:15], v[44:47], v[120:123], v[12:15]\n\t" \
  "ds_read_b64_tr_b16 v[44:45], v141 offset:40968\n\t" \
  "ds_read_b64_tr_b16 v[46:47], v141 offset:43528\n\t" \
  "s_waitcnt lgkmcnt(8)\n\t" \
  "v_mfma_f32_16x16x32_bf16 v[16:19], v[48:51], v[116:119], v[16:19]\n\t" \
  "v_mfma_f32_16x16x32_bf16 v[20:23], v[48:51], v[120:123], v[20:23]\n\t" \
  "ds_read_b64_tr_b16 v[48:49], v141 offset:40976\n\t" \
  "ds_read_b64_tr_b16 v[50:51], v141 offset:43536\n\t" \
  "s_waitcnt lgkmcnt(8)\n\t" \
  "v_mfma_f32_16x16x32_bf16 v[24:27], v[52:55], v[116:119], v[24:27]\n\t" \
  "v_mfma_f32_16x16x32_bf16 v[28:31], v[52:55], v[120:123], v[28:31]\n\t" \
  "ds_read_b64_tr_b16 v[52:53], v141 offset:40984\n\t" \
  "ds_read_b64_tr_b16 v[54:55], v141 offset:43544\n\t" \
  "s_waitcnt lgkmcnt(8)\n\t" \
  "v_mfma_f32_16x16x32_bf16 v[32:35], v[56:59], v[116:119], v[32:35]\n\t" \
  "v_mfma_f32_16x16x32_bf16 v[36:39], v[56:59], v[120:123], v[36:39]\n\t" \
  "ds_read_b64_tr_b16 v[56:57], v141 offset:40992\n\t" \
  "ds_read_b64_tr_b16 v[58:59], v141 offset:43552\n\t" \
  "s_waitcnt vmcnt(2)\n\t" \
  "s_waitcnt lgkmcnt(8)\n\t" \
  "v_mfma_f32_16x16x32_bf16 v[0:3], v[40:43], v[124:127], v[0:3]\n\t" \
  "v_mfma_f32_16x16x32_bf16 v[4:7], v[40:43], v[128:131], v[4:7]\n\t" \
  "ds_read_b64_tr_b16 v[40:41], v141 offset:46080\n\t" \
  "ds_read_b64_tr_b16 v[42:43], v141 offset:48640\n\t" \
  "s_waitcnt lgkmcnt(8)\n\t" \
  "v_mfma_f32_16x16x32_bf16 v[8:11], v[44:47], v[124:127], v[8:11]\n\t" \
  "v_mfma_f32_16x16x32_bf16 v[12:15], v[44:47], v[128:131], v[12:15]\n\t" \
  "ds_read_b64_tr_b16 v[44:45], v141 offset:46088\n\t" \
  "ds_read_b64_tr_b16 v[46:47], v141 offset:48648\n\t" \
  "s_waitcnt lgkmcnt(8)\n\t" \
  "v_mfma_f32_16x16x32_bf16 v[16:19], v[48:51], v[124:127], v[16:19]\n\t" \
  "v_mfma_f32_16x16x32_bf16 v[20:23], v[48:51], v[128:131], v[20:23]\n\t" \
  "ds_read_b64_tr_b16 v[48:49], v141 offset:46096\n\t" \
  "ds_read_b64_tr_b16 v[50:51], v141 offset:48656\n\t" \
  "s_waitcnt lgkmcnt(8)\n\t" \
  "v_mfma_f32_16x16x32_bf16 v[24:27], v[52:55], v[124:127], v[24:27]\n\t" \
  "v_mfma_f32_16x16x32_bf16 v[28:31], v[52:55], v[128:131], v[28:31]\n\t" \
  "ds_read_b64_tr_b16 v[52:53], v141 offset:46104\n\t" \
  "ds_read_b64_tr_b16 v[54:55], v141 offset:48664\n\t" \
  "s_waitcnt lgkmcnt(8)\n\t" \
  "v_mfma_f32_16x16x32_bf16 v[32:35], v[56:59], v[124:127], v[32:35]\n\t" \
  "v_mfma_f32_16x16x32_bf16 v[36:39], v[56:59], v[128:131], v[36:39]\n\t" \
  "ds_read_b64_tr_b16 v[56:57], v141 offset:46112\n\t" \
  "ds_read_b64_tr_b16 v[58:59], v141 offset:48672\n\t" \
  "s_waitcnt vmcnt(0)\n\t" \
  "s_waitcnt lgkmcnt(8)\n\t" \
  "v_mfma_f32_16x16x32_bf16 v[0:3], v[40:43], v[132:135], v[0:3]\n\t" \
  "v_mfma_f32_16x16x32_bf16 v[4:7], v[40:43], v[136:139], v[4:7]\n\t" \
  "s_waitcnt lgkmcnt(6)\n\t" \
  "v_mfma_f32_16x16x32_bf16 v[8:11], v[44:47], v[132:135], v[8:11]\n\t" \
  "v_mfma_f32_16x16x32_bf16 v[12:15], v[44:47], v[136:139], v[12:15]\n\t" \
  "s_waitcnt lgkmcnt(4)\n\t" \
  "v_mfma_f32_16x16x32_bf16 v[16:19], v[48:51], v[132:135], v[16:19]\n\t" \
  "v_mfma_f32_16x16x32_bf16 v[20:23], v[48:51], v[136:139], v[20:23]\n\t" \
  "s_waitcnt lgkmcnt(2)\n\t" \
  "v_mfma_f32_16x16x32_bf16 v[24:27], v[52:55], v[132:135], v[24:27]\n\t" \
  "v_mfma_f32_16x16x32_bf16 v[28:31], v[52:55], v[136:139], v[28:31]\n\t" \
  "s_waitcnt lgkmcnt(0)\n\t" \
  "v_mfma_f32_16x16x32_bf16 v[32:35], v[56:59], v[132:135], v[32:35]\n\t" \
  "v_mfma_f32_16x16x32_bf16 v[36:39], v[56:59], v[136:139], v[36:39]\n\t" \
  "s_mov_b64 exec, 1\n\t" \
  "ds_add_u32 v142, v143\n\t" \
  "s_mov_b64 exec, -1\n\t" \
  "s_nop 7\n\t"
#define TILE_GEMM_CLOB "v40", "v41", "v42", "v43", "v44", "v45", "v46", "v47", "v48", "v49", "v50", "v51", "v52", "v53", "v54", "v55", "v56", "v57", "v58", "v59", "v60", "v61", "v62", "v63", "v64", "v65", "v66", "v67", "v68", "v69", "v70", "v71", "v72", "v73", "v74", "v75", "v76", "v77", "v78", "v79", "v80", "v81", "v82", "v83", "v84", "v85", "v86", "v87", "v88", "v89", "v90", "v91", "v92", "v93", "v94", "v95", "v96", "v97", "v98", "v99", "v100", "v101", "v102", "v103", "v104", "v105", "v106", "v107", "v108", "v109", "v110", "v111", "v112", "v113", "v114", "v115", "v116", "v117", "v118", "v119", "v120", "v121", "v122", "v123", "v124", "v125", "v126", "v127", "v128", "v129", "v130", "v131", "v132", "v133", "v134", "v135", "v136", "v137", "v138", "v139", "v143"
#define K1_LOOP_ASM \
  "v_mov_b64 v[0:1], 0\n\t" \
  "v_mov_b64 v[2:3], 0\n\t" \
  "v_mov_b64 v[4:5], 0\n\t" \
  "v_mov_b64 v[6:7], 0\n\t" \
  "v_mov_b64 v[8:9], 0\n\t" \
  "v_mov_b64 v[10:11], 0\n\t" \
  "v_mov_b64 v[12:13], 0\n\t" \
  "v_mov_b64 v[14:15], 0\n\t" \
  "v_mov_b64 v[16:17], 0\n\t" \
  "v_mov_b64 v[18:19], 0\n\t" \
  "v_mov_b64 v[20:21], 0\n\t" \
  "v_mov_b64 v[22:23], 0\n\t" \
  "v_mov_b64 v[24:25], 0\n\t" \
  "v_mov_b64 v[26:27], 0\n\t" \
  "v_mov_b64 v[28:29], 0\n\t" \
  "v_mov_b64 v[30:31], 0\n\t" \
  "v_mov_b64 v[32:33], 0\n\t" \
  "v_mov_b64 v[34:35], 0\n\t" \
  "v_mov_b64 v[36:37], 0\n\t" \
  "v_mov_b64 v[38:39], 0\n\t" \
  "v_mov_b64 v[40:41], 0\n\t" \
  "v_mov_b64 v[42:43], 0\n\t" \
  "v_mov_b64 v[44:45], 0\n\t" \
  "v_mov_b64 v[46:47], 0\n\t" \
  "v_mov_b64 v[48:49], 0\n\t" \
  "v_mov_b64 v[50:51], 0\n\t" \
  "v_mov_b64 v[52:53], 0\n\t" \
  "v_mov_b64 v[54:55], 0\n\t" \
  "v_mov_b64 v[56:57], 0\n\t" \
  "v_mov_b64 v[58:59], 0\n\t" \
  "v_mov_b64 v[60:61], 0\n\t" \
  "v_mov_b64 v[62:63], 0\n\t" \
  "v_mov_b64 v[64:65], 0\n\t" \
  "v_mov_b64 v[66:67], 0\n\t" \
  "v_mov_b64 v[68:69], 0\n\t" \
  "v_mov_b64 v[70:71], 0\n\t" \
  "v_mov_b64 v[72:73], 0\n\t" \
  "v_mov_b64 v[74:75], 0\n\t" \
  "v_mov_b64 v[76:77], 0\n\t" \
  "v_mov_b64 v[78:79], 0\n\t" \
  "v_mov_b64 v[116:117], 0\n\t" \
  "v_mov_b64 v[118:119], 0\n\t" \
  "v_mov_b64 v[120:121], 0\n\t" \
  "v_mov_b64 v[122:123], 0\n\t" \
  "v_mov_b64 v[124:125], 0\n\t" \
  "v_mov_b64 v[126:127], 0\n\t" \
  "v_mov_b64 v[128:129], 0\n\t" \
  "v_mov_b64 v[130:131], 0\n\t" \
  "v_mov_b64 v[132:133], 0\n\t" \
  "v_mov_b64 v[134:135], 0\n\t" \
  "v_mov_b64 v[136:137], 0\n\t" \
  "v_mov_b64 v[138:139], 0\n\t" \
  "v_mov_b64 v[140:141], 0\n\t" \
  "v_mov_b64 v[142:143], 0\n\t" \
  "v_mov_b64 v[144:145], 0\n\t" \
  "v_mov_b64 v[146:147], 0\n\t" \
  "v_mov_b64 v[148:149], 0\n\t" \
  "v_mov_b64 v[150:151], 0\n\t" \
  "s_mov_b32 %[cnt], 0\n\t" \
  "1:\n\t" \
  "s_barrier\n\t" \
  "v_mfma_f32_16x16x32_bf16 v[0:3], v[132:135], v[116:119], v[0:3]\n\t" \
  "ds_read_b128 v[80:83], v152\n\t" \
  "v_mfma_f32_16x16x32_bf16 v[4:7], v[132:135], v[120:123], v[4:7]\n\t" \
  "ds_read_b64_tr_b16 v[96:97], v154 offset:0\n\t" \
  "v_mfma_f32_16x16x32_bf16 v[8:11], v[132:135], v[124:127], v[8:11]\n\t" \
  "ds_read_b64_tr_b16 v[98:99], v154 offset:2560\n\t" \
  "v_mfma_f32_16x16x32_bf16 v[12:15], v[132:135], v[128:131], v[12:15]\n\t" \
  "ds_read_b128 v[84:87], v152 offset:2048\n\t" \
  "v_mfma_f32_16x16x32_bf16 v[16:19], v[136:139], v[116:119], v[16:19]\n\t" \
  "ds_read_b128 v[88:91], v152 offset:4096\n\t" \
  "v_mfma_f32_16x16x32_bf16 v[20:23], v[136:139], v[120:123], v[20:23]\n\t" \
  "ds_read_b128 v[92:95], v152 offset:6144\n\t" \
  "v_mfma_f32_16x16x32_bf16 v[24:27], v[136:139], v[124:127], v[24:27]\n\t" \
  "ds_read_b64_tr_b16 v[100:101], v154 offset:8\n\t" \
  "v_mfma_f32_16x16x32_bf16 v[28:31], v[136:139], v[128:131], v[28:31]\n\t" \
  "ds_read_b64_tr_b16 v[102:103], v154 offset:2568\n\t" \
  "v_mfma_f32_16x16x32_bf16 v[32:35], v[140:143], v[116:119], v[32:35]\n\t" \
  "ds_read_b64_tr_b16 v[104:105], v154 offset:16\n\t" \
  "v_mfma_f32_16x16x32_bf16 v[36:39], v[140:143], v[120:123], v[36:39]\n\t" \
  "ds_read_b64_tr_b16 v[106:107], v154 offset:2576\n\t" \
  "v_mfma_f32_16x16x32_bf16 v[40:43], v[140:143], v[124:127], v[40:43]\n\t" \
  "ds_read_b64_tr_b16 v[108:109], v154 offset:24\n\t" \
  "v_mfma_f32_16x16x32_bf16 v[44:47], v[140:143], v[128:131], v[44:47]\n\t" \
  "ds_read_b64_tr_b16 v[110:111], v154 offset:2584\n\t" \
  "v_mfma_f32_16x16x32_bf16 v[48:51], v[144:147], v[116:119], v[48:51]\n\t" \
  "ds_read_b64_tr_b16 v[112:113], v154 offset:32\n\t" \
  "v_mfma_f32_16x16x32_bf16 v[52:55], v[144:147], v[120:123], v[52:55]\n\t" \
  "ds_read_b64_tr_b16 v[114:115], v154 offset:2592\n\t" \
  "v_mfma_f32_16x16x32_bf16 v[56:59], v[144:147], v[124:127], v[56:59]\n\t" \
  "s_and_b32 %[inc], %[cnt], 3\n\t" \
  "v_mfma_f32_16x16x32_bf16 v[60:63], v[144:147], v[128:131], v[60:63]\n\t" \
  "s_cmp_eq_u32 %[inc], 3\n\t" \
  "v_mfma_f32_16x16x32_bf16 v[64:67], v[148:151], v[116:119], v[64:67]\n\t" \
  "s_cselect_b32 %[inc], %[wrap], %[stp]\n\t" \
  "v_mfma_f32_16x16x32_bf16 v[68:71], v[148:151], v[120:123], v[68:71]\n\t" \
  "v_add_u32 v152, %[inc], v152\n\t" \
  "v_mfma_f32_16x16x32_bf16 v[72:75], v[148:151], v[124:127], v[72:75]\n\t" \
  "v_mfma_f32_16x16x32_bf16 v[76:79], v[148:151], v[128:131], v[76:79]\n\t" \
  "s_waitcnt lgkmcnt(0)\n\t" \
  "v_mfma_f32_16x16x32_bf16 v[0:3], v[96:99], v[80:83], v[0:3]\n\t" \
  "ds_read_b128 v[116:119], v153\n\t" \
  "v_mfma_f32_16x16x32_bf16 v[4:7], v[96:99], v[84:87], v[4:7]\n\t" \
  "ds_read_b64_tr_b16 v[132:133], v154 offset:5120\n\t" \
  "v_mfma_f32_16x16x32_bf16 v[8:11], v[96:99], v[88:91], v[8:11]\n\t" \
  "ds_read_b64_tr_b16 v[134:135], v154 offset:7680\n\t" \
  "v_mfma_f32_16x16x32_bf16 v[12:15], v[96:99], v[92:95], v[12:15]\n\t" \
  "ds_read_b128 v[120:123], v153 offset:2048\n\t" \
  "v_mfma_f32_16x16x32_bf16 v[16:19], v[100:103], v[80:83], v[16:19]\n\t" \
  "ds_read_b128 v[124:127], v153 offset:4096\n\t" \
  "v_mfma_f32_16x16x32_bf16 v[20:23], v[100:103], v[84:87], v[20:23]\n\t" \
  "ds_read_b128 v[128:131], v153 offset:6144\n\t" \
  "v_mfma_f32_16x16x32_bf16 v[24:27], v[100:103], v[88:91], v[24:27]\n\t" \
  "ds_read_b64_tr_b16 v[136:137], v154 offset:5128\n\t" \
  "v_mfma_f32_16x16x32_bf16 v[28:31], v[100:103], v[92:95], v[28:31]\n\t" \
  "ds_read_b64_tr_b16 v[138:139], v154 offset:7688\n\t" \
  "v_mfma_f32_16x16x32_bf16 v[32:35], v[104:107], v[80:83], v[32:35]\n\t" \
  "ds_read_b64_tr_b16 v[140:141], v154 offset:5136\n\t" \
  "v_mfma_f32_16x16x32_bf16 v[36:39], v[104:107], v[84:87], v[36:39]\n\t" \
  "ds_read_b64_tr_b16 v[142:143], v154 offset:7696\n\t" \
  "v_mfma_f32_16x16x32_bf16 v[40:43], v[104:107], v[88:91], v[40:43]\n\t" \
  "ds_read_b64_tr_b16 v[144:145], v154 offset:5144\n\t" \
  "v_mfma_f32_16x16x32_bf16 v[44:47], v[104:107], v[92:95], v[44:47]\n\t" \
  "ds_read_b64_tr_b16 v[146:147], v154 offset:7704\n\t" \
  "v_mfma_f32_16x16x32_bf16 v[48:51], v[108:111], v[80:83], v[48:51]\n\t" \
  "ds_read_b64_tr_b16 v[148:149], v154 offset:5152\n\t" \
  "v_mfma_f32_16x16x32_bf16 v[52:55], v[108:111], v[84:87], v[52:55]\n\t" \
  "ds_read_b64_tr_b16 v[150:151], v154 offset:7712\n\t" \
  "v_mfma_f32_16x16x32_bf16 v[56:59], v[108:111], v[88:91], v[56:59]\n\t" \
  "v_add_u32 v153, %[inc], v153\n\t" \
  "v_mfma_f32_16x16x32_bf16 v[60:63], v[108:111], v[92:95], v[60:63]\n\t" \
  "v_add_u32 v154, %[inc], v154\n\t" \
  "v_mfma_f32_16x16x32_bf16 v[64:67], v[112:115], v[80:83], v[64:67]\n\t" \
  "s_add_u32 %[cnt], %[cnt], 1\n\t" \
  "v_mfma_f32_16x16x32_bf16 v[68:71], v[112:115], v[84:87], v[68:71]\n\t" \
  "s_cmp_lt_u32 %[cnt], 10\n\t" \
  "v_mfma_f32_16x16x32_bf16 v[72:75], v[112:115], v[88:91], v[72:75]\n\t" \
  "v_mfma_f32_16x16x32_bf16 v[76:79], v[112:115], v[92:95], v[76:79]\n\t" \
  "s_waitcnt lgkmcnt(0)\n\t" \
  "s_cbranch_scc1 1b\n\t" \
  "v_mfma_f32_16x16x32_bf16 v[0:3], v[132:135], v[116:119], v[0:3]\n\t" \
  "v_mfma_f32_16x16x32_bf16 v[16:19], v[136:139], v[116:119], v[16:19]\n\t" \
  "v_mfma_f32_16x16x32_bf16 v[32:35], v[140:143], v[116:119], v[32:35]\n\t" \
  "v_mfma_f32_16x16x32_bf16 v[48:51], v[144:147], v[116:119], v[48:51]\n\t" \
  "v_mfma_f32_16x16x32_bf16 v[64:67], v[148:151], v[116:119], v[64:67]\n\t" \
  "v_mfma_f32_16x16x32_bf16 v[4:7], v[132:135], v[120:123], v[4:7]\n\t" \
  "v_mfma_f32_16x16x32_bf16 v[20:23], v[136:139], v[120:123], v[20:23]\n\t" \
  "v_mfma_f32_16x16x32_bf16 v[36:39], v[140:143], v[120:123], v[36:39]\n\t" \
  "v_mfma_f32_16x16x32_bf16 v[52:55], v[144:147], v[120:123], v[52:55]\n\t" \
  "v_mfma_f32_16x16x32_bf16 v[68:71], v[148:151], v[120:123], v[68:71]\n\t" \
  "v_mfma_f32_16x16x32_bf16 v[8:11], v[132:135], v[124:127], v[8:11]\n\t" \
  "v_mfma_f32_16x16x32_bf16 v[24:27], v[136:139], v[124:127], v[24:27]\n\t" \
  "v_mfma_f32_16x16x32_bf16 v[40:43], v[140:143], v[124:127], v[40:43]\n\t" \
  "v_mfma_f32_16x16x32_bf16 v[56:59], v[144:147], v[124:127], v[56:59]\n\t" \
  "v_mfma_f32_16x16x32_bf16 v[72:75], v[148:151], v[124:127], v[72:75]\n\t" \
  "v_mfma_f32_16x16x32_bf16 v[12:15], v[132:135], v[128:131], v[12:15]\n\t" \
  "v_mfma_f32_16x16x32_bf16 v[28:31], v[136:139], v[128:131], v[28:31]\n\t" \
  "v_mfma_f32_16x16x32_bf16 v[44:47], v[140:143], v[128:131], v[44:47]\n\t" \
  "v_mfma_f32_16x16x32_bf16 v[60:63], v[144:147], v[128:131], v[60:63]\n\t" \
  "v_mfma_f32_16x16x32_bf16 v[76:79], v[148:151], v[128:131], v[76:79]\n\t" \
  "s_nop 7\n\t"
#define K1_LOOP_CLOB "v80", "v81", "v82", "v83", "v84", "v85", "v86", "v87", "v88", "v89", "v90", "v91", "v92", "v93", "v94", "v95", "v96", "v97", "v98", "v99", "v100", "v101", "v102", "v103", "v104", "v105", "v106", "v107", "v108", "v109", "v110", "v111", "v112", "v113", "v114", "v115", "v116", "v117", "v118", "v119", "v120", "v121", "v122", "v123", "v124", "v125", "v126", "v127", "v128", "v129", "v130", "v131", "v132", "v133", "v134", "v135", "v136", "v137", "v138", "v139", "v140", "v141", "v142", "v143", "v144", "v145", "v146", "v147", "v148", "v149", "v150", "v151"
#define LDELAY 2
#define QOKW 9u
#define FINW 4u
#define FSTAMP(i) do {} while (0)
#define HSTAMP(i) STAMP(i)
#define TSTAMP(i) do {} while (0)
#define FSLEEP 0
#define BSLEEP 1
#define K1_ROT ((tt + 3 * (jb >> 3)) % 10)
constexpr u32 K1_SLOT = 36864, K1_W = 16384;
constexpr u32 K1_EX = 3 * K1_SLOT, K1_EXP = 4 * K1_SLOT;
constexpr u32 K1_VI = 0, K1_VI_P = 208, K1_PHK = 27648, K1_PHK_P = 336;
constexpr u32 K1_QST = 0;
constexpr u32 K1_PHQ = 16384;
constexpr u32 K1_KVT = 57344;
constexpr u32 K1_KVF = 70656;
constexpr u32 K1_XCH = 73728;
constexpr u32 K1_WPHI = 156160;
constexpr u32 L_K01 = 160256;
constexpr u32 SL = 4 * REC;
constexpr u32 L_IMG = 0;
constexpr u32 L_RING = 102400;
constexpr u32 L_MRG = L_RING + SL;
constexpr u32 L_FLG = 162816;
constexpr u32 F_GBAR = L_FLG + 96, F_APUB = L_FLG + 100, F_AFREE = L_FLG + 104  , F_P1 = L_FLG + 112, F_PHB = L_FLG + 116,
              F_P2 = L_FLG + 120, F_KVOK = L_FLG + 124, F_BIMG = L_FLG + 128, F_QOK = L_FLG + 132, F_TAG = L_FLG + 136, F_FIN = L_FLG + 152, F_K01 = L_FLG + 156, F_AOK = L_FLG + 160, F_P3 = L_FLG + 164, L_GT = L_FLG + 256;
constexpr u32 LDS_BYTES = 163840;

__device__ __forceinline__ f32x16 attn_exact(rsrc_t rW, u32x4 qx, u32 q2lo, int lane) {
  const f32x16 zero16 = {0.f, 0.f, 0.f, 0.f, 0.f, 0.f, 0.f, 0.f, 0.f, 0.f, 0.f, 0.f, 0.f, 0.f, 0.f, 0.f};
  const int r32 = lane & 31, hh = lane >> 5;
  const u32x2 qz = {q2lo, 0u};
  float m = -INFINITY;
#pragma unroll 1
  for (u32 rec = 0; rec < 64; ++rec) {
    const u32x4 a1 = gl128(rW, lane * 16, WS_KVIM + rec * REC);
    const u32 a2 = gl32(rW, lane * 4, WS_KVIM + rec * REC + 1024);
    f32x16 s = mfma32(a1, qx, zero16);
    s = mfma32k8((u32x2){a2, 0u}, qz, s);
    m = fmaxf(m, max16(s));
  }
  m = xhalf_max(m);
  f32x16 O = zero16;
  const u32 av = 1280u + (u32)hh * 352u + (u32)(r32 <= 20 ? r32 : 21) * 16u;
#pragma unroll 1
  for (u32 t2 = 0; t2 < 64; ++t2) {
    const u32 rec = (t2 + 62) & 63;
    const u32x4 a1 = gl128(rW, lane * 16, WS_KVIM + rec * REC);
    const u32 a2 = gl32(rW, lane * 4, WS_KVIM + rec * REC + 1024);
    const u32x4 b1 = gl128(rW, av, WS_KVIM + t2 * REC), b2 = gl128(rW, av, WS_KVIM + t2 * REC + 704);
    f32x16 s = mfma32(a1, qx, zero16);
    s = mfma32k8((u32x2){a2, 0u}, qz, s);
#pragma unroll
    for (int r = 0; r < 16; ++r) s[r] = __builtin_amdgcn_exp2f(s[r] - m);
    O = smf(b1, lo8(s), O, 0xDDDD);
    O = smf(b2, hi8(s), O, 0xDDDD);
  }
  return O;
}

__global__ void __launch_bounds__(768, 3)
gm_fused(const char* __restrict__ x, const char* __restrict__ wqkv, const char* __restrict__ wphi, const char* __restrict__ wq,
         const char* __restrict__ wk, const char* __restrict__ wv, const char* __restrict__ gw, const char* __restrict__ awo,
         const char* __restrict__ bwo, char* ws, char* __restrict__ out) {
  extern __shared__ __attribute__((aligned(16))) char smem[];
  ldsp lds = (ldsp)smem;
  const u32 lds0 = (u32)(uintptr_t)lds;
  const int w = __builtin_amdgcn_readfirstlane(threadIdx.x >> 6);
  const int bid = blockIdx.x, xcd = bid & 7, jb = bid >> 3;
  const rsrc_t rW = __builtin_amdgcn_make_buffer_rsrc(ws, 0, WS_END, 0x00020000);
  const f32x16 zero16 = {0.f, 0.f, 0.f, 0.f, 0.f, 0.f, 0.f, 0.f, 0.f, 0.f, 0.f, 0.f, 0.f, 0.f, 0.f, 0.f};
  STAMP_INIT();
  const int tt = (xcd & 1) * 8 + (jb & 7), combo = (xcd >> 1) * 4 + (jb >> 3), type = combo & 1, hg = combo >> 1;
  const u32x4 rsWS = mkrsrc(ws, WS_END);
  {
    if (w < 4) {
      const int cg = w >> 1, th = w & 1;
      f32x16 ac[5];
      {
        const int lane = lane_id(), g = lane >> 4, i16 = lane & 15;
        u32 aW = lds0 + K1_W + (u32)cg * 10240u + (u32)(4 * g + (i16 >> 2)) * 160u + (u32)(i16 & 3) * 40u;
        u32 aX0 = lds0 + (u32)(64 * th + i16) * 128u + (u32)((g ^ (i16 >> 1)) * 16), aX1 = aX0 ^ 64u;
        u32 cnt, inc;
        asm volatile(K1_LOOP_ASM
                     : "=&{v[0:15]}"(ac[0]), "=&{v[16:31]}"(ac[1]), "=&{v[32:47]}"(ac[2]), "=&{v[48:63]}"(ac[3]), "=&{v[64:79]}"(ac[4]),
                       "+{v152}"(aX0), "+{v153}"(aX1), "+{v154}"(aW), [cnt] "=&s"(cnt), [inc] "=&s"(inc)
                     : [wrap] "s"(0u - 3u * K1_SLOT), [stp] "s"(K1_SLOT)
                     : "memory", "scc", K1_LOOP_CLOB);
      }
      HSTAMP(2); FSTAMP(2);
      const int lane = lane_id(), g = lane >> 4, i16 = lane & 15;
      const float sc = (cg && !type) ? QSCALE : 1.f;
#pragma unroll
      for (int n = 0; n < 4; ++n) {
        u32 v[10];
#pragma unroll
        for (int T = 0; T < 5; ++T) { v[2 * T] = pk2(ac[T][4 * n] * sc, ac[T][4 * n + 1] * sc); v[2 * T + 1] = pk2(ac[T][4 * n + 2] * sc, ac[T][4 * n + 3] * sc); }
        const int t32 = 16 * (n & 1) + i16;
        if (cg == 0) {
          ldsp xw = lds + K1_XCH + (u32)(2 * n + th) * 2560u;
          *(LDSAS u32x4*)(xw + lane * 16) = (u32x4){v[0], v[1], v[2], v[3]};
          *(LDSAS u32x4*)(xw + 1024 + lane * 16) = (u32x4){v[4], v[5], v[6], v[7]};
          *(LDSAS u32x2*)(xw + 2048 + lane * 8) = (u32x2){v[8], v[9]};
        } else if (type) {
          ldsp dst = lds + K1_VI + (u32)(64 * th + 16 * n + i16) * K1_VI_P + (u32)g * 48u;
          *(LDSAS u32x4*)(dst) = (u32x4){v[0], v[1], v[2], v[3]};
          *(LDSAS u32x4*)(dst + 16) = (u32x4){v[4], v[5], v[6], v[7]};
          *(LDSAS u32x4*)(dst + 32) = (u32x4){v[8], v[9], 0x00003F80u, 0u};
        } else if (th == 0) {
          ldsp dst = lds + K1_QST + (u32)(2 * g + (n >> 1)) * 1280u;
          *(LDSAS u32x4*)(dst + t32 * 16) = (u32x4){v[0], v[1], v[2], v[3]};
          *(LDSAS u32x4*)(dst + 512 + t32 * 16) = (u32x4){v[4], v[5], v[6], v[7]};
          *(LDSAS u32x2*)(dst + 1024 + t32 * 8) = (u32x2){v[8], v[9]};
        } else {
          const u32 dst = WS_QB + (u32)(tt * 8 + hg) * 10240u + (u32)(2 * g + (n >> 1)) * 1280u;
          st128(rW, dst + t32 * 16, (u32x4){v[0], v[1], v[2], v[3]});
          st128(rW, dst + 512 + t32 * 16, (u32x4){v[4], v[5], v[6], v[7]});
          st64(rW, dst + 1024 + t32 * 8, (u32x2){v[8], v[9]});
        }
      }
      LBAR();
      HSTAMP(3);
      if (w == 3) {
        const u32x2 tag = ld64(lds + F_TAG);
        const u32 fq = WS_FQ + (u32)(tt * 8 + hg) * 128u;
        if (type == 0) { DRAIN(); st64(rW, lane == 0 ? fq : OOB, tag); FSTAMP(3); }
        else { flags_wait<1>(rW, fq, 1, tag); lds_st(lds + F_QOK, 1u); FSTAMP(4); }
      }
    } else {
      const int lane = lane_id(), g = lane >> 4, i16 = lane & 15;
      if (w >= 8) {
        __builtin_amdgcn_s_setprio(1);
        const int lw = w - 8, g1 = lw >> 1, hb = lw & 1;
        const u32x4 rsX = mkrsrc(x - 4096, SEQ * DM * 2 + 4096);
        const bool useq = g1 && !type;
        const u32x4 rsW = useq ? mkrsrc(wq - 4096, DM * DM * 2 + 4096) : mkrsrc(wqkv - 4096, DM * 3 * DM * 2 + 4096);
        const u32 pitch = useq ? 1280u : 3840u;
        const u32 col = (type ? (g1 ? 1280u : 640u) : 0u) + 80u * hg;
        u32 vx[4], vw[5];
        const u32 vx0 = 4096u + (u32)(lane >> 3) * 1280u + (u32)(((lane & 7) ^ (lane >> 4)) * 16);
#pragma unroll
        for (int i = 0; i < 4; ++i) vx[i] = (vx0 ^ (u32)((i & 1) * 64)) + (u32)i * 9216u;
#pragma unroll
        for (int j = 0; j < 5; ++j) {
          const u32 c = 64u * j + lane, rho = (c * 205u) >> 11, cc = c - rho * 10u;
          const u32 k = 8u * ((rho >> 2) & 3u) + 4u * (rho >> 4) + (rho & 3u);
          vw[j] = 4096u + k * pitch + cc * 16u - (u32)(j & 3) * 1024u;
        }
        const u32 sx0 = (u32)(128 * tt + 32 * lw) * 1280u, sw0 = 32u * hb * pitch + col * 2u;
        int ks = K1_ROT;
        auto issue = [&](int s) {
          const u32 base = lds0 + (u32)(s & 3) * K1_SLOT;
          u32 keep;
          asm volatile(
              "s_mov_b32 %0, m0\n\ts_mov_b32 m0, %[mx]\n\ts_nop 2\n\t"
              "buffer_load_dwordx4 %[x0], %[rx], %[sx] offen lds\n\t"
              "buffer_load_dwordx4 %[x1], %[rx], %[sx] offen offset:1024 lds\n\t"
              "buffer_load_dwordx4 %[x2], %[rx], %[sx] offen offset:2048 lds\n\t"
              "buffer_load_dwordx4 %[x3], %[rx], %[sx] offen offset:3072 lds\n\t"
              "s_mov_b32 m0, %[mw]\n\ts_nop 0\n\t"
              "buffer_load_dwordx4 %[w0], %[rw], %[sw] offen lds\n\t"
              "buffer_load_dwordx4 %[w1], %[rw], %[sw] offen offset:1024 lds\n\t"
              "buffer_load_dwordx4 %[w2], %[rw], %[sw] offen offset:2048 lds\n\t"
              "buffer_load_dwordx4 %[w3], %[rw], %[sw] offen offset:3072 lds\n\t"
              "s_add_u32 m0, %[mw], 4096\n\ts_nop 0\n\t"
              "buffer_load_dwordx4 %[w4], %[rw], %[sw] offen lds\n\t"
              "s_mov_b32 m0, %0"
              : "=&s"(keep)
              : [mx] "s"(base + (u32)lw * 4096u), [mw] "s"(base + K1_W + (u32)lw * 5120u),
                [rx] "s"(rsX), [rw] "s"(rsW), [sx] "s"(sx0 + (u32)ks * 128u), [sw] "s"(sw0 + (u32)ks * 64u * pitch),
                [x0] "v"(vx[0]), [x1] "v"(vx[1]), [x2] "v"(vx[2]), [x3] "v"(vx[3]), [w0] "v"(vw[0]), [w1] "v"(vw[1]), [w2] "v"(vw[2]), [w3] "v"(vw[3]),
                [w4] "v"(vw[4])
              : "memory", "scc");
          ks = ks == 9 ? 0 : ks + 1;
        };
        __builtin_amdgcn_s_sleep(LDELAY);
        issue(0); issue(1);
        WAIT_BAR(9);
        HSTAMP(2); FSTAMP(2);
        issue(2);
#pragma unroll 1
        for (int s = 1; s < 8; ++s) {
          issue(s + 2);
          WAIT_BAR(18);
        }
        WAIT_BAR(9);
        WAIT_BAR(0);
        lds_wait_ge<1>(lds + F_KVOK, 1);
        {
          const u32 o = (u32)lw * 13312u, vl = (u32)lane * 16u;
#pragma unroll 1
          for (u32 c = 0; c < 3; ++c) dma4(rsWS, vl, WS_KVIM + o + c * 4096u, lds0 + L_RING + o + c * 4096u);
          dma1s(rsWS, vl, WS_KVIM + o + 12288u, lds0 + L_RING + o + 12288u);
          dma1s(rsWS, vl, WS_KVIM + 52736u, lds0 + L_RING + 52736u);
        }
        if (lane < 8) lds_st(lds + L_FLG + 32 + lane * 4, 5u);
        DRAIN();
      } else {
        const int v4 = w - 4, tok0 = 128 * tt + 16 * hg;
        __builtin_amdgcn_s_setprio(3);
        {
          const u32x4 rsE = mkrsrc((type ? wv : wk) + v4 * 6400, 6400);
          const u32 la = lds0 + K1_EX + (u32)v4 * 7168u;
#pragma unroll 1
          for (u32 c = 0; c < 7; ++c) dma1(rsE, (u32)lane * 16u, c * 1024u, la + c * 1024u);
          u32x4 xf[5], gf[5];
#pragma unroll
          for (int s = 0; s < 5; ++s) {
            xf[s] = *(const u32x4*)(x + (size_t)(tok0 + i16) * 1280 + v4 * 320 + s * 64 + g * 16);
            gf[s] = *(const u32x4*)(gw + v4 * 320 + s * 64 + g * 16);
          }
          u64 e = 0;
          if (w == 7) e = __hip_atomic_load((const gu64*)(ws + WS_EPOCH), __ATOMIC_RELAXED, __HIP_MEMORY_SCOPE_AGENT) + 0x9E3779B97F4A7C15ull;
          DRAIN();
          if (w == 7) lds_st(lds + L_FLG + lane * 4, lane == 34 ? (u32)e : (lane == 35 ? (u32)(e >> 32) : 0u));
          ldsp pe = lds + K1_EX + (u32)v4 * 7168u + (u32)(8 * g + (i16 >> 2)) * 40u + (u32)(i16 & 3) * 8u;
          f32x4 c0 = {0.f, 0.f, 0.f, 0.f}, c1 = c0, c2 = c0;
#pragma unroll
          for (int s = 0; s < 5; ++s) {
            const u32x4 w0 = trd2(pe + s * 1280, pe + s * 1280 + 160), w1 = trd2(pe + s * 1280 + 32, pe + s * 1280 + 192);
            if (type) { c0 = mfma16(xf[s], w0, c0); c1 = mfma16(xf[s], w1, c1); }
            else { c0 = mfma16(w0, xf[s], c0); c1 = mfma16(w1, xf[s], c1); }
            c2 = mfma16(gf[s], xf[s], c2);
          }
          ldsp pp = lds + K1_EXP + (u32)v4 * 1024u + lane * 16;
          *(LDSAS f32x4*)(pp) = c0; *(LDSAS f32x4*)(pp + 4096) = c1; *(LDSAS f32x4*)(pp + 8192) = c2;
        }
        LBAR();
        if (v4 < 3) {
          ldsp pp = lds + K1_EXP + (u32)v4 * 4096u + lane * 16;
          const f32x4 e0 = (*(LDSAS f32x4*)(pp) + *(LDSAS f32x4*)(pp + 1024)) + (*(LDSAS f32x4*)(pp + 2048) + *(LDSAS f32x4*)(pp + 3072));
          const u32x2 ev = {pk2(e0[0], e0[1]), pk2(e0[2], e0[3])};
          const int tile = tok0 >> 5, k0 = tok0 & 31;
          if (v4 == 2) {
            st32(rW, (type == 0 && g == 0) ? WS_GATE + (u32)(tok0 + i16) * 4u : OOB, __float_as_uint(__builtin_amdgcn_rcpf(1.f + __builtin_amdgcn_exp2f(-e0[0] * LOG2E))));
          } else if (type == 0) {
            const u32 rec = WS_KVIM + (u32)((tile + 62) & 63) * REC;
            if (v4 == 0) st64(rW, rec + (g >> 1) * 512 + (k0 + i16) * 16 + (g & 1) * 8, ev);
            else { const u32 o = g ? OOB : rec + 1024 + (k0 + i16) * 4; st32(rW, o, ev[0]); st32(rW, o + 128, ev[1]); }
          } else {
            const int d = 16 * v4 + i16, k32 = k0 + 4 * g;
            const u32x2 val = d == 20 ? (u32x2){0x3F803F80u, 0x3F803F80u} : (d == 21 ? (u32x2){0u, 0u} : ev);
            st64(rW, d <= 21 ? WS_KVIM + (u32)tile * REC + 1280 + (k32 >> 3) * 352 + d * 16 + (k32 & 7) * 2 : OOB, val);
          }
        }
        LBAR();
        {
          const u32 be = (u32)(v4 * 64 + lane) * 16u;
          dma1(mkrsrc(wphi, NH * 400 * 2), be < 3200u ? be : OOB, (u32)hg * 3200u, lds0 + K1_WPHI + (u32)v4 * 1024u);
        }
        LBAR();
        WAIT_BAR(0);
        const u32x2 tag = ld64(lds + F_TAG);
        if (w == 7) {
          st64(rW, lane < 8 ? WS_F1 + (u32)lane * 2048u + (u32)bid * 8u : OOB, tag);
          FSTAMP(3);
          LBAR(); LBAR(); LBAR(); LBAR(); LBAR();
          const u32 fo = WS_F1 + (u32)xcd * 2048u;
          u32x4 fa = gl128(rW, lane * 16, fo), fb = gl128(rW, lane * 16, fo + 1024);
          LBAR();
          PIN(fa); PIN(fb);
          const u32 dd = (fa[0] ^ tag[0]) | (fa[1] ^ tag[1]) | (fa[2] ^ tag[0]) | (fa[3] ^ tag[1]) | (fb[0] ^ tag[0]) | (fb[1] ^ tag[1]) | (fb[2] ^ tag[0]) | (fb[3] ^ tag[1]);
          if (!__all(dd == 0u)) flags_wait256<2>(rW, fo, tag);
          asm volatile("" ::: "memory");
          FSTAMP(4);
          const u32x4 ka = gl128(rW, lane * 16, WS_KVIM + 62 * REC), kb = gl128(rW, lane * 16, WS_KVIM + 63 * REC);
          const u32 kc = gl32(rW, lane * 4, WS_KVIM + 62 * REC + 1024), kd = gl32(rW, lane * 4, WS_KVIM + 63 * REC + 1024);
          const u32 gtv = gl32(rW, lane * 4, WS_GATE + (u32)(2 * tt + type) * 256u);
          asm volatile("" ::: "memory");
          lds_st(lds + F_KVOK, 1u);
          if (bid == 0 && lane == 0)
            __hip_atomic_store((gu64*)(ws + WS_EPOCH), (u64)tag[0] | ((u64)tag[1] << 32), __ATOMIC_RELAXED, __HIP_MEMORY_SCOPE_AGENT);
          *(LDSAS u32x4*)(lds + L_K01 + lane * 16) = ka; *(LDSAS u32*)(lds + L_K01 + 1024 + lane * 4) = kc;
          *(LDSAS u32x4*)(lds + L_K01 + 1280 + lane * 16) = kb; *(LDSAS u32*)(lds + L_K01 + 2304 + lane * 4) = kd;
          *(LDSAS u32*)(lds + L_GT + lane * 4) = gtv;
        } else {
          LBAR(); LBAR(); LBAR(); LBAR(); LBAR(); LBAR();
        }
      }
      __builtin_amdgcn_s_setprio(2);
      const int j8 = w - 4;
      const u32 aP = K1_WPHI + (((i16 & 3) == g) ? (u32)g * 800u + (u32)(i16 >> 2) * 40u : 3200u);
      u32x4 a0[5], a1[5]; u32x2 a2[5];
#pragma unroll
      for (int T = 0; T < 5; ++T) {
        a0[T] = trd2(lds + aP + T * 8, lds + aP + T * 8 + 160);
        a1[T] = trd2(lds + aP + T * 8 + 320, lds + aP + T * 8 + 480);
        a2[T] = trd(lds + aP + T * 8 + 640);
      }
      LBAR();
      HSTAMP(3);
      ldsp xr = lds + K1_XCH + (u32)j8 * 2560u;
      const u32x4 bq0 = ld128(xr + lane * 16), bq1 = ld128(xr + 1024 + lane * 16);
      const u32x2 bq2 = ld64(xr + 2048 + lane * 8);
      f32x4 pq[5];
#pragma unroll
      for (int T = 0; T < 5; ++T) pq[T] = mfma16(a0[T], bq0, (f32x4){0.f, 0.f, 0.f, 0.f});
#pragma unroll
      for (int T = 0; T < 5; ++T) pq[T] = mfma16(a1[T], bq1, pq[T]);
#pragma unroll
      for (int T = 0; T < 5; ++T) pq[T] = mfma16((u32x4){a2[T][0], a2[T][1], 0u, 0u}, (u32x4){bq2[0], bq2[1], 0u, 0u}, pq[T]);
      float mx = fmaxf(pq[0][0], pq[0][1]), mn = fminf(pq[0][0], pq[0][1]);
      mx = fmaxf(fmaxf(mx, pq[0][2]), pq[0][3]); mn = fminf(fminf(mn, pq[0][2]), pq[0][3]);
#pragma unroll
      for (int T = 1; T < 5; ++T) {
        mx = fmaxf(fmaxf(mx, pq[T][0]), pq[T][1]); mx = fmaxf(fmaxf(mx, pq[T][2]), pq[T][3]);
        mn = fminf(fminf(mn, pq[T][0]), pq[T][1]); mn = fminf(fminf(mn, pq[T][2]), pq[T][3]);
      }
      const float cp = -mx * LOG2E, cn = mn * LOG2E;
      f32x4 ep[5], en[5];
#pragma unroll
      for (int T = 0; T < 5; ++T) { ep[T] = pq[T] * LOG2E + cp; en[T] = pq[T] * (-LOG2E) + cn; PIN(ep[T]); PIN(en[T]); }
#pragma unroll
      for (int T = 0; T < 5; ++T)
#pragma unroll
        for (int i = 0; i < 4; ++i) { ep[T][i] = __builtin_amdgcn_exp2f(ep[T][i]); en[T][i] = __builtin_amdgcn_exp2f(en[T][i]); }
      const f32x4 sp = (ep[0] + ep[1]) + (ep[2] + ep[3]) + ep[4], sn = (en[0] + en[1]) + (en[2] + en[3]) + en[4];
      const float rp = __builtin_amdgcn_rcpf((sp[0] + sp[1]) + (sp[2] + sp[3])), rn = __builtin_amdgcn_rcpf((sn[0] + sn[1]) + (sn[2] + sn[3]));
      u32 ph[20];
#pragma unroll
      for (int T = 0; T < 5; ++T) {
        const f32x4 a = ep[T] * rp, b = en[T] * rn;
        ph[2 * T] = pk2(a[0], a[1]); ph[2 * T + 1] = pk2(a[2], a[3]);
        ph[10 + 2 * T] = pk2(b[0], b[1]); ph[11 + 2 * T] = pk2(b[2], b[3]);
      }
      const int tl = 64 * (j8 & 1) + 16 * (j8 >> 1) + i16;
      if (type == 0) {
        ldsp dst = lds + K1_PHQ + (u32)(4 * g + (tl >> 5)) * 2560u + (tl & 31) * 16;
#pragma unroll
        for (int c = 0; c < 5; ++c) *(LDSAS u32x4*)(dst + c * 512) = (u32x4){ph[4 * c], ph[4 * c + 1], ph[4 * c + 2], ph[4 * c + 3]};
      } else {
        ldsp dst = lds + K1_PHK + (u32)tl * K1_PHK_P + (u32)g * 80u;
#pragma unroll
        for (int c = 0; c < 5; ++c) *(LDSAS u32x4*)(dst + 16 * c) = (u32x4){ph[4 * c], ph[4 * c + 1], ph[4 * c + 2], ph[4 * c + 3]};
        lds_arrive(lds + F_PHB);
        lds_wait_ge<1>(lds + F_PHB, 8);
        const int hl = j8 >> 1, dt = j8 & 1;
        const u32 trow = (u32)(8 * g + (i16 >> 2));
        ldsp pa = lds + K1_PHK + trow * K1_PHK_P + (u32)hl * 80u + (u32)(i16 & 3) * 8u;
        ldsp pb = lds + K1_VI + trow * K1_VI_P + (u32)hl * 48u + (u32)dt * 32u + (u32)(i16 & 3) * 8u;
        f32x4 kv[3];
#pragma unroll
        for (int ft = 0; ft < 3; ++ft) kv[ft] = (f32x4){0.f, 0.f, 0.f, 0.f};
#pragma unroll
        for (int ks = 0; ks < 4; ++ks) {
          u32x4 bv = trd2(pb + ks * 32 * K1_VI_P, pb + (ks * 32 + 4) * K1_VI_P);
#pragma unroll
          for (int ft = 0; ft < 3; ++ft) {
            u32x4 av = trd2(pa + ks * 32 * K1_PHK_P + ft * 32, pa + (ks * 32 + 4) * K1_PHK_P + ft * 32);
            kv[ft] = mfma16(av, bv, kv[ft]);
          }
        }
#pragma unroll
        for (int ft = 0; ft < 3; ++ft) *(LDSAS f32x4*)(lds + K1_KVF + (u32)j8 * 3072u + ft * 1024 + lane * 16) = kv[ft];
      }
      lds_arrive(lds + F_P2);
      __builtin_amdgcn_s_setprio(0);
      HSTAMP(4);
      if (w >= 8 && w < 11) {
        __builtin_amdgcn_s_setprio(3);
        const int hw = w - 8;
        const u32x2 tag = ld64(lds + F_TAG);
        lds_wait_ge<4>(lds + F_P2, 8);
        if (type) {
          lds_wait_ge<4>(lds + F_QOK, QOKW);
#pragma unroll 1
          for (int j = hw; j < 8; j += 3) {
            const int d = 16 * (j & 1) + i16;
            const u32 dst = d <= 20 ? WS_KVP + (u32)((hg * 16 + tt) * 4 + (j >> 1)) * 3360u + d * 160 + 16 * g : OOB;
            ldsp src = lds + K1_KVF + (u32)j * 3072u + lane * 16;
            st128(rW, dst, ld128(src)); st128(rW, dst + 64, ld128(src + 1024)); st128(rW, g < 2 ? dst + 128 : OOB, ld128(src + 2048));
          }
          DRAIN();
          if (lds_arrive(lds + F_P3) == 2) {
            st64(rW, lane == 0 ? WS_F2 + (u32)(hg * 16 + tt) * 8u : OOB, tag);
            flags_wait<4>(rW, WS_F2 + (u32)hg * 128u, 16, tag);
            const int it = tt * 53 + lane;
            const bool ok = lane < 53 && it < 840;
            const u32 vo = ok ? (u32)it * 16u : OOB, pb = WS_KVP + (u32)hg * 16u * 13440u;
            f32x4 a = __builtin_bit_cast(f32x4, gl128(rW, vo, pb));
#pragma unroll
            for (int p = 1; p < 16; ++p) a += __builtin_bit_cast(f32x4, gl128(rW, vo, pb + p * 13440));
            st64(rW, ok ? WS_KVT + (u32)hg * 6720u + (u32)it * 8u : OOB, (u32x2){pk2(a[0], a[1]), pk2(a[2], a[3])});
            DRAIN();
            st64(rW, lane == 0 ? WS_F3 + (u32)(hg * 16 + tt) * 8u : OOB, tag);
            lds_arrive(lds + F_P3);
          }
          lds_wait_ge<4>(lds + F_P3, 4);
        } else {
          const int r32 = lane & 31, hh = lane >> 5;
          {
            const u32 qi = (u32)((jb >> 4) * 8 + (jb & 7));
            const u32x4 rs = mkrsrc((qi & 1) ? bwo : awo, DM * DM * 2);
            u32 dummy;
#pragma unroll 1
            for (u32 c = hw; c < 13; c += 3) {
              const u32 l = c * 64u + lane;
              asm volatile("s_nop 4\n\tbuffer_load_dword %0, %1, %2, 0 offen" : "=v"(dummy) : "v"(l < 800u ? ((qi >> 1) * 800u + l) * 128u : OOB), "s"(rs) : "memory");
            }
            DRAIN();
          }
          if (hw == 0) {
            const u32x4 rsK = mkrsrc(ws + WS_KVT + (u32)hg * 6720u, 6720);
            *(LDSAS u32x2*)(lds + K1_KVT + 7168 + lane * 8) = gl64(rW, lane * 8, WS_GATE + (u32)tt * 512u);
            flags_wait<4>(rW, WS_F3 + (u32)hg * 128u, 16, tag);
            dma4(rsK, (u32)lane * 16u, 0u, lds0 + K1_KVT);
            dma1s(rsK, (u32)lane * 16u, 4096u, lds0 + K1_KVT + 4096u); dma1s(rsK, (u32)lane * 16u, 5120u, lds0 + K1_KVT + 5120u);
            dma1s(rsK, (u32)lane * 16u, 6144u, lds0 + K1_KVT + 6144u);
            DRAIN();
            lds_arrive(lds + F_P3);
          }
          lds_wait_ge<4>(lds + F_P3, 1);
          HSTAMP(5);
          const u32 ao = K1_KVT + (u32)(r32 < 20 ? r32 : 20) * 80u + (u32)hh * 16u;
          const u32 vo0 = WS_OFR + (u32)(8 * tt + (r32 >> 4)) * 20480u + (r32 & 15) * 16 + (u32)hg * 2560u;
#pragma unroll 1
          for (int j = hw; j < 16; j += 3) {
            const int hl = j >> 2, t4 = j & 3;
            ldsp ak = lds + ao + hl * 1680, pk = lds + K1_PHQ + j * 2560;
            const u32x4 a0 = ld128(ak), a1 = ld128(ak + 32);
            u32x4 a2 = ld128(ak + 64);
            const u32x4 p0 = ld128(pk + lane * 16), p1 = ld128(pk + 1024 + lane * 16), p2 = ld128(pk + 2048 + r32 * 16);
            const float gt = *(LDSAS float*)(lds + K1_KVT + 7168 + (32 * t4 + r32) * 4);
            if (hh) a2 = (u32x4){0u, 0u, 0u, 0u};
            f32x16 N = mfma32(a0, p0, zero16);
            N = mfma32(a1, p1, N);
            N = mfma32(a2, p2, N);
            const float sa = gt * __builtin_amdgcn_rcpf(N[12] + 1e-6f);
            const u32 vo = vo0 + (u32)(t4 * 40960 + (hl >> 1) * 1280);
            st128(rW, vo + hh * 256 + (hl & 1) * 512, (u32x4){pk2(N[0] * sa, N[1] * sa), pk2(N[2] * sa, N[3] * sa), pk2(N[4] * sa, N[5] * sa), pk2(N[6] * sa, N[7] * sa)});
            st64(rW, hh ? OOB : vo + 1024 + (hl & 1) * 8, (u32x2){pk2(N[8] * sa, N[9] * sa), pk2(N[10] * sa, N[11] * sa)});
          }
          DRAIN();
          if (lds_arrive(lds + F_APUB) == 2) st64(rW, lane == 0 ? WS_FLGA + (u32)(tt * 16 + hg) * 8u : OOB, tag);
          lds_wait_ge<4>(lds + F_APUB, 3);
        }
        HSTAMP(6);
      }
    }
  }

  if (w >= 8) {
    __builtin_amdgcn_s_setprio(3);
    const int hw = w - 8, kh = hw >> 1, t64 = xcd * 4 + (jb & 3), cgp = jb >> 2;
    const u32x2 tag = ld64(lds + F_TAG);
    f32x16 ac0 = zero16, ac1 = zero16; f32x8 ac2 = {0.f, 0.f, 0.f, 0.f, 0.f, 0.f, 0.f, 0.f};
    u32 s = 5, slot = 0, ps = 5;
#pragma unroll 1
    for (int part = 0; part < 2; ++part) {
      const int ln = lane_id();
      const u32 vl = (u32)ln * 16u;
      if (w == 11) {
#pragma unroll 1
        for (u32 it = 0; it < SPIN_CAP && ps < 16; ++it) {
          const u32 fl = lds_ld(lds + (ln < 8 ? L_FLG + ln * 4 : F_AOK));
          const u32 ns = s;
          if (s < 16 && __all(ln >= 8 || fl + 4 >= s)) {
            const u32 so = WS_KVIM + s * SL, la = lds0 + L_RING + slot * SL;
            dma4(rsWS, vl, so, la); dma4(rsWS, vl, so + 4096u, la + 4096u); dma2h(rsWS, vl, so + 8192u, la + 8192u);
            asm volatile("s_waitcnt vmcnt(11)" ::: "memory");
            ++s; slot = slot == 4 ? 0 : slot + 1;
          } else {
            DRAIN();
            if (ps == ns) {
              if (part == 0 && __all(ln < 8 || fl >= 4)) break;
              __builtin_amdgcn_s_sleep(6);
            }
          }
          if (ps != ns) {
            ps = ns;
            if (ln < 8) lds_st(lds + L_FLG + 32 + ln * 4, ps);
          }
        }
      } else {
        if (part) lds_wait_ge<4>(lds + F_AFREE, 4u); else lds_wait_ge<4>(lds + L_FLG + 32, 6u);
        const u32x4 rsO = mkrsrc(part ? bwo : awo, DM * DM * 2);
#pragma unroll 1
        for (u32 pp = hw; pp < 25; pp += 3) {
          const u32 c = 64u * pp + ln, rho = (c * 6554u) >> 16, cc = c - rho * 10u;
          const u32 U = 4u * (rho >> 5) + ((rho >> 2) & 3u), u = (rho >> 4) & 1u, q = rho & 3u;
          const u32 hp = (U * 205u) >> 10, un = U - hp * 5u;
          const u32 row = 40u * hp + q + ((un < 4u) ? (un >> 1) * 20u + 4u * (un & 1u) + 8u * u : u * 20u + 16u);
          const u32 vo = row * 1280u + cc * 16u;
#pragma unroll
          for (u32 per = 0; per < 4; ++per) dma1(rsO, vo, per * 204800u + (u32)cgp * 160u, lds0 + L_IMG + (per * 25u + pp) * 1024u);
        }
        DRAIN();
        lds_arrive(lds + (part ? F_BIMG : F_AOK));
        if (part == 0 && hw == 0) { flags_wait<4>(rW, WS_FLGA + (u32)(t64 >> 1) * 128u, 8, tag); lds_arrive(lds + F_AOK); }
      }
      if (part == 0) lds_wait_ge<4>(lds + F_AOK, 4u);
      else { lds_wait_ge<4>(lds + F_FIN, FINW); TSTAMP(3); flags_wait<BSLEEP>(rW, WS_FLGB + (u32)t64 * 256u + (u32)kh * 128u, 16, tag); TSTAMP(4); FSTAMP(7); }
      u32 s0 = WS_OFR + (u32)(part * 2621440 + (4 * t64 + 2 * (hw & 1)) * 20480 + kh * 10240), s1 = s0 + 20480u, it;
      u32 pa = lds0 + F_BIMG;
      asm volatile(TILE_GEMM_ASM
                   : "+{v[0:15]}"(ac0), "+{v[16:31]}"(ac1), "+{v[32:39]}"(ac2), "+{v142}"(pa), [s0] "+s"(s0), [s1] "+s"(s1), [it] "=&s"(it)
                   : "{v140}"(vl), "{v141}"(lds0 + L_IMG + (u32)(4 * (ln >> 4) + ((ln & 15) >> 2)) * 160u + (u32)(ln & 3) * 40u + (u32)kh * 51200u), [rs] "s"(rsWS), [want] "s"(part ? 3u : 0u),
                     [afree] "s"(lds0 + F_AFREE)
                   : "memory", "scc", "vcc", TILE_GEMM_CLOB);
      if (part == 0) HSTAMP(7);
    }
    TSTAMP(5);
    {
      const int ln = lane_id();
      f32x4 acc[5][2];
#pragma unroll
      for (int n = 0; n < 2; ++n) {
        acc[0][n] = (f32x4){ac0[4 * n], ac0[4 * n + 1], ac0[4 * n + 2], ac0[4 * n + 3]}; acc[1][n] = (f32x4){ac0[8 + 4 * n], ac0[9 + 4 * n], ac0[10 + 4 * n], ac0[11 + 4 * n]};
        acc[2][n] = (f32x4){ac1[4 * n], ac1[4 * n + 1], ac1[4 * n + 2], ac1[4 * n + 3]}; acc[3][n] = (f32x4){ac1[8 + 4 * n], ac1[9 + 4 * n], ac1[10 + 4 * n], ac1[11 + 4 * n]};
        acc[4][n] = (f32x4){ac2[4 * n], ac2[4 * n + 1], ac2[4 * n + 2], ac2[4 * n + 3]};
      }
      ldsp xb = lds + L_RING + 3 * SL;
      ldsp xw = xb + (u32)hw * 5120u + ln * 16, xr = xb + (u32)(hw ^ 2) * 5120u + ln * 16;
#pragma unroll
      for (int T = 0; T < 5; ++T) *(LDSAS f32x4*)(xw + T * 1024) = kh ? acc[T][0] : acc[T][1];
      lds_arrive(lds + F_GBAR + 48 + (hw & 1) * 4);
      lds_wait_ge<0>(lds + F_GBAR + 48 + (hw & 1) * 4, 2u);
      TSTAMP(6);
      u32 o[10];
#pragma unroll
      for (int T = 0; T < 5; ++T) {
        const f32x4 v = (kh ? acc[T][1] : acc[T][0]) + *(LDSAS f32x4*)(xr + T * 1024);
        o[2 * T] = pk2(v[0], v[1]); o[2 * T + 1] = pk2(v[2], v[3]);
      }
      typedef u32x4 u32x4a __attribute__((aligned(8)));
      char* dst = out + (size_t)(64 * t64 + 32 * (hw & 1) + 16 * kh + (ln & 15)) * 1280 + cgp * 160 + (ln >> 4) * 40;
      *(u32x4a*)dst = (u32x4){o[0], o[1], o[2], o[3]};
      *(u32x4a*)(dst + 16) = (u32x4){o[4], o[5], o[6], o[7]};
      *(u32x2*)(dst + 32) = (u32x2){o[8], o[9]};
    }
    STAMP_FLUSH();
    return;
  }

  const int pr = w & 3, par = w >> 2, head = 4 * hg + pr, t64 = 2 * tt + type;
  u32x4 q1[2]; u32x2 q2[2];
  f32x16 O0, O1;
  {
    const int lane = lane_id(), r32 = lane & 31, hh = lane >> 5;
    if (type) {
      lds_wait_ge<1>(lds + F_QOK, 1);
#pragma unroll
      for (int j = 0; j < 2; ++j) {
        const u32 qrow = WS_QB + (u32)(tt * 8 + hg) * 10240u + (u32)(2 * pr + j) * 1280u;
        q1[j] = gl128(rW, lane * 16, qrow);
        q2[j] = (u32x2){gl32(rW, r32 * 8 + hh * 4, qrow + 1024), 0u};
      }
      lds_arrive(lds + F_QOK);
    } else {
#pragma unroll
      for (int j = 0; j < 2; ++j) {
        ldsp qrow = lds + K1_QST + (u32)(2 * pr + j) * 1280u;
        q1[j] = ld128(qrow + lane * 16);
        q2[j] = (u32x2){*(LDSAS u32*)(qrow + 1024 + r32 * 8 + hh * 4), 0u};
      }
    }
    const u32x4 k1a = ld128(lds + L_K01 + lane * 16);
    const u32 k2a = *(LDSAS u32*)(lds + L_K01 + 1024 + lane * 4);
    u32x4 k1 = ld128(lds + L_K01 + par * 1280 + lane * 16);
    u32x2 k2 = {*(LDSAS u32*)(lds + L_K01 + par * 1280 + 1024 + lane * 4), hh ? 0u : 0x00003F80u};
    f32x16 S0, S1, S2, S3;
#pragma unroll
    for (int j = 0; j < 2; ++j) {
      f32x16 t = mfma32(k1a, q1[j], zero16);
      t = mfma32k8((u32x2){k2a, 0u}, q2[j], t);
      const float m = xhalf_max(max16(t));
      q2[j][1] = hh ? 0u : (pk2(-m, 0.f) & 0xffffu);
    }
    S0 = mfma32(k1, q1[0], zero16);
    S0 = mfma32k8(k2, q2[0], S0);
    u32x4 vlo, vhi;
    const u32 rb = lds0 + L_RING + (u32)par * REC;
    u32 aK1 = rb + (u32)lane * 16u, aK2 = rb + 1024u + (u32)lane * 4u;
    u32 aV = rb + 1280u + (u32)hh * 352u + (u32)(r32 <= 20 ? r32 : 21) * 16u;
    u32 aF = lds0 + L_FLG + (u32)w * 4u, tmp;
    HSTAMP(5); FSTAMP(5);
    u32 cnt, inc, slot, rdy;
    asm volatile(ATTN_LOOP_ASM
                 : "=&{v[0:15]}"(O0), "=&{v[16:31]}"(O1), "+{v[32:47]}"(S0), "=&{v[48:63]}"(S1), "=&{v[64:79]}"(S2), "=&{v[80:95]}"(S3),
                   "+{v[108:111]}"(k1), "+{v[112:113]}"(k2), "=&{v[116:119]}"(vlo), "=&{v[120:123]}"(vhi),
                   "+{v124}"(aK1), "+{v125}"(aK2), "+{v126}"(aV), "=&{v115}"(tmp), [cnt] "=&s"(cnt), [inc] "=&s"(inc), [slot] "=&s"(slot), [rdy] "=&s"(rdy)
                 : "{v[96:99]}"(q1[0]), "{v[100:103]}"(q1[1]), "{v[104:105]}"(q2[0]), "{v[106:107]}"(q2[1]), "{v114}"(0xDDDD), "{v127}"(aF),
                   [wrap] "s"(0u - 4u * SL), [stg] "s"(SL), [par] "s"(par)
                 : "memory", "scc");
  }
  __builtin_amdgcn_s_setprio(3);
  HSTAMP(6); TSTAMP(2); FSTAMP(6);
  const int lane = lane_id(), r32 = lane & 31, hh = lane >> 5;
  ldsp mg = lds + L_MRG + (u32)pr * (2 * REC);
  if (lds_arrive(lds + L_FLG + 64 + pr * 4) == 0) {
#pragma unroll 1
    for (u32 it = 0; it < SPIN_CAP; ++it) {
      if (__all(lds_ld(lds + L_FLG + (lane & 7) * 4) >= 13u)) break;
      __builtin_amdgcn_s_sleep(2);
    }
    asm volatile("" ::: "memory");
#pragma unroll
    for (int j = 0; j < 2; ++j) {
      const f32x16 snd = j ? O1 : O0;
      ldsp mw = mg + j * REC;
      *(LDSAS f32x4*)(mw + lane * 16) = (f32x4){snd[0], snd[1], snd[2], snd[3]};
      *(LDSAS f32x4*)(mw + 1024 + lane * 16) = (f32x4){snd[4], snd[5], snd[6], snd[7]};
      if (!hh) *(LDSAS f32x4*)(mw + 2048 + r32 * 16) = (f32x4){snd[8], snd[9], snd[10], snd[11]};
      else *(LDSAS float*)(mw + 2560 + r32 * 4) = snd[8];
    }
    asm volatile("s_waitcnt lgkmcnt(0)" ::: "memory");
    lds_st(lds + L_FLG + 80 + pr * 4, 1u);
    TSTAMP(3);
    STAMP_FLUSH();
    return;
  }
  lds_wait_ge<FSLEEP>(lds + L_FLG + 80 + pr * 4, 1u);
  TSTAMP(5);
  const u32x2 tag = ld64(lds + F_TAG);
#pragma unroll
  for (int j = 0; j < 2; ++j) {
    f32x16 O = j ? O1 : O0;
    ldsp mr = mg + j * REC;
    const f32x4 a = *(LDSAS f32x4*)(mr + lane * 16), b = *(LDSAS f32x4*)(mr + 1024 + lane * 16), c = *(LDSAS f32x4*)(mr + 2048 + r32 * 16);
    float l = from_hi(O[8]) + *(LDSAS float*)(mr + 2560 + r32 * 4);
    const float gt = *(LDSAS float*)(lds + L_GT + (32 * j + r32) * 4);
#pragma unroll
    for (int i = 0; i < 4; ++i) { O[i] += a[i]; O[4 + i] += b[i]; O[8 + i] += c[i]; }
    float t = 0.f;
#pragma unroll
    for (int r = 0; r < 12; ++r) t = __builtin_fmaf(O[r], 0.f, t);
    const bool bad = !(t == 0.f) || !(l < 1.0e30f);
    if (__builtin_expect(__any(bad), 0)) {
      O = attn_exact(rW, q1[j], q2[j][0], lane);
      l = from_hi(O[8]);
    }
    const float sb = (1.f - gt) * __builtin_amdgcn_rcpf(l);
    const u32 vo = WS_OFR + 2621440u + (u32)(4 * t64 + 2 * j + (r32 >> 4)) * 20480u + (r32 & 15) * 16 + (head >> 1) * 1280;
    st128(rW, vo + hh * 256 + (head & 1) * 512, (u32x4){pk2(O[0] * sb, O[1] * sb), pk2(O[2] * sb, O[3] * sb), pk2(O[4] * sb, O[5] * sb), pk2(O[6] * sb, O[7] * sb)});
    st64(rW, hh ? OOB : vo + 1024 + (head & 1) * 8, (u32x2){pk2(O[8] * sb, O[9] * sb), pk2(O[10] * sb, O[11] * sb)});
  }
  HSTAMP(7); TSTAMP(6);
  DRAIN();
  TSTAMP(7); FSTAMP(7);
  st64(rW, lane == 0 ? WS_FLGB + (u32)(t64 * 32 + head) * 8u : OOB, tag);
  lds_arrive(lds + F_FIN);
  STAMP_FLUSH();
}

extern "C" void kernel_launch(void* const* d_in, const int* in_sizes, int n_in, void* d_out, int out_size, void* d_ws,
                              size_t ws_size, hipStream_t stream) {
  (void)in_sizes; (void)n_in; (void)out_size; (void)ws_size;
  (void)hipFuncSetAttribute((const void*)gm_fused, hipFuncAttributeMaxDynamicSharedMemorySize, LDS_BYTES);
  gm_fused<<<256, 768, LDS_BYTES, stream>>>((const char*)d_in[0], (const char*)d_in[1], (const char*)d_in[2], (const char*)d_in[5], (const char*)d_in[6],
      (const char*)d_in[7], (const char*)d_in[9], (const char*)d_in[3], (const char*)d_in[8], (char*)d_ws, (char*)d_out);
}
```

```cpp
#include <hip/hip_runtime.h>
#include <hip/hip_bf16.h>
#include <stdint.h>

typedef unsigned int u32;
typedef unsigned long long u64;
typedef u32 u32x2 __attribute__((ext_vector_type(2)));
typedef u32 u32x4 __attribute__((ext_vector_type(4)));
typedef float f32x2 __attribute__((ext_vector_type(2)));
typedef float f32x4 __attribute__((ext_vector_type(4)));
typedef float f32x8 __attribute__((ext_vector_type(8)));
typedef float f32x16 __attribute__((ext_vector_type(16)));
typedef __bf16 bf16x2 __attribute__((ext_vector_type(2)));
typedef __bf16 bf16x8 __attribute__((ext_vector_type(8)));
typedef __bf16 bf16x16 __attribute__((ext_vector_type(16)));
typedef short s16x4 __attribute__((ext_vector_type(4)));
#define LDSAS __attribute__((address_space(3)))
typedef LDSAS char* ldsp;
typedef __attribute__((address_space(1))) u64 gu64;

constexpr int SEQ = 2048, DM = 640, NH = 32, DH = 20;
constexpr float LOG2E = 1.4426950408889634f;
constexpr float QSCALE = 1.4426950408889634f * 0.22360679774997896f;

constexpr u32 REC = 2688;
constexpr u32 WS_QB   = 0;
constexpr u32 WS_KVIM = WS_QB + 1310720;
constexpr u32 WS_KVP  = WS_KVIM + 64 * REC;
constexpr u32 WS_KVT  = WS_KVP + 32 * 16 * 3360;
constexpr u32 WS_GATE = WS_KVT + 32 * 1680;
constexpr u32 WS_OFR  = WS_GATE + 8192;
constexpr u32 WS_F1   = WS_OFR + 5242880;
constexpr u32 WS_F2   = WS_F1 + 16384;
constexpr u32 WS_F3   = WS_F2 + 1024;
constexpr u32 WS_FLGA = WS_F3 + 1024;
constexpr u32 WS_FLGB = WS_FLGA + 2048;
constexpr u32 WS_FQ   = WS_FLGB + 8192;
constexpr u32 WS_EPOCH = WS_FQ + 16384;
constexpr u32 WS_END  = WS_EPOCH + 256;
constexpr u32 OOB = 0x40000000u;
constexpr u32 SPIN_CAP = 1u << 20;

#define SBAR() __builtin_amdgcn_sched_barrier(0)
#define PIN(x) asm volatile("" : "+v"(x))
#define LBAR() asm volatile("s_waitcnt lgkmcnt(0)\n\ts_barrier" ::: "memory")
#define WAIT_BAR(N) asm volatile("s_waitcnt vmcnt(" #N ") lgkmcnt(0)\n\ts_barrier" ::: "memory")
#define DRAIN() asm volatile("s_waitcnt vmcnt(0)" ::: "memory")

__device__ __forceinline__ int lane_id() {
  int l;
  asm volatile("v_mbcnt_lo_u32_b32 %0, -1, 0\n\tv_mbcnt_hi_u32_b32 %0, -1, %0" : "=v"(l));
  return l;
}
__device__ __forceinline__ u32 pk2(float a, float b) {
  f32x2 v = {a, b};
  return __builtin_bit_cast(u32, __builtin_convertvector(v, bf16x2));
}
__device__ __forceinline__ f32x4 mfma16(u32x4 a, u32x4 b, f32x4 c) {
  return __builtin_amdgcn_mfma_f32_16x16x32_bf16(__builtin_bit_cast(bf16x8, a), __builtin_bit_cast(bf16x8, b), c, 0, 0, 0);
}
__device__ __forceinline__ f32x16 mfma32(u32x4 a, u32x4 b, f32x16 c) {
  return __builtin_amdgcn_mfma_f32_32x32x16_bf16(__builtin_bit_cast(bf16x8, a), __builtin_bit_cast(bf16x8, b), c, 0, 0, 0);
}
__device__ __forceinline__ f32x16 mfma32k8(u32x2 a, u32x2 b, f32x16 c) {
  return __builtin_amdgcn_mfma_f32_32x32x8bf16_1k(__builtin_bit_cast(s16x4, a), __builtin_bit_cast(s16x4, b), c, 0, 0, 0);
}
__device__ __forceinline__ f32x16 smf(u32x4 a, f32x8 b, f32x16 c, int idx) {
  return __builtin_amdgcn_smfmac_f32_32x32x32_bf16(__builtin_bit_cast(bf16x8, a), __builtin_bit_cast(bf16x16, b), c, idx, 0, 0);
}
__device__ __forceinline__ u32x2 trd(ldsp p) {
  return __builtin_bit_cast(u32x2, __builtin_amdgcn_ds_read_tr16_b64_v4i16((LDSAS s16x4*)p));
}
__device__ __forceinline__ u32x4 trd2(ldsp p0, ldsp p1) {
  u32x2 a = trd(p0), b = trd(p1);
  return (u32x4){a[0], a[1], b[0], b[1]};
}
__device__ __forceinline__ u32x4 ld128(ldsp p) { return *(LDSAS u32x4*)p; }
__device__ __forceinline__ u32x2 ld64(ldsp p) { return *(LDSAS u32x2*)p; }
__device__ __forceinline__ float bflo(u32 v) { return __uint_as_float(v << 16); }
__device__ __forceinline__ float bfhi(u32 v) { return __uint_as_float(v & 0xffff0000u); }

__device__ __forceinline__ u32x4 mkrsrc(const void* p, u32 bytes) {
  uint64_t a = (uint64_t)p;
  return (u32x4){(u32)__builtin_amdgcn_readfirstlane((u32)a), (u32)__builtin_amdgcn_readfirstlane((u32)(a >> 32) & 0xffffu), bytes, 0x00020000u};
}
__device__ __forceinline__ void dma1(u32x4 rs, u32 voff, u32 soff, u32 ldsaddr) {
  u32 keep;
  asm volatile("s_mov_b32 %0, m0\n\ts_mov_b32 m0, %4\n\ts_nop 2\n\tbuffer_load_dwordx4 %1, %2, %3 offen lds\n\ts_mov_b32 m0, %0"
               : "=&s"(keep) : "v"(voff), "s"(rs), "s"(__builtin_amdgcn_readfirstlane(soff)), "s"(__builtin_amdgcn_readfirstlane(ldsaddr)) : "memory");
}
__device__ __forceinline__ void dma1s(u32x4 rs, u32 voff, u32 soff, u32 ldsaddr) {
  u32 keep;
  asm volatile("s_mov_b32 %0, m0\n\ts_mov_b32 m0, %4\n\ts_nop 2\n\tbuffer_load_dwordx4 %1, %2, %3 offen sc1 lds\n\ts_mov_b32 m0, %0"
               : "=&s"(keep) : "v"(voff), "s"(rs), "s"(__builtin_amdgcn_readfirstlane(soff)), "s"(__builtin_amdgcn_readfirstlane(ldsaddr)) : "memory");
}
__device__ __forceinline__ void dma4(u32x4 rs, u32 voff, u32 soff, u32 ldsaddr) {
  u32 keep;
  asm volatile("s_mov_b32 %0, m0\n\ts_mov_b32 m0, %4\n\ts_nop 2\n\t"
               "buffer_load_dwordx4 %1, %2, %3 offen sc1 lds\n\tbuffer_load_dwordx4 %1, %2, %3 offen offset:1024 sc1 lds\n\t"
               "buffer_load_dwordx4 %1, %2, %3 offen offset:2048 sc1 lds\n\tbuffer_load_dwordx4 %1, %2, %3 offen offset:3072 sc1 lds\n\t"
               "s_mov_b32 m0, %0"
               : "=&s"(keep) : "v"(voff), "s"(rs), "s"(__builtin_amdgcn_readfirstlane(soff)), "s"(__builtin_amdgcn_readfirstlane(ldsaddr)) : "memory");
}
__device__ __forceinline__ void dma2h(u32x4 rs, u32 voff, u32 soff, u32 ldsaddr) {
  u32 keep;
  asm volatile("s_mov_b32 %0, m0\n\ts_mov_b32 m0, %4\n\ts_nop 2\n\t"
               "buffer_load_dwordx4 %1, %2, %3 offen sc1 lds\n\tbuffer_load_dwordx4 %1, %2, %3 offen offset:1024 sc1 lds\n\t"
               "s_mov_b32 exec_hi, 0\n\tbuffer_load_dwordx4 %1, %2, %3 offen offset:2048 sc1 lds\n\ts_mov_b32 exec_hi, -1\n\t"
               "s_mov_b32 m0, %0"
               : "=&s"(keep) : "v"(voff), "s"(rs), "s"(__builtin_amdgcn_readfirstlane(soff)), "s"(__builtin_amdgcn_readfirstlane(ldsaddr)) : "memory");
}
typedef __amdgpu_buffer_rsrc_t rsrc_t;
__device__ __forceinline__ void st128(rsrc_t r, u32 off, u32x4 v) { __builtin_amdgcn_raw_buffer_store_b128(v, r, (int)off, 0, 16); }
__device__ __forceinline__ void st64(rsrc_t r, u32 off, u32x2 v) { __builtin_amdgcn_raw_buffer_store_b64(v, r, (int)off, 0, 16); }
__device__ __forceinline__ void st32(rsrc_t r, u32 off, u32 v) { __builtin_amdgcn_raw_buffer_store_b32(v, r, (int)off, 0, 16); }
__device__ __forceinline__ void st16(rsrc_t r, u32 off, u32 v) { __builtin_amdgcn_raw_buffer_store_b16((unsigned short)v, r, (int)off, 0, 16); }
__device__ __forceinline__ u32x4 gl128(rsrc_t r, u32 voff, u32 soff) { return __builtin_amdgcn_raw_buffer_load_b128(r, (int)voff, (int)soff, 16); }
__device__ __forceinline__ u32x2 gl64(rsrc_t r, u32 voff, u32 soff) { return __builtin_amdgcn_raw_buffer_load_b64(r, (int)voff, (int)soff, 16); }
__device__ __forceinline__ u32 gl32(rsrc_t r, u32 voff, u32 soff) { return __builtin_amdgcn_raw_buffer_load_b32(r, (int)voff, (int)soff, 16); }

__device__ __forceinline__ f32x8 lo8(const f32x16& s) { return __builtin_shufflevector(s, s, 0, 1, 2, 3, 4, 5, 6, 7); }
__device__ __forceinline__ f32x8 hi8(const f32x16& s) { return __builtin_shufflevector(s, s, 8, 9, 10, 11, 12, 13, 14, 15); }
__device__ __forceinline__ float max16(const f32x16& s) {
  float a = fmaxf(fmaxf(s[0], s[1]), s[2]), b = fmaxf(fmaxf(s[3], s[4]), s[5]);
  a = fmaxf(fmaxf(a, s[6]), s[7]); b = fmaxf(fmaxf(b, s[8]), s[9]);
  a = fmaxf(fmaxf(a, s[10]), s[11]); b = fmaxf(fmaxf(b, s[12]), s[13]);
  a = fmaxf(fmaxf(a, s[14]), s[15]);
  return fmaxf(a, b);
}
__device__ __forceinline__ float xhalf_max(float m) {
  auto r = __builtin_amdgcn_permlane32_swap(__float_as_uint(m), __float_as_uint(m), false, false);
  return fmaxf(__uint_as_float(r[0]), __uint_as_float(r[1]));
}
__device__ __forceinline__ float from_hi(float v) {
  auto r = __builtin_amdgcn_permlane32_swap(__float_as_uint(v), __float_as_uint(v), false, false);
  return __uint_as_float(r[1]);
}
__device__ __forceinline__ u32 lds_ld(ldsp p) { return __hip_atomic_load((LDSAS u32*)p, __ATOMIC_RELAXED, __HIP_MEMORY_SCOPE_WORKGROUP); }
__device__ __forceinline__ void lds_st(ldsp p, u32 v) { __hip_atomic_store((LDSAS u32*)p, v, __ATOMIC_RELAXED, __HIP_MEMORY_SCOPE_WORKGROUP); }
template <int SLEEP>
__device__ __forceinline__ void lds_wait_ge(ldsp p, u32 want) {
#pragma unroll 1
  for (u32 it = 0; it < SPIN_CAP; ++it) {
    if ((u32)__builtin_amdgcn_readfirstlane(lds_ld(p)) >= want) break;
    __builtin_amdgcn_s_sleep(SLEEP);
  }
  asm volatile("" ::: "memory");
}
__device__ __forceinline__ u32 lds_arrive(ldsp p) {
  u32 old; u64 keep;
  asm volatile("v_mov_b32 %0, 1\n\ts_mov_b64 %1, exec\n\ts_mov_b64 exec, 1\n\ts_waitcnt lgkmcnt(0)\n\tds_add_rtn_u32 %0, %2, %0\n\t"
               "s_waitcnt lgkmcnt(0)\n\ts_mov_b64 exec, %1" : "=&v"(old), "=&s"(keep) : "v"((u32)(uintptr_t)p) : "memory");
  return (u32)__builtin_amdgcn_readfirstlane(old);
}
template <int SLEEP>
__device__ __forceinline__ void flags_wait(rsrc_t r, u32 off, int n, u32x2 tag) {
  const int lane = lane_id();
  const u32 vo = (u32)(lane < n ? lane : 0) * 8u;
#pragma unroll 1
  for (u32 it = 0; it < SPIN_CAP; ++it) {
    asm volatile("" ::: "memory");
    const u32x2 v = gl64(r, vo, off);
    if (__all(v[0] == tag[0] && v[1] == tag[1])) break;
    __builtin_amdgcn_s_sleep(SLEEP);
  }
  asm volatile("" ::: "memory");
}
template <int SLEEP>
__device__ __forceinline__ void flags_wait256(rsrc_t r, u32 off, u32x2 tag) {
  const u32 vo = (u32)lane_id() * 16u;
#pragma unroll 1
  for (u32 it = 0; it < SPIN_CAP; ++it) {
    asm volatile("" ::: "memory");
    const u32x4 a = gl128(r, vo, off), b = gl128(r, vo, off + 1024);
    const u32 d = (a[0] ^ tag[0]) | (a[1] ^ tag[1]) | (a[2] ^ tag[0]) | (a[3] ^ tag[1]) | (b[0] ^ tag[0]) | (b[1] ^ tag[1]) | (b[2] ^ tag[0]) | (b[3] ^ tag[1]);
    if (__all(d == 0u)) break;
    __builtin_amdgcn_s_sleep(SLEEP);
  }
  asm volatile("" ::: "memory");
}

#define STAMP(i) do {} while (0)
#define RSTAMP(i) do {} while (0)
#define STAMP_INIT()
#define STAMP_FLUSH()

#define ATTN_LOOP_ASM \
  "s_nop 11\n\t" \
  "v_mov_b64 v[0:1], 0\n\t" \
  "v_mov_b64 v[2:3], 0\n\t" \
  "v_mov_b64 v[4:5], 0\n\t" \
  "v_mov_b64 v[6:7], 0\n\t" \
  "v_mov_b64 v[8:9], 0\n\t" \
  "v_mov_b64 v[10:11], 0\n\t" \
  "v_mov_b64 v[12:13], 0\n\t" \
  "v_mov_b64 v[14:15], 0\n\t" \
  "v_mov_b64 v[16:17], 0\n\t" \
  "v_mov_b64 v[18:19], 0\n\t" \
  "v_mov_b64 v[20:21], 0\n\t" \
  "v_mov_b64 v[22:23], 0\n\t" \
  "v_mov_b64 v[24:25], 0\n\t" \
  "v_mov_b64 v[26:27], 0\n\t" \
  "v_mov_b64 v[28:29], 0\n\t" \
  "v_mov_b64 v[30:31], 0\n\t" \
  "v_mov_b64 v[80:81], 0\n\t" \
  "v_mov_b64 v[82:83], 0\n\t" \
  "v_mov_b64 v[84:85], 0\n\t" \
  "v_mov_b64 v[86:87], 0\n\t" \
  "v_mov_b64 v[88:89], 0\n\t" \
  "v_mov_b64 v[90:91], 0\n\t" \
  "v_mov_b64 v[92:93], 0\n\t" \
  "v_mov_b64 v[94:95], 0\n\t" \
  "v_mov_b64 v[116:117], 0\n\t" \
  "v_mov_b64 v[118:119], 0\n\t" \
  "v_mov_b64 v[120:121], 0\n\t" \
  "v_mov_b64 v[122:123], 0\n\t" \
  "s_mov_b32 %[cnt], 0\n\t" \
  "s_mov_b32 %[slot], 0\n\t" \
  "2:\n\t" \
  "s_mov_b32 %[inc], 0\n\t" \
  "3:\n\t" \
  "ds_read_b32 v115, v127 offset:32\n\t" \
  "s_add_u32 %[inc], %[inc], 1\n\t" \
  "s_waitcnt lgkmcnt(0)\n\t" \
  "v_readfirstlane_b32 %[rdy], v115\n\t" \
  "s_nop 3\n\t" \
  "s_cmp_gt_u32 %[rdy], %[cnt]\n\t" \
  "s_cbranch_scc1 6f\n\t" \
  "s_sleep 1\n\t" \
  "s_cmp_lt_u32 %[inc], 0x100000\n\t" \
  "s_cbranch_scc1 3b\n\t" \
  "s_branch 9f\n\t" \
  "6:\n\t" \
  "s_cmp_lg_u32 %[par], 0\n\t" \
  "s_cbranch_scc1 5f\n\t" \
  "4:\n\t" \
  "v_mfma_f32_32x32x16_bf16 v[48:63], v[108:111], v[100:103], 0\n\t" \
  "ds_read_b128 v[108:111], v124 offset:0\n\t" \
  "v_exp_f32 v32, v32\n\t" \
  "v_exp_f32 v33, v33\n\t" \
  "v_exp_f32 v34, v34\n\t" \
  "v_exp_f32 v35, v35\n\t" \
  "v_mfma_f32_32x32x8_bf16 v[48:63], v[112:113], v[106:107], v[48:63]\n\t" \
  "ds_read_b32 v112, v125 offset:0\n\t" \
  "v_exp_f32 v36, v36\n\t" \
  "v_exp_f32 v37, v37\n\t" \
  "v_exp_f32 v38, v38\n\t" \
  "v_exp_f32 v39, v39\n\t" \
  "v_smfmac_f32_32x32x32_bf16 v[16:31], v[116:119], v[80:87], v114\n\t" \
  "ds_read_b128 v[116:119], v126 offset:0\n\t" \
  "v_exp_f32 v40, v40\n\t" \
  "v_exp_f32 v41, v41\n\t" \
  "v_exp_f32 v42, v42\n\t" \
  "v_exp_f32 v43, v43\n\t" \
  "v_smfmac_f32_32x32x32_bf16 v[16:31], v[120:123], v[88:95], v114\n\t" \
  "ds_read_b128 v[120:123], v126 offset:704\n\t" \
  "v_exp_f32 v44, v44\n\t" \
  "v_exp_f32 v45, v45\n\t" \
  "v_exp_f32 v46, v46\n\t" \
  "v_exp_f32 v47, v47\n\t" \
  "s_waitcnt lgkmcnt(2)\n\t" \
  "v_mfma_f32_32x32x16_bf16 v[64:79], v[108:111], v[96:99], 0\n\t" \
  "v_exp_f32 v48, v48\n\t" \
  "v_exp_f32 v49, v49\n\t" \
  "v_exp_f32 v50, v50\n\t" \
  "v_exp_f32 v51, v51\n\t" \
  "v_mfma_f32_32x32x8_bf16 v[64:79], v[112:113], v[104:105], v[64:79]\n\t" \
  "v_exp_f32 v52, v52\n\t" \
  "v_exp_f32 v53, v53\n\t" \
  "v_exp_f32 v54, v54\n\t" \
  "v_exp_f32 v55, v55\n\t" \
  "s_waitcnt lgkmcnt(0)\n\t" \
  "v_smfmac_f32_32x32x32_bf16 v[0:15], v[116:119], v[32:39], v114\n\t" \
  "v_exp_f32 v56, v56\n\t" \
  "v_exp_f32 v57, v57\n\t" \
  "v_exp_f32 v58, v58\n\t" \
  "v_exp_f32 v59, v59\n\t" \
  "v_smfmac_f32_32x32x32_bf16 v[0:15], v[120:123], v[40:47], v114\n\t" \
  "v_exp_f32 v60, v60\n\t" \
  "v_exp_f32 v61, v61\n\t" \
  "v_exp_f32 v62, v62\n\t" \
  "v_exp_f32 v63, v63\n\t" \
  "v_mfma_f32_32x32x16_bf16 v[80:95], v[108:111], v[100:103], 0\n\t" \
  "ds_read_b128 v[108:111], v124 offset:5376\n\t" \
  "v_exp_f32 v64, v64\n\t" \
  "v_exp_f32 v65, v65\n\t" \
  "v_exp_f32 v66, v66\n\t" \
  "v_exp_f32 v67, v67\n\t" \
  "v_mfma_f32_32x32x8_bf16 v[80:95], v[112:113], v[106:107], v[80:95]\n\t" \
  "ds_read_b32 v112, v125 offset:5376\n\t" \
  "v_exp_f32 v68, v68\n\t" \
  "v_exp_f32 v69, v69\n\t" \
  "v_exp_f32 v70, v70\n\t" \
  "v_exp_f32 v71, v71\n\t" \
  "v_smfmac_f32_32x32x32_bf16 v[16:31], v[116:119], v[48:55], v114\n\t" \
  "ds_read_b128 v[116:119], v126 offset:5376\n\t" \
  "v_exp_f32 v72, v72\n\t" \
  "v_exp_f32 v73, v73\n\t" \
  "v_exp_f32 v74, v74\n\t" \
  "v_exp_f32 v75, v75\n\t" \
  "v_smfmac_f32_32x32x32_bf16 v[16:31], v[120:123], v[56:63], v114\n\t" \
  "ds_read_b128 v[120:123], v126 offset:6080\n\t" \
  "s_add_u32 %[cnt], %[cnt], 1\n\t" \
  "s_cmp_eq_u32 %[slot], 4\n\t" \
  "s_cselect_b32 %[inc], %[wrap], %[stg]\n\t" \
  "v_exp_f32 v76, v76\n\t" \
  "v_exp_f32 v77, v77\n\t" \
  "v_exp_f32 v78, v78\n\t" \
  "v_exp_f32 v79, v79\n\t" \
  "s_waitcnt lgkmcnt(2)\n\t" \
  "v_mfma_f32_32x32x16_bf16 v[32:47], v[108:111], v[96:99], 0\n\t" \
  "ds_read_b32 v115, v127 offset:32\n\t" \
  "s_cselect_b32 %[slot], -1, %[slot]\n\t" \
  "s_add_u32 %[slot], %[slot], 1\n\t" \
  "v_exp_f32 v80, v80\n\t" \
  "v_exp_f32 v81, v81\n\t" \
  "v_exp_f32 v82, v82\n\t" \
  "v_exp_f32 v83, v83\n\t" \
  "v_mfma_f32_32x32x8_bf16 v[32:47], v[112:113], v[104:105], v[32:47]\n\t" \
  "v_add_u32 v124, %[inc], v124\n\t" \
  "v_add_u32 v125, %[inc], v125\n\t" \
  "v_add_u32 v126, %[inc], v126\n\t" \
  "v_exp_f32 v84, v84\n\t" \
  "v_exp_f32 v85, v85\n\t" \
  "v_exp_f32 v86, v86\n\t" \
  "v_exp_f32 v87, v87\n\t" \
  "s_waitcnt lgkmcnt(1)\n\t" \
  "v_smfmac_f32_32x32x32_bf16 v[0:15], v[116:119], v[64:71], v114\n\t" \
  "v_exp_f32 v88, v88\n\t" \
  "v_exp_f32 v89, v89\n\t" \
  "v_exp_f32 v90, v90\n\t" \
  "v_exp_f32 v91, v91\n\t" \
  "s_waitcnt lgkmcnt(0)\n\t" \
  "v_readfirstlane_b32 %[rdy], v115\n\t" \
  "v_mov_b32 v115, %[cnt]\n\t" \
  "v_smfmac_f32_32x32x32_bf16 v[0:15], v[120:123], v[72:79], v114\n\t" \
  "ds_write_b32 v127, v115\n\t" \
  "s_cmp_gt_u32 %[rdy], %[cnt]\n\t" \
  "v_exp_f32 v92, v92\n\t" \
  "v_exp_f32 v93, v93\n\t" \
  "v_exp_f32 v94, v94\n\t" \
  "v_exp_f32 v95, v95\n\t" \
  "s_cbranch_scc1 4b\n\t" \
  "s_cmp_ge_u32 %[cnt], 16\n\t" \
  "s_cbranch_scc1 9f\n\t" \
  "s_branch 2b\n\t" \
  "5:\n\t" \
  "v_mfma_f32_32x32x16_bf16 v[48:63], v[108:111], v[100:103], 0\n\t" \
  "ds_read_b128 v[108:111], v124 offset:0\n\t" \
  "s_setprio 1\n\t" \
  "v_exp_f32 v32, v32\n\t" \
  "v_exp_f32 v33, v33\n\t" \
  "v_exp_f32 v34, v34\n\t" \
  "v_exp_f32 v35, v35\n\t" \
  "v_mfma_f32_32x32x8_bf16 v[48:63], v[112:113], v[106:107], v[48:63]\n\t" \
  "ds_read_b32 v112, v125 offset:0\n\t" \
  "v_exp_f32 v36, v36\n\t" \
  "v_exp_f32 v37, v37\n\t" \
  "v_exp_f32 v38, v38\n\t" \
  "v_exp_f32 v39, v39\n\t" \
  "v_smfmac_f32_32x32x32_bf16 v[16:31], v[116:119], v[80:87], v114\n\t" \
  "ds_read_b128 v[116:119], v126 offset:0\n\t" \
  "v_exp_f32 v40, v40\n\t" \
  "v_exp_f32 v41, v41\n\t" \
  "v_exp_f32 v42, v42\n\t" \
  "v_exp_f32 v43, v43\n\t" \
  "v_smfmac_f32_32x32x32_bf16 v[16:31], v[120:123], v[88:95], v114\n\t" \
  "ds_read_b128 v[120:123], v126 offset:704\n\t" \
  "v_exp_f32 v44, v44\n\t" \
  "v_exp_f32 v45, v45\n\t" \
  "v_exp_f32 v46, v46\n\t" \
  "v_exp_f32 v47, v47\n\t" \
  "s_waitcnt lgkmcnt(2)\n\t" \
  "v_mfma_f32_32x32x16_bf16 v[64:79], v[108:111], v[96:99], 0\n\t" \
  "v_exp_f32 v48, v48\n\t" \
  "v_exp_f32 v49, v49\n\t" \
  "v_exp_f32 v50, v50\n\t" \
  "v_exp_f32 v51, v51\n\t" \
  "v_mfma_f32_32x32x8_bf16 v[64:79], v[112:113], v[104:105], v[64:79]\n\t" \
  "v_exp_f32 v52, v52\n\t" \
  "v_exp_f32 v53, v53\n\t" \
  "v_exp_f32 v54, v54\n\t" \
  "v_exp_f32 v55, v55\n\t" \
  "s_waitcnt lgkmcnt(0)\n\t" \
  "v_smfmac_f32_32x32x32_bf16 v[0:15], v[116:119], v[32:39], v114\n\t" \
  "v_exp_f32 v56, v56\n\t" \
  "v_exp_f32 v57, v57\n\t" \
  "v_exp_f32 v58, v58\n\t" \
  "v_exp_f32 v59, v59\n\t" \
  "v_smfmac_f32_32x32x32_bf16 v[0:15], v[120:123], v[40:47], v114\n\t" \
  "v_exp_f32 v60, v60\n\t" \
  "v_exp_f32 v61, v61\n\t" \
  "v_exp_f32 v62, v62\n\t" \
  "v_exp_f32 v63, v63\n\t" \
  "v_mfma_f32_32x32x16_bf16 v[80:95], v[108:111], v[100:103], 0\n\t" \
  "ds_read_b128 v[108:111], v124 offset:5376\n\t" \
  "v_exp_f32 v64, v64\n\t" \
  "v_exp_f32 v65, v65\n\t" \
  "v_exp_f32 v66, v66\n\t" \
  "v_exp_f32 v67, v67\n\t" \
  "v_mfma_f32_32x32x8_bf16 v[80:95], v[112:113], v[106:107], v[80:95]\n\t" \
  "ds_read_b32 v112, v125 offset:5376\n\t" \
  "v_exp_f32 v68, v68\n\t" \
  "v_exp_f32 v69, v69\n\t" \
  "v_exp_f32 v70, v70\n\t" \
  "v_exp_f32 v71, v71\n\t" \
  "v_smfmac_f32_32x32x32_bf16 v[16:31], v[116:119], v[48:55], v114\n\t" \
  "ds_read_b128 v[116:119], v126 offset:5376\n\t" \
  "v_exp_f32 v72, v72\n\t" \
  "v_exp_f32 v73, v73\n\t" \
  "v_exp_f32 v74, v74\n\t" \
  "v_exp_f32 v75, v75\n\t" \
  "v_smfmac_f32_32x32x32_bf16 v[16:31], v[120:123], v[56:63], v114\n\t" \
  "ds_read_b128 v[120:123], v126 offset:6080\n\t" \
  "s_add_u32 %[cnt], %[cnt], 1\n\t" \
  "s_cmp_eq_u32 %[slot], 4\n\t" \
  "s_cselect_b32 %[inc], %[wrap], %[stg]\n\t" \
  "v_exp_f32 v76, v76\n\t" \
  "v_exp_f32 v77, v77\n\t" \
  "v_exp_f32 v78, v78\n\t" \
  "v_exp_f32 v79, v79\n\t" \
  "s_waitcnt lgkmcnt(2)\n\t" \
  "v_mfma_f32_32x32x16_bf16 v[32:47], v[108:111], v[96:99], 0\n\t" \
  "ds_read_b32 v115, v127 offset:32\n\t" \
  "s_cselect_b32 %[slot], -1, %[slot]\n\t" \
  "s_add_u32 %[slot], %[slot], 1\n\t" \
  "v_exp_f32 v80, v80\n\t" \
  "v_exp_f32 v81, v81\n\t" \
  "v_exp_f32 v82, v82\n\t" \
  "v_exp_f32 v83, v83\n\t" \
  "v_mfma_f32_32x32x8_bf16 v[32:47], v[112:113], v[104:105], v[32:47]\n\t" \
  "v_add_u32 v124, %[inc], v124\n\t" \
  "v_add_u32 v125, %[inc], v125\n\t" \
  "v_add_u32 v126, %[inc], v126\n\t" \
  "s_setprio 0\n\t" \
  "v_exp_f32 v84, v84\n\t" \
  "v_exp_f32 v85, v85\n\t" \
  "v_exp_f32 v86, v86\n\t" \
  "v_exp_f32 v87, v87\n\t" \
  "s_waitcnt lgkmcnt(1)\n\t" \
  "v_smfmac_f32_32x32x32_bf16 v[0:15], v[116:119], v[64:71], v114\n\t" \
  "v_exp_f32 v88, v88\n\t" \
  "v_exp_f32 v89, v89\n\t" \
  "v_exp_f32 v90, v90\n\t" \
  "v_exp_f32 v91, v91\n\t" \
  "s_waitcnt lgkmcnt(0)\n\t" \
  "v_readfirstlane_b32 %[rdy], v115\n\t" \
  "v_mov_b32 v115, %[cnt]\n\t" \
  "v_smfmac_f32_32x32x32_bf16 v[0:15], v[120:123], v[72:79], v114\n\t" \
  "ds_write_b32 v127, v115\n\t" \
  "s_cmp_gt_u32 %[rdy], %[cnt]\n\t" \
  "v_exp_f32 v92, v92\n\t" \
  "v_exp_f32 v93, v93\n\t" \
  "v_exp_f32 v94, v94\n\t" \
  "v_exp_f32 v95, v95\n\t" \
  "s_cbranch_scc1 5b\n\t" \
  "s_cmp_ge_u32 %[cnt], 16\n\t" \
  "s_cbranch_scc1 9f\n\t" \
  "s_branch 2b\n\t" \
  "9:\n\t" \
  "s_nop 1\n\t" \
  "v_smfmac_f32_32x32x32_bf16 v[16:31], v[116:119], v[80:87], v114\n\t" \
  "v_smfmac_f32_32x32x32_bf16 v[16:31], v[120:123], v[88:95], v114\n\t" \
  "s_waitcnt lgkmcnt(0)\n\t" \
  "s_nop 15\n\t"
#define TILE_GEMM_ASM \
  "s_nop 4\n\t" \
  "buffer_load_dwordx4 v[60:63], v140, %[rs], %[s0] offen offset:0 sc1\n\t" \
  "buffer_load_dwordx4 v[64:67], v140, %[rs], %[s1] offen offset:0 sc1\n\t" \
  "buffer_load_dwordx4 v[68:71], v140, %[rs], %[s0] offen offset:1024 sc1\n\t" \
  "buffer_load_dwordx4 v[72:75], v140, %[rs], %[s1] offen offset:1024 sc1\n\t" \
  "buffer_load_dwordx4 v[76:79], v140, %[rs], %[s0] offen offset:2048 sc1\n\t" \
  "buffer_load_dwordx4 v[80:83], v140, %[rs], %[s1] offen offset:2048 sc1\n\t" \
  "buffer_load_dwordx4 v[84:87], v140, %[rs], %[s0] offen offset:3072 sc1\n\t" \
  "buffer_load_dwordx4 v[88:91], v140, %[rs], %[s1] offen offset:3072 sc1\n\t" \
  "s_add_u32 %[s0], %[s0], 4096\n\t" \
  "buffer_load_dwordx4 v[92:95], v140, %[rs], %[s0] offen offset:0 sc1\n\t" \
  "s_add_u32 %[s1], %[s1], 4096\n\t" \
  "buffer_load_dwordx4 v[96:99], v140, %[rs], %[s1] offen offset:0 sc1\n\t" \
  "buffer_load_dwordx4 v[100:103], v140, %[rs], %[s0] offen offset:1024 sc1\n\t" \
  "buffer_load_dwordx4 v[104:107], v140, %[rs], %[s1] offen offset:1024 sc1\n\t" \
  "buffer_load_dwordx4 v[108:111], v140, %[rs], %[s0] offen offset:2048 sc1\n\t" \
  "buffer_load_dwordx4 v[112:115], v140, %[rs], %[s1] offen offset:2048 sc1\n\t" \
  "buffer_load_dwordx4 v[116:119], v140, %[rs], %[s0] offen offset:3072 sc1\n\t" \
  "buffer_load_dwordx4 v[120:123], v140, %[rs], %[s1] offen offset:3072 sc1\n\t" \
  "s_add_u32 %[s0], %[s0], 4096\n\t" \
  "buffer_load_dwordx4 v[124:127], v140, %[rs], %[s0] offen offset:0 sc1\n\t" \
  "s_add_u32 %[s1], %[s1], 4096\n\t" \
  "buffer_load_dwordx4 v[128:131], v140, %[rs], %[s1] offen offset:0 sc1\n\t" \
  "buffer_load_dwordx4 v[132:135], v140, %[rs], %[s0] offen offset:1024 sc1\n\t" \
  "buffer_load_dwordx4 v[136:139], v140, %[rs], %[s1] offen offset:1024 sc1\n\t" \
  "s_mov_b32 %[it], 0\n\t" \
  "1:\n\t" \
  "ds_read_b32 v143, v142\n\t" \
  "s_add_u32 %[it], %[it], 1\n\t" \
  "s_waitcnt lgkmcnt(0)\n\t" \
  "v_cmp_gt_u32 vcc, %[want], v143\n\t" \
  "s_cbranch_vccz 2f\n\t" \
  "s_sleep 4\n\t" \
  "s_cmp_lt_u32 %[it], 0x100000\n\t" \
  "s_cbranch_scc1 1b\n\t" \
  "2:\n\t" \
  "ds_read_b64_tr_b16 v[40:41], v141 offset:0\n\t" \
  "ds_read_b64_tr_b16 v[42:43], v141 offset:2560\n\t" \
  "ds_read_b64_tr_b16 v[44:45], v141 offset:8\n\t" \
  "ds_read_b64_tr_b16 v[46:47], v141 offset:2568\n\t" \
  "ds_read_b64_tr_b16 v[48:49], v141 offset:16\n\t" \
  "ds_read_b64_tr_b16 v[50:51], v141 offset:2576\n\t" \
  "ds_read_b64_tr_b16 v[52:53], v141 offset:24\n\t" \
  "ds_read_b64_tr_b16 v[54:55], v141 offset:2584\n\t" \
  "ds_read_b64_tr_b16 v[56:57], v141 offset:32\n\t" \
  "ds_read_b64_tr_b16 v[58:59], v141 offset:2592\n\t" \
  "v_mov_b32 v143, 1\n\t" \
  "v_mov_b32 v142, %[afree]\n\t" \
  "s_waitcnt vmcnt(18)\n\t" \
  "s_waitcnt lgkmcnt(8)\n\t" \
  "v_mfma_f32_16x16x32_bf16 v[0:3], v[40:43], v[60:63], v[0:3]\n\t" \
  "v_mfma_f32_16x16x32_bf16 v[4:7], v[40:43], v[64:67], v[4:7]\n\t" \
  "ds_read_b64_tr_b16 v[40:41], v141 offset:5120\n\t" \
  "ds_read_b64_tr_b16 v[42:43], v141 offset:7680\n\t" \
  "s_waitcnt lgkmcnt(8)\n\t" \
  "v_mfma_f32_16x16x32_bf16 v[8:11], v[44:47], v[60:63], v[8:11]\n\t" \
  "v_mfma_f32_16x16x32_bf16 v[12:15], v[44:47], v[64:67], v[12:15]\n\t" \
  "ds_read_b64_tr_b16 v[44:45], v141 offset:5128\n\t" \
  "ds_read_b64_tr_b16 v[46:47], v141 offset:7688\n\t" \
  "s_waitcnt lgkmcnt(8)\n\t" \
  "v_mfma_f32_16x16x32_bf16 v[16:19], v[48:51], v[60:63], v[16:19]\n\t" \
  "v_mfma_f32_16x16x32_bf16 v[20:23], v[48:51], v[64:67], v[20:23]\n\t" \
  "ds_read_b64_tr_b16 v[48:49], v141 offset:5136\n\t" \
  "ds_read_b64_tr_b16 v[50:51], v141 offset:7696\n\t" \
  "s_waitcnt lgkmcnt(8)\n\t" \
  "v_mfma_f32_16x16x32_bf16 v[24:27], v[52:55], v[60:63], v[24:27]\n\t" \
  "v_mfma_f32_16x16x32_bf16 v[28:31], v[52:55], v[64:67], v[28:31]\n\t" \
  "ds_read_b64_tr_b16 v[52:53], v141 offset:5144\n\t" \
  "ds_read_b64_tr_b16 v[54:55], v141 offset:7704\n\t" \
  "s_waitcnt lgkmcnt(8)\n\t" \
  "v_mfma_f32_16x16x32_bf16 v[32:35], v[56:59], v[60:63], v[32:35]\n\t" \
  "v_mfma_f32_16x16x32_bf16 v[36:39], v[56:59], v[64:67], v[36:39]\n\t" \
  "ds_read_b64_tr_b16 v[56:57], v141 offset:5152\n\t" \
  "ds_read_b64_tr_b16 v[58:59], v141 offset:7712\n\t" \
  "s_waitcnt vmcnt(16)\n\t" \
  "s_waitcnt lgkmcnt(8)\n\t" \
  "v_mfma_f32_16x16x32_bf16 v[0:3], v[40:43], v[68:71], v[0:3]\n\t" \
  "v_mfma_f32_16x16x32_bf16 v[4:7], v[40:43], v[72:75], v[4:7]\n\t" \
  "ds_read_b64_tr_b16 v[40:41], v141 offset:10240\n\t" \
  "ds_read_b64_tr_b16 v[42:43], v141 offset:12800\n\t" \
  "s_waitcnt lgkmcnt(8)\n\t" \
  "v_mfma_f32_16x16x32_bf16 v[8:11], v[44:47], v[68:71], v[8:11]\n\t" \
  "v_mfma_f32_16x16x32_bf16 v[12:15], v[44:47], v[72:75], v[12:15]\n\t" \
  "ds_read_b64_tr_b16 v[44:45], v141 offset:10248\n\t" \
  "ds_read_b64_tr_b16 v[46:47], v141 offset:12808\n\t" \
  "s_waitcnt lgkmcnt(8)\n\t" \
  "v_mfma_f32_16x16x32_bf16 v[16:19], v[48:51], v[68:71], v[16:19]\n\t" \
  "v_mfma_f32_16x16x32_bf16 v[20:23], v[48:51], v[72:75], v[20:23]\n\t" \
  "ds_read_b64_tr_b16 v[48:49], v141 offset:10256\n\t" \
  "ds_read_b64_tr_b16 v[50:51], v141 offset:12816\n\t" \
  "s_waitcnt lgkmcnt(8)\n\t" \
  "v_mfma_f32_16x16x32_bf16 v[24:27], v[52:55], v[68:71], v[24:27]\n\t" \
  "v_mfma_f32_16x16x32_bf16 v[28:31], v[52:55], v[72:75], v[28:31]\n\t" \
  "ds_read_b64_tr_b16 v[52:53], v141 offset:10264\n\t" \
  "ds_read_b64_tr_b16 v[54:55], v141 offset:12824\n\t" \
  "s_waitcnt lgkmcnt(8)\n\t" \
  "v_mfma_f32_16x16x32_bf16 v[32:35], v[56:59], v[68:71], v[32:35]\n\t" \
  "v_mfma_f32_16x16x32_bf16 v[36:39], v[56:59], v[72:75], v[36:39]\n\t" \
  "ds_read_b64_tr_b16 v[56:57], v141 offset:10272\n\t" \
  "ds_read_b64_tr_b16 v[58:59], v141 offset:12832\n\t" \
  "s_waitcnt vmcnt(14)\n\t" \
  "s_waitcnt lgkmcnt(8)\n\t" \
  "v_mfma_f32_16x16x32_bf16 v[0:3], v[40:43], v[76:79], v[0:3]\n\t" \
  "v_mfma_f32_16x16x32_bf16 v[4:7], v[40:43], v[80:83], v[4:7]\n\t" \
  "ds_read_b64_tr_b16 v[40:41], v141 offset:15360\n\t" \
  "ds_read_b64_tr_b16 v[42:43], v141 offset:17920\n\t" \
  "s_waitcnt lgkmcnt(8)\n\t" \
  "v_mfma_f32_16x16x32_bf16 v[8:11], v[44:47], v[76:79], v[8:11]\n\t" \
  "v_mfma_f32_16x16x32_bf16 v[12:15], v[44:47], v[80:83], v[12:15]\n\t" \
  "ds_read_b64_tr_b16 v[44:45], v141 offset:15368\n\t" \
  "ds_read_b64_tr_b16 v[46:47], v141 offset:17928\n\t" \
  "s_waitcnt lgkmcnt(8)\n\t" \
  "v_mfma_f32_16x16x32_bf16 v[16:19], v[48:51], v[76:79], v[16:19]\n\t" \
  "v_mfma_f32_16x16x32_bf16 v[20:23], v[48:51], v[80:83], v[20:23]\n\t" \
  "ds_read_b64_tr_b16 v[48:49], v141 offset:15376\n\t" \
  "ds_read_b64_tr_b16 v[50:51], v141 offset:17936\n\t" \
  "s_waitcnt lgkmcnt(8)\n\t" \
  "v_mfma_f32_16x16x32_bf16 v[24:27], v[52:55], v[76:79], v[24:27]\n\t" \
  "v_mfma_f32_16x16x32_bf16 v[28:31], v[52:55], v[80:83], v[28:31]\n\t" \
  "ds_read_b64_tr_b16 v[52:53], v141 offset:15384\n\t" \
  "ds_read_b64_tr_b16 v[54:55], v141 offset:17944\n\t" \
  "s_waitcnt lgkmcnt(8)\n\t" \
  "v_mfma_f32_16x16x32_bf16 v[32:35], v[56:59], v[76:79], v[32:35]\n\t" \
  "v_mfma_f32_16x16x32_bf16 v[36:39], v[56:59], v[80:83], v[36:39]\n\t" \
  "ds_read_b64_tr_b16 v[56:57], v141 offset:15392\n\t" \
  "ds_read_b64_tr_b16 v[58:59], v141 offset:17952\n\t" \
  "s_waitcnt vmcnt(12)\n\t" \
  "s_waitcnt lgkmcnt(8)\n\t" \
  "v_mfma_f32_16x16x32_bf16 v[0:3], v[40:43], v[84:87], v[0:3]\n\t" \
  "v_mfma_f32_16x16x32_bf16 v[4:7], v[40:43], v[88:91], v[4:7]\n\t" \
  "ds_read_b64_tr_b16 v[40:41], v141 offset:20480\n\t" \
  "ds_read_b64_tr_b16 v[42:43], v141 offset:23040\n\t" \
  "s_waitcnt lgkmcnt(8)\n\t" \
  "v_mfma_f32_16x16x32_bf16 v[8:11], v[44:47], v[84:87], v[8:11]\n\t" \
  "v_mfma_f32_16x16x32_bf16 v[12:15], v[44:47], v[88:91], v[12:15]\n\t" \
  "ds_read_b64_tr_b16 v[44:45], v141 offset:20488\n\t" \
  "ds_read_b64_tr_b16 v[46:47], v141 offset:23048\n\t" \
  "s_waitcnt lgkmcnt(8)\n\t" \
  "v_mfma_f32_16x16x32_bf16 v[16:19], v[48:51], v[84:87], v[16:19]\n\t" \
  "v_mfma_f32_16x16x32_bf16 v[20:23], v[48:51], v[88:91], v[20:23]\n\t" \
  "ds_read_b64_tr_b16 v[48:49], v141 offset:20496\n\t" \
  "ds_read_b64_tr_b16 v[50:51], v141 offset:23056\n\t" \
  "s_waitcnt lgkmcnt(8)\n\t" \
  "v_mfma_f32_16x16x32_bf16 v[24:27], v[52:55], v[84:87], v[24:27]\n\t" \
  "v_mfma_f32_16x16x32_bf16 v[28:31], v[52:55], v[88:91], v[28:31]\n\t" \
  "ds_read_b64_tr_b16 v[52:53], v141 offset:20504\n\t" \
  "ds_read_b64_tr_b16 v[54:55], v141 offset:23064\n\t" \
  "s_waitcnt lgkmcnt(8)\n\t" \
  "v_mfma_f32_16x16x32_bf16 v[32:35], v[56:59], v[84:87], v[32:35]\n\t" \
  "v_mfma_f32_16x16x32_bf16 v[36:39], v[56:59], v[88:91], v[36:39]\n\t" \
  "ds_read_b64_tr_b16 v[56:57], v141 offset:20512\n\t" \
  "ds_read_b64_tr_b16 v[58:59], v141 offset:23072\n\t" \
  "s_waitcnt vmcnt(10)\n\t" \
  "s_waitcnt lgkmcnt(8)\n\t" \
  "v_mfma_f32_16x16x32_bf16 v[0:3], v[40:43], v[92:95], v[0:3]\n\t" \
  "v_mfma_f32_16x16x32_bf16 v[4:7], v[40:43], v[96:99], v[4:7]\n\t" \
  "ds_read_b64_tr_b16 v[40:41], v141 offset:25600\n\t" \
  "ds_read_b64_tr_b16 v[42:43], v141 offset:28160\n\t" \
  "s_waitcnt lgkmcnt(8)\n\t" \
  "v_mfma_f32_16x16x32_bf16 v[8:11], v[44:47], v[92:95], v[8:11]\n\t" \
  "v_mfma_f32_16x16x32_bf16 v[12:15], v[44:47], v[96:99], v[12:15]\n\t" \
  "ds_read_b64_tr_b16 v[44:45], v141 offset:25608\n\t" \
  "ds_read_b64_tr_b16 v[46:47], v141 offset:28168\n\t" \
  "s_waitcnt lgkmcnt(8)\n\t" \
  "v_mfma_f32_16x16x32_bf16 v[16:19], v[48:51], v[92:95], v[16:19]\n\t" \
  "v_mfma_f32_16x16x32_bf16 v[20:23], v[48:51], v[96:99], v[20:23]\n\t" \
  "ds_read_b64_tr_b16 v[48:49], v141 offset:25616\n\t" \
  "ds_read_b64_tr_b16 v[50:51], v141 offset:28176\n\t" \
  "s_waitcnt lgkmcnt(8)\n\t" \
  "v_mfma_f32_16x16x32_bf16 v[24:27], v[52:55], v[92:95], v[24:27]\n\t" \
  "v_mfma_f32_16x16x32_bf16 v[28:31], v[52:55], v[96:99], v[28:31]\n\t" \
  "ds_read_b64_tr_b16 v[52:53], v141 offset:25624\n\t" \
  "ds_read_b64_tr_b16 v[54:55], v141 offset:28184\n\t" \
  "s_waitcnt lgkmcnt(8)\n\t" \
  "v_mfma_f32_16x16x32_bf16 v[32:35], v[56:59], v[92:95], v[32:35]\n\t" \
  "v_mfma_f32_16x16x32_bf16 v[36:39], v[56:59], v[96:99], v[36:39]\n\t" \
  "ds_read_b64_tr_b16 v[56:57], v141 offset:25632\n\t" \
  "ds_read_b64_tr_b16 v[58:59], v141 offset:28192\n\t" \
  "s_waitcnt vmcnt(8)\n\t" \
  "s_waitcnt lgkmcnt(8)\n\t" \
  "v_mfma_f32_16x16x32_bf16 v[0:3], v[40:43], v[100:103], v[0:3]\n\t" \
  "v_mfma_f32_16x16x32_bf16 v[4:7], v[40:43], v[104:107], v[4:7]\n\t" \
  "ds_read_b64_tr_b16 v[40:41], v141 offset:30720\n\t" \
  "ds_read_b64_tr_b16 v[42:43], v141 offset:33280\n\t" \
  "s_waitcnt lgkmcnt(8)\n\t" \
  "v_mfma_f32_16x16x32_bf16 v[8:11], v[44:47], v[100:103], v[8:11]\n\t" \
  "v_mfma_f32_16x16x32_bf16 v[12:15], v[44:47], v[104:107], v[12:15]\n\t" \
  "ds_read_b64_tr_b16 v[44:45], v141 offset:30728\n\t" \
  "ds_read_b64_tr_b16 v[46:47], v141 offset:33288\n\t" \
  "s_waitcnt lgkmcnt(8)\n\t" \
  "v_mfma_f32_16x16x32_bf16 v[16:19], v[48:51], v[100:103], v[16:19]\n\t" \
  "v_mfma_f32_16x16x32_bf16 v[20:23], v[48:51], v[104:107], v[20:23]\n\t" \
  "ds_read_b64_tr_b16 v[48:49], v141 offset:30736\n\t" \
  "ds_read_b64_tr_b16 v[50:51], v141 offset:33296\n\t" \
  "s_waitcnt lgkmcnt(8)\n\t" \
  "v_mfma_f32_16x16x32_bf16 v[24:27], v[52:55], v[100:103], v[24:27]\n\t" \
  "v_mfma_f32_16x16x32_bf16 v[28:31], v[52:55], v[104:107], v[28:31]\n\t" \
  "ds_read_b64_tr_b16 v[52:53], v141 offset:30744\n\t" \
  "ds_read_b64_tr_b16 v[54:55], v141 offset:33304\n\t" \
  "s_waitcnt lgkmcnt(8)\n\t" \
  "v_mfma_f32_16x16x32_bf16 v[32:35], v[56:59], v[100:103], v[32:35]\n\t" \
  "v_mfma_f32_16x16x32_bf16 v[36:39], v[56:59], v[104:107], v[36:39]\n\t" \
  "ds_read_b64_tr_b16 v[56:57], v141 offset:30752\n\t" \
  "ds_read_b64_tr_b16 v[58:59], v141 offset:33312\n\t" \
  "s_waitcnt vmcnt(6)\n\t" \
  "s_waitcnt lgkmcnt(8)\n\t" \
  "v_mfma_f32_16x16x32_bf16 v[0:3], v[40:43], v[108:111], v[0:3]\n\t" \
  "v_mfma_f32_16x16x32_bf16 v[4:7], v[40:43], v[112:115], v[4:7]\n\t" \
  "ds_read_b64_tr_b16 v[40:41], v141 offset:35840\n\t" \
  "ds_read_b64_tr_b16 v[42:43], v141 offset:38400\n\t" \
  "s_waitcnt lgkmcnt(8)\n\t" \
  "v_mfma_f32_16x16x32_bf16 v[8:11], v[44:47], v[108:111], v[8:11]\n\t" \
  "v_mfma_f32_16x16x32_bf16 v[12:15], v[44:47], v[112:115], v[12:15]\n\t" \
  "ds_read_b64_tr_b16 v[44:45], v141 offset:35848\n\t" \
  "ds_read_b64_tr_b16 v[46:47], v141 offset:38408\n\t" \
  "s_waitcnt lgkmcnt(8)\n\t" \
  "v_mfma_f32_16x16x32_bf16 v[16:19], v[48:51], v[108:111], v[16:19]\n\t" \
  "v_mfma_f32_16x16x32_bf16 v[20:23], v[48:51], v[112:115], v[20:23]\n\t" \
  "ds_read_b64_tr_b16 v[48:49], v141 offset:35856\n\t" \
  "ds_read_b64_tr_b16 v[50:51], v141 offset:38416\n\t" \
  "s_waitcnt lgkmcnt(8)\n\t" \
  "v_mfma_f32_16x16x32_bf16 v[24:27], v[52:55], v[108:111], v[24:27]\n\t" \
  "v_mfma_f32_16x16x32_bf16 v[28:31], v[52:55], v[112:115], v[28:31]\n\t" \
  "ds_read_b64_tr_b16 v[52:53], v141 offset:35864\n\t" \
  "ds_read_b64_tr_b16 v[54:55], v141 offset:38424\n\t" \
  "s_waitcnt lgkmcnt(8)\n\t" \
  "v_mfma_f32_16x16x32_bf16 v[32:35], v[56:59], v[108:111], v[32:35]\n\t" \
  "v_mfma_f32_16x16x32_bf16 v[36:39], v[56:59], v[112:115], v[36:39]\n\t" \
  "ds_read_b64_tr_b16 v[56:57], v141 offset:35872\n\t" \
  "ds_read_b64_tr_b16 v[58:59], v141 offset:38432\n\t" \
  "s_waitcnt vmcnt(4)\n\t" \
  "s_waitcnt lgkmcnt(8)\n\t" \
  "v_mfma_f32_16x16x32_bf16 v[0:3], v[40:43], v[116:119], v[0:3]\n\t" \
  "v_mfma_f32_16x16x32_bf16 v[4:7], v[40:43], v[120:123], v[4:7]\n\t" \
  "ds_read_b64_tr_b16 v[40:41], v141 offset:40960\n\t" \
  "ds_read_b64_tr_b16 v[42:43], v141 offset:43520\n\t" \
  "s_waitcnt lgkmcnt(8)\n\t" \
  "v_mfma_f32_16x16x32_bf16 v[8:11], v[44:47], v[116:119], v[8:11]\n\t" \
  "v_mfma_f32_16x16x32_bf16 v[12:15], v[44:47], v[120:123], v[12:15]\n\t" \
  "ds_read_b64_tr_b16 v[44:45], v141 offset:40968\n\t" \
  "ds_read_b64_tr_b16 v[46:47], v141 offset:43528\n\t" \
  "s_waitcnt lgkmcnt(8)\n\t" \
  "v_mfma_f32_16x16x32_bf16 v[16:19], v[48:51], v[116:119], v[16:19]\n\t" \
  "v_mfma_f32_16x16x32_bf16 v[20:23], v[48:51], v[120:123], v[20:23]\n\t" \
  "ds_read_b64_tr_b16 v[48:49], v141 offset:40976\n\t" \
  "ds_read_b64_tr_b16 v[50:51], v141 offset:43536\n\t" \
  "s_waitcnt lgkmcnt(8)\n\t" \
  "v_mfma_f32_16x16x32_bf16 v[24:27], v[52:55], v[116:119], v[24:27]\n\t" \
  "v_mfma_f32_16x16x32_bf16 v[28:31], v[52:55], v[120:123], v[28:31]\n\t" \
  "ds_read_b64_tr_b16 v[52:53], v141 offset:40984\n\t" \
  "ds_read_b64_tr_b16 v[54:55], v141 offset:43544\n\t" \
  "s_waitcnt lgkmcnt(8)\n\t" \
  "v_mfma_f32_16x16x32_bf16 v[32:35], v[56:59], v[116:119], v[32:35]\n\t" \
  "v_mfma_f32_16x16x32_bf16 v[36:39], v[56:59], v[120:123], v[36:39]\n\t" \
  "ds_read_b64_tr_b16 v[56:57], v141 offset:40992\n\t" \
  "ds_read_b64_tr_b16 v[58:59], v141 offset:43552\n\t" \
  "s_waitcnt vmcnt(2)\n\t" \
  "s_waitcnt lgkmcnt(8)\n\t" \
  "v_mfma_f32_16x16x32_bf16 v[0:3], v[40:43], v[124:127], v[0:3]\n\t" \
  "v_mfma_f32_16x16x32_bf16 v[4:7], v[40:43], v[128:131], v[4:7]\n\t" \
  "ds_read_b64_tr_b16 v[40:41], v141 offset:46080\n\t" \
  "ds_read_b64_tr_b16 v[42:43], v141 offset:48640\n\t" \
  "s_waitcnt lgkmcnt(8)\n\t" \
  "v_mfma_f32_16x16x32_bf16 v[8:11], v[44:47], v[124:127], v[8:11]\n\t" \
  "v_mfma_f32_16x16x32_bf16 v[12:15], v[44:47], v[128:131], v[12:15]\n\t" \
  "ds_read_b64_tr_b16 v[44:45], v141 offset:46088\n\t" \
  "ds_read_b64_tr_b16 v[46:47], v141 offset:48648\n\t" \
  "s_waitcnt lgkmcnt(8)\n\t" \
  "v_mfma_f32_16x16x32_bf16 v[16:19], v[48:51], v[124:127], v[16:19]\n\t" \
  "v_mfma_f32_16x16x32_bf16 v[20:23], v[48:51], v[128:131], v[20:23]\n\t" \
  "ds_read_b64_tr_b16 v[48:49], v141 offset:46096\n\t" \
  "ds_read_b64_tr_b16 v[50:51], v141 offset:48656\n\t" \
  "s_waitcnt lgkmcnt(8)\n\t" \
  "v_mfma_f32_16x16x32_bf16 v[24:27], v[52:55], v[124:127], v[24:27]\n\t" \
  "v_mfma_f32_16x16x32_bf16 v[28:31], v[52:55], v[128:131], v[28:31]\n\t" \
  "ds_read_b64_tr_b16 v[52:53], v141 offset:46104\n\t" \
  "ds_read_b64_tr_b16 v[54:55], v141 offset:48664\n\t" \
  "s_waitcnt lgkmcnt(8)\n\t" \
  "v_mfma_f32_16x16x32_bf16 v[32:35], v[56:59], v[124:127], v[32:35]\n\t" \
  "v_mfma_f32_16x16x32_bf16 v[36:39], v[56:59], v[128:131], v[36:39]\n\t" \
  "ds_read_b64_tr_b16 v[56:57], v141 offset:46112\n\t" \
  "ds_read_b64_tr_b16 v[58:59], v141 offset:48672\n\t" \
  "s_waitcnt vmcnt(0)\n\t" \
  "s_waitcnt lgkmcnt(8)\n\t" \
  "v_mfma_f32_16x16x32_bf16 v[0:3], v[40:43], v[132:135], v[0:3]\n\t" \
  "v_mfma_f32_16x16x32_bf16 v[4:7], v[40:43], v[136:139], v[4:7]\n\t" \
  "s_waitcnt lgkmcnt(6)\n\t" \
  "v_mfma_f32_16x16x32_bf16 v[8:11], v[44:47], v[132:135], v[8:11]\n\t" \
  "v_mfma_f32_16x16x32_bf16 v[12:15], v[44:47], v[136:139], v[12:15]\n\t" \
  "s_waitcnt lgkmcnt(4)\n\t" \
  "v_mfma_f32_16x16x32_bf16 v[16:19], v[48:51], v[132:135], v[16:19]\n\t" \
  "v_mfma_f32_16x16x32_bf16 v[20:23], v[48:51], v[136:139], v[20:23]\n\t" \
  "s_waitcnt lgkmcnt(2)\n\t" \
  "v_mfma_f32_16x16x32_bf16 v[24:27], v[52:55], v[132:135], v[24:27]\n\t" \
  "v_mfma_f32_16x16x32_bf16 v[28:31], v[52:55], v[136:139], v[28:31]\n\t" \
  "s_waitcnt lgkmcnt(0)\n\t" \
  "v_mfma_f32_16x16x32_bf16 v[32:35], v[56:59], v[132:135], v[32:35]\n\t" \
  "v_mfma_f32_16x16x32_bf16 v[36:39], v[56:59], v[136:139], v[36:39]\n\t" \
  "s_mov_b64 exec, 1\n\t" \
  "ds_add_u32 v142, v143\n\t" \
  "s_mov_b64 exec, -1\n\t" \
  "s_nop 7\n\t"
#define TILE_GEMM_CLOB "v40", "v41", "v42", "v43", "v44", "v45", "v46", "v47", "v48", "v49", "v50", "v51", "v52", "v53", "v54", "v55", "v56", "v57", "v58", "v59", "v60", "v61", "v62", "v63", "v64", "v65", "v66", "v67", "v68", "v69", "v70", "v71", "v72", "v73", "v74", "v75", "v76", "v77", "v78", "v79", "v80", "v81", "v82", "v83", "v84", "v85", "v86", "v87", "v88", "v89", "v90", "v91", "v92", "v93", "v94", "v95", "v96", "v97", "v98", "v99", "v100", "v101", "v102", "v103", "v104", "v105", "v106", "v107", "v108", "v109", "v110", "v111", "v112", "v113", "v114", "v115", "v116", "v117", "v118", "v119", "v120", "v121", "v122", "v123", "v124", "v125", "v126", "v127", "v128", "v129", "v130", "v131", "v132", "v133", "v134", "v135", "v136", "v137", "v138", "v139", "v143"
#define K1_LOOP_ASM \
  "v_mov_b64 v[0:1], 0\n\t" \
  "v_mov_b64 v[2:3], 0\n\t" \
  "v_mov_b64 v[4:5], 0\n\t" \
  "v_mov_b64 v[6:7], 0\n\t" \
  "v_mov_b64 v[8:9], 0\n\t" \
  "v_mov_b64 v[10:11], 0\n\t" \
  "v_mov_b64 v[12:13], 0\n\t" \
  "v_mov_b64 v[14:15], 0\n\t" \
  "v_mov_b64 v[16:17], 0\n\t" \
  "v_mov_b64 v[18:19], 0\n\t" \
  "v_mov_b64 v[20:21], 0\n\t" \
  "v_mov_b64 v[22:23], 0\n\t" \
  "v_mov_b64 v[24:25], 0\n\t" \
  "v_mov_b64 v[26:27], 0\n\t" \
  "v_mov_b64 v[28:29], 0\n\t" \
  "v_mov_b64 v[30:31], 0\n\t" \
  "v_mov_b64 v[32:33], 0\n\t" \
  "v_mov_b64 v[34:35], 0\n\t" \
  "v_mov_b64 v[36:37], 0\n\t" \
  "v_mov_b64 v[38:39], 0\n\t" \
  "v_mov_b64 v[40:41], 0\n\t" \
  "v_mov_b64 v[42:43], 0\n\t" \
  "v_mov_b64 v[44:45], 0\n\t" \
  "v_mov_b64 v[46:47], 0\n\t" \
  "v_mov_b64 v[48:49], 0\n\t" \
  "v_mov_b64 v[50:51], 0\n\t" \
  "v_mov_b64 v[52:53], 0\n\t" \
  "v_mov_b64 v[54:55], 0\n\t" \
  "v_mov_b64 v[56:57], 0\n\t" \
  "v_mov_b64 v[58:59], 0\n\t" \
  "v_mov_b64 v[60:61], 0\n\t" \
  "v_mov_b64 v[62:63], 0\n\t" \
  "v_mov_b64 v[64:65], 0\n\t" \
  "v_mov_b64 v[66:67], 0\n\t" \
  "v_mov_b64 v[68:69], 0\n\t" \
  "v_mov_b64 v[70:71], 0\n\t" \
  "v_mov_b64 v[72:73], 0\n\t" \
  "v_mov_b64 v[74:75], 0\n\t" \
  "v_mov_b64 v[76:77], 0\n\t" \
  "v_mov_b64 v[78:79], 0\n\t" \
  "v_mov_b64 v[116:117], 0\n\t" \
  "v_mov_b64 v[118:119], 0\n\t" \
  "v_mov_b64 v[120:121], 0\n\t" \
  "v_mov_b64 v[122:123], 0\n\t" \
  "v_mov_b64 v[124:125], 0\n\t" \
  "v_mov_b64 v[126:127], 0\n\t" \
  "v_mov_b64 v[128:129], 0\n\t" \
  "v_mov_b64 v[130:131], 0\n\t" \
  "v_mov_b64 v[132:133], 0\n\t" \
  "v_mov_b64 v[134:135], 0\n\t" \
  "v_mov_b64 v[136:137], 0\n\t" \
  "v_mov_b64 v[138:139], 0\n\t" \
  "v_mov_b64 v[140:141], 0\n\t" \
  "v_mov_b64 v[142:143], 0\n\t" \
  "v_mov_b64 v[144:145], 0\n\t" \
  "v_mov_b64 v[146:147], 0\n\t" \
  "v_mov_b64 v[148:149], 0\n\t" \
  "v_mov_b64 v[150:151], 0\n\t" \
  "s_mov_b32 %[cnt], 0\n\t" \
  "1:\n\t" \
  "s_barrier\n\t" \
  "v_mfma_f32_16x16x32_bf16 v[0:3], v[132:135], v[116:119], v[0:3]\n\t" \
  "ds_read_b128 v[80:83], v152\n\t" \
  "v_mfma_f32_16x16x32_bf16 v[4:7], v[132:135], v[120:123], v[4:7]\n\t" \
  "ds_read_b64_tr_b16 v[96:97], v154 offset:0\n\t" \
  "v_mfma_f32_16x16x32_bf16 v[8:11], v[132:135], v[124:127], v[8:11]\n\t" \
  "ds_read_b64_tr_b16 v[98:99], v154 offset:2560\n\t" \
  "v_mfma_f32_16x16x32_bf16 v[12:15], v[132:135], v[128:131], v[12:15]\n\t" \
  "ds_read_b128 v[84:87], v152 offset:2048\n\t" \
  "v_mfma_f32_16x16x32_bf16 v[16:19], v[136:139], v[116:119], v[16:19]\n\t" \
  "ds_read_b128 v[88:91], v152 offset:4096\n\t" \
  "v_mfma_f32_16x16x32_bf16 v[20:23], v[136:139], v[120:123], v[20:23]\n\t" \
  "ds_read_b128 v[92:95], v152 offset:6144\n\t" \
  "v_mfma_f32_16x16x32_bf16 v[24:27], v[136:139], v[124:127], v[24:27]\n\t" \
  "ds_read_b64_tr_b16 v[100:101], v154 offset:8\n\t" \
  "v_mfma_f32_16x16x32_bf16 v[28:31], v[136:139], v[128:131], v[28:31]\n\t" \
  "ds_read_b64_tr_b16 v[102:103], v154 offset:2568\n\t" \
  "v_mfma_f32_16x16x32_bf16 v[32:35], v[140:143], v[116:119], v[32:35]\n\t" \
  "ds_read_b64_tr_b16 v[104:105], v154 offset:16\n\t" \
  "v_mfma_f32_16x16x32_bf16 v[36:39], v[140:143], v[120:123], v[36:39]\n\t" \
  "ds_read_b64_tr_b16 v[106:107], v154 offset:2576\n\t" \
  "v_mfma_f32_16x16x32_bf16 v[40:43], v[140:143], v[124:127], v[40:43]\n\t" \
  "ds_read_b64_tr_b16 v[108:109], v154 offset:24\n\t" \
  "v_mfma_f32_16x16x32_bf16 v[44:47], v[140:143], v[128:131], v[44:47]\n\t" \
  "ds_read_b64_tr_b16 v[110:111], v154 offset:2584\n\t" \
  "v_mfma_f32_16x16x32_bf16 v[48:51], v[144:147], v[116:119], v[48:51]\n\t" \
  "ds_read_b64_tr_b16 v[112:113], v154 offset:32\n\t" \
  "v_mfma_f32_16x16x32_bf16 v[52:55], v[144:147], v[120:123], v[52:55]\n\t" \
  "ds_read_b64_tr_b16 v[114:115], v154 offset:2592\n\t" \
  "v_mfma_f32_16x16x32_bf16 v[56:59], v[144:147], v[124:127], v[56:59]\n\t" \
  "s_and_b32 %[inc], %[cnt], 3\n\t" \
  "v_mfma_f32_16x16x32_bf16 v[60:63], v[144:147], v[128:131], v[60:63]\n\t" \
  "s_cmp_eq_u32 %[inc], 3\n\t" \
  "v_mfma_f32_16x16x32_bf16 v[64:67], v[148:151], v[116:119], v[64:67]\n\t" \
  "s_cselect_b32 %[inc], %[wrap], %[stp]\n\t" \
  "v_mfma_f32_16x16x32_bf16 v[68:71], v[148:151], v[120:123], v[68:71]\n\t" \
  "v_add_u32 v152, %[inc], v152\n\t" \
  "v_mfma_f32_16x16x32_bf16 v[72:75], v[148:151], v[124:127], v[72:75]\n\t" \
  "v_mfma_f32_16x16x32_bf16 v[76:79], v[148:151], v[128:131], v[76:79]\n\t" \
  "s_waitcnt lgkmcnt(0)\n\t" \
  "v_mfma_f32_16x16x32_bf16 v[0:3], v[96:99], v[80:83], v[0:3]\n\t" \
  "ds_read_b128 v[116:119], v153\n\t" \
  "v_mfma_f32_16x16x32_bf16 v[4:7], v[96:99], v[84:87], v[4:7]\n\t" \
  "ds_read_b64_tr_b16 v[132:133], v154 offset:5120\n\t" \
  "v_mfma_f32_16x16x32_bf16 v[8:11], v[96:99], v[88:91], v[8:11]\n\t" \
  "ds_read_b64_tr_b16 v[134:135], v154 offset:7680\n\t" \
  "v_mfma_f32_16x16x32_bf16 v[12:15], v[96:99], v[92:95], v[12:15]\n\t" \
  "ds_read_b128 v[120:123], v153 offset:2048\n\t" \
  "v_mfma_f32_16x16x32_bf16 v[16:19], v[100:103], v[80:83], v[16:19]\n\t" \
  "ds_read_b128 v[124:127], v153 offset:4096\n\t" \
  "v_mfma_f32_16x16x32_bf16 v[20:23], v[100:103], v[84:87], v[20:23]\n\t" \
  "ds_read_b128 v[128:131], v153 offset:6144\n\t" \
  "v_mfma_f32_16x16x32_bf16 v[24:27], v[100:103], v[88:91], v[24:27]\n\t" \
  "ds_read_b64_tr_b16 v[136:137], v154 offset:5128\n\t" \
  "v_mfma_f32_16x16x32_bf16 v[28:31], v[100:103], v[92:95], v[28:31]\n\t" \
  "ds_read_b64_tr_b16 v[138:139], v154 offset:7688\n\t" \
  "v_mfma_f32_16x16x32_bf16 v[32:35], v[104:107], v[80:83], v[32:35]\n\t" \
  "ds_read_b64_tr_b16 v[140:141], v154 offset:5136\n\t" \
  "v_mfma_f32_16x16x32_bf16 v[36:39], v[104:107], v[84:87], v[36:39]\n\t" \
  "ds_read_b64_tr_b16 v[142:143], v154 offset:7696\n\t" \
  "v_mfma_f32_16x16x32_bf16 v[40:43], v[104:107], v[88:91], v[40:43]\n\t" \
  "ds_read_b64_tr_b16 v[144:145], v154 offset:5144\n\t" \
  "v_mfma_f32_16x16x32_bf16 v[44:47], v[104:107], v[92:95], v[44:47]\n\t" \
  "ds_read_b64_tr_b16 v[146:147], v154 offset:7704\n\t" \
  "v_mfma_f32_16x16x32_bf16 v[48:51], v[108:111], v[80:83], v[48:51]\n\t" \
  "ds_read_b64_tr_b16 v[148:149], v154 offset:5152\n\t" \
  "v_mfma_f32_16x16x32_bf16 v[52:55], v[108:111], v[84:87], v[52:55]\n\t" \
  "ds_read_b64_tr_b16 v[150:151], v154 offset:7712\n\t" \
  "v_mfma_f32_16x16x32_bf16 v[56:59], v[108:111], v[88:91], v[56:59]\n\t" \
  "v_add_u32 v153, %[inc], v153\n\t" \
  "v_mfma_f32_16x16x32_bf16 v[60:63], v[108:111], v[92:95], v[60:63]\n\t" \
  "v_add_u32 v154, %[inc], v154\n\t" \
  "v_mfma_f32_16x16x32_bf16 v[64:67], v[112:115], v[80:83], v[64:67]\n\t" \
  "s_add_u32 %[cnt], %[cnt], 1\n\t" \
  "v_mfma_f32_16x16x32_bf16 v[68:71], v[112:115], v[84:87], v[68:71]\n\t" \
  "s_cmp_lt_u32 %[cnt], 10\n\t" \
  "v_mfma_f32_16x16x32_bf16 v[72:75], v[112:115], v[88:91], v[72:75]\n\t" \
  "v_mfma_f32_16x16x32_bf16 v[76:79], v[112:115], v[92:95], v[76:79]\n\t" \
  "s_waitcnt lgkmcnt(0)\n\t" \
  "s_cbranch_scc1 1b\n\t" \
  "v_mfma_f32_16x16x32_bf16 v[0:3], v[132:135], v[116:119], v[0:3]\n\t" \
  "v_mfma_f32_16x16x32_bf16 v[16:19], v[136:139], v[116:119], v[16:19]\n\t" \
  "v_mfma_f32_16x16x32_bf16 v[32:35], v[140:143], v[116:119], v[32:35]\n\t" \
  "v_mfma_f32_16x16x32_bf16 v[48:51], v[144:147], v[116:119], v[48:51]\n\t" \
  "v_mfma_f32_16x16x32_bf16 v[64:67], v[148:151], v[116:119], v[64:67]\n\t" \
  "v_mfma_f32_16x16x32_bf16 v[4:7], v[132:135], v[120:123], v[4:7]\n\t" \
  "v_mfma_f32_16x16x32_bf16 v[20:23], v[136:139], v[120:123], v[20:23]\n\t" \
  "v_mfma_f32_16x16x32_bf16 v[36:39], v[140:143], v[120:123], v[36:39]\n\t" \
  "v_mfma_f32_16x16x32_bf16 v[52:55], v[144:147], v[120:123], v[52:55]\n\t" \
  "v_mfma_f32_16x16x32_bf16 v[68:71], v[148:151], v[120:123], v[68:71]\n\t" \
  "v_mfma_f32_16x16x32_bf16 v[8:11], v[132:135], v[124:127], v[8:11]\n\t" \
  "v_mfma_f32_16x16x32_bf16 v[24:27], v[136:139], v[124:127], v[24:27]\n\t" \
  "v_mfma_f32_16x16x32_bf16 v[40:43], v[140:143], v[124:127], v[40:43]\n\t" \
  "v_mfma_f32_16x16x32_bf16 v[56:59], v[144:147], v[124:127], v[56:59]\n\t" \
  "v_mfma_f32_16x16x32_bf16 v[72:75], v[148:151], v[124:127], v[72:75]\n\t" \
  "v_mfma_f32_16x16x32_bf16 v[12:15], v[132:135], v[128:131], v[12:15]\n\t" \
  "v_mfma_f32_16x16x32_bf16 v[28:31], v[136:139], v[128:131], v[28:31]\n\t" \
  "v_mfma_f32_16x16x32_bf16 v[44:47], v[140:143], v[128:131], v[44:47]\n\t" \
  "v_mfma_f32_16x16x32_bf16 v[60:63], v[144:147], v[128:131], v[60:63]\n\t" \
  "v_mfma_f32_16x16x32_bf16 v[76:79], v[148:151], v[128:131], v[76:79]\n\t" \
  "s_nop 7\n\t"
#define K1_LOOP_CLOB "v80", "v81", "v82", "v83", "v84", "v85", "v86", "v87", "v88", "v89", "v90", "v91", "v92", "v93", "v94", "v95", "v96", "v97", "v98", "v99", "v100", "v101", "v102", "v103", "v104", "v105", "v106", "v107", "v108", "v109", "v110", "v111", "v112", "v113", "v114", "v115", "v116", "v117", "v118", "v119", "v120", "v121", "v122", "v123", "v124", "v125", "v126", "v127", "v128", "v129", "v130", "v131", "v132", "v133", "v134", "v135", "v136", "v137", "v138", "v139", "v140", "v141", "v142", "v143", "v144", "v145", "v146", "v147", "v148", "v149", "v150", "v151"
#define CODE_BYTES 23552
#define LDELAY 2
#define QOKW 9u
#define FINW 4u
#define FSTAMP(i) do {} while (0)
#define HSTAMP(i) STAMP(i)
#define TSTAMP(i) do {} while (0)
#define FSLEEP 0
#define BSLEEP 1
#define K1_ROT ((tt + 3 * (jb >> 3)) % 10)
constexpr u32 K1_SLOT = 36864, K1_W = 16384;
constexpr u32 K1_EX = 3 * K1_SLOT, K1_EXP = 4 * K1_SLOT;
constexpr u32 K1_VI = 0, K1_VI_P = 208, K1_PHK = 27648, K1_PHK_P = 336;
constexpr u32 K1_QST = 0;
constexpr u32 K1_PHQ = 16384;
constexpr u32 K1_KVT = 57344;
constexpr u32 K1_KVF = 70656;
constexpr u32 K1_XCH = 73728;
constexpr u32 K1_WPHI = 156160;
constexpr u32 L_K01 = 160256;
constexpr u32 SL = 4 * REC;
constexpr u32 L_IMG = 0;
constexpr u32 L_RING = 102400;
constexpr u32 L_MRG = L_RING + SL;
constexpr u32 L_FLG = 162816;
constexpr u32 F_GBAR = L_FLG + 96, F_APUB = L_FLG + 100, F_AFREE = L_FLG + 104  , F_P1 = L_FLG + 112, F_PHB = L_FLG + 116,
              F_P2 = L_FLG + 120, F_KVOK = L_FLG + 124, F_BIMG = L_FLG + 128, F_QOK = L_FLG + 132, F_TAG = L_FLG + 136, F_FIN = L_FLG + 152, F_K01 = L_FLG + 156, F_AOK = L_FLG + 160, F_P3 = L_FLG + 164, L_GT = L_FLG + 256;
constexpr u32 LDS_BYTES = 163840;

__device__ __forceinline__ f32x16 attn_exact(rsrc_t rW, u32x4 qx, u32 q2lo, int lane) {
  const f32x16 zero16 = {0.f, 0.f, 0.f, 0.f, 0.f, 0.f, 0.f, 0.f, 0.f, 0.f, 0.f, 0.f, 0.f, 0.f, 0.f, 0.f};
  const int r32 = lane & 31, hh = lane >> 5;
  const u32x2 qz = {q2lo, 0u};
  float m = -INFINITY;
#pragma unroll 1
  for (u32 rec = 0; rec < 64; ++rec) {
    const u32x4 a1 = gl128(rW, lane * 16, WS_KVIM + rec * REC);
    const u32 a2 = gl32(rW, lane * 4, WS_KVIM + rec * REC + 1024);
    f32x16 s = mfma32(a1, qx, zero16);
    s = mfma32k8((u32x2){a2, 0u}, qz, s);
    m = fmaxf(m, max16(s));
  }
  m = xhalf_max(m);
  f32x16 O = zero16;
  const u32 av = 1280u + (u32)hh * 352u + (u32)(r32 <= 20 ? r32 : 21) * 16u;
#pragma unroll 1
  for (u32 t2 = 0; t2 < 64; ++t2) {
    const u32 rec = (t2 + 62) & 63;
    const u32x4 a1 = gl128(rW, lane * 16, WS_KVIM + rec * REC);
    const u32 a2 = gl32(rW, lane * 4, WS_KVIM + rec * REC + 1024);
    const u32x4 b1 = gl128(rW, av, WS_KVIM + t2 * REC), b2 = gl128(rW, av, WS_KVIM + t2 * REC + 704);
    f32x16 s = mfma32(a1, qx, zero16);
    s = mfma32k8((u32x2){a2, 0u}, qz, s);
#pragma unroll
    for (int r = 0; r < 16; ++r) s[r] = __builtin_amdgcn_exp2f(s[r] - m);
    O = smf(b1, lo8(s), O, 0xDDDD);
    O = smf(b2, hi8(s), O, 0xDDDD);
  }
  return O;
}

extern "C" __global__ void __launch_bounds__(768, 3)
gm_fused(const char* __restrict__ x, const char* __restrict__ wqkv, const char* __restrict__ wphi, const char* __restrict__ wq,
         const char* __restrict__ wk, const char* __restrict__ wv, const char* __restrict__ gw, const char* __restrict__ awo,
         const char* __restrict__ bwo, char* ws, char* __restrict__ out) {
  extern __shared__ __attribute__((aligned(16))) char smem[];
  ldsp lds = (ldsp)smem;
  const u32 lds0 = (u32)(uintptr_t)lds;
  const int w = __builtin_amdgcn_readfirstlane(threadIdx.x >> 6);
  const int bid = blockIdx.x, xcd = bid & 7, jb = bid >> 3;
  const rsrc_t rW = __builtin_amdgcn_make_buffer_rsrc(ws, 0, WS_END, 0x00020000);
  const f32x16 zero16 = {0.f, 0.f, 0.f, 0.f, 0.f, 0.f, 0.f, 0.f, 0.f, 0.f, 0.f, 0.f, 0.f, 0.f, 0.f, 0.f};
  STAMP_INIT();
  const int tt = (xcd & 1) * 8 + (jb & 7), combo = (xcd >> 1) * 4 + (jb >> 3), type = combo & 1, hg = combo >> 1;
  const u32x4 rsWS = mkrsrc(ws, WS_END);
  {
    if (w < 4) {
      const int cg = w >> 1, th = w & 1;
      f32x16 ac[5];
      {
        const int lane = lane_id(), g = lane >> 4, i16 = lane & 15;
        u32 aW = lds0 + K1_W + (u32)cg * 10240u + (u32)(4 * g + (i16 >> 2)) * 160u + (u32)(i16 & 3) * 40u;
        u32 aX0 = lds0 + (u32)(64 * th + i16) * 128u + (u32)((g ^ (i16 >> 1)) * 16), aX1 = aX0 ^ 64u;
        u32 cnt, inc;
        asm volatile(K1_LOOP_ASM
                     : "=&{v[0:15]}"(ac[0]), "=&{v[16:31]}"(ac[1]), "=&{v[32:47]}"(ac[2]), "=&{v[48:63]}"(ac[3]), "=&{v[64:79]}"(ac[4]),
                       "+{v152}"(aX0), "+{v153}"(aX1), "+{v154}"(aW), [cnt] "=&s"(cnt), [inc] "=&s"(inc)
                     : [wrap] "s"(0u - 3u * K1_SLOT), [stp] "s"(K1_SLOT)
                     : "memory", "scc", "v160", "v161", K1_LOOP_CLOB);
      }
      DRAIN();
      HSTAMP(2); FSTAMP(2);
      const int lane = lane_id(), g = lane >> 4, i16 = lane & 15;
      const float sc = (cg && !type) ? QSCALE : 1.f;
#pragma unroll
      for (int n = 0; n < 4; ++n) {
        u32 v[10];
#pragma unroll
        for (int T = 0; T < 5; ++T) { v[2 * T] = pk2(ac[T][4 * n] * sc, ac[T][4 * n + 1] * sc); v[2 * T + 1] = pk2(ac[T][4 * n + 2] * sc, ac[T][4 * n + 3] * sc); }
        const int t32 = 16 * (n & 1) + i16;
        if (cg == 0) {
          ldsp xw = lds + K1_XCH + (u32)(2 * n + th) * 2560u;
          *(LDSAS u32x4*)(xw + lane * 16) = (u32x4){v[0], v[1], v[2], v[3]};
          *(LDSAS u32x4*)(xw + 1024 + lane * 16) = (u32x4){v[4], v[5], v[6], v[7]};
          *(LDSAS u32x2*)(xw + 2048 + lane * 8) = (u32x2){v[8], v[9]};
        } else if (type) {
          ldsp dst = lds + K1_VI + (u32)(64 * th + 16 * n + i16) * K1_VI_P + (u32)g * 48u;
          *(LDSAS u32x4*)(dst) = (u32x4){v[0], v[1], v[2], v[3]};
          *(LDSAS u32x4*)(dst + 16) = (u32x4){v[4], v[5], v[6], v[7]};
          *(LDSAS u32x4*)(dst + 32) = (u32x4){v[8], v[9], 0x00003F80u, 0u};
        } else if (th == 0) {
          ldsp dst = lds + K1_QST + (u32)(2 * g + (n >> 1)) * 1280u;
          *(LDSAS u32x4*)(dst + t32 * 16) = (u32x4){v[0], v[1], v[2], v[3]};
          *(LDSAS u32x4*)(dst + 512 + t32 * 16) = (u32x4){v[4], v[5], v[6], v[7]};
          *(LDSAS u32x2*)(dst + 1024 + t32 * 8) = (u32x2){v[8], v[9]};
        } else {
          const u32 dst = WS_QB + (u32)(tt * 8 + hg) * 10240u + (u32)(2 * g + (n >> 1)) * 1280u;
          st128(rW, dst + t32 * 16, (u32x4){v[0], v[1], v[2], v[3]});
          st128(rW, dst + 512 + t32 * 16, (u32x4){v[4], v[5], v[6], v[7]});
          st64(rW, dst + 1024 + t32 * 8, (u32x2){v[8], v[9]});
        }
      }
      LBAR();
      HSTAMP(3);
      if (w == 3) {
        const u32x2 tag = ld64(lds + F_TAG);
        const u32 fq = WS_FQ + (u32)(tt * 8 + hg) * 128u;
        if (type == 0) { DRAIN(); st64(rW, lane == 0 ? fq : OOB, tag); FSTAMP(3); }
        else { flags_wait<1>(rW, fq, 1, tag); lds_st(lds + F_QOK, 1u); FSTAMP(4); }
      }
    } else {
      const int lane = lane_id(), g = lane >> 4, i16 = lane & 15;
      if (w >= 8) {
        __builtin_amdgcn_s_setprio(1);
        const int lw = w - 8, g1 = lw >> 1, hb = lw & 1;
        const u32x4 rsX = mkrsrc(x - 4096, SEQ * DM * 2 + 4096);
        const bool useq = g1 && !type;
        const u32x4 rsW = useq ? mkrsrc(wq - 4096, DM * DM * 2 + 4096) : mkrsrc(wqkv - 4096, DM * 3 * DM * 2 + 4096);
        const u32 pitch = useq ? 1280u : 3840u;
        const u32 col = (type ? (g1 ? 1280u : 640u) : 0u) + 80u * hg;
        u32 vx[4], vw[5];
        const u32 vx0 = 4096u + (u32)(lane >> 3) * 1280u + (u32)(((lane & 7) ^ (lane >> 4)) * 16);
#pragma unroll
        for (int i = 0; i < 4; ++i) vx[i] = (vx0 ^ (u32)((i & 1) * 64)) + (u32)i * 9216u;
#pragma unroll
        for (int j = 0; j < 5; ++j) {
          const u32 c = 64u * j + lane, rho = (c * 205u) >> 11, cc = c - rho * 10u;
          const u32 k = 8u * ((rho >> 2) & 3u) + 4u * (rho >> 4) + (rho & 3u);
          vw[j] = 4096u + k * pitch + cc * 16u - (u32)(j & 3) * 1024u;
        }
        const u32 sx0 = (u32)(128 * tt + 32 * lw) * 1280u, sw0 = 32u * hb * pitch + col * 2u;
        int ks = K1_ROT;
        auto issue = [&](int s) {
          const u32 base = lds0 + (u32)(s & 3) * K1_SLOT;
          u32 keep;
          asm volatile(
              "s_mov_b32 %0, m0\n\ts_mov_b32 m0, %[mx]\n\ts_nop 2\n\t"
              "buffer_load_dwordx4 %[x0], %[rx], %[sx] offen lds\n\t"
              "buffer_load_dwordx4 %[x1], %[rx], %[sx] offen offset:1024 lds\n\t"
              "buffer_load_dwordx4 %[x2], %[rx], %[sx] offen offset:2048 lds\n\t"
              "buffer_load_dwordx4 %[x3], %[rx], %[sx] offen offset:3072 lds\n\t"
              "s_mov_b32 m0, %[mw]\n\ts_nop 0\n\t"
              "buffer_load_dwordx4 %[w0], %[rw], %[sw] offen lds\n\t"
              "buffer_load_dwordx4 %[w1], %[rw], %[sw] offen offset:1024 lds\n\t"
              "buffer_load_dwordx4 %[w2], %[rw], %[sw] offen offset:2048 lds\n\t"
              "buffer_load_dwordx4 %[w3], %[rw], %[sw] offen offset:3072 lds\n\t"
              "s_add_u32 m0, %[mw], 4096\n\ts_nop 0\n\t"
              "buffer_load_dwordx4 %[w4], %[rw], %[sw] offen lds\n\t"
              "s_mov_b32 m0, %0"
              : "=&s"(keep)
              : [mx] "s"(base + (u32)lw * 4096u), [mw] "s"(base + K1_W + (u32)lw * 5120u),
                [rx] "s"(rsX), [rw] "s"(rsW), [sx] "s"(sx0 + (u32)ks * 128u), [sw] "s"(sw0 + (u32)ks * 64u * pitch),
                [x0] "v"(vx[0]), [x1] "v"(vx[1]), [x2] "v"(vx[2]), [x3] "v"(vx[3]), [w0] "v"(vw[0]), [w1] "v"(vw[1]), [w2] "v"(vw[2]), [w3] "v"(vw[3]),
                [w4] "v"(vw[4])
              : "memory", "scc");
          ks = ks == 9 ? 0 : ks + 1;
        };
        __builtin_amdgcn_s_sleep(LDELAY);
        issue(0); issue(1);
        WAIT_BAR(9);
        HSTAMP(2); FSTAMP(2);
        issue(2);
#pragma unroll 1
        for (int s = 1; s < 8; ++s) {
          issue(s + 2);
          WAIT_BAR(18);
        }
        WAIT_BAR(9);
        WAIT_BAR(0);
        lds_wait_ge<1>(lds + F_KVOK, 1);
        {
          const u32 o = (u32)lw * 13312u, vl = (u32)lane * 16u;
#pragma unroll 1
          for (u32 c = 0; c < 3; ++c) dma4(rsWS, vl, WS_KVIM + o + c * 4096u, lds0 + L_RING + o + c * 4096u);
          dma1s(rsWS, vl, WS_KVIM + o + 12288u, lds0 + L_RING + o + 12288u);
          dma1s(rsWS, vl, WS_KVIM + 52736u, lds0 + L_RING + 52736u);
        }
        if (lane < 8) lds_st(lds + L_FLG + 32 + lane * 4, 5u);
        DRAIN();
      } else {
        const int v4 = w - 4, tok0 = 128 * tt + 16 * hg;
        __builtin_amdgcn_s_setprio(3);
        {
          const u32x4 rsE = mkrsrc((type ? wv : wk) + v4 * 6400, 6400);
          const u32 la = lds0 + K1_EX + (u32)v4 * 7168u;
#pragma unroll 1
          for (u32 c = 0; c < 7; ++c) dma1(rsE, (u32)lane * 16u, c * 1024u, la + c * 1024u);
          u32x4 xf[5], gf[5];
#pragma unroll
          for (int s = 0; s < 5; ++s) {
            xf[s] = *(const u32x4*)(x + (size_t)(tok0 + i16) * 1280 + v4 * 320 + s * 64 + g * 16);
            gf[s] = *(const u32x4*)(gw + v4 * 320 + s * 64 + g * 16);
          }
          u64 e = 0;
          if (w == 7) e = __hip_atomic_load((const gu64*)(ws + WS_EPOCH), __ATOMIC_RELAXED, __HIP_MEMORY_SCOPE_AGENT) + 0x9E3779B97F4A7C15ull;
          DRAIN();
          if (w == 7) lds_st(lds + L_FLG + lane * 4, lane == 34 ? (u32)e : (lane == 35 ? (u32)(e >> 32) : 0u));
          ldsp pe = lds + K1_EX + (u32)v4 * 7168u + (u32)(8 * g + (i16 >> 2)) * 40u + (u32)(i16 & 3) * 8u;
          f32x4 c0 = {0.f, 0.f, 0.f, 0.f}, c1 = c0, c2 = c0;
#pragma unroll
          for (int s = 0; s < 5; ++s) {
            const u32x4 w0 = trd2(pe + s * 1280, pe + s * 1280 + 160), w1 = trd2(pe + s * 1280 + 32, pe + s * 1280 + 192);
            if (type) { c0 = mfma16(xf[s], w0, c0); c1 = mfma16(xf[s], w1, c1); }
            else { c0 = mfma16(w0, xf[s], c0); c1 = mfma16(w1, xf[s], c1); }
            c2 = mfma16(gf[s], xf[s], c2);
          }
          ldsp pp = lds + K1_EXP + (u32)v4 * 1024u + lane * 16;
          *(LDSAS f32x4*)(pp) = c0; *(LDSAS f32x4*)(pp + 4096) = c1; *(LDSAS f32x4*)(pp + 8192) = c2;
        }
        LBAR();
        if (v4 < 3) {
          ldsp pp = lds + K1_EXP + (u32)v4 * 4096u + lane * 16;
          const f32x4 e0 = (*(LDSAS f32x4*)(pp) + *(LDSAS f32x4*)(pp + 1024)) + (*(LDSAS f32x4*)(pp + 2048) + *(LDSAS f32x4*)(pp + 3072));
          const u32x2 ev = {pk2(e0[0], e0[1]), pk2(e0[2], e0[3])};
          const int tile = tok0 >> 5, k0 = tok0 & 31;
          if (v4 == 2) {
            st32(rW, (type == 0 && g == 0) ? WS_GATE + (u32)(tok0 + i16) * 4u : OOB, __float_as_uint(__builtin_amdgcn_rcpf(1.f + __builtin_amdgcn_exp2f(-e0[0] * LOG2E))));
          } else if (type == 0) {
            const u32 rec = WS_KVIM + (u32)((tile + 62) & 63) * REC;
            if (v4 == 0) st64(rW, rec + (g >> 1) * 512 + (k0 + i16) * 16 + (g & 1) * 8, ev);
            else { const u32 o = g ? OOB : rec + 1024 + (k0 + i16) * 4; st32(rW, o, ev[0]); st32(rW, o + 128, ev[1]); }
          } else {
            const int d = 16 * v4 + i16, k32 = k0 + 4 * g;
            const u32x2 val = d == 20 ? (u32x2){0x3F803F80u, 0x3F803F80u} : (d == 21 ? (u32x2){0u, 0u} : ev);
            st64(rW, d <= 21 ? WS_KVIM + (u32)tile * REC + 1280 + (k32 >> 3) * 352 + d * 16 + (k32 & 7) * 2 : OOB, val);
          }
        }
        LBAR();
        {
          const u32 be = (u32)(v4 * 64 + lane) * 16u;
          dma1(mkrsrc(wphi, NH * 400 * 2), be < 3200u ? be : OOB, (u32)hg * 3200u, lds0 + K1_WPHI + (u32)v4 * 1024u);
        }
        LBAR();
        WAIT_BAR(0);
        const u32x2 tag = ld64(lds + F_TAG);
        if (w == 7) {
          st64(rW, lane < 8 ? WS_F1 + (u32)lane * 2048u + (u32)bid * 8u : OOB, tag);
          FSTAMP(3);
          LBAR(); LBAR(); LBAR(); LBAR(); LBAR();
          const u32 fo = WS_F1 + (u32)xcd * 2048u;
          u32x4 fa = gl128(rW, lane * 16, fo), fb = gl128(rW, lane * 16, fo + 1024);
          LBAR();
          PIN(fa); PIN(fb);
          const u32 dd = (fa[0] ^ tag[0]) | (fa[1] ^ tag[1]) | (fa[2] ^ tag[0]) | (fa[3] ^ tag[1]) | (fb[0] ^ tag[0]) | (fb[1] ^ tag[1]) | (fb[2] ^ tag[0]) | (fb[3] ^ tag[1]);
          if (!__all(dd == 0u)) flags_wait256<2>(rW, fo, tag);
          asm volatile("" ::: "memory");
          FSTAMP(4);
          const u32x4 ka = gl128(rW, lane * 16, WS_KVIM + 62 * REC), kb = gl128(rW, lane * 16, WS_KVIM + 63 * REC);
          const u32 kc = gl32(rW, lane * 4, WS_KVIM + 62 * REC + 1024), kd = gl32(rW, lane * 4, WS_KVIM + 63 * REC + 1024);
          const u32 gtv = gl32(rW, lane * 4, WS_GATE + (u32)(2 * tt + type) * 256u);
          asm volatile("" ::: "memory");
          lds_st(lds + F_KVOK, 1u);
          if (bid == 0 && lane == 0)
            __hip_atomic_store((gu64*)(ws + WS_EPOCH), (u64)tag[0] | ((u64)tag[1] << 32), __ATOMIC_RELAXED, __HIP_MEMORY_SCOPE_AGENT);
          *(LDSAS u32x4*)(lds + L_K01 + lane * 16) = ka; *(LDSAS u32*)(lds + L_K01 + 1024 + lane * 4) = kc;
          *(LDSAS u32x4*)(lds + L_K01 + 1280 + lane * 16) = kb; *(LDSAS u32*)(lds + L_K01 + 2304 + lane * 4) = kd;
          *(LDSAS u32*)(lds + L_GT + lane * 4) = gtv;
        } else {
          LBAR(); LBAR(); LBAR(); LBAR(); LBAR(); LBAR();
        }
      }
      __builtin_amdgcn_s_setprio(2);
      const int j8 = w - 4;
      const u32 aP = K1_WPHI + (((i16 & 3) == g) ? (u32)g * 800u + (u32)(i16 >> 2) * 40u : 3200u);
      u32x4 a0[5], a1[5]; u32x2 a2[5];
#pragma unroll
      for (int T = 0; T < 5; ++T) {
        a0[T] = trd2(lds + aP + T * 8, lds + aP + T * 8 + 160);
        a1[T] = trd2(lds + aP + T * 8 + 320, lds + aP + T * 8 + 480);
        a2[T] = trd(lds + aP + T * 8 + 640);
      }
      LBAR();
      HSTAMP(3);
      ldsp xr = lds + K1_XCH + (u32)j8 * 2560u;
      const u32x4 bq0 = ld128(xr + lane * 16), bq1 = ld128(xr + 1024 + lane * 16);
      const u32x2 bq2 = ld64(xr + 2048 + lane * 8);
      f32x4 pq[5];
#pragma unroll
      for (int T = 0; T < 5; ++T) pq[T] = mfma16(a0[T], bq0, (f32x4){0.f, 0.f, 0.f, 0.f});
#pragma unroll
      for (int T = 0; T < 5; ++T) pq[T] = mfma16(a1[T], bq1, pq[T]);
#pragma unroll
      for (int T = 0; T < 5; ++T) pq[T] = mfma16((u32x4){a2[T][0], a2[T][1], 0u, 0u}, (u32x4){bq2[0], bq2[1], 0u, 0u}, pq[T]);
      float mx = fmaxf(pq[0][0], pq[0][1]), mn = fminf(pq[0][0], pq[0][1]);
      mx = fmaxf(fmaxf(mx, pq[0][2]), pq[0][3]); mn = fminf(fminf(mn, pq[0][2]), pq[0][3]);
#pragma unroll
      for (int T = 1; T < 5; ++T) {
        mx = fmaxf(fmaxf(mx, pq[T][0]), pq[T][1]); mx = fmaxf(fmaxf(mx, pq[T][2]), pq[T][3]);
        mn = fminf(fminf(mn, pq[T][0]), pq[T][1]); mn = fminf(fminf(mn, pq[T][2]), pq[T][3]);
      }
      const float cp = -mx * LOG2E, cn = mn * LOG2E;
      f32x4 ep[5], en[5];
#pragma unroll
      for (int T = 0; T < 5; ++T) { ep[T] = pq[T] * LOG2E + cp; en[T] = pq[T] * (-LOG2E) + cn; PIN(ep[T]); PIN(en[T]); }
#pragma unroll
      for (int T = 0; T < 5; ++T)
#pragma unroll
        for (int i = 0; i < 4; ++i) { ep[T][i] = __builtin_amdgcn_exp2f(ep[T][i]); en[T][i] = __builtin_amdgcn_exp2f(en[T][i]); }
      const f32x4 sp = (ep[0] + ep[1]) + (ep[2] + ep[3]) + ep[4], sn = (en[0] + en[1]) + (en[2] + en[3]) + en[4];
      const float rp = __builtin_amdgcn_rcpf((sp[0] + sp[1]) + (sp[2] + sp[3])), rn = __builtin_amdgcn_rcpf((sn[0] + sn[1]) + (sn[2] + sn[3]));
      u32 ph[20];
#pragma unroll
      for (int T = 0; T < 5; ++T) {
        const f32x4 a = ep[T] * rp, b = en[T] * rn;
        ph[2 * T] = pk2(a[0], a[1]); ph[2 * T + 1] = pk2(a[2], a[3]);
        ph[10 + 2 * T] = pk2(b[0], b[1]); ph[11 + 2 * T] = pk2(b[2], b[3]);
      }
      const int tl = 64 * (j8 & 1) + 16 * (j8 >> 1) + i16;
      if (type == 0) {
        ldsp dst = lds + K1_PHQ + (u32)(4 * g + (tl >> 5)) * 2560u + (tl & 31) * 16;
#pragma unroll
        for (int c = 0; c < 5; ++c) *(LDSAS u32x4*)(dst + c * 512) = (u32x4){ph[4 * c], ph[4 * c + 1], ph[4 * c + 2], ph[4 * c + 3]};
      } else {
        ldsp dst = lds + K1_PHK + (u32)tl * K1_PHK_P + (u32)g * 80u;
#pragma unroll
        for (int c = 0; c < 5; ++c) *(LDSAS u32x4*)(dst + 16 * c) = (u32x4){ph[4 * c], ph[4 * c + 1], ph[4 * c + 2], ph[4 * c + 3]};
        lds_arrive(lds + F_PHB);
        lds_wait_ge<1>(lds + F_PHB, 8);
        const int hl = j8 >> 1, dt = j8 & 1;
        const u32 trow = (u32)(8 * g + (i16 >> 2));
        ldsp pa = lds + K1_PHK + trow * K1_PHK_P + (u32)hl * 80u + (u32)(i16 & 3) * 8u;
        ldsp pb = lds + K1_VI + trow * K1_VI_P + (u32)hl * 48u + (u32)dt * 32u + (u32)(i16 & 3) * 8u;
        f32x4 kv[3];
#pragma unroll
        for (int ft = 0; ft < 3; ++ft) kv[ft] = (f32x4){0.f, 0.f, 0.f, 0.f};
#pragma unroll
        for (int ks = 0; ks < 4; ++ks) {
          u32x4 bv = trd2(pb + ks * 32 * K1_VI_P, pb + (ks * 32 + 4) * K1_VI_P);
#pragma unroll
          for (int ft = 0; ft < 3; ++ft) {
            u32x4 av = trd2(pa + ks * 32 * K1_PHK_P + ft * 32, pa + (ks * 32 + 4) * K1_PHK_P + ft * 32);
            kv[ft] = mfma16(av, bv, kv[ft]);
          }
        }
#pragma unroll
        for (int ft = 0; ft < 3; ++ft) *(LDSAS f32x4*)(lds + K1_KVF + (u32)j8 * 3072u + ft * 1024 + lane * 16) = kv[ft];
      }
      lds_arrive(lds + F_P2);
      __builtin_amdgcn_s_setprio(0);
      HSTAMP(4);
      if (w >= 8 && w < 11) {
        __builtin_amdgcn_s_setprio(3);
        const int hw = w - 8;
        const u32x2 tag = ld64(lds + F_TAG);
        lds_wait_ge<4>(lds + F_P2, 8);
        if (type) {
          lds_wait_ge<4>(lds + F_QOK, QOKW);
#pragma unroll 1
          for (int j = hw; j < 8; j += 3) {
            const int d = 16 * (j & 1) + i16;
            const u32 dst = d <= 20 ? WS_KVP + (u32)((hg * 16 + tt) * 4 + (j >> 1)) * 3360u + d * 160 + 16 * g : OOB;
            ldsp src = lds + K1_KVF + (u32)j * 3072u + lane * 16;
            st128(rW, dst, ld128(src)); st128(rW, dst + 64, ld128(src + 1024)); st128(rW, g < 2 ? dst + 128 : OOB, ld128(src + 2048));
          }
          DRAIN();
          if (lds_arrive(lds + F_P3) == 2) {
            st64(rW, lane == 0 ? WS_F2 + (u32)(hg * 16 + tt) * 8u : OOB, tag);
            flags_wait<4>(rW, WS_F2 + (u32)hg * 128u, 16, tag);
            const int it = tt * 53 + lane;
            const bool ok = lane < 53 && it < 840;
            const u32 vo = ok ? (u32)it * 16u : OOB, pb = WS_KVP + (u32)hg * 16u * 13440u;
            f32x4 a = __builtin_bit_cast(f32x4, gl128(rW, vo, pb));
#pragma unroll
            for (int p = 1; p < 16; ++p) a += __builtin_bit_cast(f32x4, gl128(rW, vo, pb + p * 13440));
            st64(rW, ok ? WS_KVT + (u32)hg * 6720u + (u32)it * 8u : OOB, (u32x2){pk2(a[0], a[1]), pk2(a[2], a[3])});
            DRAIN();
            st64(rW, lane == 0 ? WS_F3 + (u32)(hg * 16 + tt) * 8u : OOB, tag);
            lds_arrive(lds + F_P3);
          }
          lds_wait_ge<4>(lds + F_P3, 4);
        } else {
          const int r32 = lane & 31, hh = lane >> 5;
          {
            const u32 qi = (u32)((jb >> 4) * 8 + (jb & 7));
            const u32x4 rs = mkrsrc((qi & 1) ? bwo : awo, DM * DM * 2);
            u32 dummy;
#pragma unroll 1
            for (u32 c = hw; c < 13; c += 3) {
              const u32 l = c * 64u + lane;
              asm volatile("s_nop 4\n\tbuffer_load_dword %0, %1, %2, 0 offen" : "=v"(dummy) : "v"(l < 800u ? ((qi >> 1) * 800u + l) * 128u : OOB), "s"(rs) : "memory");
            }
            DRAIN();
          }
          if (hw == 0) {
            const u32x4 rsK = mkrsrc(ws + WS_KVT + (u32)hg * 6720u, 6720);
            *(LDSAS u32x2*)(lds + K1_KVT + 7168 + lane * 8) = gl64(rW, lane * 8, WS_GATE + (u32)tt * 512u);
            flags_wait<4>(rW, WS_F3 + (u32)hg * 128u, 16, tag);
            dma4(rsK, (u32)lane * 16u, 0u, lds0 + K1_KVT);
            dma1s(rsK, (u32)lane * 16u, 4096u, lds0 + K1_KVT + 4096u); dma1s(rsK, (u32)lane * 16u, 5120u, lds0 + K1_KVT + 5120u);
            dma1s(rsK, (u32)lane * 16u, 6144u, lds0 + K1_KVT + 6144u);
            DRAIN();
            lds_arrive(lds + F_P3);
          }
          lds_wait_ge<4>(lds + F_P3, 1);
          HSTAMP(5);
          const u32 ao = K1_KVT + (u32)(r32 < 20 ? r32 : 20) * 80u + (u32)hh * 16u;
          const u32 vo0 = WS_OFR + (u32)(8 * tt + (r32 >> 4)) * 20480u + (r32 & 15) * 16 + (u32)hg * 2560u;
#pragma unroll 1
          for (int j = hw; j < 16; j += 3) {
            const int hl = j >> 2, t4 = j & 3;
            ldsp ak = lds + ao + hl * 1680, pk = lds + K1_PHQ + j * 2560;
            const u32x4 a0 = ld128(ak), a1 = ld128(ak + 32);
            u32x4 a2 = ld128(ak + 64);
            const u32x4 p0 = ld128(pk + lane * 16), p1 = ld128(pk + 1024 + lane * 16), p2 = ld128(pk + 2048 + r32 * 16);
            const float gt = *(LDSAS float*)(lds + K1_KVT + 7168 + (32 * t4 + r32) * 4);
            if (hh) a2 = (u32x4){0u, 0u, 0u, 0u};
            f32x16 N = mfma32(a0, p0, zero16);
            N = mfma32(a1, p1, N);
            N = mfma32(a2, p2, N);
            const float sa = gt * __builtin_amdgcn_rcpf(N[12] + 1e-6f);
            const u32 vo = vo0 + (u32)(t4 * 40960 + (hl >> 1) * 1280);
            st128(rW, vo + hh * 256 + (hl & 1) * 512, (u32x4){pk2(N[0] * sa, N[1] * sa), pk2(N[2] * sa, N[3] * sa), pk2(N[4] * sa, N[5] * sa), pk2(N[6] * sa, N[7] * sa)});
            st64(rW, hh ? OOB : vo + 1024 + (hl & 1) * 8, (u32x2){pk2(N[8] * sa, N[9] * sa), pk2(N[10] * sa, N[11] * sa)});
          }
          DRAIN();
          if (lds_arrive(lds + F_APUB) == 2) st64(rW, lane == 0 ? WS_FLGA + (u32)(tt * 16 + hg) * 8u : OOB, tag);
          lds_wait_ge<4>(lds + F_APUB, 3);
        }
        HSTAMP(6);
      }
    }
  }

  if (w >= 8) {
    __builtin_amdgcn_s_setprio(3);
    const int hw = w - 8, kh = hw >> 1, t64 = xcd * 4 + (jb & 3), cgp = jb >> 2;
    const u32x2 tag = ld64(lds + F_TAG);
    f32x16 ac0 = zero16, ac1 = zero16; f32x8 ac2 = {0.f, 0.f, 0.f, 0.f, 0.f, 0.f, 0.f, 0.f};
    u32 s = 5, slot = 0, ps = 5;
#pragma unroll 1
    for (int part = 0; part < 2; ++part) {
      const int ln = lane_id();
      const u32 vl = (u32)ln * 16u;
      if (w == 11) {
#pragma unroll 1
        for (u32 it = 0; it < SPIN_CAP && ps < 16; ++it) {
          const u32 fl = lds_ld(lds + (ln < 8 ? L_FLG + ln * 4 : F_AOK));
          const u32 ns = s;
          if (s < 16 && __all(ln >= 8 || fl + 4 >= s)) {
            const u32 so = WS_KVIM + s * SL, la = lds0 + L_RING + slot * SL;
            dma4(rsWS, vl, so, la); dma4(rsWS, vl, so + 4096u, la + 4096u); dma2h(rsWS, vl, so + 8192u, la + 8192u);
            asm volatile("s_waitcnt vmcnt(11)" ::: "memory");
            ++s; slot = slot == 4 ? 0 : slot + 1;
          } else {
            DRAIN();
            if (ps == ns) {
              if (part == 0 && __all(ln < 8 || fl >= 4)) break;
              __builtin_amdgcn_s_sleep(6);
            }
          }
          if (ps != ns) {
            ps = ns;
            if (ln < 8) lds_st(lds + L_FLG + 32 + ln * 4, ps);
          }
        }
      } else {
        if (part) lds_wait_ge<4>(lds + F_AFREE, 4u); else lds_wait_ge<4>(lds + L_FLG + 32, 6u);
        const u32x4 rsO = mkrsrc(part ? bwo : awo, DM * DM * 2);
#pragma unroll 1
        for (u32 pp = hw; pp < 25; pp += 3) {
          const u32 c = 64u * pp + ln, rho = (c * 6554u) >> 16, cc = c - rho * 10u;
          const u32 U = 4u * (rho >> 5) + ((rho >> 2) & 3u), u = (rho >> 4) & 1u, q = rho & 3u;
          const u32 hp = (U * 205u) >> 10, un = U - hp * 5u;
          const u32 row = 40u * hp + q + ((un < 4u) ? (un >> 1) * 20u + 4u * (un & 1u) + 8u * u : u * 20u + 16u);
          const u32 vo = row * 1280u + cc * 16u;
#pragma unroll
          for (u32 per = 0; per < 4; ++per) dma1(rsO, vo, per * 204800u + (u32)cgp * 160u, lds0 + L_IMG + (per * 25u + pp) * 1024u);
        }
        DRAIN();
        lds_arrive(lds + (part ? F_BIMG : F_AOK));
        if (part == 0 && hw == 0) { flags_wait<4>(rW, WS_FLGA + (u32)(t64 >> 1) * 128u, 8, tag); lds_arrive(lds + F_AOK); }
      }
      if (part == 0) lds_wait_ge<4>(lds + F_AOK, 4u);
      else { lds_wait_ge<4>(lds + F_FIN, FINW); TSTAMP(3); flags_wait<BSLEEP>(rW, WS_FLGB + (u32)t64 * 256u + (u32)kh * 128u, 16, tag); TSTAMP(4); FSTAMP(7); }
      u32 s0 = WS_OFR + (u32)(part * 2621440 + (4 * t64 + 2 * (hw & 1)) * 20480 + kh * 10240), s1 = s0 + 20480u, it;
      u32 pa = lds0 + F_BIMG;
      asm volatile(TILE_GEMM_ASM
                   : "+{v[0:15]}"(ac0), "+{v[16:31]}"(ac1), "+{v[32:39]}"(ac2), "+{v142}"(pa), [s0] "+s"(s0), [s1] "+s"(s1), [it] "=&s"(it)
                   : "{v140}"(vl), "{v141}"(lds0 + L_IMG + (u32)(4 * (ln >> 4) + ((ln & 15) >> 2)) * 160u + (u32)(ln & 3) * 40u + (u32)kh * 51200u), [rs] "s"(rsWS), [want] "s"(part ? 3u : 0u),
                     [afree] "s"(lds0 + F_AFREE)
                   : "memory", "scc", "vcc", TILE_GEMM_CLOB);
      if (part == 0) HSTAMP(7);
    }
    TSTAMP(5);
    {
      const int ln = lane_id();
      f32x4 acc[5][2];
#pragma unroll
      for (int n = 0; n < 2; ++n) {
        acc[0][n] = (f32x4){ac0[4 * n], ac0[4 * n + 1], ac0[4 * n + 2], ac0[4 * n + 3]}; acc[1][n] = (f32x4){ac0[8 + 4 * n], ac0[9 + 4 * n], ac0[10 + 4 * n], ac0[11 + 4 * n]};
        acc[2][n] = (f32x4){ac1[4 * n], ac1[4 * n + 1], ac1[4 * n + 2], ac1[4 * n + 3]}; acc[3][n] = (f32x4){ac1[8 + 4 * n], ac1[9 + 4 * n], ac1[10 + 4 * n], ac1[11 + 4 * n]};
        acc[4][n] = (f32x4){ac2[4 * n], ac2[4 * n + 1], ac2[4 * n + 2], ac2[4 * n + 3]};
      }
      ldsp xb = lds + L_RING + 3 * SL;
      ldsp xw = xb + (u32)hw * 5120u + ln * 16, xr = xb + (u32)(hw ^ 2) * 5120u + ln * 16;
#pragma unroll
      for (int T = 0; T < 5; ++T) *(LDSAS f32x4*)(xw + T * 1024) = kh ? acc[T][0] : acc[T][1];
      lds_arrive(lds + F_GBAR + 48 + (hw & 1) * 4);
      lds_wait_ge<0>(lds + F_GBAR + 48 + (hw & 1) * 4, 2u);
      TSTAMP(6);
      u32 o[10];
#pragma unroll
      for (int T = 0; T < 5; ++T) {
        const f32x4 v = (kh ? acc[T][1] : acc[T][0]) + *(LDSAS f32x4*)(xr + T * 1024);
        o[2 * T] = pk2(v[0], v[1]); o[2 * T + 1] = pk2(v[2], v[3]);
      }
      typedef u32x4 u32x4a __attribute__((aligned(8)));
      char* dst = out + (size_t)(64 * t64 + 32 * (hw & 1) + 16 * kh + (ln & 15)) * 1280 + cgp * 160 + (ln >> 4) * 40;
      *(u32x4a*)dst = (u32x4){o[0], o[1], o[2], o[3]};
      *(u32x4a*)(dst + 16) = (u32x4){o[4], o[5], o[6], o[7]};
      *(u32x2*)(dst + 32) = (u32x2){o[8], o[9]};
    }
    STAMP_FLUSH();
    return;
  }

  const int pr = w & 3, par = w >> 2, head = 4 * hg + pr, t64 = 2 * tt + type;
  u32x4 q1[2]; u32x2 q2[2];
  f32x16 O0, O1;
  {
    const int lane = lane_id(), r32 = lane & 31, hh = lane >> 5;
    if (type) {
      lds_wait_ge<1>(lds + F_QOK, 1);
#pragma unroll
      for (int j = 0; j < 2; ++j) {
        const u32 qrow = WS_QB + (u32)(tt * 8 + hg) * 10240u + (u32)(2 * pr + j) * 1280u;
        q1[j] = gl128(rW, lane * 16, qrow);
        q2[j] = (u32x2){gl32(rW, r32 * 8 + hh * 4, qrow + 1024), 0u};
      }
      lds_arrive(lds + F_QOK);
    } else {
#pragma unroll
      for (int j = 0; j < 2; ++j) {
        ldsp qrow = lds + K1_QST + (u32)(2 * pr + j) * 1280u;
        q1[j] = ld128(qrow + lane * 16);
        q2[j] = (u32x2){*(LDSAS u32*)(qrow + 1024 + r32 * 8 + hh * 4), 0u};
      }
    }
    const u32x4 k1a = ld128(lds + L_K01 + lane * 16);
    const u32 k2a = *(LDSAS u32*)(lds + L_K01 + 1024 + lane * 4);
    u32x4 k1 = ld128(lds + L_K01 + par * 1280 + lane * 16);
    u32x2 k2 = {*(LDSAS u32*)(lds + L_K01 + par * 1280 + 1024 + lane * 4), hh ? 0u : 0x00003F80u};
    f32x16 S0, S1, S2, S3;
#pragma unroll
    for (int j = 0; j < 2; ++j) {
      f32x16 t = mfma32(k1a, q1[j], zero16);
      t = mfma32k8((u32x2){k2a, 0u}, q2[j], t);
      const float m = xhalf_max(max16(t));
      q2[j][1] = hh ? 0u : (pk2(-m, 0.f) & 0xffffu);
    }
    S0 = mfma32(k1, q1[0], zero16);
    S0 = mfma32k8(k2, q2[0], S0);
    u32x4 vlo, vhi;
    const u32 rb = lds0 + L_RING + (u32)par * REC;
    u32 aK1 = rb + (u32)lane * 16u, aK2 = rb + 1024u + (u32)lane * 4u;
    u32 aV = rb + 1280u + (u32)hh * 352u + (u32)(r32 <= 20 ? r32 : 21) * 16u;
    u32 aF = lds0 + L_FLG + (u32)w * 4u, tmp;
    HSTAMP(5); FSTAMP(5);
    u32 cnt, inc, slot, rdy;
    asm volatile(ATTN_LOOP_ASM
                 : "=&{v[0:15]}"(O0), "=&{v[16:31]}"(O1), "+{v[32:47]}"(S0), "=&{v[48:63]}"(S1), "=&{v[64:79]}"(S2), "=&{v[80:95]}"(S3),
                   "+{v[108:111]}"(k1), "+{v[112:113]}"(k2), "=&{v[116:119]}"(vlo), "=&{v[120:123]}"(vhi),
                   "+{v124}"(aK1), "+{v125}"(aK2), "+{v126}"(aV), "=&{v115}"(tmp), [cnt] "=&s"(cnt), [inc] "=&s"(inc), [slot] "=&s"(slot), [rdy] "=&s"(rdy)
                 : "{v[96:99]}"(q1[0]), "{v[100:103]}"(q1[1]), "{v[104:105]}"(q2[0]), "{v[106:107]}"(q2[1]), "{v114}"(0xDDDD), "{v127}"(aF),
                   [wrap] "s"(0u - 4u * SL), [stg] "s"(SL), [par] "s"(par)
                 : "memory", "scc");
  }
  __builtin_amdgcn_s_setprio(3);
  HSTAMP(6); TSTAMP(2); FSTAMP(6);
  const int lane = lane_id(), r32 = lane & 31, hh = lane >> 5;
  ldsp mg = lds + L_MRG + (u32)pr * (2 * REC);
  if (lds_arrive(lds + L_FLG + 64 + pr * 4) == 0) {
#pragma unroll 1
    for (u32 it = 0; it < SPIN_CAP; ++it) {
      if (__all(lds_ld(lds + L_FLG + (lane & 7) * 4) >= 13u)) break;
      __builtin_amdgcn_s_sleep(2);
    }
    asm volatile("" ::: "memory");
#pragma unroll
    for (int j = 0; j < 2; ++j) {
      const f32x16 snd = j ? O1 : O0;
      ldsp mw = mg + j * REC;
      *(LDSAS f32x4*)(mw + lane * 16) = (f32x4){snd[0], snd[1], snd[2], snd[3]};
      *(LDSAS f32x4*)(mw + 1024 + lane * 16) = (f32x4){snd[4], snd[5], snd[6], snd[7]};
      if (!hh) *(LDSAS f32x4*)(mw + 2048 + r32 * 16) = (f32x4){snd[8], snd[9], snd[10], snd[11]};
      else *(LDSAS float*)(mw + 2560 + r32 * 4) = snd[8];
    }
    asm volatile("s_waitcnt lgkmcnt(0)" ::: "memory");
    lds_st(lds + L_FLG + 80 + pr * 4, 1u);
    TSTAMP(3);
    STAMP_FLUSH();
    return;
  }
  lds_wait_ge<FSLEEP>(lds + L_FLG + 80 + pr * 4, 1u);
  TSTAMP(5);
  const u32x2 tag = ld64(lds + F_TAG);
#pragma unroll
  for (int j = 0; j < 2; ++j) {
    f32x16 O = j ? O1 : O0;
    ldsp mr = mg + j * REC;
    const f32x4 a = *(LDSAS f32x4*)(mr + lane * 16), b = *(LDSAS f32x4*)(mr + 1024 + lane * 16), c = *(LDSAS f32x4*)(mr + 2048 + r32 * 16);
    float l = from_hi(O[8]) + *(LDSAS float*)(mr + 2560 + r32 * 4);
    const float gt = *(LDSAS float*)(lds + L_GT + (32 * j + r32) * 4);
#pragma unroll
    for (int i = 0; i < 4; ++i) { O[i] += a[i]; O[4 + i] += b[i]; O[8 + i] += c[i]; }
    float t = 0.f;
#pragma unroll
    for (int r = 0; r < 12; ++r) t = __builtin_fmaf(O[r], 0.f, t);
    const bool bad = !(t == 0.f) || !(l < 1.0e30f);
    if (__builtin_expect(__any(bad), 0)) {
      O = attn_exact(rW, q1[j], q2[j][0], lane);
      l = from_hi(O[8]);
    }
    const float sb = (1.f - gt) * __builtin_amdgcn_rcpf(l);
    const u32 vo = WS_OFR + 2621440u + (u32)(4 * t64 + 2 * j + (r32 >> 4)) * 20480u + (r32 & 15) * 16 + (head >> 1) * 1280;
    st128(rW, vo + hh * 256 + (head & 1) * 512, (u32x4){pk2(O[0] * sb, O[1] * sb), pk2(O[2] * sb, O[3] * sb), pk2(O[4] * sb, O[5] * sb), pk2(O[6] * sb, O[7] * sb)});
    st64(rW, hh ? OOB : vo + 1024 + (head & 1) * 8, (u32x2){pk2(O[8] * sb, O[9] * sb), pk2(O[10] * sb, O[11] * sb)});
  }
  HSTAMP(7); TSTAMP(6);
  DRAIN();
  TSTAMP(7); FSTAMP(7);
  st64(rW, lane == 0 ? WS_FLGB + (u32)(t64 * 32 + head) * 8u : OOB, tag);
  lds_arrive(lds + F_FIN);
  STAMP_FLUSH();
}

extern "C" void kernel_launch(void* const* d_in, const int* in_sizes, int n_in, void* d_out, int out_size, void* d_ws,
                              size_t ws_size, hipStream_t stream) {
  (void)in_sizes; (void)n_in; (void)out_size; (void)ws_size;
  (void)hipFuncSetAttribute((const void*)gm_fused, hipFuncAttributeMaxDynamicSharedMemorySize, LDS_BYTES);
  gm_fused<<<256, 768, LDS_BYTES, stream>>>((const char*)d_in[0], (const char*)d_in[1], (const char*)d_in[2], (const char*)d_in[5], (const char*)d_in[6],
      (const char*)d_in[7], (const char*)d_in[9], (const char*)d_in[3], (const char*)d_in[8], (char*)d_ws, (char*)d_out);
}
```

```cpp
#include <hip/hip_runtime.h>
#include <hip/hip_bf16.h>
#include <stdint.h>

typedef unsigned int u32;
typedef unsigned long long u64;
typedef u32 u32x2 __attribute__((ext_vector_type(2)));
typedef u32 u32x4 __attribute__((ext_vector_type(4)));
typedef float f32x2 __attribute__((ext_vector_type(2)));
typedef float f32x4 __attribute__((ext_vector_type(4)));
typedef float f32x8 __attribute__((ext_vector_type(8)));
typedef float f32x16 __attribute__((ext_vector_type(16)));
typedef __bf16 bf16x2 __attribute__((ext_vector_type(2)));
typedef __bf16 bf16x8 __attribute__((ext_vector_type(8)));
typedef __bf16 bf16x16 __attribute__((ext_vector_type(16)));
typedef short s16x4 __attribute__((ext_vector_type(4)));
#define LDSAS __attribute__((address_space(3)))
typedef LDSAS char* ldsp;
typedef __attribute__((address_space(1))) u64 gu64;

constexpr int SEQ = 2048, DM = 640, NH = 32, DH = 20;
constexpr float LOG2E = 1.4426950408889634f;
constexpr float QSCALE = 1.4426950408889634f * 0.22360679774997896f;

constexpr u32 REC = 2688;
constexpr u32 WS_QB   = 0;
constexpr u32 WS_KVIM = WS_QB + 1310720;
constexpr u32 WS_KVP  = WS_KVIM + 64 * REC;
constexpr u32 WS_KVT  = WS_KVP + 32 * 16 * 3360;
constexpr u32 WS_GATE = WS_KVT + 32 * 1680;
constexpr u32 WS_OFR  = WS_GATE + 8192;
constexpr u32 WS_F1   = WS_OFR + 5242880;
constexpr u32 WS_F2   = WS_F1 + 16384;
constexpr u32 WS_F3   = WS_F2 + 1024;
constexpr u32 WS_FLGA = WS_F3 + 1024;
constexpr u32 WS_FLGB = WS_FLGA + 2048;
constexpr u32 WS_FQ   = WS_FLGB + 8192;
constexpr u32 WS_EPOCH = WS_FQ + 16384;
constexpr u32 WS_END  = WS_EPOCH + 256;
constexpr u32 OOB = 0x40000000u;
constexpr u32 SPIN_CAP = 1u << 20;

#define SBAR() __builtin_amdgcn_sched_barrier(0)
#define PIN(x) asm volatile("" : "+v"(x))
#define LBAR() asm volatile("s_waitcnt lgkmcnt(0)\n\ts_barrier" ::: "memory")
#define WAIT_BAR(N) asm volatile("s_waitcnt vmcnt(" #N ") lgkmcnt(0)\n\ts_barrier" ::: "memory")
#define DRAIN() asm volatile("s_waitcnt vmcnt(0)" ::: "memory")

__device__ __forceinline__ int lane_id() {
  int l;
  asm volatile("v_mbcnt_lo_u32_b32 %0, -1, 0\n\tv_mbcnt_hi_u32_b32 %0, -1, %0" : "=v"(l));
  return l;
}
__device__ __forceinline__ u32 pk2(float a, float b) {
  f32x2 v = {a, b};
  return __builtin_bit_cast(u32, __builtin_convertvector(v, bf16x2));
}
__device__ __forceinline__ f32x4 mfma16(u32x4 a, u32x4 b, f32x4 c) {
  return __builtin_amdgcn_mfma_f32_16x16x32_bf16(__builtin_bit_cast(bf16x8, a), __builtin_bit_cast(bf16x8, b), c, 0, 0, 0);
}
__device__ __forceinline__ f32x16 mfma32(u32x4 a, u32x4 b, f32x16 c) {
  return __builtin_amdgcn_mfma_f32_32x32x16_bf16(__builtin_bit_cast(bf16x8, a), __builtin_bit_cast(bf16x8, b), c, 0, 0, 0);
}
__device__ __forceinline__ f32x16 mfma32k8(u32x2 a, u32x2 b, f32x16 c) {
  return __builtin_amdgcn_mfma_f32_32x32x8bf16_1k(__builtin_bit_cast(s16x4, a), __builtin_bit_cast(s16x4, b), c, 0, 0, 0);
}
__device__ __forceinline__ f32x16 smf(u32x4 a, f32x8 b, f32x16 c, int idx) {
  return __builtin_amdgcn_smfmac_f32_32x32x32_bf16(__builtin_bit_cast(bf16x8, a), __builtin_bit_cast(bf16x16, b), c, idx, 0, 0);
}
__device__ __forceinline__ u32x2 trd(ldsp p) {
  return __builtin_bit_cast(u32x2, __builtin_amdgcn_ds_read_tr16_b64_v4i16((LDSAS s16x4*)p));
}
__device__ __forceinline__ u32x4 trd2(ldsp p0, ldsp p1) {
  u32x2 a = trd(p0), b = trd(p1);
  return (u32x4){a[0], a[1], b[0], b[1]};
}
__device__ __forceinline__ u32x4 ld128(ldsp p) { return *(LDSAS u32x4*)p; }
__device__ __forceinline__ u32x2 ld64(ldsp p) { return *(LDSAS u32x2*)p; }
__device__ __forceinline__ float bflo(u32 v) { return __uint_as_float(v << 16); }
__device__ __forceinline__ float bfhi(u32 v) { return __uint_as_float(v & 0xffff0000u); }

__device__ __forceinline__ u32x4 mkrsrc(const void* p, u32 bytes) {
  uint64_t a = (uint64_t)p;
  return (u32x4){(u32)__builtin_amdgcn_readfirstlane((u32)a), (u32)__builtin_amdgcn_readfirstlane((u32)(a >> 32) & 0xffffu), bytes, 0x00020000u};
}
__device__ __forceinline__ void dma1(u32x4 rs, u32 voff, u32 soff, u32 ldsaddr) {
  u32 keep;
  asm volatile("s_mov_b32 %0, m0\n\ts_mov_b32 m0, %4\n\ts_nop 2\n\tbuffer_load_dwordx4 %1, %2, %3 offen lds\n\ts_mov_b32 m0, %0"
               : "=&s"(keep) : "v"(voff), "s"(rs), "s"(__builtin_amdgcn_readfirstlane(soff)), "s"(__builtin_amdgcn_readfirstlane(ldsaddr)) : "memory");
}
__device__ __forceinline__ void dma1s(u32x4 rs, u32 voff, u32 soff, u32 ldsaddr) {
  u32 keep;
  asm volatile("s_mov_b32 %0, m0\n\ts_mov_b32 m0, %4\n\ts_nop 2\n\tbuffer_load_dwordx4 %1, %2, %3 offen sc1 lds\n\ts_mov_b32 m0, %0"
               : "=&s"(keep) : "v"(voff), "s"(rs), "s"(__builtin_amdgcn_readfirstlane(soff)), "s"(__builtin_amdgcn_readfirstlane(ldsaddr)) : "memory");
}
__device__ __forceinline__ void dma4(u32x4 rs, u32 voff, u32 soff, u32 ldsaddr) {
  u32 keep;
  asm volatile("s_mov_b32 %0, m0\n\ts_mov_b32 m0, %4\n\ts_nop 2\n\t"
               "buffer_load_dwordx4 %1, %2, %3 offen sc1 lds\n\tbuffer_load_dwordx4 %1, %2, %3 offen offset:1024 sc1 lds\n\t"
               "buffer_load_dwordx4 %1, %2, %3 offen offset:2048 sc1 lds\n\tbuffer_load_dwordx4 %1, %2, %3 offen offset:3072 sc1 lds\n\t"
               "s_mov_b32 m0, %0"
               : "=&s"(keep) : "v"(voff), "s"(rs), "s"(__builtin_amdgcn_readfirstlane(soff)), "s"(__builtin_amdgcn_readfirstlane(ldsaddr)) : "memory");
}
__device__ __forceinline__ void dma2h(u32x4 rs, u32 voff, u32 soff, u32 ldsaddr) {
  u32 keep;
  asm volatile("s_mov_b32 %0, m0\n\ts_mov_b32 m0, %4\n\ts_nop 2\n\t"
               "buffer_load_dwordx4 %1, %2, %3 offen sc1 lds\n\tbuffer_load_dwordx4 %1, %2, %3 offen offset:1024 sc1 lds\n\t"
               "s_mov_b32 exec_hi, 0\n\tbuffer_load_dwordx4 %1, %2, %3 offen offset:2048 sc1 lds\n\ts_mov_b32 exec_hi, -1\n\t"
               "s_mov_b32 m0, %0"
               : "=&s"(keep) : "v"(voff), "s"(rs), "s"(__builtin_amdgcn_readfirstlane(soff)), "s"(__builtin_amdgcn_readfirstlane(ldsaddr)) : "memory");
}
typedef __amdgpu_buffer_rsrc_t rsrc_t;
__device__ __forceinline__ void st128(rsrc_t r, u32 off, u32x4 v) { __builtin_amdgcn_raw_buffer_store_b128(v, r, (int)off, 0, 16); }
__device__ __forceinline__ void st64(rsrc_t r, u32 off, u32x2 v) { __builtin_amdgcn_raw_buffer_store_b64(v, r, (int)off, 0, 16); }
__device__ __forceinline__ void st32(rsrc_t r, u32 off, u32 v) { __builtin_amdgcn_raw_buffer_store_b32(v, r, (int)off, 0, 16); }
__device__ __forceinline__ void st16(rsrc_t r, u32 off, u32 v) { __builtin_amdgcn_raw_buffer_store_b16((unsigned short)v, r, (int)off, 0, 16); }
__device__ __forceinline__ u32x4 gl128(rsrc_t r, u32 voff, u32 soff) { return __builtin_amdgcn_raw_buffer_load_b128(r, (int)voff, (int)soff, 16); }
__device__ __forceinline__ u32x2 gl64(rsrc_t r, u32 voff, u32 soff) { return __builtin_amdgcn_raw_buffer_load_b64(r, (int)voff, (int)soff, 16); }
__device__ __forceinline__ u32 gl32(rsrc_t r, u32 voff, u32 soff) { return __builtin_amdgcn_raw_buffer_load_b32(r, (int)voff, (int)soff, 16); }

__device__ __forceinline__ f32x8 lo8(const f32x16& s) { return __builtin_shufflevector(s, s, 0, 1, 2, 3, 4, 5, 6, 7); }
__device__ __forceinline__ f32x8 hi8(const f32x16& s) { return __builtin_shufflevector(s, s, 8, 9, 10, 11, 12, 13, 14, 15); }
__device__ __forceinline__ float max16(const f32x16& s) {
  float a = fmaxf(fmaxf(s[0], s[1]), s[2]), b = fmaxf(fmaxf(s[3], s[4]), s[5]);
  a = fmaxf(fmaxf(a, s[6]), s[7]); b = fmaxf(fmaxf(b, s[8]), s[9]);
  a = fmaxf(fmaxf(a, s[10]), s[11]); b = fmaxf(fmaxf(b, s[12]), s[13]);
  a = fmaxf(fmaxf(a, s[14]), s[15]);
  return fmaxf(a, b);
}
__device__ __forceinline__ float xhalf_max(float m) {
  auto r = __builtin_amdgcn_permlane32_swap(__float_as_uint(m), __float_as_uint(m), false, false);
  return fmaxf(__uint_as_float(r[0]), __uint_as_float(r[1]));
}
__device__ __forceinline__ float from_hi(float v) {
  auto r = __builtin_amdgcn_permlane32_swap(__float_as_uint(v), __float_as_uint(v), false, false);
  return __uint_as_float(r[1]);
}
__device__ __forceinline__ u32 lds_ld(ldsp p) { return __hip_atomic_load((LDSAS u32*)p, __ATOMIC_RELAXED, __HIP_MEMORY_SCOPE_WORKGROUP); }
__device__ __forceinline__ void lds_st(ldsp p, u32 v) { __hip_atomic_store((LDSAS u32*)p, v, __ATOMIC_RELAXED, __HIP_MEMORY_SCOPE_WORKGROUP); }
template <int SLEEP>
__device__ __forceinline__ void lds_wait_ge(ldsp p, u32 want) {
#pragma unroll 1
  for (u32 it = 0; it < SPIN_CAP; ++it) {
    if ((u32)__builtin_amdgcn_readfirstlane(lds_ld(p)) >= want) break;
    __builtin_amdgcn_s_sleep(SLEEP);
  }
  asm volatile("" ::: "memory");
}
__device__ __forceinline__ u32 lds_arrive(ldsp p) {
  u32 old; u64 keep;
  asm volatile("v_mov_b32 %0, 1\n\ts_mov_b64 %1, exec\n\ts_mov_b64 exec, 1\n\ts_waitcnt lgkmcnt(0)\n\tds_add_rtn_u32 %0, %2, %0\n\t"
               "s_waitcnt lgkmcnt(0)\n\ts_mov_b64 exec, %1" : "=&v"(old), "=&s"(keep) : "v"((u32)(uintptr_t)p) : "memory");
  return (u32)__builtin_amdgcn_readfirstlane(old);
}
template <int SLEEP>
__device__ __forceinline__ void flags_wait(rsrc_t r, u32 off, int n, u32x2 tag) {
  const int lane = lane_id();
  const u32 vo = (u32)(lane < n ? lane : 0) * 8u;
#pragma unroll 1
  for (u32 it = 0; it < SPIN_CAP; ++it) {
    asm volatile("" ::: "memory");
    const u32x2 v = gl64(r, vo, off);
    if (__all(v[0] == tag[0] && v[1] == tag[1])) break;
    __builtin_amdgcn_s_sleep(SLEEP);
  }
  asm volatile("" ::: "memory");
}
template <int SLEEP>
__device__ __forceinline__ void flags_wait256(rsrc_t r, u32 off, u32x2 tag) {
  const u32 vo = (u32)lane_id() * 16u;
#pragma unroll 1
  for (u32 it = 0; it < SPIN_CAP; ++it) {
    asm volatile("" ::: "memory");
    const u32x4 a = gl128(r, vo, off), b = gl128(r, vo, off + 1024);
    const u32 d = (a[0] ^ tag[0]) | (a[1] ^ tag[1]) | (a[2] ^ tag[0]) | (a[3] ^ tag[1]) | (b[0] ^ tag[0]) | (b[1] ^ tag[1]) | (b[2] ^ tag[0]) | (b[3] ^ tag[1]);
    if (__all(d == 0u)) break;
    __builtin_amdgcn_s_sleep(SLEEP);
  }
  asm volatile("" ::: "memory");
}

#define STAMP(i) do {} while (0)
#define RSTAMP(i) do {} while (0)
#define STAMP_INIT()
#define STAMP_FLUSH()

#define ATTN_LOOP_ASM \
  "s_nop 11\n\t" \
  "v_mov_b64 v[0:1], 0\n\t" \
  "v_mov_b64 v[2:3], 0\n\t" \
  "v_mov_b64 v[4:5], 0\n\t" \
  "v_mov_b64 v[6:7], 0\n\t" \
  "v_mov_b64 v[8:9], 0\n\t" \
  "v_mov_b64 v[10:11], 0\n\t" \
  "v_mov_b64 v[12:13], 0\n\t" \
  "v_mov_b64 v[14:15], 0\n\t" \
  "v_mov_b64 v[16:17], 0\n\t" \
  "v_mov_b64 v[18:19], 0\n\t" \
  "v_mov_b64 v[20:21], 0\n\t" \
  "v_mov_b64 v[22:23], 0\n\t" \
  "v_mov_b64 v[24:25], 0\n\t" \
  "v_mov_b64 v[26:27], 0\n\t" \
  "v_mov_b64 v[28:29], 0\n\t" \
  "v_mov_b64 v[30:31], 0\n\t" \
  "v_mov_b64 v[80:81], 0\n\t" \
  "v_mov_b64 v[82:83], 0\n\t" \
  "v_mov_b64 v[84:85], 0\n\t" \
  "v_mov_b64 v[86:87], 0\n\t" \
  "v_mov_b64 v[88:89], 0\n\t" \
  "v_mov_b64 v[90:91], 0\n\t" \
  "v_mov_b64 v[92:93], 0\n\t" \
  "v_mov_b64 v[94:95], 0\n\t" \
  "v_mov_b64 v[116:117], 0\n\t" \
  "v_mov_b64 v[118:119], 0\n\t" \
  "v_mov_b64 v[120:121], 0\n\t" \
  "v_mov_b64 v[122:123], 0\n\t" \
  "s_mov_b32 %[cnt], 0\n\t" \
  "s_mov_b32 %[slot], 0\n\t" \
  "s_setprio 0\n\t" \
  "2:\n\t" \
  "s_mov_b32 %[inc], 0\n\t" \
  "3:\n\t" \
  "ds_read_b32 v115, v127 offset:32\n\t" \
  "s_add_u32 %[inc], %[inc], 1\n\t" \
  "s_waitcnt lgkmcnt(0)\n\t" \
  "v_readfirstlane_b32 %[rdy], v115\n\t" \
  "s_nop 3\n\t" \
  "s_cmp_gt_u32 %[rdy], %[cnt]\n\t" \
  "s_cbranch_scc1 6f\n\t" \
  "s_sleep 1\n\t" \
  "s_cmp_lt_u32 %[inc], 0x100000\n\t" \
  "s_cbranch_scc1 3b\n\t" \
  "s_branch 9f\n\t" \
  "6:\n\t" \
  "s_cmp_lg_u32 %[par], 0\n\t" \
  "s_cbranch_scc1 5f\n\t" \
  "4:\n\t" \
  "v_mfma_f32_32x32x16_bf16 v[48:63], v[108:111], v[100:103], 0\n\t" \
  "ds_read_b128 v[108:111], v124 offset:0\n\t" \
  "v_exp_f32 v32, v32\n\t" \
  "v_exp_f32 v33, v33\n\t" \
  "v_exp_f32 v34, v34\n\t" \
  "v_exp_f32 v35, v35\n\t" \
  "v_mfma_f32_32x32x8_bf16 v[48:63], v[112:113], v[106:107], v[48:63]\n\t" \
  "ds_read_b32 v112, v125 offset:0\n\t" \
  "v_exp_f32 v36, v36\n\t" \
  "v_exp_f32 v37, v37\n\t" \
  "v_exp_f32 v38, v38\n\t" \
  "v_exp_f32 v39, v39\n\t" \
  "v_smfmac_f32_32x32x32_bf16 v[16:31], v[116:119], v[80:87], v114\n\t" \
  "ds_read_b128 v[116:119], v126 offset:0\n\t" \
  "v_exp_f32 v40, v40\n\t" \
  "v_exp_f32 v41, v41\n\t" \
  "v_exp_f32 v42, v42\n\t" \
  "v_exp_f32 v43, v43\n\t" \
  "v_smfmac_f32_32x32x32_bf16 v[16:31], v[120:123], v[88:95], v114\n\t" \
  "ds_read_b128 v[120:123], v126 offset:704\n\t" \
  "v_exp_f32 v44, v44\n\t" \
  "v_exp_f32 v45, v45\n\t" \
  "v_exp_f32 v46, v46\n\t" \
  "v_exp_f32 v47, v47\n\t" \
  "s_waitcnt lgkmcnt(2)\n\t" \
  "v_mfma_f32_32x32x16_bf16 v[64:79], v[108:111], v[96:99], 0\n\t" \
  "v_exp_f32 v48, v48\n\t" \
  "v_exp_f32 v49, v49\n\t" \
  "v_exp_f32 v50, v50\n\t" \
  "v_exp_f32 v51, v51\n\t" \
  "v_mfma_f32_32x32x8_bf16 v[64:79], v[112:113], v[104:105], v[64:79]\n\t" \
  "v_exp_f32 v52, v52\n\t" \
  "v_exp_f32 v53, v53\n\t" \
  "v_exp_f32 v54, v54\n\t" \
  "v_exp_f32 v55, v55\n\t" \
  "s_waitcnt lgkmcnt(0)\n\t" \
  "v_smfmac_f32_32x32x32_bf16 v[0:15], v[116:119], v[32:39], v114\n\t" \
  "v_exp_f32 v56, v56\n\t" \
  "v_exp_f32 v57, v57\n\t" \
  "v_exp_f32 v58, v58\n\t" \
  "v_exp_f32 v59, v59\n\t" \
  "v_smfmac_f32_32x32x32_bf16 v[0:15], v[120:123], v[40:47], v114\n\t" \
  "v_exp_f32 v60, v60\n\t" \
  "v_exp_f32 v61, v61\n\t" \
  "v_exp_f32 v62, v62\n\t" \
  "v_exp_f32 v63, v63\n\t" \
  "v_mfma_f32_32x32x16_bf16 v[80:95], v[108:111], v[100:103], 0\n\t" \
  "ds_read_b128 v[108:111], v124 offset:5376\n\t" \
  "v_exp_f32 v64, v64\n\t" \
  "v_exp_f32 v65, v65\n\t" \
  "v_exp_f32 v66, v66\n\t" \
  "v_exp_f32 v67, v67\n\t" \
  "v_mfma_f32_32x32x8_bf16 v[80:95], v[112:113], v[106:107], v[80:95]\n\t" \
  "ds_read_b32 v112, v125 offset:5376\n\t" \
  "v_exp_f32 v68, v68\n\t" \
  "v_exp_f32 v69, v69\n\t" \
  "v_exp_f32 v70, v70\n\t" \
  "v_exp_f32 v71, v71\n\t" \
  "v_smfmac_f32_32x32x32_bf16 v[16:31], v[116:119], v[48:55], v114\n\t" \
  "ds_read_b128 v[116:119], v126 offset:5376\n\t" \
  "v_exp_f32 v72, v72\n\t" \
  "v_exp_f32 v73, v73\n\t" \
  "v_exp_f32 v74, v74\n\t" \
  "v_exp_f32 v75, v75\n\t" \
  "v_smfmac_f32_32x32x32_bf16 v[16:31], v[120:123], v[56:63], v114\n\t" \
  "ds_read_b128 v[120:123], v126 offset:6080\n\t" \
  "s_add_u32 %[cnt], %[cnt], 1\n\t" \
  "s_cmp_eq_u32 %[slot], 4\n\t" \
  "s_cselect_b32 %[inc], %[wrap], %[stg]\n\t" \
  "v_exp_f32 v76, v76\n\t" \
  "v_exp_f32 v77, v77\n\t" \
  "v_exp_f32 v78, v78\n\t" \
  "v_exp_f32 v79, v79\n\t" \
  "s_waitcnt lgkmcnt(2)\n\t" \
  "v_mfma_f32_32x32x16_bf16 v[32:47], v[108:111], v[96:99], 0\n\t" \
  "ds_read_b32 v115, v127 offset:32\n\t" \
  "s_cselect_b32 %[slot], -1, %[slot]\n\t" \
  "s_add_u32 %[slot], %[slot], 1\n\t" \
  "v_exp_f32 v80, v80\n\t" \
  "v_exp_f32 v81, v81\n\t" \
  "v_exp_f32 v82, v82\n\t" \
  "v_exp_f32 v83, v83\n\t" \
  "v_mfma_f32_32x32x8_bf16 v[32:47], v[112:113], v[104:105], v[32:47]\n\t" \
  "v_add_u32 v124, %[inc], v124\n\t" \
  "v_add_u32 v125, %[inc], v125\n\t" \
  "v_add_u32 v126, %[inc], v126\n\t" \
  "v_exp_f32 v84, v84\n\t" \
  "v_exp_f32 v85, v85\n\t" \
  "v_exp_f32 v86, v86\n\t" \
  "v_exp_f32 v87, v87\n\t" \
  "s_waitcnt lgkmcnt(1)\n\t" \
  "v_smfmac_f32_32x32x32_bf16 v[0:15], v[116:119], v[64:71], v114\n\t" \
  "v_exp_f32 v88, v88\n\t" \
  "v_exp_f32 v89, v89\n\t" \
  "v_exp_f32 v90, v90\n\t" \
  "v_exp_f32 v91, v91\n\t" \
  "s_waitcnt lgkmcnt(0)\n\t" \
  "v_readfirstlane_b32 %[rdy], v115\n\t" \
  "v_mov_b32 v115, %[cnt]\n\t" \
  "v_smfmac_f32_32x32x32_bf16 v[0:15], v[120:123], v[72:79], v114\n\t" \
  "ds_write_b32 v127, v115\n\t" \
  "s_cmp_gt_u32 %[rdy], %[cnt]\n\t" \
  "v_exp_f32 v92, v92\n\t" \
  "v_exp_f32 v93, v93\n\t" \
  "v_exp_f32 v94, v94\n\t" \
  "v_exp_f32 v95, v95\n\t" \
  "s_cbranch_scc1 4b\n\t" \
  "s_cmp_ge_u32 %[cnt], 16\n\t" \
  "s_cbranch_scc1 9f\n\t" \
  "s_branch 2b\n\t" \
  "5:\n\t" \
  "v_mfma_f32_32x32x16_bf16 v[48:63], v[108:111], v[100:103], 0\n\t" \
  "ds_read_b128 v[108:111], v124 offset:0\n\t" \
  "s_setprio 1\n\t" \
  "v_exp_f32 v32, v32\n\t" \
  "v_exp_f32 v33, v33\n\t" \
  "v_exp_f32 v34, v34\n\t" \
  "v_exp_f32 v35, v35\n\t" \
  "v_mfma_f32_32x32x8_bf16 v[48:63], v[112:113], v[106:107], v[48:63]\n\t" \
  "ds_read_b32 v112, v125 offset:0\n\t" \
  "v_exp_f32 v36, v36\n\t" \
  "v_exp_f32 v37, v37\n\t" \
  "v_exp_f32 v38, v38\n\t" \
  "v_exp_f32 v39, v39\n\t" \
  "v_smfmac_f32_32x32x32_bf16 v[16:31], v[116:119], v[80:87], v114\n\t" \
  "ds_read_b128 v[116:119], v126 offset:0\n\t" \
  "v_exp_f32 v40, v40\n\t" \
  "v_exp_f32 v41, v41\n\t" \
  "v_exp_f32 v42, v42\n\t" \
  "v_exp_f32 v43, v43\n\t" \
  "v_smfmac_f32_32x32x32_bf16 v[16:31], v[120:123], v[88:95], v114\n\t" \
  "ds_read_b128 v[120:123], v126 offset:704\n\t" \
  "v_exp_f32 v44, v44\n\t" \
  "v_exp_f32 v45, v45\n\t" \
  "v_exp_f32 v46, v46\n\t" \
  "v_exp_f32 v47, v47\n\t" \
  "s_waitcnt lgkmcnt(2)\n\t" \
  "v_mfma_f32_32x32x16_bf16 v[64:79], v[108:111], v[96:99], 0\n\t" \
  "v_exp_f32 v48, v48\n\t" \
  "v_exp_f32 v49, v49\n\t" \
  "v_exp_f32 v50, v50\n\t" \
  "v_exp_f32 v51, v51\n\t" \
  "v_mfma_f32_32x32x8_bf16 v[64:79], v[112:113], v[104:105], v[64:79]\n\t" \
  "v_exp_f32 v52, v52\n\t" \
  "v_exp_f32 v53, v53\n\t" \
  "v_exp_f32 v54, v54\n\t" \
  "v_exp_f32 v55, v55\n\t" \
  "s_waitcnt lgkmcnt(0)\n\t" \
  "v_smfmac_f32_32x32x32_bf16 v[0:15], v[116:119], v[32:39], v114\n\t" \
  "v_exp_f32 v56, v56\n\t" \
  "v_exp_f32 v57, v57\n\t" \
  "v_exp_f32 v58, v58\n\t" \
  "v_exp_f32 v59, v59\n\t" \
  "v_smfmac_f32_32x32x32_bf16 v[0:15], v[120:123], v[40:47], v114\n\t" \
  "v_exp_f32 v60, v60\n\t" \
  "v_exp_f32 v61, v61\n\t" \
  "v_exp_f32 v62, v62\n\t" \
  "v_exp_f32 v63, v63\n\t" \
  "v_mfma_f32_32x32x16_bf16 v[80:95], v[108:111], v[100:103], 0\n\t" \
  "ds_read_b128 v[108:111], v124 offset:5376\n\t" \
  "v_exp_f32 v64, v64\n\t" \
  "v_exp_f32 v65, v65\n\t" \
  "v_exp_f32 v66, v66\n\t" \
  "v_exp_f32 v67, v67\n\t" \
  "v_mfma_f32_32x32x8_bf16 v[80:95], v[112:113], v[106:107], v[80:95]\n\t" \
  "ds_read_b32 v112, v125 offset:5376\n\t" \
  "v_exp_f32 v68, v68\n\t" \
  "v_exp_f32 v69, v69\n\t" \
  "v_exp_f32 v70, v70\n\t" \
  "v_exp_f32 v71, v71\n\t" \
  "v_smfmac_f32_32x32x32_bf16 v[16:31], v[116:119], v[48:55], v114\n\t" \
  "ds_read_b128 v[116:119], v126 offset:5376\n\t" \
  "v_exp_f32 v72, v72\n\t" \
  "v_exp_f32 v73, v73\n\t" \
  "v_exp_f32 v74, v74\n\t" \
  "v_exp_f32 v75, v75\n\t" \
  "v_smfmac_f32_32x32x32_bf16 v[16:31], v[120:123], v[56:63], v114\n\t" \
  "ds_read_b128 v[120:123], v126 offset:6080\n\t" \
  "s_add_u32 %[cnt], %[cnt], 1\n\t" \
  "s_cmp_eq_u32 %[slot], 4\n\t" \
  "s_cselect_b32 %[inc], %[wrap], %[stg]\n\t" \
  "v_exp_f32 v76, v76\n\t" \
  "v_exp_f32 v77, v77\n\t" \
  "v_exp_f32 v78, v78\n\t" \
  "v_exp_f32 v79, v79\n\t" \
  "s_waitcnt lgkmcnt(2)\n\t" \
  "v_mfma_f32_32x32x16_bf16 v[32:47], v[108:111], v[96:99], 0\n\t" \
  "ds_read_b32 v115, v127 offset:32\n\t" \
  "s_cselect_b32 %[slot], -1, %[slot]\n\t" \
  "s_add_u32 %[slot], %[slot], 1\n\t" \
  "v_exp_f32 v80, v80\n\t" \
  "v_exp_f32 v81, v81\n\t" \
  "v_exp_f32 v82, v82\n\t" \
  "v_exp_f32 v83, v83\n\t" \
  "v_mfma_f32_32x32x8_bf16 v[32:47], v[112:113], v[104:105], v[32:47]\n\t" \
  "v_add_u32 v124, %[inc], v124\n\t" \
  "v_add_u32 v125, %[inc], v125\n\t" \
  "v_add_u32 v126, %[inc], v126\n\t" \
  "s_setprio 0\n\t" \
  "v_exp_f32 v84, v84\n\t" \
  "v_exp_f32 v85, v85\n\t" \
  "v_exp_f32 v86, v86\n\t" \
  "v_exp_f32 v87, v87\n\t" \
  "s_waitcnt lgkmcnt(1)\n\t" \
  "v_smfmac_f32_32x32x32_bf16 v[0:15], v[116:119], v[64:71], v114\n\t" \
  "v_exp_f32 v88, v88\n\t" \
  "v_exp_f32 v89, v89\n\t" \
  "v_exp_f32 v90, v90\n\t" \
  "v_exp_f32 v91, v91\n\t" \
  "s_waitcnt lgkmcnt(0)\n\t" \
  "v_readfirstlane_b32 %[rdy], v115\n\t" \
  "v_mov_b32 v115, %[cnt]\n\t" \
  "v_smfmac_f32_32x32x32_bf16 v[0:15], v[120:123], v[72:79], v114\n\t" \
  "ds_write_b32 v127, v115\n\t" \
  "s_cmp_gt_u32 %[rdy], %[cnt]\n\t" \
  "v_exp_f32 v92, v92\n\t" \
  "v_exp_f32 v93, v93\n\t" \
  "v_exp_f32 v94, v94\n\t" \
  "v_exp_f32 v95, v95\n\t" \
  "s_cbranch_scc1 5b\n\t" \
  "s_cmp_ge_u32 %[cnt], 16\n\t" \
  "s_cbranch_scc1 9f\n\t" \
  "s_branch 2b\n\t" \
  "9:\n\t" \
  "s_nop 1\n\t" \
  "v_smfmac_f32_32x32x32_bf16 v[16:31], v[116:119], v[80:87], v114\n\t" \
  "v_smfmac_f32_32x32x32_bf16 v[16:31], v[120:123], v[88:95], v114\n\t" \
  "s_waitcnt lgkmcnt(0)\n\t" \
  "s_nop 15\n\t"
#define TILE_GEMM_ASM \
  "s_nop 4\n\t" \
  "buffer_load_dwordx4 v[60:63], v140, %[rs], %[s0] offen offset:0 sc1\n\t" \
  "buffer_load_dwordx4 v[64:67], v140, %[rs], %[s1] offen offset:0 sc1\n\t" \
  "buffer_load_dwordx4 v[68:71], v140, %[rs], %[s0] offen offset:1024 sc1\n\t" \
  "buffer_load_dwordx4 v[72:75], v140, %[rs], %[s1] offen offset:1024 sc1\n\t" \
  "buffer_load_dwordx4 v[76:79], v140, %[rs], %[s0] offen offset:2048 sc1\n\t" \
  "buffer_load_dwordx4 v[80:83], v140, %[rs], %[s1] offen offset:2048 sc1\n\t" \
  "buffer_load_dwordx4 v[84:87], v140, %[rs], %[s0] offen offset:3072 sc1\n\t" \
  "buffer_load_dwordx4 v[88:91], v140, %[rs], %[s1] offen offset:3072 sc1\n\t" \
  "s_add_u32 %[s0], %[s0], 4096\n\t" \
  "buffer_load_dwordx4 v[92:95], v140, %[rs], %[s0] offen offset:0 sc1\n\t" \
  "s_add_u32 %[s1], %[s1], 4096\n\t" \
  "buffer_load_dwordx4 v[96:99], v140, %[rs], %[s1] offen offset:0 sc1\n\t" \
  "buffer_load_dwordx4 v[100:103], v140, %[rs], %[s0] offen offset:1024 sc1\n\t" \
  "buffer_load_dwordx4 v[104:107], v140, %[rs], %[s1] offen offset:1024 sc1\n\t" \
  "buffer_load_dwordx4 v[108:111], v140, %[rs], %[s0] offen offset:2048 sc1\n\t" \
  "buffer_load_dwordx4 v[112:115], v140, %[rs], %[s1] offen offset:2048 sc1\n\t" \
  "buffer_load_dwordx4 v[116:119], v140, %[rs], %[s0] offen offset:3072 sc1\n\t" \
  "buffer_load_dwordx4 v[120:123], v140, %[rs], %[s1] offen offset:3072 sc1\n\t" \
  "s_add_u32 %[s0], %[s0], 4096\n\t" \
  "buffer_load_dwordx4 v[124:127], v140, %[rs], %[s0] offen offset:0 sc1\n\t" \
  "s_add_u32 %[s1], %[s1], 4096\n\t" \
  "buffer_load_dwordx4 v[128:131], v140, %[rs], %[s1] offen offset:0 sc1\n\t" \
  "buffer_load_dwordx4 v[132:135], v140, %[rs], %[s0] offen offset:1024 sc1\n\t" \
  "buffer_load_dwordx4 v[136:139], v140, %[rs], %[s1] offen offset:1024 sc1\n\t" \
  "s_mov_b32 %[it], 0\n\t" \
  "1:\n\t" \
  "ds_read_b32 v143, v142\n\t" \
  "s_add_u32 %[it], %[it], 1\n\t" \
  "s_waitcnt lgkmcnt(0)\n\t" \
  "v_cmp_gt_u32 vcc, %[want], v143\n\t" \
  "s_cbranch_vccz 2f\n\t" \
  "s_sleep 4\n\t" \
  "s_cmp_lt_u32 %[it], 0x100000\n\t" \
  "s_cbranch_scc1 1b\n\t" \
  "2:\n\t" \
  "ds_read_b64_tr_b16 v[40:41], v141 offset:0\n\t" \
  "ds_read_b64_tr_b16 v[42:43], v141 offset:2560\n\t" \
  "ds_read_b64_tr_b16 v[44:45], v141 offset:8\n\t" \
  "ds_read_b64_tr_b16 v[46:47], v141 offset:2568\n\t" \
  "ds_read_b64_tr_b16 v[48:49], v141 offset:16\n\t" \
  "ds_read_b64_tr_b16 v[50:51], v141 offset:2576\n\t" \
  "ds_read_b64_tr_b16 v[52:53], v141 offset:24\n\t" \
  "ds_read_b64_tr_b16 v[54:55], v141 offset:2584\n\t" \
  "ds_read_b64_tr_b16 v[56:57], v141 offset:32\n\t" \
  "ds_read_b64_tr_b16 v[58:59], v141 offset:2592\n\t" \
  "v_mov_b32 v143, 1\n\t" \
  "v_mov_b32 v142, %[afree]\n\t" \
  "s_waitcnt vmcnt(18)\n\t" \
  "s_waitcnt lgkmcnt(8)\n\t" \
  "v_mfma_f32_16x16x32_bf16 v[0:3], v[40:43], v[60:63], v[0:3]\n\t" \
  "v_mfma_f32_16x16x32_bf16 v[4:7], v[40:43], v[64:67], v[4:7]\n\t" \
  "ds_read_b64_tr_b16 v[40:41], v141 offset:5120\n\t" \
  "ds_read_b64_tr_b16 v[42:43], v141 offset:7680\n\t" \
  "s_waitcnt lgkmcnt(8)\n\t" \
  "v_mfma_f32_16x16x32_bf16 v[8:11], v[44:47], v[60:63], v[8:11]\n\t" \
  "v_mfma_f32_16x16x32_bf16 v[12:15], v[44:47], v[64:67], v[12:15]\n\t" \
  "ds_read_b64_tr_b16 v[44:45], v141 offset:5128\n\t" \
  "ds_read_b64_tr_b16 v[46:47], v141 offset:7688\n\t" \
  "s_waitcnt lgkmcnt(8)\n\t" \
  "v_mfma_f32_16x16x32_bf16 v[16:19], v[48:51], v[60:63], v[16:19]\n\t" \
  "v_mfma_f32_16x16x32_bf16 v[20:23], v[48:51], v[64:67], v[20:23]\n\t" \
  "ds_read_b64_tr_b16 v[48:49], v141 offset:5136\n\t" \
  "ds_read_b64_tr_b16 v[50:51], v141 offset:7696\n\t" \
  "s_waitcnt lgkmcnt(8)\n\t" \
  "v_mfma_f32_16x16x32_bf16 v[24:27], v[52:55], v[60:63], v[24:27]\n\t" \
  "v_mfma_f32_16x16x32_bf16 v[28:31], v[52:55], v[64:67], v[28:31]\n\t" \
  "ds_read_b64_tr_b16 v[52:53], v141 offset:5144\n\t" \
  "ds_read_b64_tr_b16 v[54:55], v141 offset:7704\n\t" \
  "s_waitcnt lgkmcnt(8)\n\t" \
  "v_mfma_f32_16x16x32_bf16 v[32:35], v[56:59], v[60:63], v[32:35]\n\t" \
  "v_mfma_f32_16x16x32_bf16 v[36:39], v[56:59], v[64:67], v[36:39]\n\t" \
  "ds_read_b64_tr_b16 v[56:57], v141 offset:5152\n\t" \
  "ds_read_b64_tr_b16 v[58:59], v141 offset:7712\n\t" \
  "s_waitcnt vmcnt(16)\n\t" \
  "s_waitcnt lgkmcnt(8)\n\t" \
  "v_mfma_f32_16x16x32_bf16 v[0:3], v[40:43], v[68:71], v[0:3]\n\t" \
  "v_mfma_f32_16x16x32_bf16 v[4:7], v[40:43], v[72:75], v[4:7]\n\t" \
  "ds_read_b64_tr_b16 v[40:41], v141 offset:10240\n\t" \
  "ds_read_b64_tr_b16 v[42:43], v141 offset:12800\n\t" \
  "s_waitcnt lgkmcnt(8)\n\t" \
  "v_mfma_f32_16x16x32_bf16 v[8:11], v[44:47], v[68:71], v[8:11]\n\t" \
  "v_mfma_f32_16x16x32_bf16 v[12:15], v[44:47], v[72:75], v[12:15]\n\t" \
  "ds_read_b64_tr_b16 v[44:45], v141 offset:10248\n\t" \
  "ds_read_b64_tr_b16 v[46:47], v141 offset:12808\n\t" \
  "s_waitcnt lgkmcnt(8)\n\t" \
  "v_mfma_f32_16x16x32_bf16 v[16:19], v[48:51], v[68:71], v[16:19]\n\t" \
  "v_mfma_f32_16x16x32_bf16 v[20:23], v[48:51], v[72:75], v[20:23]\n\t" \
  "ds_read_b64_tr_b16 v[48:49], v141 offset:10256\n\t" \
  "ds_read_b64_tr_b16 v[50:51], v141 offset:12816\n\t" \
  "s_waitcnt lgkmcnt(8)\n\t" \
  "v_mfma_f32_16x16x32_bf16 v[24:27], v[52:55], v[68:71], v[24:27]\n\t" \
  "v_mfma_f32_16x16x32_bf16 v[28:31], v[52:55], v[72:75], v[28:31]\n\t" \
  "ds_read_b64_tr_b16 v[52:53], v141 offset:10264\n\t" \
  "ds_read_b64_tr_b16 v[54:55], v141 offset:12824\n\t" \
  "s_waitcnt lgkmcnt(8)\n\t" \
  "v_mfma_f32_16x16x32_bf16 v[32:35], v[56:59], v[68:71], v[32:35]\n\t" \
  "v_mfma_f32_16x16x32_bf16 v[36:39], v[56:59], v[72:75], v[36:39]\n\t" \
  "ds_read_b64_tr_b16 v[56:57], v141 offset:10272\n\t" \
  "ds_read_b64_tr_b16 v[58:59], v141 offset:12832\n\t" \
  "s_waitcnt vmcnt(14)\n\t" \
  "s_waitcnt lgkmcnt(8)\n\t" \
  "v_mfma_f32_16x16x32_bf16 v[0:3], v[40:43], v[76:79], v[0:3]\n\t" \
  "v_mfma_f32_16x16x32_bf16 v[4:7], v[40:43], v[80:83], v[4:7]\n\t" \
  "ds_read_b64_tr_b16 v[40:41], v141 offset:15360\n\t" \
  "ds_read_b64_tr_b16 v[42:43], v141 offset:17920\n\t" \
  "s_waitcnt lgkmcnt(8)\n\t" \
  "v_mfma_f32_16x16x32_bf16 v[8:11], v[44:47], v[76:79], v[8:11]\n\t" \
  "v_mfma_f32_16x16x32_bf16 v[12:15], v[44:47], v[80:83], v[12:15]\n\t" \
  "ds_read_b64_tr_b16 v[44:45], v141 offset:15368\n\t" \
  "ds_read_b64_tr_b16 v[46:47], v141 offset:17928\n\t" \
  "s_waitcnt lgkmcnt(8)\n\t" \
  "v_mfma_f32_16x16x32_bf16 v[16:19], v[48:51], v[76:79], v[16:19]\n\t" \
  "v_mfma_f32_16x16x32_bf16 v[20:23], v[48:51], v[80:83], v[20:23]\n\t" \
  "ds_read_b64_tr_b16 v[48:49], v141 offset:15376\n\t" \
  "ds_read_b64_tr_b16 v[50:51], v141 offset:17936\n\t" \
  "s_waitcnt lgkmcnt(8)\n\t" \
  "v_mfma_f32_16x16x32_bf16 v[24:27], v[52:55], v[76:79], v[24:27]\n\t" \
  "v_mfma_f32_16x16x32_bf16 v[28:31], v[52:55], v[80:83], v[28:31]\n\t" \
  "ds_read_b64_tr_b16 v[52:53], v141 offset:15384\n\t" \
  "ds_read_b64_tr_b16 v[54:55], v141 offset:17944\n\t" \
  "s_waitcnt lgkmcnt(8)\n\t" \
  "v_mfma_f32_16x16x32_bf16 v[32:35], v[56:59], v[76:79], v[32:35]\n\t" \
  "v_mfma_f32_16x16x32_bf16 v[36:39], v[56:59], v[80:83], v[36:39]\n\t" \
  "ds_read_b64_tr_b16 v[56:57], v141 offset:15392\n\t" \
  "ds_read_b64_tr_b16 v[58:59], v141 offset:17952\n\t" \
  "s_waitcnt vmcnt(12)\n\t" \
  "s_waitcnt lgkmcnt(8)\n\t" \
  "v_mfma_f32_16x16x32_bf16 v[0:3], v[40:43], v[84:87], v[0:3]\n\t" \
  "v_mfma_f32_16x16x32_bf16 v[4:7], v[40:43], v[88:91], v[4:7]\n\t" \
  "ds_read_b64_tr_b16 v[40:41], v141 offset:20480\n\t" \
  "ds_read_b64_tr_b16 v[42:43], v141 offset:23040\n\t" \
  "s_waitcnt lgkmcnt(8)\n\t" \
  "v_mfma_f32_16x16x32_bf16 v[8:11], v[44:47], v[84:87], v[8:11]\n\t" \
  "v_mfma_f32_16x16x32_bf16 v[12:15], v[44:47], v[88:91], v[12:15]\n\t" \
  "ds_read_b64_tr_b16 v[44:45], v141 offset:20488\n\t" \
  "ds_read_b64_tr_b16 v[46:47], v141 offset:23048\n\t" \
  "s_waitcnt lgkmcnt(8)\n\t" \
  "v_mfma_f32_16x16x32_bf16 v[16:19], v[48:51], v[84:87], v[16:19]\n\t" \
  "v_mfma_f32_16x16x32_bf16 v[20:23], v[48:51], v[88:91], v[20:23]\n\t" \
  "ds_read_b64_tr_b16 v[48:49], v141 offset:20496\n\t" \
  "ds_read_b64_tr_b16 v[50:51], v141 offset:23056\n\t" \
  "s_waitcnt lgkmcnt(8)\n\t" \
  "v_mfma_f32_16x16x32_bf16 v[24:27], v[52:55], v[84:87], v[24:27]\n\t" \
  "v_mfma_f32_16x16x32_bf16 v[28:31], v[52:55], v[88:91], v[28:31]\n\t" \
  "ds_read_b64_tr_b16 v[52:53], v141 offset:20504\n\t" \
  "ds_read_b64_tr_b16 v[54:55], v141 offset:23064\n\t" \
  "s_waitcnt lgkmcnt(8)\n\t" \
  "v_mfma_f32_16x16x32_bf16 v[32:35], v[56:59], v[84:87], v[32:35]\n\t" \
  "v_mfma_f32_16x16x32_bf16 v[36:39], v[56:59], v[88:91], v[36:39]\n\t" \
  "ds_read_b64_tr_b16 v[56:57], v141 offset:20512\n\t" \
  "ds_read_b64_tr_b16 v[58:59], v141 offset:23072\n\t" \
  "s_waitcnt vmcnt(10)\n\t" \
  "s_waitcnt lgkmcnt(8)\n\t" \
  "v_mfma_f32_16x16x32_bf16 v[0:3], v[40:43], v[92:95], v[0:3]\n\t" \
  "v_mfma_f32_16x16x32_bf16 v[4:7], v[40:43], v[96:99], v[4:7]\n\t" \
  "ds_read_b64_tr_b16 v[40:41], v141 offset:25600\n\t" \
  "ds_read_b64_tr_b16 v[42:43], v141 offset:28160\n\t" \
  "s_waitcnt lgkmcnt(8)\n\t" \
  "v_mfma_f32_16x16x32_bf16 v[8:11], v[44:47], v[92:95], v[8:11]\n\t" \
  "v_mfma_f32_16x16x32_bf16 v[12:15], v[44:47], v[96:99], v[12:15]\n\t" \
  "ds_read_b64_tr_b16 v[44:45], v141 offset:25608\n\t" \
  "ds_read_b64_tr_b16 v[46:47], v141 offset:28168\n\t" \
  "s_waitcnt lgkmcnt(8)\n\t" \
  "v_mfma_f32_16x16x32_bf16 v[16:19], v[48:51], v[92:95], v[16:19]\n\t" \
  "v_mfma_f32_16x16x32_bf16 v[20:23], v[48:51], v[96:99], v[20:23]\n\t" \
  "ds_read_b64_tr_b16 v[48:49], v141 offset:25616\n\t" \
  "ds_read_b64_tr_b16 v[50:51], v141 offset:28176\n\t" \
  "s_waitcnt lgkmcnt(8)\n\t" \
  "v_mfma_f32_16x16x32_bf16 v[24:27], v[52:55], v[92:95], v[24:27]\n\t" \
  "v_mfma_f32_16x16x32_bf16 v[28:31], v[52:55], v[96:99], v[28:31]\n\t" \
  "ds_read_b64_tr_b16 v[52:53], v141 offset:25624\n\t" \
  "ds_read_b64_tr_b16 v[54:55], v141 offset:28184\n\t" \
  "s_waitcnt lgkmcnt(8)\n\t" \
  "v_mfma_f32_16x16x32_bf16 v[32:35], v[56:59], v[92:95], v[32:35]\n\t" \
  "v_mfma_f32_16x16x32_bf16 v[36:39], v[56:59], v[96:99], v[36:39]\n\t" \
  "ds_read_b64_tr_b16 v[56:57], v141 offset:25632\n\t" \
  "ds_read_b64_tr_b16 v[58:59], v141 offset:28192\n\t" \
  "s_waitcnt vmcnt(8)\n\t" \
  "s_waitcnt lgkmcnt(8)\n\t" \
  "v_mfma_f32_16x16x32_bf16 v[0:3], v[40:43], v[100:103], v[0:3]\n\t" \
  "v_mfma_f32_16x16x32_bf16 v[4:7], v[40:43], v[104:107], v[4:7]\n\t" \
  "ds_read_b64_tr_b16 v[40:41], v141 offset:30720\n\t" \
  "ds_read_b64_tr_b16 v[42:43], v141 offset:33280\n\t" \
  "s_waitcnt lgkmcnt(8)\n\t" \
  "v_mfma_f32_16x16x32_bf16 v[8:11], v[44:47], v[100:103], v[8:11]\n\t" \
  "v_mfma_f32_16x16x32_bf16 v[12:15], v[44:47], v[104:107], v[12:15]\n\t" \
  "ds_read_b64_tr_b16 v[44:45], v141 offset:30728\n\t" \
  "ds_read_b64_tr_b16 v[46:47], v141 offset:33288\n\t" \
  "s_waitcnt lgkmcnt(8)\n\t" \
  "v_mfma_f32_16x16x32_bf16 v[16:19], v[48:51], v[100:103], v[16:19]\n\t" \
  "v_mfma_f32_16x16x32_bf16 v[20:23], v[48:51], v[104:107], v[20:23]\n\t" \
  "ds_read_b64_tr_b16 v[48:49], v141 offset:30736\n\t" \
  "ds_read_b64_tr_b16 v[50:51], v141 offset:33296\n\t" \
  "s_waitcnt lgkmcnt(8)\n\t" \
  "v_mfma_f32_16x16x32_bf16 v[24:27], v[52:55], v[100:103], v[24:27]\n\t" \
  "v_mfma_f32_16x16x32_bf16 v[28:31], v[52:55], v[104:107], v[28:31]\n\t" \
  "ds_read_b64_tr_b16 v[52:53], v141 offset:30744\n\t" \
  "ds_read_b64_tr_b16 v[54:55], v141 offset:33304\n\t" \
  "s_waitcnt lgkmcnt(8)\n\t" \
  "v_mfma_f32_16x16x32_bf16 v[32:35], v[56:59], v[100:103], v[32:35]\n\t" \
  "v_mfma_f32_16x16x32_bf16 v[36:39], v[56:59], v[104:107], v[36:39]\n\t" \
  "ds_read_b64_tr_b16 v[56:57], v141 offset:30752\n\t" \
  "ds_read_b64_tr_b16 v[58:59], v141 offset:33312\n\t" \
  "s_waitcnt vmcnt(6)\n\t" \
  "s_waitcnt lgkmcnt(8)\n\t" \
  "v_mfma_f32_16x16x32_bf16 v[0:3], v[40:43], v[108:111], v[0:3]\n\t" \
  "v_mfma_f32_16x16x32_bf16 v[4:7], v[40:43], v[112:115], v[4:7]\n\t" \
  "ds_read_b64_tr_b16 v[40:41], v141 offset:35840\n\t" \
  "ds_read_b64_tr_b16 v[42:43], v141 offset:38400\n\t" \
  "s_waitcnt lgkmcnt(8)\n\t" \
  "v_mfma_f32_16x16x32_bf16 v[8:11], v[44:47], v[108:111], v[8:11]\n\t" \
  "v_mfma_f32_16x16x32_bf16 v[12:15], v[44:47], v[112:115], v[12:15]\n\t" \
  "ds_read_b64_tr_b16 v[44:45], v141 offset:35848\n\t" \
  "ds_read_b64_tr_b16 v[46:47], v141 offset:38408\n\t" \
  "s_waitcnt lgkmcnt(8)\n\t" \
  "v_mfma_f32_16x16x32_bf16 v[16:19], v[48:51], v[108:111], v[16:19]\n\t" \
  "v_mfma_f32_16x16x32_bf16 v[20:23], v[48:51], v[112:115], v[20:23]\n\t" \
  "ds_read_b64_tr_b16 v[48:49], v141 offset:35856\n\t" \
  "ds_read_b64_tr_b16 v[50:51], v141 offset:38416\n\t" \
  "s_waitcnt lgkmcnt(8)\n\t" \
  "v_mfma_f32_16x16x32_bf16 v[24:27], v[52:55], v[108:111], v[24:27]\n\t" \
  "v_mfma_f32_16x16x32_bf16 v[28:31], v[52:55], v[112:115], v[28:31]\n\t" \
  "ds_read_b64_tr_b16 v[52:53], v141 offset:35864\n\t" \
  "ds_read_b64_tr_b16 v[54:55], v141 offset:38424\n\t" \
  "s_waitcnt lgkmcnt(8)\n\t" \
  "v_mfma_f32_16x16x32_bf16 v[32:35], v[56:59], v[108:111], v[32:35]\n\t" \
  "v_mfma_f32_16x16x32_bf16 v[36:39], v[56:59], v[112:115], v[36:39]\n\t" \
  "ds_read_b64_tr_b16 v[56:57], v141 offset:35872\n\t" \
  "ds_read_b64_tr_b16 v[58:59], v141 offset:38432\n\t" \
  "s_waitcnt vmcnt(4)\n\t" \
  "s_waitcnt lgkmcnt(8)\n\t" \
  "v_mfma_f32_16x16x32_bf16 v[0:3], v[40:43], v[116:119], v[0:3]\n\t" \
  "v_mfma_f32_16x16x32_bf16 v[4:7], v[40:43], v[120:123], v[4:7]\n\t" \
  "ds_read_b64_tr_b16 v[40:41], v141 offset:40960\n\t" \
  "ds_read_b64_tr_b16 v[42:43], v141 offset:43520\n\t" \
  "s_waitcnt lgkmcnt(8)\n\t" \
  "v_mfma_f32_16x16x32_bf16 v[8:11], v[44:47], v[116:119], v[8:11]\n\t" \
  "v_mfma_f32_16x16x32_bf16 v[12:15], v[44:47], v[120:123], v[12:15]\n\t" \
  "ds_read_b64_tr_b16 v[44:45], v141 offset:40968\n\t" \
  "ds_read_b64_tr_b16 v[46:47], v141 offset:43528\n\t" \
  "s_waitcnt lgkmcnt(8)\n\t" \
  "v_mfma_f32_16x16x32_bf16 v[16:19], v[48:51], v[116:119], v[16:19]\n\t" \
  "v_mfma_f32_16x16x32_bf16 v[20:23], v[48:51], v[120:123], v[20:23]\n\t" \
  "ds_read_b64_tr_b16 v[48:49], v141 offset:40976\n\t" \
  "ds_read_b64_tr_b16 v[50:51], v141 offset:43536\n\t" \
  "s_waitcnt lgkmcnt(8)\n\t" \
  "v_mfma_f32_16x16x32_bf16 v[24:27], v[52:55], v[116:119], v[24:27]\n\t" \
  "v_mfma_f32_16x16x32_bf16 v[28:31], v[52:55], v[120:123], v[28:31]\n\t" \
  "ds_read_b64_tr_b16 v[52:53], v141 offset:40984\n\t" \
  "ds_read_b64_tr_b16 v[54:55], v141 offset:43544\n\t" \
  "s_waitcnt lgkmcnt(8)\n\t" \
  "v_mfma_f32_16x16x32_bf16 v[32:35], v[56:59], v[116:119], v[32:35]\n\t" \
  "v_mfma_f32_16x16x32_bf16 v[36:39], v[56:59], v[120:123], v[36:39]\n\t" \
  "ds_read_b64_tr_b16 v[56:57], v141 offset:40992\n\t" \
  "ds_read_b64_tr_b16 v[58:59], v141 offset:43552\n\t" \
  "s_waitcnt vmcnt(2)\n\t" \
  "s_waitcnt lgkmcnt(8)\n\t" \
  "v_mfma_f32_16x16x32_bf16 v[0:3], v[40:43], v[124:127], v[0:3]\n\t" \
  "v_mfma_f32_16x16x32_bf16 v[4:7], v[40:43], v[128:131], v[4:7]\n\t" \
  "ds_read_b64_tr_b16 v[40:41], v141 offset:46080\n\t" \
  "ds_read_b64_tr_b16 v[42:43], v141 offset:48640\n\t" \
  "s_waitcnt lgkmcnt(8)\n\t" \
  "v_mfma_f32_16x16x32_bf16 v[8:11], v[44:47], v[124:127], v[8:11]\n\t" \
  "v_mfma_f32_16x16x32_bf16 v[12:15], v[44:47], v[128:131], v[12:15]\n\t" \
  "ds_read_b64_tr_b16 v[44:45], v141 offset:46088\n\t" \
  "ds_read_b64_tr_b16 v[46:47], v141 offset:48648\n\t" \
  "s_waitcnt lgkmcnt(8)\n\t" \
  "v_mfma_f32_16x16x32_bf16 v[16:19], v[48:51], v[124:127], v[16:19]\n\t" \
  "v_mfma_f32_16x16x32_bf16 v[20:23], v[48:51], v[128:131], v[20:23]\n\t" \
  "ds_read_b64_tr_b16 v[48:49], v141 offset:46096\n\t" \
  "ds_read_b64_tr_b16 v[50:51], v141 offset:48656\n\t" \
  "s_waitcnt lgkmcnt(8)\n\t" \
  "v_mfma_f32_16x16x32_bf16 v[24:27], v[52:55], v[124:127], v[24:27]\n\t" \
  "v_mfma_f32_16x16x32_bf16 v[28:31], v[52:55], v[128:131], v[28:31]\n\t" \
  "ds_read_b64_tr_b16 v[52:53], v141 offset:46104\n\t" \
  "ds_read_b64_tr_b16 v[54:55], v141 offset:48664\n\t" \
  "s_waitcnt lgkmcnt(8)\n\t" \
  "v_mfma_f32_16x16x32_bf16 v[32:35], v[56:59], v[124:127], v[32:35]\n\t" \
  "v_mfma_f32_16x16x32_bf16 v[36:39], v[56:59], v[128:131], v[36:39]\n\t" \
  "ds_read_b64_tr_b16 v[56:57], v141 offset:46112\n\t" \
  "ds_read_b64_tr_b16 v[58:59], v141 offset:48672\n\t" \
  "s_waitcnt vmcnt(0)\n\t" \
  "s_waitcnt lgkmcnt(8)\n\t" \
  "v_mfma_f32_16x16x32_bf16 v[0:3], v[40:43], v[132:135], v[0:3]\n\t" \
  "v_mfma_f32_16x16x32_bf16 v[4:7], v[40:43], v[136:139], v[4:7]\n\t" \
  "s_waitcnt lgkmcnt(6)\n\t" \
  "v_mfma_f32_16x16x32_bf16 v[8:11], v[44:47], v[132:135], v[8:11]\n\t" \
  "v_mfma_f32_16x16x32_bf16 v[12:15], v[44:47], v[136:139], v[12:15]\n\t" \
  "s_waitcnt lgkmcnt(4)\n\t" \
  "v_mfma_f32_16x16x32_bf16 v[16:19], v[48:51], v[132:135], v[16:19]\n\t" \
  "v_mfma_f32_16x16x32_bf16 v[20:23], v[48:51], v[136:139], v[20:23]\n\t" \
  "s_waitcnt lgkmcnt(2)\n\t" \
  "v_mfma_f32_16x16x32_bf16 v[24:27], v[52:55], v[132:135], v[24:27]\n\t" \
  "v_mfma_f32_16x16x32_bf16 v[28:31], v[52:55], v[136:139], v[28:31]\n\t" \
  "s_waitcnt lgkmcnt(0)\n\t" \
  "v_mfma_f32_16x16x32_bf16 v[32:35], v[56:59], v[132:135], v[32:35]\n\t" \
  "v_mfma_f32_16x16x32_bf16 v[36:39], v[56:59], v[136:139], v[36:39]\n\t" \
  "s_mov_b64 exec, 1\n\t" \
  "ds_add_u32 v142, v143\n\t" \
  "s_mov_b64 exec, -1\n\t" \
  "s_nop 7\n\t"
#define TILE_GEMM_CLOB "v40", "v41", "v42", "v43", "v44", "v45", "v46", "v47", "v48", "v49", "v50", "v51", "v52", "v53", "v54", "v55", "v56", "v57", "v58", "v59", "v60", "v61", "v62", "v63", "v64", "v65", "v66", "v67", "v68", "v69", "v70", "v71", "v72", "v73", "v74", "v75", "v76", "v77", "v78", "v79", "v80", "v81", "v82", "v83", "v84", "v85", "v86", "v87", "v88", "v89", "v90", "v91", "v92", "v93", "v94", "v95", "v96", "v97", "v98", "v99", "v100", "v101", "v102", "v103", "v104", "v105", "v106", "v107", "v108", "v109", "v110", "v111", "v112", "v113", "v114", "v115", "v116", "v117", "v118", "v119", "v120", "v121", "v122", "v123", "v124", "v125", "v126", "v127", "v128", "v129", "v130", "v131", "v132", "v133", "v134", "v135", "v136", "v137", "v138", "v139", "v143"
#define K1_LOOP_ASM \
  "v_mov_b64 v[0:1], 0\n\t" \
  "v_mov_b64 v[2:3], 0\n\t" \
  "v_mov_b64 v[4:5], 0\n\t" \
  "v_mov_b64 v[6:7], 0\n\t" \
  "v_mov_b64 v[8:9], 0\n\t" \
  "v_mov_b64 v[10:11], 0\n\t" \
  "v_mov_b64 v[12:13], 0\n\t" \
  "v_mov_b64 v[14:15], 0\n\t" \
  "v_mov_b64 v[16:17], 0\n\t" \
  "v_mov_b64 v[18:19], 0\n\t" \
  "v_mov_b64 v[20:21], 0\n\t" \
  "v_mov_b64 v[22:23], 0\n\t" \
  "v_mov_b64 v[24:25], 0\n\t" \
  "v_mov_b64 v[26:27], 0\n\t" \
  "v_mov_b64 v[28:29], 0\n\t" \
  "v_mov_b64 v[30:31], 0\n\t" \
  "v_mov_b64 v[32:33], 0\n\t" \
  "v_mov_b64 v[34:35], 0\n\t" \
  "v_mov_b64 v[36:37], 0\n\t" \
  "v_mov_b64 v[38:39], 0\n\t" \
  "v_mov_b64 v[40:41], 0\n\t" \
  "v_mov_b64 v[42:43], 0\n\t" \
  "v_mov_b64 v[44:45], 0\n\t" \
  "v_mov_b64 v[46:47], 0\n\t" \
  "v_mov_b64 v[48:49], 0\n\t" \
  "v_mov_b64 v[50:51], 0\n\t" \
  "v_mov_b64 v[52:53], 0\n\t" \
  "v_mov_b64 v[54:55], 0\n\t" \
  "v_mov_b64 v[56:57], 0\n\t" \
  "v_mov_b64 v[58:59], 0\n\t" \
  "v_mov_b64 v[60:61], 0\n\t" \
  "v_mov_b64 v[62:63], 0\n\t" \
  "v_mov_b64 v[64:65], 0\n\t" \
  "v_mov_b64 v[66:67], 0\n\t" \
  "v_mov_b64 v[68:69], 0\n\t" \
  "v_mov_b64 v[70:71], 0\n\t" \
  "v_mov_b64 v[72:73], 0\n\t" \
  "v_mov_b64 v[74:75], 0\n\t" \
  "v_mov_b64 v[76:77], 0\n\t" \
  "v_mov_b64 v[78:79], 0\n\t" \
  "v_mov_b64 v[116:117], 0\n\t" \
  "v_mov_b64 v[118:119], 0\n\t" \
  "v_mov_b64 v[120:121], 0\n\t" \
  "v_mov_b64 v[122:123], 0\n\t" \
  "v_mov_b64 v[124:125], 0\n\t" \
  "v_mov_b64 v[126:127], 0\n\t" \
  "v_mov_b64 v[128:129], 0\n\t" \
  "v_mov_b64 v[130:131], 0\n\t" \
  "v_mov_b64 v[132:133], 0\n\t" \
  "v_mov_b64 v[134:135], 0\n\t" \
  "v_mov_b64 v[136:137], 0\n\t" \
  "v_mov_b64 v[138:139], 0\n\t" \
  "v_mov_b64 v[140:141], 0\n\t" \
  "v_mov_b64 v[142:143], 0\n\t" \
  "v_mov_b64 v[144:145], 0\n\t" \
  "v_mov_b64 v[146:147], 0\n\t" \
  "v_mov_b64 v[148:149], 0\n\t" \
  "v_mov_b64 v[150:151], 0\n\t" \
  "s_mov_b32 %[cnt], 0\n\t" \
  "1:\n\t" \
  "s_barrier\n\t" \
  "v_mfma_f32_16x16x32_bf16 v[0:3], v[132:135], v[116:119], v[0:3]\n\t" \
  "ds_read_b128 v[80:83], v152\n\t" \
  "v_mfma_f32_16x16x32_bf16 v[4:7], v[132:135], v[120:123], v[4:7]\n\t" \
  "ds_read_b64_tr_b16 v[96:97], v154 offset:0\n\t" \
  "v_mfma_f32_16x16x32_bf16 v[8:11], v[132:135], v[124:127], v[8:11]\n\t" \
  "ds_read_b64_tr_b16 v[98:99], v154 offset:2560\n\t" \
  "v_mfma_f32_16x16x32_bf16 v[12:15], v[132:135], v[128:131], v[12:15]\n\t" \
  "ds_read_b128 v[84:87], v152 offset:2048\n\t" \
  "v_mfma_f32_16x16x32_bf16 v[16:19], v[136:139], v[116:119], v[16:19]\n\t" \
  "ds_read_b128 v[88:91], v152 offset:4096\n\t" \
  "v_mfma_f32_16x16x32_bf16 v[20:23], v[136:139], v[120:123], v[20:23]\n\t" \
  "ds_read_b128 v[92:95], v152 offset:6144\n\t" \
  "v_mfma_f32_16x16x32_bf16 v[24:27], v[136:139], v[124:127], v[24:27]\n\t" \
  "ds_read_b64_tr_b16 v[100:101], v154 offset:8\n\t" \
  "v_mfma_f32_16x16x32_bf16 v[28:31], v[136:139], v[128:131], v[28:31]\n\t" \
  "ds_read_b64_tr_b16 v[102:103], v154 offset:2568\n\t" \
  "v_mfma_f32_16x16x32_bf16 v[32:35], v[140:143], v[116:119], v[32:35]\n\t" \
  "ds_read_b64_tr_b16 v[104:105], v154 offset:16\n\t" \
  "v_mfma_f32_16x16x32_bf16 v[36:39], v[140:143], v[120:123], v[36:39]\n\t" \
  "ds_read_b64_tr_b16 v[106:107], v154 offset:2576\n\t" \
  "v_mfma_f32_16x16x32_bf16 v[40:43], v[140:143], v[124:127], v[40:43]\n\t" \
  "ds_read_b64_tr_b16 v[108:109], v154 offset:24\n\t" \
  "v_mfma_f32_16x16x32_bf16 v[44:47], v[140:143], v[128:131], v[44:47]\n\t" \
  "ds_read_b64_tr_b16 v[110:111], v154 offset:2584\n\t" \
  "v_mfma_f32_16x16x32_bf16 v[48:51], v[144:147], v[116:119], v[48:51]\n\t" \
  "ds_read_b64_tr_b16 v[112:113], v154 offset:32\n\t" \
  "v_mfma_f32_16x16x32_bf16 v[52:55], v[144:147], v[120:123], v[52:55]\n\t" \
  "ds_read_b64_tr_b16 v[114:115], v154 offset:2592\n\t" \
  "v_mfma_f32_16x16x32_bf16 v[56:59], v[144:147], v[124:127], v[56:59]\n\t" \
  "s_and_b32 %[inc], %[cnt], 3\n\t" \
  "v_mfma_f32_16x16x32_bf16 v[60:63], v[144:147], v[128:131], v[60:63]\n\t" \
  "s_cmp_eq_u32 %[inc], 3\n\t" \
  "v_mfma_f32_16x16x32_bf16 v[64:67], v[148:151], v[116:119], v[64:67]\n\t" \
  "s_cselect_b32 %[inc], %[wrap], %[stp]\n\t" \
  "v_mfma_f32_16x16x32_bf16 v[68:71], v[148:151], v[120:123], v[68:71]\n\t" \
  "v_add_u32 v152, %[inc], v152\n\t" \
  "v_mfma_f32_16x16x32_bf16 v[72:75], v[148:151], v[124:127], v[72:75]\n\t" \
  "v_mfma_f32_16x16x32_bf16 v[76:79], v[148:151], v[128:131], v[76:79]\n\t" \
  "s_waitcnt lgkmcnt(0)\n\t" \
  "v_mfma_f32_16x16x32_bf16 v[0:3], v[96:99], v[80:83], v[0:3]\n\t" \
  "ds_read_b128 v[116:119], v153\n\t" \
  "v_mfma_f32_16x16x32_bf16 v[4:7], v[96:99], v[84:87], v[4:7]\n\t" \
  "ds_read_b64_tr_b16 v[132:133], v154 offset:5120\n\t" \
  "v_mfma_f32_16x16x32_bf16 v[8:11], v[96:99], v[88:91], v[8:11]\n\t" \
  "ds_read_b64_tr_b16 v[134:135], v154 offset:7680\n\t" \
  "v_mfma_f32_16x16x32_bf16 v[12:15], v[96:99], v[92:95], v[12:15]\n\t" \
  "ds_read_b128 v[120:123], v153 offset:2048\n\t" \
  "v_mfma_f32_16x16x32_bf16 v[16:19], v[100:103], v[80:83], v[16:19]\n\t" \
  "ds_read_b128 v[124:127], v153 offset:4096\n\t" \
  "v_mfma_f32_16x16x32_bf16 v[20:23], v[100:103], v[84:87], v[20:23]\n\t" \
  "ds_read_b128 v[128:131], v153 offset:6144\n\t" \
  "v_mfma_f32_16x16x32_bf16 v[24:27], v[100:103], v[88:91], v[24:27]\n\t" \
  "ds_read_b64_tr_b16 v[136:137], v154 offset:5128\n\t" \
  "v_mfma_f32_16x16x32_bf16 v[28:31], v[100:103], v[92:95], v[28:31]\n\t" \
  "ds_read_b64_tr_b16 v[138:139], v154 offset:7688\n\t" \
  "v_mfma_f32_16x16x32_bf16 v[32:35], v[104:107], v[80:83], v[32:35]\n\t" \
  "ds_read_b64_tr_b16 v[140:141], v154 offset:5136\n\t" \
  "v_mfma_f32_16x16x32_bf16 v[36:39], v[104:107], v[84:87], v[36:39]\n\t" \
  "ds_read_b64_tr_b16 v[142:143], v154 offset:7696\n\t" \
  "v_mfma_f32_16x16x32_bf16 v[40:43], v[104:107], v[88:91], v[40:43]\n\t" \
  "ds_read_b64_tr_b16 v[144:145], v154 offset:5144\n\t" \
  "v_mfma_f32_16x16x32_bf16 v[44:47], v[104:107], v[92:95], v[44:47]\n\t" \
  "ds_read_b64_tr_b16 v[146:147], v154 offset:7704\n\t" \
  "v_mfma_f32_16x16x32_bf16 v[48:51], v[108:111], v[80:83], v[48:51]\n\t" \
  "ds_read_b64_tr_b16 v[148:149], v154 offset:5152\n\t" \
  "v_mfma_f32_16x16x32_bf16 v[52:55], v[108:111], v[84:87], v[52:55]\n\t" \
  "ds_read_b64_tr_b16 v[150:151], v154 offset:7712\n\t" \
  "v_mfma_f32_16x16x32_bf16 v[56:59], v[108:111], v[88:91], v[56:59]\n\t" \
  "v_add_u32 v153, %[inc], v153\n\t" \
  "v_mfma_f32_16x16x32_bf16 v[60:63], v[108:111], v[92:95], v[60:63]\n\t" \
  "v_add_u32 v154, %[inc], v154\n\t" \
  "v_mfma_f32_16x16x32_bf16 v[64:67], v[112:115], v[80:83], v[64:67]\n\t" \
  "s_add_u32 %[cnt], %[cnt], 1\n\t" \
  "v_mfma_f32_16x16x32_bf16 v[68:71], v[112:115], v[84:87], v[68:71]\n\t" \
  "s_cmp_lt_u32 %[cnt], 10\n\t" \
  "v_mfma_f32_16x16x32_bf16 v[72:75], v[112:115], v[88:91], v[72:75]\n\t" \
  "v_mfma_f32_16x16x32_bf16 v[76:79], v[112:115], v[92:95], v[76:79]\n\t" \
  "s_waitcnt lgkmcnt(0)\n\t" \
  "s_cbranch_scc1 1b\n\t" \
  "v_mfma_f32_16x16x32_bf16 v[0:3], v[132:135], v[116:119], v[0:3]\n\t" \
  "v_mfma_f32_16x16x32_bf16 v[16:19], v[136:139], v[116:119], v[16:19]\n\t" \
  "v_mfma_f32_16x16x32_bf16 v[32:35], v[140:143], v[116:119], v[32:35]\n\t" \
  "v_mfma_f32_16x16x32_bf16 v[48:51], v[144:147], v[116:119], v[48:51]\n\t" \
  "v_mfma_f32_16x16x32_bf16 v[64:67], v[148:151], v[116:119], v[64:67]\n\t" \
  "v_mfma_f32_16x16x32_bf16 v[4:7], v[132:135], v[120:123], v[4:7]\n\t" \
  "v_mfma_f32_16x16x32_bf16 v[20:23], v[136:139], v[120:123], v[20:23]\n\t" \
  "v_mfma_f32_16x16x32_bf16 v[36:39], v[140:143], v[120:123], v[36:39]\n\t" \
  "v_mfma_f32_16x16x32_bf16 v[52:55], v[144:147], v[120:123], v[52:55]\n\t" \
  "v_mfma_f32_16x16x32_bf16 v[68:71], v[148:151], v[120:123], v[68:71]\n\t" \
  "v_mfma_f32_16x16x32_bf16 v[8:11], v[132:135], v[124:127], v[8:11]\n\t" \
  "v_mfma_f32_16x16x32_bf16 v[24:27], v[136:139], v[124:127], v[24:27]\n\t" \
  "v_mfma_f32_16x16x32_bf16 v[40:43], v[140:143], v[124:127], v[40:43]\n\t" \
  "v_mfma_f32_16x16x32_bf16 v[56:59], v[144:147], v[124:127], v[56:59]\n\t" \
  "v_mfma_f32_16x16x32_bf16 v[72:75], v[148:151], v[124:127], v[72:75]\n\t" \
  "v_mfma_f32_16x16x32_bf16 v[12:15], v[132:135], v[128:131], v[12:15]\n\t" \
  "v_mfma_f32_16x16x32_bf16 v[28:31], v[136:139], v[128:131], v[28:31]\n\t" \
  "v_mfma_f32_16x16x32_bf16 v[44:47], v[140:143], v[128:131], v[44:47]\n\t" \
  "v_mfma_f32_16x16x32_bf16 v[60:63], v[144:147], v[128:131], v[60:63]\n\t" \
  "v_mfma_f32_16x16x32_bf16 v[76:79], v[148:151], v[128:131], v[76:79]\n\t" \
  "s_nop 7\n\t"
#define K1_LOOP_CLOB "v80", "v81", "v82", "v83", "v84", "v85", "v86", "v87", "v88", "v89", "v90", "v91", "v92", "v93", "v94", "v95", "v96", "v97", "v98", "v99", "v100", "v101", "v102", "v103", "v104", "v105", "v106", "v107", "v108", "v109", "v110", "v111", "v112", "v113", "v114", "v115", "v116", "v117", "v118", "v119", "v120", "v121", "v122", "v123", "v124", "v125", "v126", "v127", "v128", "v129", "v130", "v131", "v132", "v133", "v134", "v135", "v136", "v137", "v138", "v139", "v140", "v141", "v142", "v143", "v144", "v145", "v146", "v147", "v148", "v149", "v150", "v151"
#define LDELAY 2
#define QOKW 9u
#define FINW 4u
#define G2(i) do {} while (0)
#define GSTAMP(i) do {} while (0)
#define FSTAMP(i) do {} while (0)
#define HSTAMP(i) STAMP(i)
#define TSTAMP(i) do {} while (0)
#define FSLEEP 0
#define BSLEEP 1
#define K1_ROT ((tt + 3 * (jb >> 3)) % 10)
constexpr u32 K1_SLOT = 36864, K1_W = 16384;
constexpr u32 K1_EX = 3 * K1_SLOT, K1_EXP = 4 * K1_SLOT;
constexpr u32 K1_VI = 0, K1_VI_P = 208, K1_PHK = 27648, K1_PHK_P = 336;
constexpr u32 K1_QST = 0;
constexpr u32 K1_PHQ = 16384;
constexpr u32 K1_KVF = 70656;
constexpr u32 K1_XCH = 73728;
constexpr u32 K1_WPHI = 156160;
constexpr u32 L_K01 = 160256;
constexpr u32 SL = 4 * REC;
constexpr u32 L_IMG = 0;
constexpr u32 L_RING = 102400;
constexpr u32 L_MRG = L_RING + SL;
constexpr u32 L_FLG = 162816;
constexpr u32 F_GBAR = L_FLG + 96, F_APUB = L_FLG + 100, F_AFREE = L_FLG + 104  , F_P1 = L_FLG + 112, F_PHB = L_FLG + 116,
              F_P2 = L_FLG + 120, F_KVOK = L_FLG + 124, F_BIMG = L_FLG + 128, F_QOK = L_FLG + 132, F_TAG = L_FLG + 136, F_FIN = L_FLG + 152, F_K01 = L_FLG + 156, F_AOK = L_FLG + 160, F_P3 = L_FLG + 164, L_GT = L_FLG + 256;
constexpr u32 LDS_BYTES = 163840;

__device__ __forceinline__ f32x16 attn_exact(rsrc_t rW, u32x4 qx, u32 q2lo, int lane) {
  const f32x16 zero16 = {0.f, 0.f, 0.f, 0.f, 0.f, 0.f, 0.f, 0.f, 0.f, 0.f, 0.f, 0.f, 0.f, 0.f, 0.f, 0.f};
  const int r32 = lane & 31, hh = lane >> 5;
  const u32x2 qz = {q2lo, 0u};
  float m = -INFINITY;
#pragma unroll 1
  for (u32 rec = 0; rec < 64; ++rec) {
    const u32x4 a1 = gl128(rW, lane * 16, WS_KVIM + rec * REC);
    const u32 a2 = gl32(rW, lane * 4, WS_KVIM + rec * REC + 1024);
    f32x16 s = mfma32(a1, qx, zero16);
    s = mfma32k8((u32x2){a2, 0u}, qz, s);
    m = fmaxf(m, max16(s));
  }
  m = xhalf_max(m);
  f32x16 O = zero16;
  const u32 av = 1280u + (u32)hh * 352u + (u32)(r32 <= 20 ? r32 : 21) * 16u;
#pragma unroll 1
  for (u32 t2 = 0; t2 < 64; ++t2) {
    const u32 rec = (t2 + 62) & 63;
    const u32x4 a1 = gl128(rW, lane * 16, WS_KVIM + rec * REC);
    const u32 a2 = gl32(rW, lane * 4, WS_KVIM + rec * REC + 1024);
    const u32x4 b1 = gl128(rW, av, WS_KVIM + t2 * REC), b2 = gl128(rW, av, WS_KVIM + t2 * REC + 704);
    f32x16 s = mfma32(a1, qx, zero16);
    s = mfma32k8((u32x2){a2, 0u}, qz, s);
#pragma unroll
    for (int r = 0; r < 16; ++r) s[r] = __builtin_amdgcn_exp2f(s[r] - m);
    O = smf(b1, lo8(s), O, 0xDDDD);
    O = smf(b2, hi8(s), O, 0xDDDD);
  }
  return O;
}

extern "C" __global__ void __launch_bounds__(768, 3)
gm_fused(const char* __restrict__ x, const char* __restrict__ wqkv, const char* __restrict__ wphi, const char* __restrict__ wq,
         const char* __restrict__ wk, const char* __restrict__ wv, const char* __restrict__ gw, const char* __restrict__ awo,
         const char* __restrict__ bwo, char* ws, char* __restrict__ out) {
  extern __shared__ __attribute__((aligned(16))) char smem[];
  ldsp lds = (ldsp)smem;
  const u32 lds0 = (u32)(uintptr_t)lds;
  const int w = __builtin_amdgcn_readfirstlane(threadIdx.x >> 6);
  const int bid = blockIdx.x, xcd = bid & 7, jb = bid >> 3;
  const rsrc_t rW = __builtin_amdgcn_make_buffer_rsrc(ws, 0, WS_END, 0x00020000);
  const f32x16 zero16 = {0.f, 0.f, 0.f, 0.f, 0.f, 0.f, 0.f, 0.f, 0.f, 0.f, 0.f, 0.f, 0.f, 0.f, 0.f, 0.f};
  STAMP_INIT();
  const int tt = (xcd & 1) * 8 + (jb & 7), combo = (xcd >> 1) * 4 + (jb >> 3), type = combo & 1, hg = combo >> 1;
  const u32x4 rsWS = mkrsrc(ws, WS_END);
  {
    if (w < 4) {
      const int cg = w >> 1, th = w & 1;
      f32x16 ac[5];
      {
        const int lane = lane_id(), g = lane >> 4, i16 = lane & 15;
        u32 aW = lds0 + K1_W + (u32)cg * 10240u + (u32)(4 * g + (i16 >> 2)) * 160u + (u32)(i16 & 3) * 40u;
        u32 aX0 = lds0 + (u32)(64 * th + i16) * 128u + (u32)((g ^ (i16 >> 1)) * 16), aX1 = aX0 ^ 64u;
        u32 cnt, inc;
        asm volatile(K1_LOOP_ASM
                     : "=&{v[0:15]}"(ac[0]), "=&{v[16:31]}"(ac[1]), "=&{v[32:47]}"(ac[2]), "=&{v[48:63]}"(ac[3]), "=&{v[64:79]}"(ac[4]),
                       "+{v152}"(aX0), "+{v153}"(aX1), "+{v154}"(aW), [cnt] "=&s"(cnt), [inc] "=&s"(inc)
                     : [wrap] "s"(0u - 3u * K1_SLOT), [stp] "s"(K1_SLOT)
                     : "memory", "scc", K1_LOOP_CLOB);
      }
      HSTAMP(2); FSTAMP(2);
      const int lane = lane_id(), g = lane >> 4, i16 = lane & 15;
      const float sc = (cg && !type) ? QSCALE : 1.f;
#pragma unroll
      for (int n = 0; n < 4; ++n) {
        u32 v[10];
#pragma unroll
        for (int T = 0; T < 5; ++T) { v[2 * T] = pk2(ac[T][4 * n] * sc, ac[T][4 * n + 1] * sc); v[2 * T + 1] = pk2(ac[T][4 * n + 2] * sc, ac[T][4 * n + 3] * sc); }
        const int t32 = 16 * (n & 1) + i16;
        if (cg == 0) {
          ldsp xw = lds + K1_XCH + (u32)(2 * n + th) * 2560u;
          *(LDSAS u32x4*)(xw + lane * 16) = (u32x4){v[0], v[1], v[2], v[3]};
          *(LDSAS u32x4*)(xw + 1024 + lane * 16) = (u32x4){v[4], v[5], v[6], v[7]};
          *(LDSAS u32x2*)(xw + 2048 + lane * 8) = (u32x2){v[8], v[9]};
        } else if (type) {
          ldsp dst = lds + K1_VI + (u32)(64 * th + 16 * n + i16) * K1_VI_P + (u32)g * 48u;
          *(LDSAS u32x4*)(dst) = (u32x4){v[0], v[1], v[2], v[3]};
          *(LDSAS u32x4*)(dst + 16) = (u32x4){v[4], v[5], v[6], v[7]};
          *(LDSAS u32x4*)(dst + 32) = (u32x4){v[8], v[9], 0x00003F80u, 0u};
        } else if (th == 0) {
          ldsp dst = lds + K1_QST + (u32)(2 * g + (n >> 1)) * 1280u;
          *(LDSAS u32x4*)(dst + t32 * 16) = (u32x4){v[0], v[1], v[2], v[3]};
          *(LDSAS u32x4*)(dst + 512 + t32 * 16) = (u32x4){v[4], v[5], v[6], v[7]};
          *(LDSAS u32x2*)(dst + 1024 + t32 * 8) = (u32x2){v[8], v[9]};
        } else {
          const u32 dst = WS_QB + (u32)(tt * 8 + hg) * 10240u + (u32)(2 * g + (n >> 1)) * 1280u;
          st128(rW, dst + t32 * 16, (u32x4){v[0], v[1], v[2], v[3]});
          st128(rW, dst + 512 + t32 * 16, (u32x4){v[4], v[5], v[6], v[7]});
          st64(rW, dst + 1024 + t32 * 8, (u32x2){v[8], v[9]});
        }
      }
      LBAR();
      HSTAMP(3);
      if (w == 3) {
        const u32x2 tag = ld64(lds + F_TAG);
        const u32 fq = WS_FQ + (u32)(tt * 8 + hg) * 128u;
        if (type == 0) { DRAIN(); st64(rW, lane == 0 ? fq : OOB, tag); FSTAMP(3); }
        else { flags_wait<1>(rW, fq, 1, tag); lds_st(lds + F_QOK, 1u); FSTAMP(4); }
      }
    } else {
      const int lane = lane_id(), g = lane >> 4, i16 = lane & 15;
      if (w >= 8) {
        __builtin_amdgcn_s_setprio(1);
        const int lw = w - 8, g1 = lw >> 1, hb = lw & 1;
        const u32x4 rsX = mkrsrc(x - 4096, SEQ * DM * 2 + 4096);
        const bool useq = g1 && !type;
        const u32x4 rsW = useq ? mkrsrc(wq - 4096, DM * DM * 2 + 4096) : mkrsrc(wqkv - 4096, DM * 3 * DM * 2 + 4096);
        const u32 pitch = useq ? 1280u : 3840u;
        const u32 col = (type ? (g1 ? 1280u : 640u) : 0u) + 80u * hg;
        u32 vx[4], vw[5];
        const u32 vx0 = 4096u + (u32)(lane >> 3) * 1280u + (u32)(((lane & 7) ^ (lane >> 4)) * 16);
#pragma unroll
        for (int i = 0; i < 4; ++i) vx[i] = (vx0 ^ (u32)((i & 1) * 64)) + (u32)i * 9216u;
#pragma unroll
        for (int j = 0; j < 5; ++j) {
          const u32 c = 64u * j + lane, rho = (c * 205u) >> 11, cc = c - rho * 10u;
          const u32 k = 8u * ((rho >> 2) & 3u) + 4u * (rho >> 4) + (rho & 3u);
          vw[j] = 4096u + k * pitch + cc * 16u - (u32)(j & 3) * 1024u;
        }
        const u32 sx0 = (u32)(128 * tt + 32 * lw) * 1280u, sw0 = 32u * hb * pitch + col * 2u;
        int ks = K1_ROT;
        auto issue = [&](int s) {
          const u32 base = lds0 + (u32)(s & 3) * K1_SLOT;
          u32 keep;
          asm volatile(
              "s_mov_b32 %0, m0\n\ts_mov_b32 m0, %[mx]\n\ts_nop 2\n\t"
              "buffer_load_dwordx4 %[x0], %[rx], %[sx] offen lds\n\t"
              "buffer_load_dwordx4 %[x1], %[rx], %[sx] offen offset:1024 lds\n\t"
              "buffer_load_dwordx4 %[x2], %[rx], %[sx] offen offset:2048 lds\n\t"
              "buffer_load_dwordx4 %[x3], %[rx], %[sx] offen offset:3072 lds\n\t"
              "s_mov_b32 m0, %[mw]\n\ts_nop 0\n\t"
              "buffer_load_dwordx4 %[w0], %[rw], %[sw] offen lds\n\t"
              "buffer_load_dwordx4 %[w1], %[rw], %[sw] offen offset:1024 lds\n\t"
              "buffer_load_dwordx4 %[w2], %[rw], %[sw] offen offset:2048 lds\n\t"
              "buffer_load_dwordx4 %[w3], %[rw], %[sw] offen offset:3072 lds\n\t"
              "s_add_u32 m0, %[mw], 4096\n\ts_nop 0\n\t"
              "buffer_load_dwordx4 %[w4], %[rw], %[sw] offen lds\n\t"
              "s_mov_b32 m0, %0"
              : "=&s"(keep)
              : [mx] "s"(base + (u32)lw * 4096u), [mw] "s"(base + K1_W + (u32)lw * 5120u),
                [rx] "s"(rsX), [rw] "s"(rsW), [sx] "s"(sx0 + (u32)ks * 128u), [sw] "s"(sw0 + (u32)ks * 64u * pitch),
                [x0] "v"(vx[0]), [x1] "v"(vx[1]), [x2] "v"(vx[2]), [x3] "v"(vx[3]), [w0] "v"(vw[0]), [w1] "v"(vw[1]), [w2] "v"(vw[2]), [w3] "v"(vw[3]),
                [w4] "v"(vw[4])
              : "memory", "scc");
          ks = ks == 9 ? 0 : ks + 1;
        };
        __builtin_amdgcn_s_sleep(LDELAY);
        issue(0); issue(1);
        WAIT_BAR(9);
        HSTAMP(2); FSTAMP(2);
        issue(2);
#pragma unroll 1
        for (int s = 1; s < 8; ++s) {
          issue(s + 2);
          WAIT_BAR(18);
        }
        WAIT_BAR(9);
        WAIT_BAR(0);
        lds_wait_ge<1>(lds + F_KVOK, 1);
        {
          const u32 o = (u32)lw * 13312u, vl = (u32)lane * 16u;
#pragma unroll 1
          for (u32 c = 0; c < 3; ++c) dma4(rsWS, vl, WS_KVIM + o + c * 4096u, lds0 + L_RING + o + c * 4096u);
          dma1s(rsWS, vl, WS_KVIM + o + 12288u, lds0 + L_RING + o + 12288u);
          dma1s(rsWS, vl, WS_KVIM + 52736u, lds0 + L_RING + 52736u);
        }
        if (lane < 8) lds_st(lds + L_FLG + 32 + lane * 4, 5u);
        DRAIN();
      } else {
        const int v4 = w - 4, tok0 = 128 * tt + 16 * hg;
        __builtin_amdgcn_s_setprio(3);
        {
          const u32x4 rsE = mkrsrc((type ? wv : wk) + v4 * 6400, 6400);
          const u32 la = lds0 + K1_EX + (u32)v4 * 7168u;
#pragma unroll 1
          for (u32 c = 0; c < 7; ++c) dma1(rsE, (u32)lane * 16u, c * 1024u, la + c * 1024u);
          u32x4 xf[5], gf[5];
#pragma unroll
          for (int s = 0; s < 5; ++s) {
            xf[s] = *(const u32x4*)(x + (size_t)(tok0 + i16) * 1280 + v4 * 320 + s * 64 + g * 16);
            gf[s] = *(const u32x4*)(gw + v4 * 320 + s * 64 + g * 16);
          }
          u64 e = 0;
          if (w == 7) e = __hip_atomic_load((const gu64*)(ws + WS_EPOCH), __ATOMIC_RELAXED, __HIP_MEMORY_SCOPE_AGENT) + 0x9E3779B97F4A7C15ull;
          DRAIN();
          if (w == 7) lds_st(lds + L_FLG + lane * 4, lane == 34 ? (u32)e : (lane == 35 ? (u32)(e >> 32) : 0u));
          ldsp pe = lds + K1_EX + (u32)v4 * 7168u + (u32)(8 * g + (i16 >> 2)) * 40u + (u32)(i16 & 3) * 8u;
          f32x4 c0 = {0.f, 0.f, 0.f, 0.f}, c1 = c0, c2 = c0;
#pragma unroll
          for (int s = 0; s < 5; ++s) {
            const u32x4 w0 = trd2(pe + s * 1280, pe + s * 1280 + 160), w1 = trd2(pe + s * 1280 + 32, pe + s * 1280 + 192);
            if (type) { c0 = mfma16(xf[s], w0, c0); c1 = mfma16(xf[s], w1, c1); }
            else { c0 = mfma16(w0, xf[s], c0); c1 = mfma16(w1, xf[s], c1); }
            c2 = mfma16(gf[s], xf[s], c2);
          }
          ldsp pp = lds + K1_EXP + (u32)v4 * 1024u + lane * 16;
          *(LDSAS f32x4*)(pp) = c0; *(LDSAS f32x4*)(pp + 4096) = c1; *(LDSAS f32x4*)(pp + 8192) = c2;
        }
        LBAR();
        if (v4 < 3) {
          ldsp pp = lds + K1_EXP + (u32)v4 * 4096u + lane * 16;
          const f32x4 e0 = (*(LDSAS f32x4*)(pp) + *(LDSAS f32x4*)(pp + 1024)) + (*(LDSAS f32x4*)(pp + 2048) + *(LDSAS f32x4*)(pp + 3072));
          const u32x2 ev = {pk2(e0[0], e0[1]), pk2(e0[2], e0[3])};
          const int tile = tok0 >> 5, k0 = tok0 & 31;
          if (v4 == 2) {
            st32(rW, (type == 0 && g == 0) ? WS_GATE + (u32)(tok0 + i16) * 4u : OOB, __float_as_uint(__builtin_amdgcn_rcpf(1.f + __builtin_amdgcn_exp2f(-e0[0] * LOG2E))));
          } else if (type == 0) {
            const u32 rec = WS_KVIM + (u32)((tile + 62) & 63) * REC;
            if (v4 == 0) st64(rW, rec + (g >> 1) * 512 + (k0 + i16) * 16 + (g & 1) * 8, ev);
            else { const u32 o = g ? OOB : rec + 1024 + (k0 + i16) * 4; st32(rW, o, ev[0]); st32(rW, o + 128, ev[1]); }
          } else {
            const int d = 16 * v4 + i16, k32 = k0 + 4 * g;
            const u32x2 val = d == 20 ? (u32x2){0x3F803F80u, 0x3F803F80u} : (d == 21 ? (u32x2){0u, 0u} : ev);
            st64(rW, d <= 21 ? WS_KVIM + (u32)tile * REC + 1280 + (k32 >> 3) * 352 + d * 16 + (k32 & 7) * 2 : OOB, val);
          }
        }
        LBAR();
        {
          const u32 be = (u32)(v4 * 64 + lane) * 16u;
          dma1(mkrsrc(wphi, NH * 400 * 2), be < 3200u ? be : OOB, (u32)hg * 3200u, lds0 + K1_WPHI + (u32)v4 * 1024u);
        }
        LBAR();
        WAIT_BAR(0);
        const u32x2 tag = ld64(lds + F_TAG);
        if (w == 7) {
          st64(rW, lane < 8 ? WS_F1 + (u32)lane * 2048u + (u32)bid * 8u : OOB, tag);
          FSTAMP(3);
          LBAR(); LBAR(); LBAR(); LBAR(); LBAR();
          const u32 fo = WS_F1 + (u32)xcd * 2048u;
          u32x4 fa = gl128(rW, lane * 16, fo), fb = gl128(rW, lane * 16, fo + 1024);
          LBAR();
          PIN(fa); PIN(fb);
          const u32 dd = (fa[0] ^ tag[0]) | (fa[1] ^ tag[1]) | (fa[2] ^ tag[0]) | (fa[3] ^ tag[1]) | (fb[0] ^ tag[0]) | (fb[1] ^ tag[1]) | (fb[2] ^ tag[0]) | (fb[3] ^ tag[1]);
          if (!__all(dd == 0u)) flags_wait256<2>(rW, fo, tag);
          asm volatile("" ::: "memory");
          FSTAMP(4);
          const u32x4 ka = gl128(rW, lane * 16, WS_KVIM + 62 * REC), kb = gl128(rW, lane * 16, WS_KVIM + 63 * REC);
          const u32 kc = gl32(rW, lane * 4, WS_KVIM + 62 * REC + 1024), kd = gl32(rW, lane * 4, WS_KVIM + 63 * REC + 1024);
          const u32 gtv = gl32(rW, lane * 4, WS_GATE + (u32)(2 * tt + type) * 256u);
          u32 wrm = gl32(rW, lane < 29 ? (u32)(jb * 29 + lane) * 128u : OOB, WS_KVIM + 5 * SL);
          asm volatile("" ::: "memory");
          lds_st(lds + F_KVOK, 1u);
          if (bid == 0 && lane == 0)
            __hip_atomic_store((gu64*)(ws + WS_EPOCH), (u64)tag[0] | ((u64)tag[1] << 32), __ATOMIC_RELAXED, __HIP_MEMORY_SCOPE_AGENT);
          *(LDSAS u32x4*)(lds + L_K01 + lane * 16) = ka; *(LDSAS u32*)(lds + L_K01 + 1024 + lane * 4) = kc;
          *(LDSAS u32x4*)(lds + L_K01 + 1280 + lane * 16) = kb; *(LDSAS u32*)(lds + L_K01 + 2304 + lane * 4) = kd;
          *(LDSAS u32*)(lds + L_GT + lane * 4) = gtv;
          PIN(wrm);
        } else {
          LBAR(); LBAR(); LBAR(); LBAR(); LBAR(); LBAR();
        }
      }
      __builtin_amdgcn_s_setprio(2);
      const int j8 = w - 4;
      const u32 aP = K1_WPHI + (((i16 & 3) == g) ? (u32)g * 800u + (u32)(i16 >> 2) * 40u : 3200u);
      u32x4 a0[5], a1[5]; u32x2 a2[5];
#pragma unroll
      for (int T = 0; T < 5; ++T) {
        a0[T] = trd2(lds + aP + T * 8, lds + aP + T * 8 + 160);
        a1[T] = trd2(lds + aP + T * 8 + 320, lds + aP + T * 8 + 480);
        a2[T] = trd(lds + aP + T * 8 + 640);
      }
      LBAR();
      HSTAMP(3);
      ldsp xr = lds + K1_XCH + (u32)j8 * 2560u;
      const u32x4 bq0 = ld128(xr + lane * 16), bq1 = ld128(xr + 1024 + lane * 16);
      const u32x2 bq2 = ld64(xr + 2048 + lane * 8);
      f32x4 pq[5];
#pragma unroll
      for (int T = 0; T < 5; ++T) pq[T] = mfma16(a0[T], bq0, (f32x4){0.f, 0.f, 0.f, 0.f});
#pragma unroll
      for (int T = 0; T < 5; ++T) pq[T] = mfma16(a1[T], bq1, pq[T]);
#pragma unroll
      for (int T = 0; T < 5; ++T) pq[T] = mfma16((u32x4){a2[T][0], a2[T][1], 0u, 0u}, (u32x4){bq2[0], bq2[1], 0u, 0u}, pq[T]);
      float mx = fmaxf(pq[0][0], pq[0][1]), mn = fminf(pq[0][0], pq[0][1]);
      mx = fmaxf(fmaxf(mx, pq[0][2]), pq[0][3]); mn = fminf(fminf(mn, pq[0][2]), pq[0][3]);
#pragma unroll
      for (int T = 1; T < 5; ++T) {
        mx = fmaxf(fmaxf(mx, pq[T][0]), pq[T][1]); mx = fmaxf(fmaxf(mx, pq[T][2]), pq[T][3]);
        mn = fminf(fminf(mn, pq[T][0]), pq[T][1]); mn = fminf(fminf(mn, pq[T][2]), pq[T][3]);
      }
      const float cp = -mx * LOG2E, cn = mn * LOG2E;
      f32x4 ep[5], en[5];
#pragma unroll
      for (int T = 0; T < 5; ++T) { ep[T] = pq[T] * LOG2E + cp; en[T] = pq[T] * (-LOG2E) + cn; PIN(ep[T]); PIN(en[T]); }
#pragma unroll
      for (int T = 0; T < 5; ++T)
#pragma unroll
        for (int i = 0; i < 4; ++i) { ep[T][i] = __builtin_amdgcn_exp2f(ep[T][i]); en[T][i] = __builtin_amdgcn_exp2f(en[T][i]); }
      const f32x4 sp = (ep[0] + ep[1]) + (ep[2] + ep[3]) + ep[4], sn = (en[0] + en[1]) + (en[2] + en[3]) + en[4];
      const float rp = __builtin_amdgcn_rcpf((sp[0] + sp[1]) + (sp[2] + sp[3])), rn = __builtin_amdgcn_rcpf((sn[0] + sn[1]) + (sn[2] + sn[3]));
      u32 ph[20];
#pragma unroll
      for (int T = 0; T < 5; ++T) {
        const f32x4 a = ep[T] * rp, b = en[T] * rn;
        ph[2 * T] = pk2(a[0], a[1]); ph[2 * T + 1] = pk2(a[2], a[3]);
        ph[10 + 2 * T] = pk2(b[0], b[1]); ph[11 + 2 * T] = pk2(b[2], b[3]);
      }
      const int tl = 64 * (j8 & 1) + 16 * (j8 >> 1) + i16;
      if (type == 0) {
        ldsp dst = lds + K1_PHQ + (u32)(4 * g + (tl >> 5)) * 2560u + (tl & 31) * 16;
#pragma unroll
        for (int c = 0; c < 5; ++c) *(LDSAS u32x4*)(dst + c * 512) = (u32x4){ph[4 * c], ph[4 * c + 1], ph[4 * c + 2], ph[4 * c + 3]};
      } else {
        ldsp dst = lds + K1_PHK + (u32)tl * K1_PHK_P + (u32)g * 80u;
#pragma unroll
        for (int c = 0; c < 5; ++c) *(LDSAS u32x4*)(dst + 16 * c) = (u32x4){ph[4 * c], ph[4 * c + 1], ph[4 * c + 2], ph[4 * c + 3]};
        lds_arrive(lds + F_PHB);
        lds_wait_ge<1>(lds + F_PHB, 8);
        const int hl = j8 >> 1, dt = j8 & 1;
        const u32 trow = (u32)(8 * g + (i16 >> 2));
        ldsp pa = lds + K1_PHK + trow * K1_PHK_P + (u32)hl * 80u + (u32)(i16 & 3) * 8u;
        ldsp pb = lds + K1_VI + trow * K1_VI_P + (u32)hl * 48u + (u32)dt * 32u + (u32)(i16 & 3) * 8u;
        f32x4 kv[3];
#pragma unroll
        for (int ft = 0; ft < 3; ++ft) kv[ft] = (f32x4){0.f, 0.f, 0.f, 0.f};
#pragma unroll
        for (int ks = 0; ks < 4; ++ks) {
          u32x4 bv = trd2(pb + ks * 32 * K1_VI_P, pb + (ks * 32 + 4) * K1_VI_P);
#pragma unroll
          for (int ft = 0; ft < 3; ++ft) {
            u32x4 av = trd2(pa + ks * 32 * K1_PHK_P + ft * 32, pa + (ks * 32 + 4) * K1_PHK_P + ft * 32);
            kv[ft] = mfma16(av, bv, kv[ft]);
          }
        }
#pragma unroll
        for (int ft = 0; ft < 3; ++ft) *(LDSAS f32x4*)(lds + K1_KVF + (u32)j8 * 3072u + ft * 1024 + lane * 16) = kv[ft];
      }
      lds_arrive(lds + F_P2);
      HSTAMP(4);
    }
  }

  if (w >= 8) {
    __builtin_amdgcn_s_setprio(3);
    const int hw = w - 8, kh = hw >> 1, t64 = xcd * 4 + (jb & 3), cgp = jb >> 2;
    const u32x2 tag = ld64(lds + F_TAG);
    u32 s = 5, slot = 0, ps = 5;
    auto stream = [&](u32 word, u32 want) {
      const int ln = lane_id();
      const u32 vl = (u32)ln * 16u;
#pragma unroll 1
      for (u32 it = 0; it < SPIN_CAP && ps < 16; ++it) {
        const u32 fl = lds_ld(lds + (ln < 8 ? L_FLG + ln * 4 : word));
        const u32 ns = s;
        const bool go = __all(ln < 8 ? fl + 3 <= s : fl >= want);
        if (!go && s < 16 && __all(ln >= 8 || fl + 4 >= s)) {
          const u32 so = WS_KVIM + s * SL, la = lds0 + L_RING + slot * SL;
          dma4(rsWS, vl, so, la); dma4(rsWS, vl, so + 4096u, la + 4096u); dma2h(rsWS, vl, so + 8192u, la + 8192u);
          asm volatile("s_waitcnt vmcnt(11)" ::: "memory");
          ++s; slot = slot == 4 ? 0 : slot + 1;
        } else {
          DRAIN();
          if (ps == ns) {
            if (__all(ln < 8 || fl >= want)) break;
            __builtin_amdgcn_s_sleep(4);
          }
        }
        if (ps != ns) { ps = ns; if (ln < 8) lds_st(lds + L_FLG + 32 + ln * 4, ps); }
      }
    };
    auto image = [&](const char* wo) {
      const int ln = lane_id();
      const u32x4 rsO = mkrsrc(wo, DM * DM * 2);
#pragma unroll 1
      for (u32 pp = hw; pp < 25; pp += 3) {
        const u32 c = 64u * pp + ln, rho = (c * 6554u) >> 16, cc = c - rho * 10u;
        const u32 U = 4u * (rho >> 5) + ((rho >> 2) & 3u), u = (rho >> 4) & 1u, q = rho & 3u;
        const u32 hp = (U * 205u) >> 10, un = U - hp * 5u;
        const u32 row = 40u * hp + q + ((un < 4u) ? (un >> 1) * 20u + 4u * (un & 1u) + 8u * u : u * 20u + 16u);
        const u32 vo = row * 1280u + cc * 16u;
#pragma unroll
        for (u32 per = 0; per < 4; ++per) dma1(rsO, vo, per * 204800u + (u32)cgp * 160u, lds0 + L_IMG + (per * 25u + pp) * 1024u);
      }
      DRAIN();
    };
    lds_wait_ge<2>(lds + F_P2, 8);
    {
      const int lane = lane_id(), g = lane >> 4, i16 = lane & 15, r32 = lane & 31, hh = lane >> 5;
      u32x4 p0[4], p1[4], p2[4]; u32 gt[4];
      if (type) {
#pragma unroll
        for (int i = 0; i < 2; ++i) {
          const int d = 16 * i + i16;
          const u32 dst = d <= 20 ? WS_KVP + (u32)((hg * 16 + tt) * 4 + hw) * 3360u + d * 160 + 16 * g : OOB;
          ldsp src = lds + K1_KVF + (u32)(2 * hw + i) * 3072u + lane * 16;
          st128(rW, dst, ld128(src)); st128(rW, dst + 64, ld128(src + 1024)); st128(rW, g < 2 ? dst + 128 : OOB, ld128(src + 2048));
        }
        DRAIN();
        if (lds_arrive(lds + F_P3) == 3) { st64(rW, lane == 0 ? WS_F2 + (u32)(hg * 16 + tt) * 8u : OOB, tag); GSTAMP(3); }
        if (hw == 0) {
          G2(2);
          flags_wait<1>(rW, WS_F2 + (u32)hg * 128u, 16, tag);
          G2(3);
          const int it = tt * 53 + lane;
          const bool ok = lane < 53 && it < 840;
          const u32 vo = ok ? (u32)it * 16u : OOB, pb = WS_KVP + (u32)hg * 16u * 13440u;
          f32x4 a = __builtin_bit_cast(f32x4, gl128(rW, vo, pb));
#pragma unroll
          for (int p = 1; p < 16; ++p) a += __builtin_bit_cast(f32x4, gl128(rW, vo, pb + p * 13440));
          st64(rW, ok ? WS_KVT + (u32)hg * 6720u + (u32)it * 8u : OOB, (u32x2){pk2(a[0], a[1]), pk2(a[2], a[3])});
          G2(4);
          DRAIN();
          G2(5);
          st64(rW, lane == 0 ? WS_F3 + (u32)(hg * 16 + tt) * 8u : OOB, tag);
          GSTAMP(4);
          lds_arrive(lds + F_P3);
        }
      } else {
#pragma unroll
        for (int t4 = 0; t4 < 4; ++t4) {
          ldsp pk = lds + K1_PHQ + (u32)(4 * hw + t4) * 2560u;
          p0[t4] = ld128(pk + lane * 16); p1[t4] = ld128(pk + 1024 + lane * 16); p2[t4] = ld128(pk + 2048 + r32 * 16);
          gt[t4] = gl32(rW, r32 * 4, WS_GATE + (u32)(128 * tt + 32 * t4) * 4u);
          PIN(p0[t4]); PIN(p1[t4]); PIN(p2[t4]);
        }
        lds_arrive(lds + F_P3);
        if (hw == 0) { lds_wait_ge<4>(lds + F_QOK, 8); lds_arrive(lds + F_P3); }
      }
      if (w < 11) {
        lds_wait_ge<4>(lds + F_P3, 5);
        image(awo);
        lds_arrive(lds + F_AOK);
      }
      if (type == 0) {
        if (hw == 0) { flags_wait<1>(rW, WS_F3 + (u32)hg * 128u, 16, tag); lds_st(lds + F_GBAR, 1u); }
        if (hw == 1 || hw == 2) {
          const u32 qi = (u32)((jb >> 4) * 8 + (jb & 7)) * 400u + (u32)(hw - 1) * 200u;
          u32 dummy;
          asm volatile("s_nop 4\n\tbuffer_load_dword %0, %1, %2, 0 offen\n\tbuffer_load_dword %0, %1, %2, %4 offen\n\tbuffer_load_dword %0, %1, %2, %5 offen\n\t"
                       "buffer_load_dword %0, %3, %2, 0 offen\n\ts_waitcnt vmcnt(0)"
                       : "=&v"(dummy) : "v"((qi + lane) * 128u), "s"(mkrsrc(bwo, DM * DM * 2)), "v"(lane < 8 ? (qi + 192u + lane) * 128u : OOB), "s"(8192u), "s"(16384u) : "memory");
        }
        if (w == 11) stream(F_GBAR, 1u);
        if (hw >= 2) G2(2);
        lds_wait_ge<2>(lds + F_GBAR, 1);
        if (hw >= 2) G2(3);
        const u32 ao = (u32)(r32 < 20 ? r32 : 20) * 80u + (u32)hh * 16u, ab = WS_KVT + (u32)(4 * hg + hw) * 1680u;
        const u32x4 a0 = gl128(rW, ao, ab), a1 = gl128(rW, ao, ab + 32), a2 = gl128(rW, hh ? OOB : ao, ab + 64);
        const u32 vo0 = WS_OFR + (u32)(8 * tt + (r32 >> 4)) * 20480u + (r32 & 15) * 16 + (u32)(2 * hg + (hw >> 1)) * 1280u;
        const u32 vo1 = vo0 + hh * 256 + (hw & 1) * 512, vo2 = hh ? OOB : vo0 + 1024 + (hw & 1) * 8;
        f32x16 N[4];
#pragma unroll
        for (int t4 = 0; t4 < 4; ++t4) N[t4] = mfma32(a0, p0[t4], zero16);
#pragma unroll
        for (int t4 = 0; t4 < 4; ++t4) N[t4] = mfma32(a1, p1[t4], N[t4]);
#pragma unroll
        for (int t4 = 0; t4 < 4; ++t4) N[t4] = mfma32(a2, p2[t4], N[t4]);
#pragma unroll
        for (int t4 = 0; t4 < 4; ++t4) {
          const float sa = __uint_as_float(gt[t4]) * __builtin_amdgcn_rcpf(N[t4][12] + 1e-6f);
          st128(rW, vo1 + t4 * 40960, (u32x4){pk2(N[t4][0] * sa, N[t4][1] * sa), pk2(N[t4][2] * sa, N[t4][3] * sa), pk2(N[t4][4] * sa, N[t4][5] * sa), pk2(N[t4][6] * sa, N[t4][7] * sa)});
          st64(rW, vo2 + t4 * 40960, (u32x2){pk2(N[t4][8] * sa, N[t4][9] * sa), pk2(N[t4][10] * sa, N[t4][11] * sa)});
        }
        if (hw >= 2) G2(5);
        DRAIN();
        if (hw >= 2) G2(6);
        if (lds_arrive(lds + F_APUB) == 3) { st64(rW, lane == 0 ? WS_FLGA + (u32)(tt * 16 + hg) * 8u : OOB, tag); GSTAMP(5); }
      }
      if (hw == 0) { flags_wait<1>(rW, WS_FLGA + (u32)(t64 >> 1) * 128u, 8, tag); lds_arrive(lds + F_AOK); }
      HSTAMP(6);
    }
    f32x16 ac0 = zero16, ac1 = zero16; f32x8 ac2 = {0.f, 0.f, 0.f, 0.f, 0.f, 0.f, 0.f, 0.f};
#pragma unroll 1
    for (int part = 0; part < 2; ++part) {
      if (w == 11) stream(F_AOK, part ? 0xffffffffu : 4u);
      else if (part) { lds_wait_ge<2>(lds + F_AFREE, 4u); image(bwo); lds_arrive(lds + F_BIMG); GSTAMP(2); }
      if (part == 0) { lds_wait_ge<2>(lds + F_AOK, 4u); GSTAMP(6); if (hw == 0) G2(6); }
      else { lds_wait_ge<4>(lds + F_FIN, FINW); TSTAMP(3); flags_wait<BSLEEP>(rW, WS_FLGB + (u32)t64 * 256u + (u32)kh * 128u, 16, tag); TSTAMP(4); FSTAMP(7); }
      const int ln = lane_id();
      const u32 vl = (u32)ln * 16u;
      u32 s0 = WS_OFR + (u32)(part * 2621440 + (4 * t64 + 2 * (hw & 1)) * 20480 + kh * 10240), s1 = s0 + 20480u, it;
      u32 pa = lds0 + F_BIMG;
      asm volatile(TILE_GEMM_ASM
                   : "+{v[0:15]}"(ac0), "+{v[16:31]}"(ac1), "+{v[32:39]}"(ac2), "+{v142}"(pa), [s0] "+s"(s0), [s1] "+s"(s1), [it] "=&s"(it)
                   : "{v140}"(vl), "{v141}"(lds0 + L_IMG + (u32)(4 * (ln >> 4) + ((ln & 15) >> 2)) * 160u + (u32)(ln & 3) * 40u + (u32)kh * 51200u), [rs] "s"(rsWS), [want] "s"(part ? 3u : 0u),
                     [afree] "s"(lds0 + F_AFREE)
                   : "memory", "scc", "vcc", TILE_GEMM_CLOB);
      if (part == 0) { HSTAMP(7); G2(7); }
    }
    TSTAMP(5);
    {
      const int ln = lane_id();
      f32x4 acc[5][2];
#pragma unroll
      for (int n = 0; n < 2; ++n) {
        acc[0][n] = (f32x4){ac0[4 * n], ac0[4 * n + 1], ac0[4 * n + 2], ac0[4 * n + 3]}; acc[1][n] = (f32x4){ac0[8 + 4 * n], ac0[9 + 4 * n], ac0[10 + 4 * n], ac0[11 + 4 * n]};
        acc[2][n] = (f32x4){ac1[4 * n], ac1[4 * n + 1], ac1[4 * n + 2], ac1[4 * n + 3]}; acc[3][n] = (f32x4){ac1[8 + 4 * n], ac1[9 + 4 * n], ac1[10 + 4 * n], ac1[11 + 4 * n]};
        acc[4][n] = (f32x4){ac2[4 * n], ac2[4 * n + 1], ac2[4 * n + 2], ac2[4 * n + 3]};
      }
      ldsp xb = lds + L_RING + 3 * SL;
      ldsp xw = xb + (u32)hw * 5120u + ln * 16, xr = xb + (u32)(hw ^ 2) * 5120u + ln * 16;
#pragma unroll
      for (int T = 0; T < 5; ++T) *(LDSAS f32x4*)(xw + T * 1024) = kh ? acc[T][0] : acc[T][1];
      lds_arrive(lds + F_GBAR + 48 + (hw & 1) * 4);
      lds_wait_ge<0>(lds + F_GBAR + 48 + (hw & 1) * 4, 2u);
      TSTAMP(6);
      u32 o[10];
#pragma unroll
      for (int T = 0; T < 5; ++T) {
        const f32x4 v = (kh ? acc[T][1] : acc[T][0]) + *(LDSAS f32x4*)(xr + T * 1024);
        o[2 * T] = pk2(v[0], v[1]); o[2 * T + 1] = pk2(v[2], v[3]);
      }
      typedef u32x4 u32x4a __attribute__((aligned(8)));
      char* dst = out + (size_t)(64 * t64 + 32 * (hw & 1) + 16 * kh + (ln & 15)) * 1280 + cgp * 160 + (ln >> 4) * 40;
      *(u32x4a*)dst = (u32x4){o[0], o[1], o[2], o[3]};
      *(u32x4a*)(dst + 16) = (u32x4){o[4], o[5], o[6], o[7]};
      *(u32x2*)(dst + 32) = (u32x2){o[8], o[9]};
    }
    STAMP_FLUSH();
    return;
  }

  const int pr = w & 3, par = w >> 2, head = 4 * hg + pr, t64 = 2 * tt + type;
  u32x4 q1[2]; u32x2 q2[2];
  f32x16 O0, O1;
  {
    const int lane = lane_id(), r32 = lane & 31, hh = lane >> 5;
    if (type) {
      lds_wait_ge<1>(lds + F_QOK, 1);
#pragma unroll
      for (int j = 0; j < 2; ++j) {
        const u32 qrow = WS_QB + (u32)(tt * 8 + hg) * 10240u + (u32)(2 * pr + j) * 1280u;
        q1[j] = gl128(rW, lane * 16, qrow);
        q2[j] = (u32x2){gl32(rW, r32 * 8 + hh * 4, qrow + 1024), 0u};
      }
    } else {
#pragma unroll
      for (int j = 0; j < 2; ++j) {
        ldsp qrow = lds + K1_QST + (u32)(2 * pr + j) * 1280u;
        q1[j] = ld128(qrow + lane * 16);
        q2[j] = (u32x2){*(LDSAS u32*)(qrow + 1024 + r32 * 8 + hh * 4), 0u};
      }
      PIN(q1[0]); PIN(q1[1]); PIN(q2[0]); PIN(q2[1]);
      lds_arrive(lds + F_QOK);
    }
    const u32x4 k1a = ld128(lds + L_K01 + lane * 16);
    const u32 k2a = *(LDSAS u32*)(lds + L_K01 + 1024 + lane * 4);
    u32x4 k1 = ld128(lds + L_K01 + par * 1280 + lane * 16);
    u32x2 k2 = {*(LDSAS u32*)(lds + L_K01 + par * 1280 + 1024 + lane * 4), hh ? 0u : 0x00003F80u};
    f32x16 S0, S1, S2, S3;
#pragma unroll
    for (int j = 0; j < 2; ++j) {
      f32x16 t = mfma32(k1a, q1[j], zero16);
      t = mfma32k8((u32x2){k2a, 0u}, q2[j], t);
      const float m = xhalf_max(max16(t));
      q2[j][1] = hh ? 0u : (pk2(-m, 0.f) & 0xffffu);
    }
    S0 = mfma32(k1, q1[0], zero16);
    S0 = mfma32k8(k2, q2[0], S0);
    u32x4 vlo, vhi;
    const u32 rb = lds0 + L_RING + (u32)par * REC;
    u32 aK1 = rb + (u32)lane * 16u, aK2 = rb + 1024u + (u32)lane * 4u;
    u32 aV = rb + 1280u + (u32)hh * 352u + (u32)(r32 <= 20 ? r32 : 21) * 16u;
    u32 aF = lds0 + L_FLG + (u32)w * 4u, tmp;
    HSTAMP(5); FSTAMP(5);
    u32 cnt, inc, slot, rdy;
    asm volatile(ATTN_LOOP_ASM
                 : "=&{v[0:15]}"(O0), "=&{v[16:31]}"(O1), "+{v[32:47]}"(S0), "=&{v[48:63]}"(S1), "=&{v[64:79]}"(S2), "=&{v[80:95]}"(S3),
                   "+{v[108:111]}"(k1), "+{v[112:113]}"(k2), "=&{v[116:119]}"(vlo), "=&{v[120:123]}"(vhi),
                   "+{v124}"(aK1), "+{v125}"(aK2), "+{v126}"(aV), "=&{v115}"(tmp), [cnt] "=&s"(cnt), [inc] "=&s"(inc), [slot] "=&s"(slot), [rdy] "=&s"(rdy)
                 : "{v[96:99]}"(q1[0]), "{v[100:103]}"(q1[1]), "{v[104:105]}"(q2[0]), "{v[106:107]}"(q2[1]), "{v114}"(0xDDDD), "{v127}"(aF),
                   [wrap] "s"(0u - 4u * SL), [stg] "s"(SL), [par] "s"(par)
                 : "memory", "scc");
  }
  __builtin_amdgcn_s_setprio(3);
  HSTAMP(6); TSTAMP(2); FSTAMP(6);
  const int lane = lane_id(), r32 = lane & 31, hh = lane >> 5;
  ldsp mg = lds + L_MRG + (u32)pr * (2 * REC);
  if (lds_arrive(lds + L_FLG + 64 + pr * 4) == 0) {
#pragma unroll 1
    for (u32 it = 0; it < SPIN_CAP; ++it) {
      if (__all(lds_ld(lds + L_FLG + (lane & 7) * 4) >= 13u)) break;
      __builtin_amdgcn_s_sleep(2);
    }
    asm volatile("" ::: "memory");
#pragma unroll
    for (int j = 0; j < 2; ++j) {
      const f32x16 snd = j ? O1 : O0;
      ldsp mw = mg + j * REC;
      *(LDSAS f32x4*)(mw + lane * 16) = (f32x4){snd[0], snd[1], snd[2], snd[3]};
      *(LDSAS f32x4*)(mw + 1024 + lane * 16) = (f32x4){snd[4], snd[5], snd[6], snd[7]};
      if (!hh) *(LDSAS f32x4*)(mw + 2048 + r32 * 16) = (f32x4){snd[8], snd[9], snd[10], snd[11]};
      else *(LDSAS float*)(mw + 2560 + r32 * 4) = snd[8];
    }
    asm volatile("s_waitcnt lgkmcnt(0)" ::: "memory");
    lds_st(lds + L_FLG + 80 + pr * 4, 1u);
    TSTAMP(3);
    STAMP_FLUSH();
    return;
  }
  lds_wait_ge<FSLEEP>(lds + L_FLG + 80 + pr * 4, 1u);
  TSTAMP(5);
  const u32x2 tag = ld64(lds + F_TAG);
#pragma unroll
  for (int j = 0; j < 2; ++j) {
    f32x16 O = j ? O1 : O0;
    ldsp mr = mg + j * REC;
    const f32x4 a = *(LDSAS f32x4*)(mr + lane * 16), b = *(LDSAS f32x4*)(mr + 1024 + lane * 16), c = *(LDSAS f32x4*)(mr + 2048 + r32 * 16);
    float l = from_hi(O[8]) + *(LDSAS float*)(mr + 2560 + r32 * 4);
    const float gt = *(LDSAS float*)(lds + L_GT + (32 * j + r32) * 4);
#pragma unroll
    for (int i = 0; i < 4; ++i) { O[i] += a[i]; O[4 + i] += b[i]; O[8 + i] += c[i]; }
    float t = 0.f;
#pragma unroll
    for (int r = 0; r < 12; ++r) t = __builtin_fmaf(O[r], 0.f, t);
    const bool bad = !(t == 0.f) || !(l < 1.0e30f);
    if (__builtin_expect(__any(bad), 0)) {
      O = attn_exact(rW, q1[j], q2[j][0], lane);
      l = from_hi(O[8]);
    }
    const float sb = (1.f - gt) * __builtin_amdgcn_rcpf(l);
    const u32 vo = WS_OFR + 2621440u + (u32)(4 * t64 + 2 * j + (r32 >> 4)) * 20480u + (r32 & 15) * 16 + (head >> 1) * 1280;
    st128(rW, vo + hh * 256 + (head & 1) * 512, (u32x4){pk2(O[0] * sb, O[1] * sb), pk2(O[2] * sb, O[3] * sb), pk2(O[4] * sb, O[5] * sb), pk2(O[6] * sb, O[7] * sb)});
    st64(rW, hh ? OOB : vo + 1024 + (head & 1) * 8, (u32x2){pk2(O[8] * sb, O[9] * sb), pk2(O[10] * sb, O[11] * sb)});
  }
  HSTAMP(7); TSTAMP(6);
  DRAIN();
  TSTAMP(7); FSTAMP(7);
  st64(rW, lane == 0 ? WS_FLGB + (u32)(t64 * 32 + head) * 8u : OOB, tag);
  lds_arrive(lds + F_FIN);
  STAMP_FLUSH();
}

extern "C" void kernel_launch(void* const* d_in, const int* in_sizes, int n_in, void* d_out, int out_size, void* d_ws,
                              size_t ws_size, hipStream_t stream) {
  (void)in_sizes; (void)n_in; (void)out_size; (void)ws_size;
  (void)hipFuncSetAttribute((const void*)gm_fused, hipFuncAttributeMaxDynamicSharedMemorySize, LDS_BYTES);
  gm_fused<<<256, 768, LDS_BYTES, stream>>>((const char*)d_in[0], (const char*)d_in[1], (const char*)d_in[2], (const char*)d_in[5], (const char*)d_in[6],
      (const char*)d_in[7], (const char*)d_in[9], (const char*)d_in[3], (const char*)d_in[8], (char*)d_ws, (char*)d_out);
}
```

```cpp
#include <hip/hip_runtime.h>
#include <hip/hip_bf16.h>
#include <stdint.h>

typedef unsigned int u32;
typedef unsigned long long u64;
typedef u32 u32x2 __attribute__((ext_vector_type(2)));
typedef u32 u32x4 __attribute__((ext_vector_type(4)));
typedef float f32x2 __attribute__((ext_vector_type(2)));
typedef float f32x4 __attribute__((ext_vector_type(4)));
typedef float f32x8 __attribute__((ext_vector_type(8)));
typedef float f32x16 __attribute__((ext_vector_type(16)));
typedef __bf16 bf16x2 __attribute__((ext_vector_type(2)));
typedef __bf16 bf16x8 __attribute__((ext_vector_type(8)));
typedef __bf16 bf16x16 __attribute__((ext_vector_type(16)));
typedef short s16x4 __attribute__((ext_vector_type(4)));
#define LDSAS __attribute__((address_space(3)))
typedef LDSAS char* ldsp;
typedef __attribute__((address_space(1))) u64 gu64;

constexpr int SEQ = 2048, DM = 640, NH = 32, DH = 20;
constexpr float LOG2E = 1.4426950408889634f;
constexpr float QSCALE = 1.4426950408889634f * 0.22360679774997896f;

constexpr u32 REC = 2688;
constexpr u32 WS_QB   = 0;
constexpr u32 WS_KVIM = WS_QB + 1310720;
constexpr u32 WS_KVP  = WS_KVIM + 64 * REC;
constexpr u32 WS_KVT  = WS_KVP + 32 * 16 * 3360;
constexpr u32 WS_GATE = WS_KVT + 32 * 1680;
constexpr u32 WS_OFR  = WS_GATE + 8192;
constexpr u32 WS_F1   = WS_OFR + 5242880;
constexpr u32 WS_F2   = WS_F1 + 16384;
constexpr u32 WS_F3   = WS_F2 + 1024;
constexpr u32 WS_FLGA = WS_F3 + 1024;
constexpr u32 WS_FLGB = WS_FLGA + 2048;
constexpr u32 WS_FQ   = WS_FLGB + 16384;
constexpr u32 WS_EPOCH = WS_FQ + 16384;
constexpr u32 WS_END  = WS_EPOCH + 256;
constexpr u32 OOB = 0x40000000u;
constexpr u32 SPIN_CAP = 1u << 20;

#define SBAR() __builtin_amdgcn_sched_barrier(0)
#define PIN(x) asm volatile("" : "+v"(x))
#define LBAR() asm volatile("s_waitcnt lgkmcnt(0)\n\ts_barrier" ::: "memory")
#define WAIT_BAR(N) asm volatile("s_waitcnt vmcnt(" #N ") lgkmcnt(0)\n\ts_barrier" ::: "memory")
#define DRAIN() asm volatile("s_waitcnt vmcnt(0)" ::: "memory")

__device__ __forceinline__ int lane_id() {
  int l;
  asm volatile("v_mbcnt_lo_u32_b32 %0, -1, 0\n\tv_mbcnt_hi_u32_b32 %0, -1, %0" : "=v"(l));
  return l;
}
__device__ __forceinline__ u32 pk2(float a, float b) {
  f32x2 v = {a, b};
  return __builtin_bit_cast(u32, __builtin_convertvector(v, bf16x2));
}
__device__ __forceinline__ f32x4 mfma16(u32x4 a, u32x4 b, f32x4 c) {
  return __builtin_amdgcn_mfma_f32_16x16x32_bf16(__builtin_bit_cast(bf16x8, a), __builtin_bit_cast(bf16x8, b), c, 0, 0, 0);
}
__device__ __forceinline__ f32x16 mfma32(u32x4 a, u32x4 b, f32x16 c) {
  return __builtin_amdgcn_mfma_f32_32x32x16_bf16(__builtin_bit_cast(bf16x8, a), __builtin_bit_cast(bf16x8, b), c, 0, 0, 0);
}
__device__ __forceinline__ f32x16 mfma32k8(u32x2 a, u32x2 b, f32x16 c) {
  return __builtin_amdgcn_mfma_f32_32x32x8bf16_1k(__builtin_bit_cast(s16x4, a), __builtin_bit_cast(s16x4, b), c, 0, 0, 0);
}
__device__ __forceinline__ f32x16 smf(u32x4 a, f32x8 b, f32x16 c, int idx) {
  return __builtin_amdgcn_smfmac_f32_32x32x32_bf16(__builtin_bit_cast(bf16x8, a), __builtin_bit_cast(bf16x16, b), c, idx, 0, 0);
}
__device__ __forceinline__ u32x2 trd(ldsp p) {
  return __builtin_bit_cast(u32x2, __builtin_amdgcn_ds_read_tr16_b64_v4i16((LDSAS s16x4*)p));
}
__device__ __forceinline__ u32x4 trd2(ldsp p0, ldsp p1) {
  u32x2 a = trd(p0), b = trd(p1);
  return (u32x4){a[0], a[1], b[0], b[1]};
}
__device__ __forceinline__ u32x4 ld128(ldsp p) { return *(LDSAS u32x4*)p; }
__device__ __forceinline__ u32x2 ld64(ldsp p) { return *(LDSAS u32x2*)p; }
__device__ __forceinline__ float bflo(u32 v) { return __uint_as_float(v << 16); }
__device__ __forceinline__ float bfhi(u32 v) { return __uint_as_float(v & 0xffff0000u); }

__device__ __forceinline__ u32x4 mkrsrc(const void* p, u32 bytes) {
  uint64_t a = (uint64_t)p;
  return (u32x4){(u32)__builtin_amdgcn_readfirstlane((u32)a), (u32)__builtin_amdgcn_readfirstlane((u32)(a >> 32) & 0xffffu), bytes, 0x00020000u};
}
__device__ __forceinline__ void dma1(u32x4 rs, u32 voff, u32 soff, u32 ldsaddr) {
  u32 keep;
  asm volatile("s_mov_b32 %0, m0\n\ts_mov_b32 m0, %4\n\ts_nop 2\n\tbuffer_load_dwordx4 %1, %2, %3 offen lds\n\ts_mov_b32 m0, %0"
               : "=&s"(keep) : "v"(voff), "s"(rs), "s"(__builtin_amdgcn_readfirstlane(soff)), "s"(__builtin_amdgcn_readfirstlane(ldsaddr)) : "memory");
}
__device__ __forceinline__ void dma1s(u32x4 rs, u32 voff, u32 soff, u32 ldsaddr) {
  u32 keep;
  asm volatile("s_mov_b32 %0, m0\n\ts_mov_b32 m0, %4\n\ts_nop 2\n\tbuffer_load_dwordx4 %1, %2, %3 offen sc1 lds\n\ts_mov_b32 m0, %0"
               : "=&s"(keep) : "v"(voff), "s"(rs), "s"(__builtin_amdgcn_readfirstlane(soff)), "s"(__builtin_amdgcn_readfirstlane(ldsaddr)) : "memory");
}
__device__ __forceinline__ void dma4(u32x4 rs, u32 voff, u32 soff, u32 ldsaddr) {
  u32 keep;
  asm volatile("s_mov_b32 %0, m0\n\ts_mov_b32 m0, %4\n\ts_nop 2\n\t"
               "buffer_load_dwordx4 %1, %2, %3 offen sc1 lds\n\tbuffer_load_dwordx4 %1, %2, %3 offen offset:1024 sc1 lds\n\t"
               "buffer_load_dwordx4 %1, %2, %3 offen offset:2048 sc1 lds\n\tbuffer_load_dwordx4 %1, %2, %3 offen offset:3072 sc1 lds\n\t"
               "s_mov_b32 m0, %0"
               : "=&s"(keep) : "v"(voff), "s"(rs), "s"(__builtin_amdgcn_readfirstlane(soff)), "s"(__builtin_amdgcn_readfirstlane(ldsaddr)) : "memory");
}
__device__ __forceinline__ void dma2h(u32x4 rs, u32 voff, u32 soff, u32 ldsaddr) {
  u32 keep;
  asm volatile("s_mov_b32 %0, m0\n\ts_mov_b32 m0, %4\n\ts_nop 2\n\t"
               "buffer_load_dwordx4 %1, %2, %3 offen sc1 lds\n\tbuffer_load_dwordx4 %1, %2, %3 offen offset:1024 sc1 lds\n\t"
               "s_mov_b32 exec_hi, 0\n\tbuffer_load_dwordx4 %1, %2, %3 offen offset:2048 sc1 lds\n\ts_mov_b32 exec_hi, -1\n\t"
               "s_mov_b32 m0, %0"
               : "=&s"(keep) : "v"(voff), "s"(rs), "s"(__builtin_amdgcn_readfirstlane(soff)), "s"(__builtin_amdgcn_readfirstlane(ldsaddr)) : "memory");
}
typedef __amdgpu_buffer_rsrc_t rsrc_t;
__device__ __forceinline__ void st128(rsrc_t r, u32 off, u32x4 v) { __builtin_amdgcn_raw_buffer_store_b128(v, r, (int)off, 0, 16); }
__device__ __forceinline__ void st64(rsrc_t r, u32 off, u32x2 v) { __builtin_amdgcn_raw_buffer_store_b64(v, r, (int)off, 0, 16); }
__device__ __forceinline__ void st32(rsrc_t r, u32 off, u32 v) { __builtin_amdgcn_raw_buffer_store_b32(v, r, (int)off, 0, 16); }
__device__ __forceinline__ void st16(rsrc_t r, u32 off, u32 v) { __builtin_amdgcn_raw_buffer_store_b16((unsigned short)v, r, (int)off, 0, 16); }
__device__ __forceinline__ u32x4 gl128(rsrc_t r, u32 voff, u32 soff) { return __builtin_amdgcn_raw_buffer_load_b128(r, (int)voff, (int)soff, 16); }
__device__ __forceinline__ u32x2 gl64(rsrc_t r, u32 voff, u32 soff) { return __builtin_amdgcn_raw_buffer_load_b64(r, (int)voff, (int)soff, 16); }
__device__ __forceinline__ u32 gl32(rsrc_t r, u32 voff, u32 soff) { return __builtin_amdgcn_raw_buffer_load_b32(r, (int)voff, (int)soff, 16); }

__device__ __forceinline__ f32x8 lo8(const f32x16& s) { return __builtin_shufflevector(s, s, 0, 1, 2, 3, 4, 5, 6, 7); }
__device__ __forceinline__ f32x8 hi8(const f32x16& s) { return __builtin_shufflevector(s, s, 8, 9, 10, 11, 12, 13, 14, 15); }
__device__ __forceinline__ float max16(const f32x16& s) {
  float a = fmaxf(fmaxf(s[0], s[1]), s[2]), b = fmaxf(fmaxf(s[3], s[4]), s[5]);
  a = fmaxf(fmaxf(a, s[6]), s[7]); b = fmaxf(fmaxf(b, s[8]), s[9]);
  a = fmaxf(fmaxf(a, s[10]), s[11]); b = fmaxf(fmaxf(b, s[12]), s[13]);
  a = fmaxf(fmaxf(a, s[14]), s[15]);
  return fmaxf(a, b);
}
__device__ __forceinline__ float xhalf_max(float m) {
  auto r = __builtin_amdgcn_permlane32_swap(__float_as_uint(m), __float_as_uint(m), false, false);
  return fmaxf(__uint_as_float(r[0]), __uint_as_float(r[1]));
}
__device__ __forceinline__ float from_hi(float v) {
  auto r = __builtin_amdgcn_permlane32_swap(__float_as_uint(v), __float_as_uint(v), false, false);
  return __uint_as_float(r[1]);
}
__device__ __forceinline__ u32 lds_ld(ldsp p) { return __hip_atomic_load((LDSAS u32*)p, __ATOMIC_RELAXED, __HIP_MEMORY_SCOPE_WORKGROUP); }
__device__ __forceinline__ void lds_st(ldsp p, u32 v) { __hip_atomic_store((LDSAS u32*)p, v, __ATOMIC_RELAXED, __HIP_MEMORY_SCOPE_WORKGROUP); }
template <int SLEEP>
__device__ __forceinline__ void lds_wait_ge(ldsp p, u32 want) {
#pragma unroll 1
  for (u32 it = 0; it < SPIN_CAP; ++it) {
    if ((u32)__builtin_amdgcn_readfirstlane(lds_ld(p)) >= want) break;
    __builtin_amdgcn_s_sleep(SLEEP);
  }
  asm volatile("" ::: "memory");
}
__device__ __forceinline__ u32 lds_arrive(ldsp p) {
  u32 old; u64 keep;
  asm volatile("v_mov_b32 %0, 1\n\ts_mov_b64 %1, exec\n\ts_mov_b64 exec, 1\n\ts_waitcnt lgkmcnt(0)\n\tds_add_rtn_u32 %0, %2, %0\n\t"
               "s_waitcnt lgkmcnt(0)\n\ts_mov_b64 exec, %1" : "=&v"(old), "=&s"(keep) : "v"((u32)(uintptr_t)p) : "memory");
  return (u32)__builtin_amdgcn_readfirstlane(old);
}
template <int SLEEP>
__device__ __forceinline__ void flags_wait(rsrc_t r, u32 off, int n, u32x2 tag) {
  const int lane = lane_id();
  const u32 vo = (u32)(lane < n ? lane : 0) * 8u;
#pragma unroll 1
  for (u32 it = 0; it < SPIN_CAP; ++it) {
    asm volatile("" ::: "memory");
    const u32x2 v = gl64(r, vo, off);
    if (__all(v[0] == tag[0] && v[1] == tag[1])) break;
    __builtin_amdgcn_s_sleep(SLEEP);
  }
  asm volatile("" ::: "memory");
}
template <int SLEEP>
__device__ __forceinline__ void flags_wait256(rsrc_t r, u32 off, u32x2 tag) {
  const u32 vo = (u32)lane_id() * 16u;
#pragma unroll 1
  for (u32 it = 0; it < SPIN_CAP; ++it) {
    asm volatile("" ::: "memory");
    const u32x4 a = gl128(r, vo, off), b = gl128(r, vo, off + 1024);
    const u32 d = (a[0] ^ tag[0]) | (a[1] ^ tag[1]) | (a[2] ^ tag[0]) | (a[3] ^ tag[1]) | (b[0] ^ tag[0]) | (b[1] ^ tag[1]) | (b[2] ^ tag[0]) | (b[3] ^ tag[1]);
    if (__all(d == 0u)) break;
    __builtin_amdgcn_s_sleep(SLEEP);
  }
  asm volatile("" ::: "memory");
}

#define STAMP(i) do {} while (0)
#define RSTAMP(i) do {} while (0)
#define STAMP_INIT()
#define STAMP_FLUSH()

#define ATTN_LOOP_ASM \
  "s_nop 11\n\t" \
  "v_mov_b64 v[0:1], 0\n\t" \
  "v_mov_b64 v[2:3], 0\n\t" \
  "v_mov_b64 v[4:5], 0\n\t" \
  "v_mov_b64 v[6:7], 0\n\t" \
  "v_mov_b64 v[8:9], 0\n\t" \
  "v_mov_b64 v[10:11], 0\n\t" \
  "v_mov_b64 v[12:13], 0\n\t" \
  "v_mov_b64 v[14:15], 0\n\t" \
  "v_mov_b64 v[16:17], 0\n\t" \
  "v_mov_b64 v[18:19], 0\n\t" \
  "v_mov_b64 v[20:21], 0\n\t" \
  "v_mov_b64 v[22:23], 0\n\t" \
  "v_mov_b64 v[24:25], 0\n\t" \
  "v_mov_b64 v[26:27], 0\n\t" \
  "v_mov_b64 v[28:29], 0\n\t" \
  "v_mov_b64 v[30:31], 0\n\t" \
  "v_mov_b64 v[80:81], 0\n\t" \
  "v_mov_b64 v[82:83], 0\n\t" \
  "v_mov_b64 v[84:85], 0\n\t" \
  "v_mov_b64 v[86:87], 0\n\t" \
  "v_mov_b64 v[88:89], 0\n\t" \
  "v_mov_b64 v[90:91], 0\n\t" \
  "v_mov_b64 v[92:93], 0\n\t" \
  "v_mov_b64 v[94:95], 0\n\t" \
  "v_mov_b64 v[116:117], 0\n\t" \
  "v_mov_b64 v[118:119], 0\n\t" \
  "v_mov_b64 v[120:121], 0\n\t" \
  "v_mov_b64 v[122:123], 0\n\t" \
  "s_mov_b32 %[cnt], 0\n\t" \
  "s_mov_b32 %[slot], 0\n\t" \
  "s_setprio 0\n\t" \
  "2:\n\t" \
  "s_mov_b32 %[inc], 0\n\t" \
  "3:\n\t" \
  "ds_read_b32 v115, v127 offset:32\n\t" \
  "s_add_u32 %[inc], %[inc], 1\n\t" \
  "s_waitcnt lgkmcnt(0)\n\t" \
  "v_readfirstlane_b32 %[rdy], v115\n\t" \
  "s_nop 3\n\t" \
  "s_cmp_gt_u32 %[rdy], %[cnt]\n\t" \
  "s_cbranch_scc1 6f\n\t" \
  "s_sleep 1\n\t" \
  "s_cmp_lt_u32 %[inc], 0x100000\n\t" \
  "s_cbranch_scc1 3b\n\t" \
  "s_branch 9f\n\t" \
  "6:\n\t" \
  "s_cmp_lg_u32 %[par], 0\n\t" \
  "s_cbranch_scc1 5f\n\t" \
  "4:\n\t" \
  "v_mfma_f32_32x32x16_bf16 v[48:63], v[108:111], v[100:103], 0\n\t" \
  "ds_read_b128 v[108:111], v124 offset:0\n\t" \
  "v_exp_f32 v32, v32\n\t" \
  "v_exp_f32 v33, v33\n\t" \
  "v_exp_f32 v34, v34\n\t" \
  "v_exp_f32 v35, v35\n\t" \
  "v_mfma_f32_32x32x8_bf16 v[48:63], v[112:113], v[106:107], v[48:63]\n\t" \
  "ds_read_b32 v112, v125 offset:0\n\t" \
  "v_exp_f32 v36, v36\n\t" \
  "v_exp_f32 v37, v37\n\t" \
  "v_exp_f32 v38, v38\n\t" \
  "v_exp_f32 v39, v39\n\t" \
  "v_smfmac_f32_32x32x32_bf16 v[16:31], v[116:119], v[80:87], v114\n\t" \
  "ds_read_b128 v[116:119], v126 offset:0\n\t" \
  "v_exp_f32 v40, v40\n\t" \
  "v_exp_f32 v41, v41\n\t" \
  "v_exp_f32 v42, v42\n\t" \
  "v_exp_f32 v43, v43\n\t" \
  "v_smfmac_f32_32x32x32_bf16 v[16:31], v[120:123], v[88:95], v114\n\t" \
  "ds_read_b128 v[120:123], v126 offset:704\n\t" \
  "v_exp_f32 v44, v44\n\t" \
  "v_exp_f32 v45, v45\n\t" \
  "v_exp_f32 v46, v46\n\t" \
  "v_exp_f32 v47, v47\n\t" \
  "s_waitcnt lgkmcnt(2)\n\t" \
  "v_mfma_f32_32x32x16_bf16 v[64:79], v[108:111], v[96:99], 0\n\t" \
  "v_exp_f32 v48, v48\n\t" \
  "v_exp_f32 v49, v49\n\t" \
  "v_exp_f32 v50, v50\n\t" \
  "v_exp_f32 v51, v51\n\t" \
  "v_mfma_f32_32x32x8_bf16 v[64:79], v[112:113], v[104:105], v[64:79]\n\t" \
  "v_exp_f32 v52, v52\n\t" \
  "v_exp_f32 v53, v53\n\t" \
  "v_exp_f32 v54, v54\n\t" \
  "v_exp_f32 v55, v55\n\t" \
  "s_waitcnt lgkmcnt(0)\n\t" \
  "v_smfmac_f32_32x32x32_bf16 v[0:15], v[116:119], v[32:39], v114\n\t" \
  "v_exp_f32 v56, v56\n\t" \
  "v_exp_f32 v57, v57\n\t" \
  "v_exp_f32 v58, v58\n\t" \
  "v_exp_f32 v59, v59\n\t" \
  "v_smfmac_f32_32x32x32_bf16 v[0:15], v[120:123], v[40:47], v114\n\t" \
  "v_exp_f32 v60, v60\n\t" \
  "v_exp_f32 v61, v61\n\t" \
  "v_exp_f32 v62, v62\n\t" \
  "v_exp_f32 v63, v63\n\t" \
  "v_mfma_f32_32x32x16_bf16 v[80:95], v[108:111], v[100:103], 0\n\t" \
  "ds_read_b128 v[108:111], v124 offset:5376\n\t" \
  "v_exp_f32 v64, v64\n\t" \
  "v_exp_f32 v65, v65\n\t" \
  "v_exp_f32 v66, v66\n\t" \
  "v_exp_f32 v67, v67\n\t" \
  "v_mfma_f32_32x32x8_bf16 v[80:95], v[112:113], v[106:107], v[80:95]\n\t" \
  "ds_read_b32 v112, v125 offset:5376\n\t" \
  "v_exp_f32 v68, v68\n\t" \
  "v_exp_f32 v69, v69\n\t" \
  "v_exp_f32 v70, v70\n\t" \
  "v_exp_f32 v71, v71\n\t" \
  "v_smfmac_f32_32x32x32_bf16 v[16:31], v[116:119], v[48:55], v114\n\t" \
  "ds_read_b128 v[116:119], v126 offset:5376\n\t" \
  "v_exp_f32 v72, v72\n\t" \
  "v_exp_f32 v73, v73\n\t" \
  "v_exp_f32 v74, v74\n\t" \
  "v_exp_f32 v75, v75\n\t" \
  "v_smfmac_f32_32x32x32_bf16 v[16:31], v[120:123], v[56:63], v114\n\t" \
  "ds_read_b128 v[120:123], v126 offset:6080\n\t" \
  "s_add_u32 %[cnt], %[cnt], 1\n\t" \
  "s_cmp_eq_u32 %[slot], 4\n\t" \
  "s_cselect_b32 %[inc], %[wrap], %[stg]\n\t" \
  "v_exp_f32 v76, v76\n\t" \
  "v_exp_f32 v77, v77\n\t" \
  "v_exp_f32 v78, v78\n\t" \
  "v_exp_f32 v79, v79\n\t" \
  "s_waitcnt lgkmcnt(2)\n\t" \
  "v_mfma_f32_32x32x16_bf16 v[32:47], v[108:111], v[96:99], 0\n\t" \
  "ds_read_b32 v115, v127 offset:32\n\t" \
  "s_cselect_b32 %[slot], -1, %[slot]\n\t" \
  "s_add_u32 %[slot], %[slot], 1\n\t" \
  "v_exp_f32 v80, v80\n\t" \
  "v_exp_f32 v81, v81\n\t" \
  "v_exp_f32 v82, v82\n\t" \
  "v_exp_f32 v83, v83\n\t" \
  "v_mfma_f32_32x32x8_bf16 v[32:47], v[112:113], v[104:105], v[32:47]\n\t" \
  "v_add_u32 v124, %[inc], v124\n\t" \
  "v_add_u32 v125, %[inc], v125\n\t" \
  "v_add_u32 v126, %[inc], v126\n\t" \
  "v_exp_f32 v84, v84\n\t" \
  "v_exp_f32 v85, v85\n\t" \
  "v_exp_f32 v86, v86\n\t" \
  "v_exp_f32 v87, v87\n\t" \
  "s_waitcnt lgkmcnt(1)\n\t" \
  "v_smfmac_f32_32x32x32_bf16 v[0:15], v[116:119], v[64:71], v114\n\t" \
  "v_exp_f32 v88, v88\n\t" \
  "v_exp_f32 v89, v89\n\t" \
  "v_exp_f32 v90, v90\n\t" \
  "v_exp_f32 v91, v91\n\t" \
  "s_waitcnt lgkmcnt(0)\n\t" \
  "v_readfirstlane_b32 %[rdy], v115\n\t" \
  "v_mov_b32 v115, %[cnt]\n\t" \
  "v_smfmac_f32_32x32x32_bf16 v[0:15], v[120:123], v[72:79], v114\n\t" \
  "ds_write_b32 v127, v115\n\t" \
  "s_cmp_gt_u32 %[rdy], %[cnt]\n\t" \
  "v_exp_f32 v92, v92\n\t" \
  "v_exp_f32 v93, v93\n\t" \
  "v_exp_f32 v94, v94\n\t" \
  "v_exp_f32 v95, v95\n\t" \
  "s_cbranch_scc1 4b\n\t" \
  "s_cmp_ge_u32 %[cnt], 16\n\t" \
  "s_cbranch_scc1 9f\n\t" \
  "s_branch 2b\n\t" \
  "5:\n\t" \
  "v_mfma_f32_32x32x16_bf16 v[48:63], v[108:111], v[100:103], 0\n\t" \
  "ds_read_b128 v[108:111], v124 offset:0\n\t" \
  "s_setprio 1\n\t" \
  "v_exp_f32 v32, v32\n\t" \
  "v_exp_f32 v33, v33\n\t" \
  "v_exp_f32 v34, v34\n\t" \
  "v_exp_f32 v35, v35\n\t" \
  "v_mfma_f32_32x32x8_bf16 v[48:63], v[112:113], v[106:107], v[48:63]\n\t" \
  "ds_read_b32 v112, v125 offset:0\n\t" \
  "v_exp_f32 v36, v36\n\t" \
  "v_exp_f32 v37, v37\n\t" \
  "v_exp_f32 v38, v38\n\t" \
  "v_exp_f32 v39, v39\n\t" \
  "v_smfmac_f32_32x32x32_bf16 v[16:31], v[116:119], v[80:87], v114\n\t" \
  "ds_read_b128 v[116:119], v126 offset:0\n\t" \
  "v_exp_f32 v40, v40\n\t" \
  "v_exp_f32 v41, v41\n\t" \
  "v_exp_f32 v42, v42\n\t" \
  "v_exp_f32 v43, v43\n\t" \
  "v_smfmac_f32_32x32x32_bf16 v[16:31], v[120:123], v[88:95], v114\n\t" \
  "ds_read_b128 v[120:123], v126 offset:704\n\t" \
  "v_exp_f32 v44, v44\n\t" \
  "v_exp_f32 v45, v45\n\t" \
  "v_exp_f32 v46, v46\n\t" \
  "v_exp_f32 v47, v47\n\t" \
  "s_waitcnt lgkmcnt(2)\n\t" \
  "v_mfma_f32_32x32x16_bf16 v[64:79], v[108:111], v[96:99], 0\n\t" \
  "v_exp_f32 v48, v48\n\t" \
  "v_exp_f32 v49, v49\n\t" \
  "v_exp_f32 v50, v50\n\t" \
  "v_exp_f32 v51, v51\n\t" \
  "v_mfma_f32_32x32x8_bf16 v[64:79], v[112:113], v[104:105], v[64:79]\n\t" \
  "v_exp_f32 v52, v52\n\t" \
  "v_exp_f32 v53, v53\n\t" \
  "v_exp_f32 v54, v54\n\t" \
  "v_exp_f32 v55, v55\n\t" \
  "s_waitcnt lgkmcnt(0)\n\t" \
  "v_smfmac_f32_32x32x32_bf16 v[0:15], v[116:119], v[32:39], v114\n\t" \
  "v_exp_f32 v56, v56\n\t" \
  "v_exp_f32 v57, v57\n\t" \
  "v_exp_f32 v58, v58\n\t" \
  "v_exp_f32 v59, v59\n\t" \
  "v_smfmac_f32_32x32x32_bf16 v[0:15], v[120:123], v[40:47], v114\n\t" \
  "v_exp_f32 v60, v60\n\t" \
  "v_exp_f32 v61, v61\n\t" \
  "v_exp_f32 v62, v62\n\t" \
  "v_exp_f32 v63, v63\n\t" \
  "v_mfma_f32_32x32x16_bf16 v[80:95], v[108:111], v[100:103], 0\n\t" \
  "ds_read_b128 v[108:111], v124 offset:5376\n\t" \
  "v_exp_f32 v64, v64\n\t" \
  "v_exp_f32 v65, v65\n\t" \
  "v_exp_f32 v66, v66\n\t" \
  "v_exp_f32 v67, v67\n\t" \
  "v_mfma_f32_32x32x8_bf16 v[80:95], v[112:113], v[106:107], v[80:95]\n\t" \
  "ds_read_b32 v112, v125 offset:5376\n\t" \
  "v_exp_f32 v68, v68\n\t" \
  "v_exp_f32 v69, v69\n\t" \
  "v_exp_f32 v70, v70\n\t" \
  "v_exp_f32 v71, v71\n\t" \
  "v_smfmac_f32_32x32x32_bf16 v[16:31], v[116:119], v[48:55], v114\n\t" \
  "ds_read_b128 v[116:119], v126 offset:5376\n\t" \
  "v_exp_f32 v72, v72\n\t" \
  "v_exp_f32 v73, v73\n\t" \
  "v_exp_f32 v74, v74\n\t" \
  "v_exp_f32 v75, v75\n\t" \
  "v_smfmac_f32_32x32x32_bf16 v[16:31], v[120:123], v[56:63], v114\n\t" \
  "ds_read_b128 v[120:123], v126 offset:6080\n\t" \
  "s_add_u32 %[cnt], %[cnt], 1\n\t" \
  "s_cmp_eq_u32 %[slot], 4\n\t" \
  "s_cselect_b32 %[inc], %[wrap], %[stg]\n\t" \
  "v_exp_f32 v76, v76\n\t" \
  "v_exp_f32 v77, v77\n\t" \
  "v_exp_f32 v78, v78\n\t" \
  "v_exp_f32 v79, v79\n\t" \
  "s_waitcnt lgkmcnt(2)\n\t" \
  "v_mfma_f32_32x32x16_bf16 v[32:47], v[108:111], v[96:99], 0\n\t" \
  "ds_read_b32 v115, v127 offset:32\n\t" \
  "s_cselect_b32 %[slot], -1, %[slot]\n\t" \
  "s_add_u32 %[slot], %[slot], 1\n\t" \
  "v_exp_f32 v80, v80\n\t" \
  "v_exp_f32 v81, v81\n\t" \
  "v_exp_f32 v82, v82\n\t" \
  "v_exp_f32 v83, v83\n\t" \
  "v_mfma_f32_32x32x8_bf16 v[32:47], v[112:113], v[104:105], v[32:47]\n\t" \
  "v_add_u32 v124, %[inc], v124\n\t" \
  "v_add_u32 v125, %[inc], v125\n\t" \
  "v_add_u32 v126, %[inc], v126\n\t" \
  "s_setprio 0\n\t" \
  "v_exp_f32 v84, v84\n\t" \
  "v_exp_f32 v85, v85\n\t" \
  "v_exp_f32 v86, v86\n\t" \
  "v_exp_f32 v87, v87\n\t" \
  "s_waitcnt lgkmcnt(1)\n\t" \
  "v_smfmac_f32_32x32x32_bf16 v[0:15], v[116:119], v[64:71], v114\n\t" \
  "v_exp_f32 v88, v88\n\t" \
  "v_exp_f32 v89, v89\n\t" \
  "v_exp_f32 v90, v90\n\t" \
  "v_exp_f32 v91, v91\n\t" \
  "s_waitcnt lgkmcnt(0)\n\t" \
  "v_readfirstlane_b32 %[rdy], v115\n\t" \
  "v_mov_b32 v115, %[cnt]\n\t" \
  "v_smfmac_f32_32x32x32_bf16 v[0:15], v[120:123], v[72:79], v114\n\t" \
  "ds_write_b32 v127, v115\n\t" \
  "s_cmp_gt_u32 %[rdy], %[cnt]\n\t" \
  "v_exp_f32 v92, v92\n\t" \
  "v_exp_f32 v93, v93\n\t" \
  "v_exp_f32 v94, v94\n\t" \
  "v_exp_f32 v95, v95\n\t" \
  "s_cbranch_scc1 5b\n\t" \
  "s_cmp_ge_u32 %[cnt], 16\n\t" \
  "s_cbranch_scc1 9f\n\t" \
  "s_branch 2b\n\t" \
  "9:\n\t" \
  "s_setprio 3\n\t" \
  "ds_read_b32 %[dn], %[dn]\n\t" \
  "v_smfmac_f32_32x32x32_bf16 v[16:31], v[116:119], v[80:87], v114\n\t" \
  "v_smfmac_f32_32x32x32_bf16 v[16:31], v[120:123], v[88:95], v114\n\t" \
  "s_waitcnt lgkmcnt(0)\n\t" \
  "s_nop 12\n\t"
#define TILE_GEMM_ASM \
  "s_nop 4\n\t" \
  "s_mov_b32 %[want], 3\n\t" \
  "s_mov_b32 %[it], 0\n\t" \
  "s_cmp_eq_u32 %[poll], 0\n\t" \
  "s_cbranch_scc1 3f\n\t" \
  "1:\n\t" \
  "buffer_load_dwordx2 v[144:145], v143, %[rs], %[fb] offen sc1\n\t" \
  "s_add_u32 %[it], %[it], 1\n\t" \
  "s_waitcnt vmcnt(0)\n\t" \
  "v_cmp_eq_u64 vcc, %[tag], v[144:145]\n\t" \
  "s_nop 3\n\t" \
  "s_and_b32 %[t], vcc_lo, 0xff\n\t" \
  "s_cmp_eq_u32 %[t], 0xff\n\t" \
  "s_cselect_b32 %[r], 1, 0\n\t" \
  "s_and_b32 %[t], vcc_lo, 0xff00\n\t" \
  "s_cmp_eq_u32 %[t], 0xff00\n\t" \
  "s_cselect_b32 %[t], 2, 0\n\t" \
  "s_or_b32 %[r], %[r], %[t]\n\t" \
  "s_and_b32 %[r], %[r], %[want]\n\t" \
  "s_cbranch_scc1 2f\n\t" \
  "s_sleep 1\n\t" \
  "s_cmp_lt_u32 %[it], 0x100000\n\t" \
  "s_cbranch_scc1 1b\n\t" \
  "s_branch 9f\n\t" \
  "2:\n\t" \
  "s_andn2_b32 %[want], %[want], %[r]\n\t" \
  "s_cmp_eq_u32 %[r], 3\n\t" \
  "s_cbranch_scc1 3f\n\t" \
  "s_cmp_eq_u32 %[r], 2\n\t" \
  "s_cbranch_scc1 4f\n\t" \
  "s_add_u32 %[t], %[s0], 0\n\t" \
  "buffer_load_dwordx4 v[100:103], v140, %[rs], %[t] offen sc1\n\t" \
  "s_add_u32 %[t], %[s0], 20480\n\t" \
  "buffer_load_dwordx4 v[104:107], v140, %[rs], %[t] offen sc1\n\t" \
  "s_add_u32 %[t], %[s0], 1024\n\t" \
  "buffer_load_dwordx4 v[108:111], v140, %[rs], %[t] offen sc1\n\t" \
  "s_add_u32 %[t], %[s0], 21504\n\t" \
  "buffer_load_dwordx4 v[112:115], v140, %[rs], %[t] offen sc1\n\t" \
  "s_add_u32 %[t], %[s0], 2048\n\t" \
  "buffer_load_dwordx4 v[116:119], v140, %[rs], %[t] offen sc1\n\t" \
  "s_add_u32 %[t], %[s0], 22528\n\t" \
  "buffer_load_dwordx4 v[120:123], v140, %[rs], %[t] offen sc1\n\t" \
  "s_add_u32 %[t], %[s0], 3072\n\t" \
  "buffer_load_dwordx4 v[124:127], v140, %[rs], %[t] offen sc1\n\t" \
  "s_add_u32 %[t], %[s0], 23552\n\t" \
  "buffer_load_dwordx4 v[128:131], v140, %[rs], %[t] offen sc1\n\t" \
  "s_add_u32 %[t], %[s0], 4096\n\t" \
  "buffer_load_dwordx4 v[132:135], v140, %[rs], %[t] offen sc1\n\t" \
  "s_add_u32 %[t], %[s0], 24576\n\t" \
  "buffer_load_dwordx4 v[136:139], v140, %[rs], %[t] offen sc1\n\t" \
  "ds_read_b64_tr_b16 v[80:81], v141 offset:0\n\t" \
  "ds_read_b64_tr_b16 v[82:83], v141 offset:2560\n\t" \
  "ds_read_b64_tr_b16 v[84:85], v141 offset:8\n\t" \
  "ds_read_b64_tr_b16 v[86:87], v141 offset:2568\n\t" \
  "ds_read_b64_tr_b16 v[88:89], v141 offset:16\n\t" \
  "ds_read_b64_tr_b16 v[90:91], v141 offset:2576\n\t" \
  "ds_read_b64_tr_b16 v[92:93], v141 offset:24\n\t" \
  "ds_read_b64_tr_b16 v[94:95], v141 offset:2584\n\t" \
  "ds_read_b64_tr_b16 v[96:97], v141 offset:32\n\t" \
  "ds_read_b64_tr_b16 v[98:99], v141 offset:2592\n\t" \
  "s_waitcnt vmcnt(8)\n\t" \
  "s_waitcnt lgkmcnt(8)\n\t" \
  "v_mfma_f32_16x16x32_bf16 v[0:3], v[80:83], v[100:103], v[0:3]\n\t" \
  "v_mfma_f32_16x16x32_bf16 v[4:7], v[80:83], v[104:107], v[4:7]\n\t" \
  "ds_read_b64_tr_b16 v[80:81], v141 offset:5120\n\t" \
  "ds_read_b64_tr_b16 v[82:83], v141 offset:7680\n\t" \
  "s_waitcnt lgkmcnt(8)\n\t" \
  "v_mfma_f32_16x16x32_bf16 v[8:11], v[84:87], v[100:103], v[8:11]\n\t" \
  "v_mfma_f32_16x16x32_bf16 v[12:15], v[84:87], v[104:107], v[12:15]\n\t" \
  "ds_read_b64_tr_b16 v[84:85], v141 offset:5128\n\t" \
  "ds_read_b64_tr_b16 v[86:87], v141 offset:7688\n\t" \
  "s_waitcnt lgkmcnt(8)\n\t" \
  "v_mfma_f32_16x16x32_bf16 v[16:19], v[88:91], v[100:103], v[16:19]\n\t" \
  "v_mfma_f32_16x16x32_bf16 v[20:23], v[88:91], v[104:107], v[20:23]\n\t" \
  "ds_read_b64_tr_b16 v[88:89], v141 offset:5136\n\t" \
  "ds_read_b64_tr_b16 v[90:91], v141 offset:7696\n\t" \
  "s_waitcnt lgkmcnt(8)\n\t" \
  "v_mfma_f32_16x16x32_bf16 v[24:27], v[92:95], v[100:103], v[24:27]\n\t" \
  "v_mfma_f32_16x16x32_bf16 v[28:31], v[92:95], v[104:107], v[28:31]\n\t" \
  "ds_read_b64_tr_b16 v[92:93], v141 offset:5144\n\t" \
  "ds_read_b64_tr_b16 v[94:95], v141 offset:7704\n\t" \
  "s_waitcnt lgkmcnt(8)\n\t" \
  "v_mfma_f32_16x16x32_bf16 v[32:35], v[96:99], v[100:103], v[32:35]\n\t" \
  "v_mfma_f32_16x16x32_bf16 v[36:39], v[96:99], v[104:107], v[36:39]\n\t" \
  "ds_read_b64_tr_b16 v[96:97], v141 offset:5152\n\t" \
  "ds_read_b64_tr_b16 v[98:99], v141 offset:7712\n\t" \
  "s_waitcnt vmcnt(6)\n\t" \
  "s_waitcnt lgkmcnt(8)\n\t" \
  "v_mfma_f32_16x16x32_bf16 v[0:3], v[80:83], v[108:111], v[0:3]\n\t" \
  "v_mfma_f32_16x16x32_bf16 v[4:7], v[80:83], v[112:115], v[4:7]\n\t" \
  "ds_read_b64_tr_b16 v[80:81], v141 offset:10240\n\t" \
  "ds_read_b64_tr_b16 v[82:83], v141 offset:12800\n\t" \
  "s_waitcnt lgkmcnt(8)\n\t" \
  "v_mfma_f32_16x16x32_bf16 v[8:11], v[84:87], v[108:111], v[8:11]\n\t" \
  "v_mfma_f32_16x16x32_bf16 v[12:15], v[84:87], v[112:115], v[12:15]\n\t" \
  "ds_read_b64_tr_b16 v[84:85], v141 offset:10248\n\t" \
  "ds_read_b64_tr_b16 v[86:87], v141 offset:12808\n\t" \
  "s_waitcnt lgkmcnt(8)\n\t" \
  "v_mfma_f32_16x16x32_bf16 v[16:19], v[88:91], v[108:111], v[16:19]\n\t" \
  "v_mfma_f32_16x16x32_bf16 v[20:23], v[88:91], v[112:115], v[20:23]\n\t" \
  "ds_read_b64_tr_b16 v[88:89], v141 offset:10256\n\t" \
  "ds_read_b64_tr_b16 v[90:91], v141 offset:12816\n\t" \
  "s_waitcnt lgkmcnt(8)\n\t" \
  "v_mfma_f32_16x16x32_bf16 v[24:27], v[92:95], v[108:111], v[24:27]\n\t" \
  "v_mfma_f32_16x16x32_bf16 v[28:31], v[92:95], v[112:115], v[28:31]\n\t" \
  "ds_read_b64_tr_b16 v[92:93], v141 offset:10264\n\t" \
  "ds_read_b64_tr_b16 v[94:95], v141 offset:12824\n\t" \
  "s_waitcnt lgkmcnt(8)\n\t" \
  "v_mfma_f32_16x16x32_bf16 v[32:35], v[96:99], v[108:111], v[32:35]\n\t" \
  "v_mfma_f32_16x16x32_bf16 v[36:39], v[96:99], v[112:115], v[36:39]\n\t" \
  "ds_read_b64_tr_b16 v[96:97], v141 offset:10272\n\t" \
  "ds_read_b64_tr_b16 v[98:99], v141 offset:12832\n\t" \
  "s_waitcnt vmcnt(4)\n\t" \
  "s_waitcnt lgkmcnt(8)\n\t" \
  "v_mfma_f32_16x16x32_bf16 v[0:3], v[80:83], v[116:119], v[0:3]\n\t" \
  "v_mfma_f32_16x16x32_bf16 v[4:7], v[80:83], v[120:123], v[4:7]\n\t" \
  "ds_read_b64_tr_b16 v[80:81], v141 offset:15360\n\t" \
  "ds_read_b64_tr_b16 v[82:83], v141 offset:17920\n\t" \
  "s_waitcnt lgkmcnt(8)\n\t" \
  "v_mfma_f32_16x16x32_bf16 v[8:11], v[84:87], v[116:119], v[8:11]\n\t" \
  "v_mfma_f32_16x16x32_bf16 v[12:15], v[84:87], v[120:123], v[12:15]\n\t" \
  "ds_read_b64_tr_b16 v[84:85], v141 offset:15368\n\t" \
  "ds_read_b64_tr_b16 v[86:87], v141 offset:17928\n\t" \
  "s_waitcnt lgkmcnt(8)\n\t" \
  "v_mfma_f32_16x16x32_bf16 v[16:19], v[88:91], v[116:119], v[16:19]\n\t" \
  "v_mfma_f32_16x16x32_bf16 v[20:23], v[88:91], v[120:123], v[20:23]\n\t" \
  "ds_read_b64_tr_b16 v[88:89], v141 offset:15376\n\t" \
  "ds_read_b64_tr_b16 v[90:91], v141 offset:17936\n\t" \
  "s_waitcnt lgkmcnt(8)\n\t" \
  "v_mfma_f32_16x16x32_bf16 v[24:27], v[92:95], v[116:119], v[24:27]\n\t" \
  "v_mfma_f32_16x16x32_bf16 v[28:31], v[92:95], v[120:123], v[28:31]\n\t" \
  "ds_read_b64_tr_b16 v[92:93], v141 offset:15384\n\t" \
  "ds_read_b64_tr_b16 v[94:95], v141 offset:17944\n\t" \
  "s_waitcnt lgkmcnt(8)\n\t" \
  "v_mfma_f32_16x16x32_bf16 v[32:35], v[96:99], v[116:119], v[32:35]\n\t" \
  "v_mfma_f32_16x16x32_bf16 v[36:39], v[96:99], v[120:123], v[36:39]\n\t" \
  "ds_read_b64_tr_b16 v[96:97], v141 offset:15392\n\t" \
  "ds_read_b64_tr_b16 v[98:99], v141 offset:17952\n\t" \
  "s_waitcnt vmcnt(2)\n\t" \
  "s_waitcnt lgkmcnt(8)\n\t" \
  "v_mfma_f32_16x16x32_bf16 v[0:3], v[80:83], v[124:127], v[0:3]\n\t" \
  "v_mfma_f32_16x16x32_bf16 v[4:7], v[80:83], v[128:131], v[4:7]\n\t" \
  "ds_read_b64_tr_b16 v[80:81], v141 offset:20480\n\t" \
  "ds_read_b64_tr_b16 v[82:83], v141 offset:23040\n\t" \
  "s_waitcnt lgkmcnt(8)\n\t" \
  "v_mfma_f32_16x16x32_bf16 v[8:11], v[84:87], v[124:127], v[8:11]\n\t" \
  "v_mfma_f32_16x16x32_bf16 v[12:15], v[84:87], v[128:131], v[12:15]\n\t" \
  "ds_read_b64_tr_b16 v[84:85], v141 offset:20488\n\t" \
  "ds_read_b64_tr_b16 v[86:87], v141 offset:23048\n\t" \
  "s_waitcnt lgkmcnt(8)\n\t" \
  "v_mfma_f32_16x16x32_bf16 v[16:19], v[88:91], v[124:127], v[16:19]\n\t" \
  "v_mfma_f32_16x16x32_bf16 v[20:23], v[88:91], v[128:131], v[20:23]\n\t" \
  "ds_read_b64_tr_b16 v[88:89], v141 offset:20496\n\t" \
  "ds_read_b64_tr_b16 v[90:91], v141 offset:23056\n\t" \
  "s_waitcnt lgkmcnt(8)\n\t" \
  "v_mfma_f32_16x16x32_bf16 v[24:27], v[92:95], v[124:127], v[24:27]\n\t" \
  "v_mfma_f32_16x16x32_bf16 v[28:31], v[92:95], v[128:131], v[28:31]\n\t" \
  "ds_read_b64_tr_b16 v[92:93], v141 offset:20504\n\t" \
  "ds_read_b64_tr_b16 v[94:95], v141 offset:23064\n\t" \
  "s_waitcnt lgkmcnt(8)\n\t" \
  "v_mfma_f32_16x16x32_bf16 v[32:35], v[96:99], v[124:127], v[32:35]\n\t" \
  "v_mfma_f32_16x16x32_bf16 v[36:39], v[96:99], v[128:131], v[36:39]\n\t" \
  "ds_read_b64_tr_b16 v[96:97], v141 offset:20512\n\t" \
  "ds_read_b64_tr_b16 v[98:99], v141 offset:23072\n\t" \
  "s_waitcnt vmcnt(0)\n\t" \
  "s_waitcnt lgkmcnt(8)\n\t" \
  "v_mfma_f32_16x16x32_bf16 v[0:3], v[80:83], v[132:135], v[0:3]\n\t" \
  "v_mfma_f32_16x16x32_bf16 v[4:7], v[80:83], v[136:139], v[4:7]\n\t" \
  "s_waitcnt lgkmcnt(6)\n\t" \
  "v_mfma_f32_16x16x32_bf16 v[8:11], v[84:87], v[132:135], v[8:11]\n\t" \
  "v_mfma_f32_16x16x32_bf16 v[12:15], v[84:87], v[136:139], v[12:15]\n\t" \
  "s_waitcnt lgkmcnt(4)\n\t" \
  "v_mfma_f32_16x16x32_bf16 v[16:19], v[88:91], v[132:135], v[16:19]\n\t" \
  "v_mfma_f32_16x16x32_bf16 v[20:23], v[88:91], v[136:139], v[20:23]\n\t" \
  "s_waitcnt lgkmcnt(2)\n\t" \
  "v_mfma_f32_16x16x32_bf16 v[24:27], v[92:95], v[132:135], v[24:27]\n\t" \
  "v_mfma_f32_16x16x32_bf16 v[28:31], v[92:95], v[136:139], v[28:31]\n\t" \
  "s_waitcnt lgkmcnt(0)\n\t" \
  "v_mfma_f32_16x16x32_bf16 v[32:35], v[96:99], v[132:135], v[32:35]\n\t" \
  "v_mfma_f32_16x16x32_bf16 v[36:39], v[96:99], v[136:139], v[36:39]\n\t" \
  "s_branch 8f\n\t" \
  "3:\n\t" \
  "s_mov_b32 %[want], 0\n\t" \
  "s_add_u32 %[t], %[s0], 0\n\t" \
  "buffer_load_dwordx4 v[100:103], v140, %[rs], %[t] offen sc1\n\t" \
  "s_add_u32 %[t], %[s0], 20480\n\t" \
  "buffer_load_dwordx4 v[104:107], v140, %[rs], %[t] offen sc1\n\t" \
  "s_add_u32 %[t], %[s0], 1024\n\t" \
  "buffer_load_dwordx4 v[108:111], v140, %[rs], %[t] offen sc1\n\t" \
  "s_add_u32 %[t], %[s0], 21504\n\t" \
  "buffer_load_dwordx4 v[112:115], v140, %[rs], %[t] offen sc1\n\t" \
  "s_add_u32 %[t], %[s0], 2048\n\t" \
  "buffer_load_dwordx4 v[116:119], v140, %[rs], %[t] offen sc1\n\t" \
  "s_add_u32 %[t], %[s0], 22528\n\t" \
  "buffer_load_dwordx4 v[120:123], v140, %[rs], %[t] offen sc1\n\t" \
  "s_add_u32 %[t], %[s0], 3072\n\t" \
  "buffer_load_dwordx4 v[124:127], v140, %[rs], %[t] offen sc1\n\t" \
  "s_add_u32 %[t], %[s0], 23552\n\t" \
  "buffer_load_dwordx4 v[128:131], v140, %[rs], %[t] offen sc1\n\t" \
  "s_add_u32 %[t], %[s0], 4096\n\t" \
  "buffer_load_dwordx4 v[132:135], v140, %[rs], %[t] offen sc1\n\t" \
  "s_add_u32 %[t], %[s0], 24576\n\t" \
  "buffer_load_dwordx4 v[136:139], v140, %[rs], %[t] offen sc1\n\t" \
  "ds_read_b64_tr_b16 v[80:81], v141 offset:0\n\t" \
  "ds_read_b64_tr_b16 v[82:83], v141 offset:2560\n\t" \
  "ds_read_b64_tr_b16 v[84:85], v141 offset:8\n\t" \
  "ds_read_b64_tr_b16 v[86:87], v141 offset:2568\n\t" \
  "ds_read_b64_tr_b16 v[88:89], v141 offset:16\n\t" \
  "ds_read_b64_tr_b16 v[90:91], v141 offset:2576\n\t" \
  "ds_read_b64_tr_b16 v[92:93], v141 offset:24\n\t" \
  "ds_read_b64_tr_b16 v[94:95], v141 offset:2584\n\t" \
  "ds_read_b64_tr_b16 v[96:97], v141 offset:32\n\t" \
  "ds_read_b64_tr_b16 v[98:99], v141 offset:2592\n\t" \
  "s_waitcnt vmcnt(8)\n\t" \
  "s_waitcnt lgkmcnt(8)\n\t" \
  "v_mfma_f32_16x16x32_bf16 v[0:3], v[80:83], v[100:103], v[0:3]\n\t" \
  "v_mfma_f32_16x16x32_bf16 v[4:7], v[80:83], v[104:107], v[4:7]\n\t" \
  "ds_read_b64_tr_b16 v[80:81], v141 offset:5120\n\t" \
  "ds_read_b64_tr_b16 v[82:83], v141 offset:7680\n\t" \
  "s_waitcnt lgkmcnt(8)\n\t" \
  "v_mfma_f32_16x16x32_bf16 v[8:11], v[84:87], v[100:103], v[8:11]\n\t" \
  "v_mfma_f32_16x16x32_bf16 v[12:15], v[84:87], v[104:107], v[12:15]\n\t" \
  "ds_read_b64_tr_b16 v[84:85], v141 offset:5128\n\t" \
  "ds_read_b64_tr_b16 v[86:87], v141 offset:7688\n\t" \
  "s_waitcnt lgkmcnt(8)\n\t" \
  "v_mfma_f32_16x16x32_bf16 v[16:19], v[88:91], v[100:103], v[16:19]\n\t" \
  "v_mfma_f32_16x16x32_bf16 v[20:23], v[88:91], v[104:107], v[20:23]\n\t" \
  "ds_read_b64_tr_b16 v[88:89], v141 offset:5136\n\t" \
  "ds_read_b64_tr_b16 v[90:91], v141 offset:7696\n\t" \
  "s_waitcnt lgkmcnt(8)\n\t" \
  "v_mfma_f32_16x16x32_bf16 v[24:27], v[92:95], v[100:103], v[24:27]\n\t" \
  "v_mfma_f32_16x16x32_bf16 v[28:31], v[92:95], v[104:107], v[28:31]\n\t" \
  "ds_read_b64_tr_b16 v[92:93], v141 offset:5144\n\t" \
  "ds_read_b64_tr_b16 v[94:95], v141 offset:7704\n\t" \
  "s_waitcnt lgkmcnt(8)\n\t" \
  "v_mfma_f32_16x16x32_bf16 v[32:35], v[96:99], v[100:103], v[32:35]\n\t" \
  "v_mfma_f32_16x16x32_bf16 v[36:39], v[96:99], v[104:107], v[36:39]\n\t" \
  "ds_read_b64_tr_b16 v[96:97], v141 offset:5152\n\t" \
  "ds_read_b64_tr_b16 v[98:99], v141 offset:7712\n\t" \
  "s_add_u32 %[t], %[s0], 10240\n\t" \
  "buffer_load_dwordx4 v[100:103], v140, %[rs], %[t] offen sc1\n\t" \
  "s_add_u32 %[t], %[s0], 30720\n\t" \
  "buffer_load_dwordx4 v[104:107], v140, %[rs], %[t] offen sc1\n\t" \
  "s_waitcnt vmcnt(8)\n\t" \
  "s_waitcnt lgkmcnt(8)\n\t" \
  "v_mfma_f32_16x16x32_bf16 v[0:3], v[80:83], v[108:111], v[0:3]\n\t" \
  "v_mfma_f32_16x16x32_bf16 v[4:7], v[80:83], v[112:115], v[4:7]\n\t" \
  "ds_read_b64_tr_b16 v[80:81], v141 offset:10240\n\t" \
  "ds_read_b64_tr_b16 v[82:83], v141 offset:12800\n\t" \
  "s_waitcnt lgkmcnt(8)\n\t" \
  "v_mfma_f32_16x16x32_bf16 v[8:11], v[84:87], v[108:111], v[8:11]\n\t" \
  "v_mfma_f32_16x16x32_bf16 v[12:15], v[84:87], v[112:115], v[12:15]\n\t" \
  "ds_read_b64_tr_b16 v[84:85], v141 offset:10248\n\t" \
  "ds_read_b64_tr_b16 v[86:87], v141 offset:12808\n\t" \
  "s_waitcnt lgkmcnt(8)\n\t" \
  "v_mfma_f32_16x16x32_bf16 v[16:19], v[88:91], v[108:111], v[16:19]\n\t" \
  "v_mfma_f32_16x16x32_bf16 v[20:23], v[88:91], v[112:115], v[20:23]\n\t" \
  "ds_read_b64_tr_b16 v[88:89], v141 offset:10256\n\t" \
  "ds_read_b64_tr_b16 v[90:91], v141 offset:12816\n\t" \
  "s_waitcnt lgkmcnt(8)\n\t" \
  "v_mfma_f32_16x16x32_bf16 v[24:27], v[92:95], v[108:111], v[24:27]\n\t" \
  "v_mfma_f32_16x16x32_bf16 v[28:31], v[92:95], v[112:115], v[28:31]\n\t" \
  "ds_read_b64_tr_b16 v[92:93], v141 offset:10264\n\t" \
  "ds_read_b64_tr_b16 v[94:95], v141 offset:12824\n\t" \
  "s_waitcnt lgkmcnt(8)\n\t" \
  "v_mfma_f32_16x16x32_bf16 v[32:35], v[96:99], v[108:111], v[32:35]\n\t" \
  "v_mfma_f32_16x16x32_bf16 v[36:39], v[96:99], v[112:115], v[36:39]\n\t" \
  "ds_read_b64_tr_b16 v[96:97], v141 offset:10272\n\t" \
  "ds_read_b64_tr_b16 v[98:99], v141 offset:12832\n\t" \
  "s_add_u32 %[t], %[s0], 11264\n\t" \
  "buffer_load_dwordx4 v[108:111], v140, %[rs], %[t] offen sc1\n\t" \
  "s_add_u32 %[t], %[s0], 31744\n\t" \
  "buffer_load_dwordx4 v[112:115], v140, %[rs], %[t] offen sc1\n\t" \
  "s_waitcnt vmcnt(8)\n\t" \
  "s_waitcnt lgkmcnt(8)\n\t" \
  "v_mfma_f32_16x16x32_bf16 v[0:3], v[80:83], v[116:119], v[0:3]\n\t" \
  "v_mfma_f32_16x16x32_bf16 v[4:7], v[80:83], v[120:123], v[4:7]\n\t" \
  "ds_read_b64_tr_b16 v[80:81], v141 offset:15360\n\t" \
  "ds_read_b64_tr_b16 v[82:83], v141 offset:17920\n\t" \
  "s_waitcnt lgkmcnt(8)\n\t" \
  "v_mfma_f32_16x16x32_bf16 v[8:11], v[84:87], v[116:119], v[8:11]\n\t" \
  "v_mfma_f32_16x16x32_bf16 v[12:15], v[84:87], v[120:123], v[12:15]\n\t" \
  "ds_read_b64_tr_b16 v[84:85], v141 offset:15368\n\t" \
  "ds_read_b64_tr_b16 v[86:87], v141 offset:17928\n\t" \
  "s_waitcnt lgkmcnt(8)\n\t" \
  "v_mfma_f32_16x16x32_bf16 v[16:19], v[88:91], v[116:119], v[16:19]\n\t" \
  "v_mfma_f32_16x16x32_bf16 v[20:23], v[88:91], v[120:123], v[20:23]\n\t" \
  "ds_read_b64_tr_b16 v[88:89], v141 offset:15376\n\t" \
  "ds_read_b64_tr_b16 v[90:91], v141 offset:17936\n\t" \
  "s_waitcnt lgkmcnt(8)\n\t" \
  "v_mfma_f32_16x16x32_bf16 v[24:27], v[92:95], v[116:119], v[24:27]\n\t" \
  "v_mfma_f32_16x16x32_bf16 v[28:31], v[92:95], v[120:123], v[28:31]\n\t" \
  "ds_read_b64_tr_b16 v[92:93], v141 offset:15384\n\t" \
  "ds_read_b64_tr_b16 v[94:95], v141 offset:17944\n\t" \
  "s_waitcnt lgkmcnt(8)\n\t" \
  "v_mfma_f32_16x16x32_bf16 v[32:35], v[96:99], v[116:119], v[32:35]\n\t" \
  "v_mfma_f32_16x16x32_bf16 v[36:39], v[96:99], v[120:123], v[36:39]\n\t" \
  "ds_read_b64_tr_b16 v[96:97], v141 offset:15392\n\t" \
  "ds_read_b64_tr_b16 v[98:99], v141 offset:17952\n\t" \
  "s_add_u32 %[t], %[s0], 12288\n\t" \
  "buffer_load_dwordx4 v[116:119], v140, %[rs], %[t] offen sc1\n\t" \
  "s_add_u32 %[t], %[s0], 32768\n\t" \
  "buffer_load_dwordx4 v[120:123], v140, %[rs], %[t] offen sc1\n\t" \
  "s_waitcnt vmcnt(8)\n\t" \
  "s_waitcnt lgkmcnt(8)\n\t" \
  "v_mfma_f32_16x16x32_bf16 v[0:3], v[80:83], v[124:127], v[0:3]\n\t" \
  "v_mfma_f32_16x16x32_bf16 v[4:7], v[80:83], v[128:131], v[4:7]\n\t" \
  "ds_read_b64_tr_b16 v[80:81], v141 offset:20480\n\t" \
  "ds_read_b64_tr_b16 v[82:83], v141 offset:23040\n\t" \
  "s_waitcnt lgkmcnt(8)\n\t" \
  "v_mfma_f32_16x16x32_bf16 v[8:11], v[84:87], v[124:127], v[8:11]\n\t" \
  "v_mfma_f32_16x16x32_bf16 v[12:15], v[84:87], v[128:131], v[12:15]\n\t" \
  "ds_read_b64_tr_b16 v[84:85], v141 offset:20488\n\t" \
  "ds_read_b64_tr_b16 v[86:87], v141 offset:23048\n\t" \
  "s_waitcnt lgkmcnt(8)\n\t" \
  "v_mfma_f32_16x16x32_bf16 v[16:19], v[88:91], v[124:127], v[16:19]\n\t" \
  "v_mfma_f32_16x16x32_bf16 v[20:23], v[88:91], v[128:131], v[20:23]\n\t" \
  "ds_read_b64_tr_b16 v[88:89], v141 offset:20496\n\t" \
  "ds_read_b64_tr_b16 v[90:91], v141 offset:23056\n\t" \
  "s_waitcnt lgkmcnt(8)\n\t" \
  "v_mfma_f32_16x16x32_bf16 v[24:27], v[92:95], v[124:127], v[24:27]\n\t" \
  "v_mfma_f32_16x16x32_bf16 v[28:31], v[92:95], v[128:131], v[28:31]\n\t" \
  "ds_read_b64_tr_b16 v[92:93], v141 offset:20504\n\t" \
  "ds_read_b64_tr_b16 v[94:95], v141 offset:23064\n\t" \
  "s_waitcnt lgkmcnt(8)\n\t" \
  "v_mfma_f32_16x16x32_bf16 v[32:35], v[96:99], v[124:127], v[32:35]\n\t" \
  "v_mfma_f32_16x16x32_bf16 v[36:39], v[96:99], v[128:131], v[36:39]\n\t" \
  "ds_read_b64_tr_b16 v[96:97], v141 offset:20512\n\t" \
  "ds_read_b64_tr_b16 v[98:99], v141 offset:23072\n\t" \
  "s_add_u32 %[t], %[s0], 13312\n\t" \
  "buffer_load_dwordx4 v[124:127], v140, %[rs], %[t] offen sc1\n\t" \
  "s_add_u32 %[t], %[s0], 33792\n\t" \
  "buffer_load_dwordx4 v[128:131], v140, %[rs], %[t] offen sc1\n\t" \
  "s_waitcnt vmcnt(8)\n\t" \
  "s_waitcnt lgkmcnt(8)\n\t" \
  "v_mfma_f32_16x16x32_bf16 v[0:3], v[80:83], v[132:135], v[0:3]\n\t" \
  "v_mfma_f32_16x16x32_bf16 v[4:7], v[80:83], v[136:139], v[4:7]\n\t" \
  "s_waitcnt lgkmcnt(6)\n\t" \
  "v_mfma_f32_16x16x32_bf16 v[8:11], v[84:87], v[132:135], v[8:11]\n\t" \
  "v_mfma_f32_16x16x32_bf16 v[12:15], v[84:87], v[136:139], v[12:15]\n\t" \
  "s_waitcnt lgkmcnt(4)\n\t" \
  "v_mfma_f32_16x16x32_bf16 v[16:19], v[88:91], v[132:135], v[16:19]\n\t" \
  "v_mfma_f32_16x16x32_bf16 v[20:23], v[88:91], v[136:139], v[20:23]\n\t" \
  "s_waitcnt lgkmcnt(2)\n\t" \
  "v_mfma_f32_16x16x32_bf16 v[24:27], v[92:95], v[132:135], v[24:27]\n\t" \
  "v_mfma_f32_16x16x32_bf16 v[28:31], v[92:95], v[136:139], v[28:31]\n\t" \
  "s_waitcnt lgkmcnt(0)\n\t" \
  "v_mfma_f32_16x16x32_bf16 v[32:35], v[96:99], v[132:135], v[32:35]\n\t" \
  "v_mfma_f32_16x16x32_bf16 v[36:39], v[96:99], v[136:139], v[36:39]\n\t" \
  "s_add_u32 %[t], %[s0], 14336\n\t" \
  "buffer_load_dwordx4 v[132:135], v140, %[rs], %[t] offen sc1\n\t" \
  "s_add_u32 %[t], %[s0], 34816\n\t" \
  "buffer_load_dwordx4 v[136:139], v140, %[rs], %[t] offen sc1\n\t" \
  "s_branch 5f\n\t" \
  "4:\n\t" \
  "s_add_u32 %[t], %[s0], 10240\n\t" \
  "buffer_load_dwordx4 v[100:103], v140, %[rs], %[t] offen sc1\n\t" \
  "s_add_u32 %[t], %[s0], 30720\n\t" \
  "buffer_load_dwordx4 v[104:107], v140, %[rs], %[t] offen sc1\n\t" \
  "s_add_u32 %[t], %[s0], 11264\n\t" \
  "buffer_load_dwordx4 v[108:111], v140, %[rs], %[t] offen sc1\n\t" \
  "s_add_u32 %[t], %[s0], 31744\n\t" \
  "buffer_load_dwordx4 v[112:115], v140, %[rs], %[t] offen sc1\n\t" \
  "s_add_u32 %[t], %[s0], 12288\n\t" \
  "buffer_load_dwordx4 v[116:119], v140, %[rs], %[t] offen sc1\n\t" \
  "s_add_u32 %[t], %[s0], 32768\n\t" \
  "buffer_load_dwordx4 v[120:123], v140, %[rs], %[t] offen sc1\n\t" \
  "s_add_u32 %[t], %[s0], 13312\n\t" \
  "buffer_load_dwordx4 v[124:127], v140, %[rs], %[t] offen sc1\n\t" \
  "s_add_u32 %[t], %[s0], 33792\n\t" \
  "buffer_load_dwordx4 v[128:131], v140, %[rs], %[t] offen sc1\n\t" \
  "s_add_u32 %[t], %[s0], 14336\n\t" \
  "buffer_load_dwordx4 v[132:135], v140, %[rs], %[t] offen sc1\n\t" \
  "s_add_u32 %[t], %[s0], 34816\n\t" \
  "buffer_load_dwordx4 v[136:139], v140, %[rs], %[t] offen sc1\n\t" \
  "5:\n\t" \
  "ds_read_b64_tr_b16 v[80:81], v142 offset:0\n\t" \
  "ds_read_b64_tr_b16 v[82:83], v142 offset:2560\n\t" \
  "ds_read_b64_tr_b16 v[84:85], v142 offset:8\n\t" \
  "ds_read_b64_tr_b16 v[86:87], v142 offset:2568\n\t" \
  "ds_read_b64_tr_b16 v[88:89], v142 offset:16\n\t" \
  "ds_read_b64_tr_b16 v[90:91], v142 offset:2576\n\t" \
  "ds_read_b64_tr_b16 v[92:93], v142 offset:24\n\t" \
  "ds_read_b64_tr_b16 v[94:95], v142 offset:2584\n\t" \
  "ds_read_b64_tr_b16 v[96:97], v142 offset:32\n\t" \
  "ds_read_b64_tr_b16 v[98:99], v142 offset:2592\n\t" \
  "s_waitcnt vmcnt(8)\n\t" \
  "s_waitcnt lgkmcnt(8)\n\t" \
  "v_mfma_f32_16x16x32_bf16 v[40:43], v[80:83], v[100:103], v[40:43]\n\t" \
  "v_mfma_f32_16x16x32_bf16 v[44:47], v[80:83], v[104:107], v[44:47]\n\t" \
  "ds_read_b64_tr_b16 v[80:81], v142 offset:5120\n\t" \
  "ds_read_b64_tr_b16 v[82:83], v142 offset:7680\n\t" \
  "s_waitcnt lgkmcnt(8)\n\t" \
  "v_mfma_f32_16x16x32_bf16 v[48:51], v[84:87], v[100:103], v[48:51]\n\t" \
  "v_mfma_f32_16x16x32_bf16 v[52:55], v[84:87], v[104:107], v[52:55]\n\t" \
  "ds_read_b64_tr_b16 v[84:85], v142 offset:5128\n\t" \
  "ds_read_b64_tr_b16 v[86:87], v142 offset:7688\n\t" \
  "s_waitcnt lgkmcnt(8)\n\t" \
  "v_mfma_f32_16x16x32_bf16 v[56:59], v[88:91], v[100:103], v[56:59]\n\t" \
  "v_mfma_f32_16x16x32_bf16 v[60:63], v[88:91], v[104:107], v[60:63]\n\t" \
  "ds_read_b64_tr_b16 v[88:89], v142 offset:5136\n\t" \
  "ds_read_b64_tr_b16 v[90:91], v142 offset:7696\n\t" \
  "s_waitcnt lgkmcnt(8)\n\t" \
  "v_mfma_f32_16x16x32_bf16 v[64:67], v[92:95], v[100:103], v[64:67]\n\t" \
  "v_mfma_f32_16x16x32_bf16 v[68:71], v[92:95], v[104:107], v[68:71]\n\t" \
  "ds_read_b64_tr_b16 v[92:93], v142 offset:5144\n\t" \
  "ds_read_b64_tr_b16 v[94:95], v142 offset:7704\n\t" \
  "s_waitcnt lgkmcnt(8)\n\t" \
  "v_mfma_f32_16x16x32_bf16 v[72:75], v[96:99], v[100:103], v[72:75]\n\t" \
  "v_mfma_f32_16x16x32_bf16 v[76:79], v[96:99], v[104:107], v[76:79]\n\t" \
  "ds_read_b64_tr_b16 v[96:97], v142 offset:5152\n\t" \
  "ds_read_b64_tr_b16 v[98:99], v142 offset:7712\n\t" \
  "s_waitcnt vmcnt(6)\n\t" \
  "s_waitcnt lgkmcnt(8)\n\t" \
  "v_mfma_f32_16x16x32_bf16 v[40:43], v[80:83], v[108:111], v[40:43]\n\t" \
  "v_mfma_f32_16x16x32_bf16 v[44:47], v[80:83], v[112:115], v[44:47]\n\t" \
  "ds_read_b64_tr_b16 v[80:81], v142 offset:10240\n\t" \
  "ds_read_b64_tr_b16 v[82:83], v142 offset:12800\n\t" \
  "s_waitcnt lgkmcnt(8)\n\t" \
  "v_mfma_f32_16x16x32_bf16 v[48:51], v[84:87], v[108:111], v[48:51]\n\t" \
  "v_mfma_f32_16x16x32_bf16 v[52:55], v[84:87], v[112:115], v[52:55]\n\t" \
  "ds_read_b64_tr_b16 v[84:85], v142 offset:10248\n\t" \
  "ds_read_b64_tr_b16 v[86:87], v142 offset:12808\n\t" \
  "s_waitcnt lgkmcnt(8)\n\t" \
  "v_mfma_f32_16x16x32_bf16 v[56:59], v[88:91], v[108:111], v[56:59]\n\t" \
  "v_mfma_f32_16x16x32_bf16 v[60:63], v[88:91], v[112:115], v[60:63]\n\t" \
  "ds_read_b64_tr_b16 v[88:89], v142 offset:10256\n\t" \
  "ds_read_b64_tr_b16 v[90:91], v142 offset:12816\n\t" \
  "s_waitcnt lgkmcnt(8)\n\t" \
  "v_mfma_f32_16x16x32_bf16 v[64:67], v[92:95], v[108:111], v[64:67]\n\t" \
  "v_mfma_f32_16x16x32_bf16 v[68:71], v[92:95], v[112:115], v[68:71]\n\t" \
  "ds_read_b64_tr_b16 v[92:93], v142 offset:10264\n\t" \
  "ds_read_b64_tr_b16 v[94:95], v142 offset:12824\n\t" \
  "s_waitcnt lgkmcnt(8)\n\t" \
  "v_mfma_f32_16x16x32_bf16 v[72:75], v[96:99], v[108:111], v[72:75]\n\t" \
  "v_mfma_f32_16x16x32_bf16 v[76:79], v[96:99], v[112:115], v[76:79]\n\t" \
  "ds_read_b64_tr_b16 v[96:97], v142 offset:10272\n\t" \
  "ds_read_b64_tr_b16 v[98:99], v142 offset:12832\n\t" \
  "s_waitcnt vmcnt(4)\n\t" \
  "s_waitcnt lgkmcnt(8)\n\t" \
  "v_mfma_f32_16x16x32_bf16 v[40:43], v[80:83], v[116:119], v[40:43]\n\t" \
  "v_mfma_f32_16x16x32_bf16 v[44:47], v[80:83], v[120:123], v[44:47]\n\t" \
  "ds_read_b64_tr_b16 v[80:81], v142 offset:15360\n\t" \
  "ds_read_b64_tr_b16 v[82:83], v142 offset:17920\n\t" \
  "s_waitcnt lgkmcnt(8)\n\t" \
  "v_mfma_f32_16x16x32_bf16 v[48:51], v[84:87], v[116:119], v[48:51]\n\t" \
  "v_mfma_f32_16x16x32_bf16 v[52:55], v[84:87], v[120:123], v[52:55]\n\t" \
  "ds_read_b64_tr_b16 v[84:85], v142 offset:15368\n\t" \
  "ds_read_b64_tr_b16 v[86:87], v142 offset:17928\n\t" \
  "s_waitcnt lgkmcnt(8)\n\t" \
  "v_mfma_f32_16x16x32_bf16 v[56:59], v[88:91], v[116:119], v[56:59]\n\t" \
  "v_mfma_f32_16x16x32_bf16 v[60:63], v[88:91], v[120:123], v[60:63]\n\t" \
  "ds_read_b64_tr_b16 v[88:89], v142 offset:15376\n\t" \
  "ds_read_b64_tr_b16 v[90:91], v142 offset:17936\n\t" \
  "s_waitcnt lgkmcnt(8)\n\t" \
  "v_mfma_f32_16x16x32_bf16 v[64:67], v[92:95], v[116:119], v[64:67]\n\t" \
  "v_mfma_f32_16x16x32_bf16 v[68:71], v[92:95], v[120:123], v[68:71]\n\t" \
  "ds_read_b64_tr_b16 v[92:93], v142 offset:15384\n\t" \
  "ds_read_b64_tr_b16 v[94:95], v142 offset:17944\n\t" \
  "s_waitcnt lgkmcnt(8)\n\t" \
  "v_mfma_f32_16x16x32_bf16 v[72:75], v[96:99], v[116:119], v[72:75]\n\t" \
  "v_mfma_f32_16x16x32_bf16 v[76:79], v[96:99], v[120:123], v[76:79]\n\t" \
  "ds_read_b64_tr_b16 v[96:97], v142 offset:15392\n\t" \
  "ds_read_b64_tr_b16 v[98:99], v142 offset:17952\n\t" \
  "s_waitcnt vmcnt(2)\n\t" \
  "s_waitcnt lgkmcnt(8)\n\t" \
  "v_mfma_f32_16x16x32_bf16 v[40:43], v[80:83], v[124:127], v[40:43]\n\t" \
  "v_mfma_f32_16x16x32_bf16 v[44:47], v[80:83], v[128:131], v[44:47]\n\t" \
  "ds_read_b64_tr_b16 v[80:81], v142 offset:20480\n\t" \
  "ds_read_b64_tr_b16 v[82:83], v142 offset:23040\n\t" \
  "s_waitcnt lgkmcnt(8)\n\t" \
  "v_mfma_f32_16x16x32_bf16 v[48:51], v[84:87], v[124:127], v[48:51]\n\t" \
  "v_mfma_f32_16x16x32_bf16 v[52:55], v[84:87], v[128:131], v[52:55]\n\t" \
  "ds_read_b64_tr_b16 v[84:85], v142 offset:20488\n\t" \
  "ds_read_b64_tr_b16 v[86:87], v142 offset:23048\n\t" \
  "s_waitcnt lgkmcnt(8)\n\t" \
  "v_mfma_f32_16x16x32_bf16 v[56:59], v[88:91], v[124:127], v[56:59]\n\t" \
  "v_mfma_f32_16x16x32_bf16 v[60:63], v[88:91], v[128:131], v[60:63]\n\t" \
  "ds_read_b64_tr_b16 v[88:89], v142 offset:20496\n\t" \
  "ds_read_b64_tr_b16 v[90:91], v142 offset:23056\n\t" \
  "s_waitcnt lgkmcnt(8)\n\t" \
  "v_mfma_f32_16x16x32_bf16 v[64:67], v[92:95], v[124:127], v[64:67]\n\t" \
  "v_mfma_f32_16x16x32_bf16 v[68:71], v[92:95], v[128:131], v[68:71]\n\t" \
  "ds_read_b64_tr_b16 v[92:93], v142 offset:20504\n\t" \
  "ds_read_b64_tr_b16 v[94:95], v142 offset:23064\n\t" \
  "s_waitcnt lgkmcnt(8)\n\t" \
  "v_mfma_f32_16x16x32_bf16 v[72:75], v[96:99], v[124:127], v[72:75]\n\t" \
  "v_mfma_f32_16x16x32_bf16 v[76:79], v[96:99], v[128:131], v[76:79]\n\t" \
  "ds_read_b64_tr_b16 v[96:97], v142 offset:20512\n\t" \
  "ds_read_b64_tr_b16 v[98:99], v142 offset:23072\n\t" \
  "s_waitcnt vmcnt(0)\n\t" \
  "s_waitcnt lgkmcnt(8)\n\t" \
  "v_mfma_f32_16x16x32_bf16 v[40:43], v[80:83], v[132:135], v[40:43]\n\t" \
  "v_mfma_f32_16x16x32_bf16 v[44:47], v[80:83], v[136:139], v[44:47]\n\t" \
  "s_waitcnt lgkmcnt(6)\n\t" \
  "v_mfma_f32_16x16x32_bf16 v[48:51], v[84:87], v[132:135], v[48:51]\n\t" \
  "v_mfma_f32_16x16x32_bf16 v[52:55], v[84:87], v[136:139], v[52:55]\n\t" \
  "s_waitcnt lgkmcnt(4)\n\t" \
  "v_mfma_f32_16x16x32_bf16 v[56:59], v[88:91], v[132:135], v[56:59]\n\t" \
  "v_mfma_f32_16x16x32_bf16 v[60:63], v[88:91], v[136:139], v[60:63]\n\t" \
  "s_waitcnt lgkmcnt(2)\n\t" \
  "v_mfma_f32_16x16x32_bf16 v[64:67], v[92:95], v[132:135], v[64:67]\n\t" \
  "v_mfma_f32_16x16x32_bf16 v[68:71], v[92:95], v[136:139], v[68:71]\n\t" \
  "s_waitcnt lgkmcnt(0)\n\t" \
  "v_mfma_f32_16x16x32_bf16 v[72:75], v[96:99], v[132:135], v[72:75]\n\t" \
  "v_mfma_f32_16x16x32_bf16 v[76:79], v[96:99], v[136:139], v[76:79]\n\t" \
  "8:\n\t" \
  "s_cmp_lg_u32 %[want], 0\n\t" \
  "s_cbranch_scc1 1b\n\t" \
  "9:\n\t" \
  "s_nop 7\n\t"
#define TILE_GEMM_CLOB "v80", "v81", "v82", "v83", "v84", "v85", "v86", "v87", "v88", "v89", "v90", "v91", "v92", "v93", "v94", "v95", "v96", "v97", "v98", "v99", "v100", "v101", "v102", "v103", "v104", "v105", "v106", "v107", "v108", "v109", "v110", "v111", "v112", "v113", "v114", "v115", "v116", "v117", "v118", "v119", "v120", "v121", "v122", "v123", "v124", "v125", "v126", "v127", "v128", "v129", "v130", "v131", "v132", "v133", "v134", "v135", "v136", "v137", "v138", "v139", "v144", "v145"
#define K1_LOOP_ASM \
  "v_mov_b64 v[0:1], 0\n\t" \
  "v_mov_b64 v[2:3], 0\n\t" \
  "v_mov_b64 v[4:5], 0\n\t" \
  "v_mov_b64 v[6:7], 0\n\t" \
  "v_mov_b64 v[8:9], 0\n\t" \
  "v_mov_b64 v[10:11], 0\n\t" \
  "v_mov_b64 v[12:13], 0\n\t" \
  "v_mov_b64 v[14:15], 0\n\t" \
  "v_mov_b64 v[16:17], 0\n\t" \
  "v_mov_b64 v[18:19], 0\n\t" \
  "v_mov_b64 v[20:21], 0\n\t" \
  "v_mov_b64 v[22:23], 0\n\t" \
  "v_mov_b64 v[24:25], 0\n\t" \
  "v_mov_b64 v[26:27], 0\n\t" \
  "v_mov_b64 v[28:29], 0\n\t" \
  "v_mov_b64 v[30:31], 0\n\t" \
  "v_mov_b64 v[32:33], 0\n\t" \
  "v_mov_b64 v[34:35], 0\n\t" \
  "v_mov_b64 v[36:37], 0\n\t" \
  "v_mov_b64 v[38:39], 0\n\t" \
  "v_mov_b64 v[40:41], 0\n\t" \
  "v_mov_b64 v[42:43], 0\n\t" \
  "v_mov_b64 v[44:45], 0\n\t" \
  "v_mov_b64 v[46:47], 0\n\t" \
  "v_mov_b64 v[48:49], 0\n\t" \
  "v_mov_b64 v[50:51], 0\n\t" \
  "v_mov_b64 v[52:53], 0\n\t" \
  "v_mov_b64 v[54:55], 0\n\t" \
  "v_mov_b64 v[56:57], 0\n\t" \
  "v_mov_b64 v[58:59], 0\n\t" \
  "v_mov_b64 v[60:61], 0\n\t" \
  "v_mov_b64 v[62:63], 0\n\t" \
  "v_mov_b64 v[64:65], 0\n\t" \
  "v_mov_b64 v[66:67], 0\n\t" \
  "v_mov_b64 v[68:69], 0\n\t" \
  "v_mov_b64 v[70:71], 0\n\t" \
  "v_mov_b64 v[72:73], 0\n\t" \
  "v_mov_b64 v[74:75], 0\n\t" \
  "v_mov_b64 v[76:77], 0\n\t" \
  "v_mov_b64 v[78:79], 0\n\t" \
  "v_mov_b64 v[116:117], 0\n\t" \
  "v_mov_b64 v[118:119], 0\n\t" \
  "v_mov_b64 v[120:121], 0\n\t" \
  "v_mov_b64 v[122:123], 0\n\t" \
  "v_mov_b64 v[124:125], 0\n\t" \
  "v_mov_b64 v[126:127], 0\n\t" \
  "v_mov_b64 v[128:129], 0\n\t" \
  "v_mov_b64 v[130:131], 0\n\t" \
  "v_mov_b64 v[132:133], 0\n\t" \
  "v_mov_b64 v[134:135], 0\n\t" \
  "v_mov_b64 v[136:137], 0\n\t" \
  "v_mov_b64 v[138:139], 0\n\t" \
  "v_mov_b64 v[140:141], 0\n\t" \
  "v_mov_b64 v[142:143], 0\n\t" \
  "v_mov_b64 v[144:145], 0\n\t" \
  "v_mov_b64 v[146:147], 0\n\t" \
  "v_mov_b64 v[148:149], 0\n\t" \
  "v_mov_b64 v[150:151], 0\n\t" \
  "s_mov_b32 %[cnt], 0\n\t" \
  "1:\n\t" \
  "s_barrier\n\t" \
  "v_mfma_f32_16x16x32_bf16 v[0:3], v[132:135], v[116:119], v[0:3]\n\t" \
  "ds_read_b128 v[80:83], v152\n\t" \
  "v_mfma_f32_16x16x32_bf16 v[4:7], v[132:135], v[120:123], v[4:7]\n\t" \
  "ds_read_b64_tr_b16 v[96:97], v154 offset:0\n\t" \
  "v_mfma_f32_16x16x32_bf16 v[8:11], v[132:135], v[124:127], v[8:11]\n\t" \
  "ds_read_b64_tr_b16 v[98:99], v154 offset:2560\n\t" \
  "v_mfma_f32_16x16x32_bf16 v[12:15], v[132:135], v[128:131], v[12:15]\n\t" \
  "ds_read_b128 v[84:87], v152 offset:2048\n\t" \
  "v_mfma_f32_16x16x32_bf16 v[16:19], v[136:139], v[116:119], v[16:19]\n\t" \
  "ds_read_b128 v[88:91], v152 offset:4096\n\t" \
  "v_mfma_f32_16x16x32_bf16 v[20:23], v[136:139], v[120:123], v[20:23]\n\t" \
  "ds_read_b128 v[92:95], v152 offset:6144\n\t" \
  "v_mfma_f32_16x16x32_bf16 v[24:27], v[136:139], v[124:127], v[24:27]\n\t" \
  "ds_read_b64_tr_b16 v[100:101], v154 offset:8\n\t" \
  "v_mfma_f32_16x16x32_bf16 v[28:31], v[136:139], v[128:131], v[28:31]\n\t" \
  "ds_read_b64_tr_b16 v[102:103], v154 offset:2568\n\t" \
  "v_mfma_f32_16x16x32_bf16 v[32:35], v[140:143], v[116:119], v[32:35]\n\t" \
  "ds_read_b64_tr_b16 v[104:105], v154 offset:16\n\t" \
  "v_mfma_f32_16x16x32_bf16 v[36:39], v[140:143], v[120:123], v[36:39]\n\t" \
  "ds_read_b64_tr_b16 v[106:107], v154 offset:2576\n\t" \
  "v_mfma_f32_16x16x32_bf16 v[40:43], v[140:143], v[124:127], v[40:43]\n\t" \
  "ds_read_b64_tr_b16 v[108:109], v154 offset:24\n\t" \
  "v_mfma_f32_16x16x32_bf16 v[44:47], v[140:143], v[128:131], v[44:47]\n\t" \
  "ds_read_b64_tr_b16 v[110:111], v154 offset:2584\n\t" \
  "v_mfma_f32_16x16x32_bf16 v[48:51], v[144:147], v[116:119], v[48:51]\n\t" \
  "ds_read_b64_tr_b16 v[112:113], v154 offset:32\n\t" \
  "v_mfma_f32_16x16x32_bf16 v[52:55], v[144:147], v[120:123], v[52:55]\n\t" \
  "ds_read_b64_tr_b16 v[114:115], v154 offset:2592\n\t" \
  "v_mfma_f32_16x16x32_bf16 v[56:59], v[144:147], v[124:127], v[56:59]\n\t" \
  "s_and_b32 %[inc], %[cnt], 3\n\t" \
  "v_mfma_f32_16x16x32_bf16 v[60:63], v[144:147], v[128:131], v[60:63]\n\t" \
  "s_cmp_eq_u32 %[inc], 3\n\t" \
  "v_mfma_f32_16x16x32_bf16 v[64:67], v[148:151], v[116:119], v[64:67]\n\t" \
  "s_cselect_b32 %[inc], %[wrap], %[stp]\n\t" \
  "v_mfma_f32_16x16x32_bf16 v[68:71], v[148:151], v[120:123], v[68:71]\n\t" \
  "v_add_u32 v152, %[inc], v152\n\t" \
  "v_mfma_f32_16x16x32_bf16 v[72:75], v[148:151], v[124:127], v[72:75]\n\t" \
  "v_mfma_f32_16x16x32_bf16 v[76:79], v[148:151], v[128:131], v[76:79]\n\t" \
  "s_waitcnt lgkmcnt(0)\n\t" \
  "v_mfma_f32_16x16x32_bf16 v[0:3], v[96:99], v[80:83], v[0:3]\n\t" \
  "ds_read_b128 v[116:119], v153\n\t" \
  "v_mfma_f32_16x16x32_bf16 v[4:7], v[96:99], v[84:87], v[4:7]\n\t" \
  "ds_read_b64_tr_b16 v[132:133], v154 offset:5120\n\t" \
  "v_mfma_f32_16x16x32_bf16 v[8:11], v[96:99], v[88:91], v[8:11]\n\t" \
  "ds_read_b64_tr_b16 v[134:135], v154 offset:7680\n\t" \
  "v_mfma_f32_16x16x32_bf16 v[12:15], v[96:99], v[92:95], v[12:15]\n\t" \
  "ds_read_b128 v[120:123], v153 offset:2048\n\t" \
  "v_mfma_f32_16x16x32_bf16 v[16:19], v[100:103], v[80:83], v[16:19]\n\t" \
  "ds_read_b128 v[124:127], v153 offset:4096\n\t" \
  "v_mfma_f32_16x16x32_bf16 v[20:23], v[100:103], v[84:87], v[20:23]\n\t" \
  "ds_read_b128 v[128:131], v153 offset:6144\n\t" \
  "v_mfma_f32_16x16x32_bf16 v[24:27], v[100:103], v[88:91], v[24:27]\n\t" \
  "ds_read_b64_tr_b16 v[136:137], v154 offset:5128\n\t" \
  "v_mfma_f32_16x16x32_bf16 v[28:31], v[100:103], v[92:95], v[28:31]\n\t" \
  "ds_read_b64_tr_b16 v[138:139], v154 offset:7688\n\t" \
  "v_mfma_f32_16x16x32_bf16 v[32:35], v[104:107], v[80:83], v[32:35]\n\t" \
  "ds_read_b64_tr_b16 v[140:141], v154 offset:5136\n\t" \
  "v_mfma_f32_16x16x32_bf16 v[36:39], v[104:107], v[84:87], v[36:39]\n\t" \
  "ds_read_b64_tr_b16 v[142:143], v154 offset:7696\n\t" \
  "v_mfma_f32_16x16x32_bf16 v[40:43], v[104:107], v[88:91], v[40:43]\n\t" \
  "ds_read_b64_tr_b16 v[144:145], v154 offset:5144\n\t" \
  "v_mfma_f32_16x16x32_bf16 v[44:47], v[104:107], v[92:95], v[44:47]\n\t" \
  "ds_read_b64_tr_b16 v[146:147], v154 offset:7704\n\t" \
  "v_mfma_f32_16x16x32_bf16 v[48:51], v[108:111], v[80:83], v[48:51]\n\t" \
  "ds_read_b64_tr_b16 v[148:149], v154 offset:5152\n\t" \
  "v_mfma_f32_16x16x32_bf16 v[52:55], v[108:111], v[84:87], v[52:55]\n\t" \
  "ds_read_b64_tr_b16 v[150:151], v154 offset:7712\n\t" \
  "v_mfma_f32_16x16x32_bf16 v[56:59], v[108:111], v[88:91], v[56:59]\n\t" \
  "v_add_u32 v153, %[inc], v153\n\t" \
  "v_mfma_f32_16x16x32_bf16 v[60:63], v[108:111], v[92:95], v[60:63]\n\t" \
  "v_add_u32 v154, %[inc], v154\n\t" \
  "v_mfma_f32_16x16x32_bf16 v[64:67], v[112:115], v[80:83], v[64:67]\n\t" \
  "s_add_u32 %[cnt], %[cnt], 1\n\t" \
  "v_mfma_f32_16x16x32_bf16 v[68:71], v[112:115], v[84:87], v[68:71]\n\t" \
  "s_cmp_lt_u32 %[cnt], 10\n\t" \
  "v_mfma_f32_16x16x32_bf16 v[72:75], v[112:115], v[88:91], v[72:75]\n\t" \
  "v_mfma_f32_16x16x32_bf16 v[76:79], v[112:115], v[92:95], v[76:79]\n\t" \
  "s_waitcnt lgkmcnt(0)\n\t" \
  "s_cbranch_scc1 1b\n\t" \
  "v_mfma_f32_16x16x32_bf16 v[0:3], v[132:135], v[116:119], v[0:3]\n\t" \
  "v_mfma_f32_16x16x32_bf16 v[16:19], v[136:139], v[116:119], v[16:19]\n\t" \
  "v_mfma_f32_16x16x32_bf16 v[32:35], v[140:143], v[116:119], v[32:35]\n\t" \
  "v_mfma_f32_16x16x32_bf16 v[48:51], v[144:147], v[116:119], v[48:51]\n\t" \
  "v_mfma_f32_16x16x32_bf16 v[64:67], v[148:151], v[116:119], v[64:67]\n\t" \
  "v_mfma_f32_16x16x32_bf16 v[4:7], v[132:135], v[120:123], v[4:7]\n\t" \
  "v_mfma_f32_16x16x32_bf16 v[20:23], v[136:139], v[120:123], v[20:23]\n\t" \
  "v_mfma_f32_16x16x32_bf16 v[36:39], v[140:143], v[120:123], v[36:39]\n\t" \
  "v_mfma_f32_16x16x32_bf16 v[52:55], v[144:147], v[120:123], v[52:55]\n\t" \
  "v_mfma_f32_16x16x32_bf16 v[68:71], v[148:151], v[120:123], v[68:71]\n\t" \
  "v_mfma_f32_16x16x32_bf16 v[8:11], v[132:135], v[124:127], v[8:11]\n\t" \
  "v_mfma_f32_16x16x32_bf16 v[24:27], v[136:139], v[124:127], v[24:27]\n\t" \
  "v_mfma_f32_16x16x32_bf16 v[40:43], v[140:143], v[124:127], v[40:43]\n\t" \
  "v_mfma_f32_16x16x32_bf16 v[56:59], v[144:147], v[124:127], v[56:59]\n\t" \
  "v_mfma_f32_16x16x32_bf16 v[72:75], v[148:151], v[124:127], v[72:75]\n\t" \
  "v_mfma_f32_16x16x32_bf16 v[12:15], v[132:135], v[128:131], v[12:15]\n\t" \
  "v_mfma_f32_16x16x32_bf16 v[28:31], v[136:139], v[128:131], v[28:31]\n\t" \
  "v_mfma_f32_16x16x32_bf16 v[44:47], v[140:143], v[128:131], v[44:47]\n\t" \
  "v_mfma_f32_16x16x32_bf16 v[60:63], v[144:147], v[128:131], v[60:63]\n\t" \
  "v_mfma_f32_16x16x32_bf16 v[76:79], v[148:151], v[128:131], v[76:79]\n\t" \
  "s_nop 7\n\t"
#define K1_LOOP_CLOB "v80", "v81", "v82", "v83", "v84", "v85", "v86", "v87", "v88", "v89", "v90", "v91", "v92", "v93", "v94", "v95", "v96", "v97", "v98", "v99", "v100", "v101", "v102", "v103", "v104", "v105", "v106", "v107", "v108", "v109", "v110", "v111", "v112", "v113", "v114", "v115", "v116", "v117", "v118", "v119", "v120", "v121", "v122", "v123", "v124", "v125", "v126", "v127", "v128", "v129", "v130", "v131", "v132", "v133", "v134", "v135", "v136", "v137", "v138", "v139", "v140", "v141", "v142", "v143", "v144", "v145", "v146", "v147", "v148", "v149", "v150", "v151"
#define VPOS 1
#define TPAR 1
#define OUT_AUX 2
#define LDELAY 2
#define QOKW 9u
#define FINW 8u
#define PHIPRIO 2
#define GPRIO 3
#define G2(i) do {} while (0)
#define GSTAMP(i) do {} while (0)
#define FSTAMP(i) do {} while (0)
#define HSTAMP(i) STAMP(i)
#define TSTAMP(i) do {} while (0)
#define FSLEEP 1
#define BSLEEP 1
#define K1_ROT ((tt + 3 * (jb >> 3)) % 10)
constexpr u32 K1_SLOT = 36864, K1_W = 16384;
constexpr u32 K1_EX = 3 * K1_SLOT, K1_EXP = 4 * K1_SLOT;
constexpr u32 K1_VI = 0, K1_VI_P = 208, K1_PHK = 27648, K1_PHK_P = 336;
constexpr u32 K1_QST = 0;
constexpr u32 K1_PHQ = 16384;
constexpr u32 K1_KVF = 70656;
constexpr u32 K1_XCH = 73728;
constexpr u32 K1_WPHI = 156160;
constexpr u32 L_K01 = 160256;
constexpr u32 SL = 4 * REC;
constexpr u32 L_IMG = 0;
constexpr u32 L_RING = 102400;
constexpr u32 L_MRG = L_RING + SL;
constexpr u32 L_FLG = 162816;
constexpr u32 F_GBAR = L_FLG + 96, F_APUB = L_FLG + 100, F_AFREE = L_FLG + 104  , F_P1 = L_FLG + 112, F_PHB = L_FLG + 116,
              F_P2 = L_FLG + 120, F_KVOK = L_FLG + 124, F_BIMG = L_FLG + 128, F_QOK = L_FLG + 132, F_TAG = L_FLG + 136, F_FIN = L_FLG + 152, F_K01 = L_FLG + 156, F_AOK = L_FLG + 160, F_P3 = L_FLG + 164, F_XF = L_FLG + 176, L_GT = L_FLG + 256;
constexpr u32 LDS_BYTES = 163840;

__device__ __forceinline__ f32x16 attn_exact(rsrc_t rW, u32x4 qx, u32 q2lo, int lane) {
  const f32x16 zero16 = {0.f, 0.f, 0.f, 0.f, 0.f, 0.f, 0.f, 0.f, 0.f, 0.f, 0.f, 0.f, 0.f, 0.f, 0.f, 0.f};
  const int r32 = lane & 31, hh = lane >> 5;
  const u32x2 qz = {q2lo, 0u};
  float m = -INFINITY;
#pragma unroll 1
  for (u32 rec = 0; rec < 64; ++rec) {
    const u32x4 a1 = gl128(rW, lane * 16, WS_KVIM + rec * REC);
    const u32 a2 = gl32(rW, lane * 4, WS_KVIM + rec * REC + 1024);
    f32x16 s = mfma32(a1, qx, zero16);
    s = mfma32k8((u32x2){a2, 0u}, qz, s);
    m = fmaxf(m, max16(s));
  }
  m = xhalf_max(m);
  f32x16 O = zero16;
  const u32 av = 1280u + (u32)hh * 352u + (u32)(r32 <= 20 ? r32 : 21) * 16u;
#pragma unroll 1
  for (u32 t2 = 0; t2 < 64; ++t2) {
    const u32 rec = (t2 + 62) & 63;
    const u32x4 a1 = gl128(rW, lane * 16, WS_KVIM + rec * REC);
    const u32 a2 = gl32(rW, lane * 4, WS_KVIM + rec * REC + 1024);
    const u32x4 b1 = gl128(rW, av, WS_KVIM + t2 * REC), b2 = gl128(rW, av, WS_KVIM + t2 * REC + 704);
    f32x16 s = mfma32(a1, qx, zero16);
    s = mfma32k8((u32x2){a2, 0u}, qz, s);
#pragma unroll
    for (int r = 0; r < 16; ++r) s[r] = __builtin_amdgcn_exp2f(s[r] - m);
    O = smf(b1, lo8(s), O, 0xDDDD);
    O = smf(b2, hi8(s), O, 0xDDDD);
  }
  return O;
}

extern "C" __global__ void __launch_bounds__(768, 3)
gm_fused(const char* __restrict__ x, const char* __restrict__ wqkv, const char* __restrict__ wphi, const char* __restrict__ wq,
         const char* __restrict__ wk, const char* __restrict__ wv, const char* __restrict__ gw, const char* __restrict__ awo,
         const char* __restrict__ bwo, char* ws, char* __restrict__ out) {
  extern __shared__ __attribute__((aligned(16))) char smem[];
  ldsp lds = (ldsp)smem;
  const u32 lds0 = (u32)(uintptr_t)lds;
  const int w = __builtin_amdgcn_readfirstlane(threadIdx.x >> 6);
  const int bid = blockIdx.x, xcd = bid & 7, jb = bid >> 3;
  const rsrc_t rW = __builtin_amdgcn_make_buffer_rsrc(ws, 0, WS_END, 0x00020000);
  const f32x16 zero16 = {0.f, 0.f, 0.f, 0.f, 0.f, 0.f, 0.f, 0.f, 0.f, 0.f, 0.f, 0.f, 0.f, 0.f, 0.f, 0.f};
  asm volatile("" :: "s"(x), "s"(wqkv), "s"(wphi), "s"(wq), "s"(wk), "s"(wv), "s"(gw), "s"(awo), "s"(bwo), "s"(ws), "s"(out));
  STAMP_INIT();
  const int type = (xcd & 1) ^ TPAR, tt = ((xcd >> 1) & 1) * 8 + (jb & 7), hg = (xcd >> 2) * 4 + (jb >> 3);
  const u32x4 rsWS = mkrsrc(ws, WS_END);
  u32 vim[9];
  {
    if (w < 4) {
      const int cg = w >> 1, th = w & 1;
      f32x16 ac[5];
      {
        const int lane = lane_id(), g = lane >> 4, i16 = lane & 15;
        u32 aW = lds0 + K1_W + (u32)cg * 10240u + (u32)(4 * g + (i16 >> 2)) * 160u + (u32)(i16 & 3) * 40u;
        u32 aX0 = lds0 + (u32)(64 * th + i16) * 128u + (u32)((g ^ (i16 >> 1)) * 16), aX1 = aX0 ^ 64u;
        u32 cnt, inc;
        asm volatile(K1_LOOP_ASM
                     : "=&{v[0:15]}"(ac[0]), "=&{v[16:31]}"(ac[1]), "=&{v[32:47]}"(ac[2]), "=&{v[48:63]}"(ac[3]), "=&{v[64:79]}"(ac[4]),
                       "+{v152}"(aX0), "+{v153}"(aX1), "+{v154}"(aW), [cnt] "=&s"(cnt), [inc] "=&s"(inc)
                     : [wrap] "s"(0u - 3u * K1_SLOT), [stp] "s"(K1_SLOT)
                     : "memory", "scc", K1_LOOP_CLOB);
      }
      HSTAMP(2); FSTAMP(2);
      const int lane = lane_id(), g = lane >> 4, i16 = lane & 15;
      if (cg && !type) {
#pragma unroll
        for (int T = 0; T < 5; ++T) ac[T] *= QSCALE;
      }
#pragma unroll
      for (int n = 0; n < 4; ++n) {
        u32 v[10];
#pragma unroll
        for (int T = 0; T < 5; ++T) { v[2 * T] = pk2(ac[T][4 * n], ac[T][4 * n + 1]); v[2 * T + 1] = pk2(ac[T][4 * n + 2], ac[T][4 * n + 3]); }
        const int t32 = 16 * (n & 1) + i16;
        if (cg == 0) {
          ldsp xw = lds + K1_XCH + (u32)(2 * n + th) * 2560u;
          *(LDSAS u32x4*)(xw + lane * 16) = (u32x4){v[0], v[1], v[2], v[3]};
          *(LDSAS u32x4*)(xw + 1024 + lane * 16) = (u32x4){v[4], v[5], v[6], v[7]};
          *(LDSAS u32x2*)(xw + 2048 + lane * 8) = (u32x2){v[8], v[9]};
        } else if (type) {
          ldsp dst = lds + K1_VI + (u32)(64 * th + 16 * n + i16) * K1_VI_P + (u32)g * 48u;
          *(LDSAS u32x4*)(dst) = (u32x4){v[0], v[1], v[2], v[3]};
          *(LDSAS u32x4*)(dst + 16) = (u32x4){v[4], v[5], v[6], v[7]};
          *(LDSAS u32x4*)(dst + 32) = (u32x4){v[8], v[9], 0x00003F80u, 0u};
        } else if (th == 0) {
          ldsp dst = lds + K1_QST + (u32)(2 * g + (n >> 1)) * 1280u;
          *(LDSAS u32x4*)(dst + t32 * 16) = (u32x4){v[0], v[1], v[2], v[3]};
          *(LDSAS u32x4*)(dst + 512 + t32 * 16) = (u32x4){v[4], v[5], v[6], v[7]};
          *(LDSAS u32x2*)(dst + 1024 + t32 * 8) = (u32x2){v[8], v[9]};
        } else {
          const u32 dst = WS_QB + (u32)(tt * 8 + hg) * 10240u + (u32)(2 * g + (n >> 1)) * 1280u;
          st128(rW, dst + t32 * 16, (u32x4){v[0], v[1], v[2], v[3]});
          st128(rW, dst + 512 + t32 * 16, (u32x4){v[4], v[5], v[6], v[7]});
          st64(rW, dst + 1024 + t32 * 8, (u32x2){v[8], v[9]});
        }
      }
      LBAR();
      HSTAMP(3);
      if (w == 3) {
        const u32x2 tag = ld64(lds + F_TAG);
        const u32 fq = WS_FQ + (u32)(tt * 8 + hg) * 128u;
        if (type == 0) { DRAIN(); st64(rW, lane == 0 ? fq : OOB, tag); FSTAMP(3); }
        else { flags_wait<1>(rW, fq, 1, tag); lds_st(lds + F_QOK, 1u); FSTAMP(4); }
      }
    } else {
      const int lane = lane_id(), g = lane >> 4, i16 = lane & 15;
      if (w >= 8) {
        __builtin_amdgcn_s_setprio(1);
        const int lw = w - 8, g1 = lw >> 1, hb = lw & 1;
        const u32x4 rsX = mkrsrc(x - 4096, SEQ * DM * 2 + 4096);
        const bool useq = g1 && !type;
        const u32x4 rsW = useq ? mkrsrc(wq - 4096, DM * DM * 2 + 4096) : mkrsrc(wqkv - 4096, DM * 3 * DM * 2 + 4096);
        const u32 pitch = useq ? 1280u : 3840u;
        const u32 col = (type ? (g1 ? 1280u : 640u) : 0u) + 80u * hg;
        u32 vx[4], vw[5];
        const u32 vx0 = 4096u + (u32)(lane >> 3) * 1280u + (u32)(((lane & 7) ^ (lane >> 4)) * 16);
#pragma unroll
        for (int i = 0; i < 4; ++i) vx[i] = (vx0 ^ (u32)((i & 1) * 64)) + (u32)i * 9216u;
#pragma unroll
        for (int j = 0; j < 5; ++j) {
          const u32 c = 64u * j + lane, rho = (c * 205u) >> 11, cc = c - rho * 10u;
          const u32 k = 8u * ((rho >> 2) & 3u) + 4u * (rho >> 4) + (rho & 3u);
          vw[j] = 4096u + k * pitch + cc * 16u - (u32)(j & 3) * 1024u;
        }
        const u32 sx0 = (u32)(128 * tt + 32 * lw) * 1280u, sw0 = 32u * hb * pitch + col * 2u;
        int ks = K1_ROT;
        auto issue = [&](int s) {
          const u32 base = lds0 + (u32)(s & 3) * K1_SLOT;
          u32 keep;
          asm volatile(
              "s_mov_b32 %0, m0\n\ts_mov_b32 m0, %[mx]\n\ts_nop 2\n\t"
              "buffer_load_dwordx4 %[x0], %[rx], %[sx] offen lds\n\t"
              "buffer_load_dwordx4 %[x1], %[rx], %[sx] offen offset:1024 lds\n\t"
              "buffer_load_dwordx4 %[x2], %[rx], %[sx] offen offset:2048 lds\n\t"
              "buffer_load_dwordx4 %[x3], %[rx], %[sx] offen offset:3072 lds\n\t"
              "s_mov_b32 m0, %[mw]\n\ts_nop 0\n\t"
              "buffer_load_dwordx4 %[w0], %[rw], %[sw] offen lds\n\t"
              "buffer_load_dwordx4 %[w1], %[rw], %[sw] offen offset:1024 lds\n\t"
              "buffer_load_dwordx4 %[w2], %[rw], %[sw] offen offset:2048 lds\n\t"
              "buffer_load_dwordx4 %[w3], %[rw], %[sw] offen offset:3072 lds\n\t"
              "s_add_u32 m0, %[mw], 4096\n\ts_nop 0\n\t"
              "buffer_load_dwordx4 %[w4], %[rw], %[sw] offen lds\n\t"
              "s_mov_b32 m0, %0"
              : "=&s"(keep)
              : [mx] "s"(base + (u32)lw * 4096u), [mw] "s"(base + K1_W + (u32)lw * 5120u),
                [rx] "s"(rsX), [rw] "s"(rsW), [sx] "s"(sx0 + (u32)ks * 128u), [sw] "s"(sw0 + (u32)ks * 64u * pitch),
                [x0] "v"(vx[0]), [x1] "v"(vx[1]), [x2] "v"(vx[2]), [x3] "v"(vx[3]), [w0] "v"(vw[0]), [w1] "v"(vw[1]), [w2] "v"(vw[2]), [w3] "v"(vw[3]),
                [w4] "v"(vw[4])
              : "memory", "scc");
          ks = ks == 9 ? 0 : ks + 1;
        };
        __builtin_amdgcn_s_sleep(LDELAY);
        issue(0);
        issue(1);
        WAIT_BAR(9);
        HSTAMP(2); FSTAMP(2);
        issue(2);
#pragma unroll 1
        for (int s = 1; s < 8; ++s) {
          issue(s + 2);
          WAIT_BAR(18);
        }
        WAIT_BAR(9);
        WAIT_BAR(0);
        lds_wait_ge<1>(lds + F_KVOK, 1);
        {
          const u32 o = (u32)lw * 13312u, vl = (u32)lane * 16u;
#pragma unroll 1
          for (u32 c = 0; c < 3; ++c) dma4(rsWS, vl, WS_KVIM + o + c * 4096u, lds0 + L_RING + o + c * 4096u);
          dma1s(rsWS, vl, WS_KVIM + o + 12288u, lds0 + L_RING + o + 12288u);
          dma1s(rsWS, vl, WS_KVIM + 52736u, lds0 + L_RING + 52736u);
        }
        __builtin_amdgcn_s_setprio(0);
#pragma unroll
        for (int i = 0; i < 9; ++i) {
          const u32 c = 64u * (u32)(lw + 3 * i) + lane, rho = (c * 6554u) >> 16, cc = c - rho * 10u;
          const u32 U = 4u * (rho >> 5) + ((rho >> 2) & 3u), u = (rho >> 4) & 1u, q = rho & 3u;
          const u32 hp = (U * 205u) >> 10, un = U - hp * 5u;
          const u32 row = 40u * hp + q + ((un < 4u) ? (un >> 1) * 20u + 4u * (un & 1u) + 8u * u : u * 20u + 16u);
          vim[i] = row * 1280u + cc * 16u;
          PIN(vim[i]);
        }
      } else {
        const int v4 = w - 4, tok0 = 128 * tt + 16 * hg;
        __builtin_amdgcn_s_setprio(3);
        {
          const u32x4 rsE = mkrsrc((type ? wv : wk) + v4 * 6400, 6400);
          const u32 la = lds0 + K1_EX + (u32)v4 * 7168u;
#pragma unroll 1
          for (u32 c = 0; c < 7; ++c) dma1(rsE, (u32)lane * 16u, c * 1024u, la + c * 1024u);
          u32x4 xf[5], gf[5];
#pragma unroll
          for (int s = 0; s < 5; ++s) {
            xf[s] = *(const u32x4*)(x + (size_t)(tok0 + i16) * 1280 + v4 * 320 + s * 64 + g * 16);
            gf[s] = *(const u32x4*)(gw + v4 * 320 + s * 64 + g * 16);
          }
          u64 e = 0;
          if (w == 7) e = __hip_atomic_load((const gu64*)(ws + WS_EPOCH), __ATOMIC_RELAXED, __HIP_MEMORY_SCOPE_AGENT) + 0x9E3779B97F4A7C15ull;
          DRAIN();
          if (w == 7) lds_st(lds + L_FLG + lane * 4, lane == 34 ? (u32)e : (lane == 35 ? (u32)(e >> 32) : 0u));
          ldsp pe = lds + K1_EX + (u32)v4 * 7168u + (u32)(8 * g + (i16 >> 2)) * 40u + (u32)(i16 & 3) * 8u;
          f32x4 c0 = {0.f, 0.f, 0.f, 0.f}, c1 = c0, c2 = c0;
#pragma unroll
          for (int s = 0; s < 5; ++s) {
            const u32x4 w0 = trd2(pe + s * 1280, pe + s * 1280 + 160), w1 = trd2(pe + s * 1280 + 32, pe + s * 1280 + 192);
            if (type) { c0 = mfma16(xf[s], w0, c0); c1 = mfma16(xf[s], w1, c1); }
            else { c0 = mfma16(w0, xf[s], c0); c1 = mfma16(w1, xf[s], c1); }
            c2 = mfma16(gf[s], xf[s], c2);
          }
          ldsp pp = lds + K1_EXP + (u32)v4 * 1024u + lane * 16;
          *(LDSAS f32x4*)(pp) = c0; *(LDSAS f32x4*)(pp + 4096) = c1; *(LDSAS f32x4*)(pp + 8192) = c2;
        }
        LBAR();
        if (v4 < 3) {
          ldsp pp = lds + K1_EXP + (u32)v4 * 4096u + lane * 16;
          const f32x4 e0 = (*(LDSAS f32x4*)(pp) + *(LDSAS f32x4*)(pp + 1024)) + (*(LDSAS f32x4*)(pp + 2048) + *(LDSAS f32x4*)(pp + 3072));
          const u32x2 ev = {pk2(e0[0], e0[1]), pk2(e0[2], e0[3])};
          const int tile = tok0 >> 5, k0 = tok0 & 31;
          if (v4 == 2) {
            st32(rW, (type == 0 && g == 0) ? WS_GATE + (u32)(tok0 + i16) * 4u : OOB, __float_as_uint(__builtin_amdgcn_rcpf(1.f + __builtin_amdgcn_exp2f(-e0[0] * LOG2E))));
          } else if (type == 0) {
            const u32 rec = WS_KVIM + (u32)((tile + 62) & 63) * REC;
            if (v4 == 0) st64(rW, rec + (g >> 1) * 512 + (k0 + i16) * 16 + (g & 1) * 8, ev);
            else { const u32 o = g ? OOB : rec + 1024 + (k0 + i16) * 4; st32(rW, o, ev[0]); st32(rW, o + 128, ev[1]); }
          } else {
            const int d = 16 * v4 + i16, k32 = k0 + 4 * g;
            const u32x2 val = d == 20 ? (u32x2){0x3F803F80u, 0x3F803F80u} : (d == 21 ? (u32x2){0u, 0u} : ev);
            st64(rW, d <= 21 ? WS_KVIM + (u32)tile * REC + 1280 + (k32 >> 3) * 352 + d * 16 + (k32 & 7) * 2 : OOB, val);
          }
        }
        LBAR();
        {
          const u32 be = (u32)(v4 * 64 + lane) * 16u;
          dma1(mkrsrc(wphi, NH * 400 * 2), be < 3200u ? be : OOB, (u32)hg * 3200u, lds0 + K1_WPHI + (u32)v4 * 1024u);
        }
        LBAR();
        WAIT_BAR(0);
        const u32x2 tag = ld64(lds + F_TAG);
        if (w == 7) {
          st64(rW, lane < 8 ? WS_F1 + (u32)lane * 2048u + (u32)bid * 8u : OOB, tag);
          FSTAMP(3);
          LBAR(); LBAR(); LBAR(); LBAR(); LBAR();
          const u32 fo = WS_F1 + (u32)xcd * 2048u;
          u32x4 fa = gl128(rW, lane * 16, fo), fb = gl128(rW, lane * 16, fo + 1024);
          LBAR();
          PIN(fa); PIN(fb);
          const u32 dd = (fa[0] ^ tag[0]) | (fa[1] ^ tag[1]) | (fa[2] ^ tag[0]) | (fa[3] ^ tag[1]) | (fb[0] ^ tag[0]) | (fb[1] ^ tag[1]) | (fb[2] ^ tag[0]) | (fb[3] ^ tag[1]);
          if (!__all(dd == 0u)) flags_wait256<2>(rW, fo, tag);
          asm volatile("" ::: "memory");
          FSTAMP(4);
          const u32x4 ka = gl128(rW, lane * 16, WS_KVIM + 62 * REC), kb = gl128(rW, lane * 16, WS_KVIM + 63 * REC);
          const u32 kc = gl32(rW, lane * 4, WS_KVIM + 62 * REC + 1024), kd = gl32(rW, lane * 4, WS_KVIM + 63 * REC + 1024);
          const u32 gtv = gl32(rW, lane * 4, WS_GATE + (u32)(2 * tt + type) * 256u);
          u32 wrm = gl32(rW, lane < 29 ? (u32)(jb * 29 + lane) * 128u : OOB, WS_KVIM + 5 * SL);
          asm volatile("" ::: "memory");
          lds_st(lds + F_KVOK, 1u);
          if (bid == 0 && lane == 0)
            __hip_atomic_store((gu64*)(ws + WS_EPOCH), (u64)tag[0] | ((u64)tag[1] << 32), __ATOMIC_RELAXED, __HIP_MEMORY_SCOPE_AGENT);
          *(LDSAS u32x4*)(lds + L_K01 + lane * 16) = ka; *(LDSAS u32*)(lds + L_K01 + 1024 + lane * 4) = kc;
          *(LDSAS u32x4*)(lds + L_K01 + 1280 + lane * 16) = kb; *(LDSAS u32*)(lds + L_K01 + 2304 + lane * 4) = kd;
          *(LDSAS u32*)(lds + L_GT + lane * 4) = gtv;
          PIN(wrm);
        } else {
          LBAR(); LBAR(); LBAR(); LBAR(); LBAR(); LBAR();
        }
      }
      __builtin_amdgcn_s_setprio(PHIPRIO);
      const int j8 = w - 4;
      const u32 aP = K1_WPHI + (((i16 & 3) == g) ? (u32)g * 800u + (u32)(i16 >> 2) * 40u : 3200u);
      u32x4 a0[5], a1[5]; u32x2 a2[5];
#pragma unroll
      for (int T = 0; T < 5; ++T) {
        a0[T] = trd2(lds + aP + T * 8, lds + aP + T * 8 + 160);
        a1[T] = trd2(lds + aP + T * 8 + 320, lds + aP + T * 8 + 480);
        a2[T] = trd(lds + aP + T * 8 + 640);
      }
      LBAR();
      HSTAMP(3);
      ldsp xr = lds + K1_XCH + (u32)j8 * 2560u;
      const u32x4 bq0 = ld128(xr + lane * 16), bq1 = ld128(xr + 1024 + lane * 16);
      const u32x2 bq2 = ld64(xr + 2048 + lane * 8);
      if (w >= 8) {
        DRAIN();
        if (lds_arrive(lds + F_K01) == 3 && lane < 8) lds_st(lds + L_FLG + 32 + lane * 4, 5u);
      }
      f32x4 pq[5];
#pragma unroll
      for (int T = 0; T < 5; ++T) pq[T] = mfma16(a0[T], bq0, (f32x4){0.f, 0.f, 0.f, 0.f});
#pragma unroll
      for (int T = 0; T < 5; ++T) pq[T] = mfma16(a1[T], bq1, pq[T]);
#pragma unroll
      for (int T = 0; T < 5; ++T) pq[T] = mfma16((u32x4){a2[T][0], a2[T][1], 0u, 0u}, (u32x4){bq2[0], bq2[1], 0u, 0u}, pq[T]);
      float mx = fmaxf(pq[0][0], pq[0][1]), mn = fminf(pq[0][0], pq[0][1]);
      mx = fmaxf(fmaxf(mx, pq[0][2]), pq[0][3]); mn = fminf(fminf(mn, pq[0][2]), pq[0][3]);
#pragma unroll
      for (int T = 1; T < 5; ++T) {
        mx = fmaxf(fmaxf(mx, pq[T][0]), pq[T][1]); mx = fmaxf(fmaxf(mx, pq[T][2]), pq[T][3]);
        mn = fminf(fminf(mn, pq[T][0]), pq[T][1]); mn = fminf(fminf(mn, pq[T][2]), pq[T][3]);
      }
      const float cp = -mx * LOG2E, cn = mn * LOG2E;
      f32x4 ep[5], en[5];
#pragma unroll
      for (int T = 0; T < 5; ++T) { ep[T] = pq[T] * LOG2E + cp; en[T] = pq[T] * (-LOG2E) + cn; PIN(ep[T]); PIN(en[T]); }
#pragma unroll
      for (int T = 0; T < 5; ++T)
#pragma unroll
        for (int i = 0; i < 4; ++i) { ep[T][i] = __builtin_amdgcn_exp2f(ep[T][i]); en[T][i] = __builtin_amdgcn_exp2f(en[T][i]); }
      const f32x4 sp = (ep[0] + ep[1]) + (ep[2] + ep[3]) + ep[4], sn = (en[0] + en[1]) + (en[2] + en[3]) + en[4];
      const float rp = __builtin_amdgcn_rcpf((sp[0] + sp[1]) + (sp[2] + sp[3])), rn = __builtin_amdgcn_rcpf((sn[0] + sn[1]) + (sn[2] + sn[3]));
      u32 ph[20];
#pragma unroll
      for (int T = 0; T < 5; ++T) {
        const f32x4 a = ep[T] * rp, b = en[T] * rn;
        ph[2 * T] = pk2(a[0], a[1]); ph[2 * T + 1] = pk2(a[2], a[3]);
        ph[10 + 2 * T] = pk2(b[0], b[1]); ph[11 + 2 * T] = pk2(b[2], b[3]);
      }
      const int tl = 64 * (j8 & 1) + 16 * (j8 >> 1) + i16;
      if (type == 0) {
        ldsp dst = lds + K1_PHQ + (u32)(4 * g + (tl >> 5)) * 2560u + (tl & 31) * 16;
#pragma unroll
        for (int c = 0; c < 5; ++c) *(LDSAS u32x4*)(dst + c * 512) = (u32x4){ph[4 * c], ph[4 * c + 1], ph[4 * c + 2], ph[4 * c + 3]};
      } else {
        ldsp dst = lds + K1_PHK + (u32)tl * K1_PHK_P + (u32)g * 80u;
#pragma unroll
        for (int c = 0; c < 5; ++c) *(LDSAS u32x4*)(dst + 16 * c) = (u32x4){ph[4 * c], ph[4 * c + 1], ph[4 * c + 2], ph[4 * c + 3]};
        lds_arrive(lds + F_PHB);
        lds_wait_ge<1>(lds + F_PHB, 8);
        const int hl = j8 >> 1, dt = j8 & 1;
        const u32 trow = (u32)(8 * g + (i16 >> 2));
        ldsp pa = lds + K1_PHK + trow * K1_PHK_P + (u32)hl * 80u + (u32)(i16 & 3) * 8u;
        ldsp pb = lds + K1_VI + trow * K1_VI_P + (u32)hl * 48u + (u32)dt * 32u + (u32)(i16 & 3) * 8u;
        f32x4 kv[3];
#pragma unroll
        for (int ft = 0; ft < 3; ++ft) kv[ft] = (f32x4){0.f, 0.f, 0.f, 0.f};
#pragma unroll
        for (int ks = 0; ks < 4; ++ks) {
          u32x4 bv = trd2(pb + ks * 32 * K1_VI_P, pb + (ks * 32 + 4) * K1_VI_P);
#pragma unroll
          for (int ft = 0; ft < 3; ++ft) {
            u32x4 av = trd2(pa + ks * 32 * K1_PHK_P + ft * 32, pa + (ks * 32 + 4) * K1_PHK_P + ft * 32);
            kv[ft] = mfma16(av, bv, kv[ft]);
          }
        }
#pragma unroll
        for (int ft = 0; ft < 3; ++ft) *(LDSAS f32x4*)(lds + K1_KVF + (u32)j8 * 3072u + ft * 1024 + lane * 16) = kv[ft];
      }
      lds_arrive(lds + F_P2);
      HSTAMP(4);
    }
  }

  if (w >= 8) {
    __builtin_amdgcn_s_setprio(GPRIO);
    const int hw = w - 8, kh = hw >> 1, t64 = xcd * 4 + (jb & 3), cgp = jb >> 2;
    const u32x2 tag = ld64(lds + F_TAG);
    u32 s = 5, slot = 0, ps = 5;
    auto stream = [&](u32 word, u32 want) {
      const int ln = lane_id();
      const u32 vl = (u32)ln * 16u;
#pragma unroll 1
      for (u32 it = 0; it < SPIN_CAP && ps < 16; ++it) {
        const u32 fl = lds_ld(lds + (ln < 8 ? L_FLG + ln * 4 : word));
        const u32 ns = s;
        const bool go = __all(ln < 8 ? fl + 3 <= s : fl >= want);
        if (!go && s < 16 && __all(ln >= 8 || fl + 4 >= s)) {
          const u32 so = WS_KVIM + s * SL, la = lds0 + L_RING + slot * SL;
          dma4(rsWS, vl, so, la); dma4(rsWS, vl, so + 4096u, la + 4096u); dma2h(rsWS, vl, so + 8192u, la + 8192u);
          asm volatile("s_waitcnt vmcnt(11)" ::: "memory");
          ++s; slot = slot == 4 ? 0 : slot + 1;
        } else {
          DRAIN();
          if (ps == ns) {
            if (__all(ln < 8 || fl >= want)) break;
            __builtin_amdgcn_s_sleep(4);
          }
        }
        if (ps != ns) { ps = ns; if (ln < 8) lds_st(lds + L_FLG + 32 + ln * 4, ps); }
      }
    };
    auto image = [&](const char* wo) {
      const u32x4 rsO = mkrsrc(wo, DM * DM * 2);
#pragma unroll 1
      for (u32 per = 0; per < 4; ++per) {
        const u32 so = per * 204800u + (u32)cgp * 160u, la = lds0 + L_IMG + (per * 25u + (u32)hw) * 1024u;
#pragma unroll
        for (int i = 0; i < 8; ++i) dma1(rsO, vim[i], so, la + i * 3072);
        if (hw == 0) dma1(rsO, vim[8], so, la + 24576);
      }
      DRAIN();
    };
    lds_wait_ge<2>(lds + F_P2, 8);
    {
      const int lane = lane_id(), g = lane >> 4, i16 = lane & 15, r32 = lane & 31, hh = lane >> 5;
      u32x4 p0[4], p1[4], p2[4]; u32 gt[4];
      if (type) {
#pragma unroll
        for (int i = 0; i < 2; ++i) {
          const int d = 16 * i + i16;
          const u32 dst = d <= 20 ? WS_KVP + (u32)((hg * 16 + tt) * 4 + hw) * 3360u + d * 160 + 16 * g : OOB;
          ldsp src = lds + K1_KVF + (u32)(2 * hw + i) * 3072u + lane * 16;
          st128(rW, dst, ld128(src)); st128(rW, dst + 64, ld128(src + 1024)); st128(rW, g < 2 ? dst + 128 : OOB, ld128(src + 2048));
        }
        DRAIN();
        if (lds_arrive(lds + F_P3) == 3) { st64(rW, lane == 0 ? WS_F2 + (u32)(hg * 16 + tt) * 8u : OOB, tag); GSTAMP(3); }
        if (hw == 0) {
          G2(2);
          flags_wait<1>(rW, WS_F2 + (u32)hg * 128u, 16, tag);
          G2(3);
          const int it = tt * 53 + lane;
          const bool ok = lane < 53 && it < 840;
          const u32 vo = ok ? (u32)it * 16u : OOB, pb = WS_KVP + (u32)hg * 16u * 13440u;
          f32x4 a = __builtin_bit_cast(f32x4, gl128(rW, vo, pb));
#pragma unroll
          for (int p = 1; p < 16; ++p) a += __builtin_bit_cast(f32x4, gl128(rW, vo, pb + p * 13440));
          st64(rW, ok ? WS_KVT + (u32)hg * 6720u + (u32)it * 8u : OOB, (u32x2){pk2(a[0], a[1]), pk2(a[2], a[3])});
          G2(4);
          DRAIN();
          G2(5);
          st64(rW, lane == 0 ? WS_F3 + (u32)(hg * 16 + tt) * 8u : OOB, tag);
          GSTAMP(4);
          lds_arrive(lds + F_P3);
        }
      } else {
#pragma unroll
        for (int t4 = 0; t4 < 4; ++t4) {
          ldsp pk = lds + K1_PHQ + (u32)(4 * hw + t4) * 2560u;
          p0[t4] = ld128(pk + lane * 16); p1[t4] = ld128(pk + 1024 + lane * 16); p2[t4] = ld128(pk + 2048 + r32 * 16);
          gt[t4] = gl32(rW, r32 * 4, WS_GATE + (u32)(128 * tt + 32 * t4) * 4u);
          PIN(p0[t4]); PIN(p1[t4]); PIN(p2[t4]);
        }
        lds_arrive(lds + F_P3);
        if (hw == 0) { lds_wait_ge<4>(lds + F_QOK, 8); lds_arrive(lds + F_P3); }
      }
      if (w < 11) {
        lds_wait_ge<4>(lds + F_P3, 5);
        image(awo);
        lds_arrive(lds + F_AOK);
        if (hw == 1) {
          const u32 qi = (u32)jb * 200u;
          u32 dummy;
          asm volatile("s_nop 4\n\tbuffer_load_dword %0, %1, %2, 0 offen\n\tbuffer_load_dword %0, %1, %2, %4 offen\n\tbuffer_load_dword %0, %1, %2, %5 offen\n\t"
                       "buffer_load_dword %0, %3, %2, 0 offen\n\ts_waitcnt vmcnt(0)"
                       : "=&v"(dummy) : "v"((qi + lane) * 128u), "s"(mkrsrc(bwo, DM * DM * 2)), "v"(lane < 8 ? (qi + 192u + lane) * 128u : OOB), "s"(8192u), "s"(16384u) : "memory");
        }
      }
      if (type == 0) {
        if (hw == 0) { flags_wait<1>(rW, WS_F3 + (u32)hg * 128u, 16, tag); lds_st(lds + F_GBAR, 1u); }
        if (w == 11) stream(F_GBAR, 1u);
        if (hw >= 2) G2(2);
        lds_wait_ge<2>(lds + F_GBAR, 1);
        if (hw >= 2) G2(3);
        const u32 ao = (u32)(r32 < 20 ? r32 : 20) * 80u + (u32)hh * 16u, ab = WS_KVT + (u32)(4 * hg + hw) * 1680u;
        const u32x4 a0 = gl128(rW, ao, ab), a1 = gl128(rW, ao, ab + 32), a2 = gl128(rW, hh ? OOB : ao, ab + 64);
        const u32 vo0 = WS_OFR + (u32)(8 * tt + (r32 >> 4)) * 20480u + (r32 & 15) * 16 + (u32)(2 * hg + (hw >> 1)) * 1280u;
        const u32 vo1 = vo0 + hh * 256 + (hw & 1) * 512, vo2 = hh ? OOB : vo0 + 1024 + (hw & 1) * 8;
        f32x16 N[4];
#pragma unroll
        for (int t4 = 0; t4 < 4; ++t4) N[t4] = mfma32(a0, p0[t4], zero16);
#pragma unroll
        for (int t4 = 0; t4 < 4; ++t4) N[t4] = mfma32(a1, p1[t4], N[t4]);
#pragma unroll
        for (int t4 = 0; t4 < 4; ++t4) N[t4] = mfma32(a2, p2[t4], N[t4]);
#pragma unroll
        for (int t4 = 0; t4 < 4; ++t4) {
          const float sa = __uint_as_float(gt[t4]) * __builtin_amdgcn_rcpf(N[t4][12] + 1e-6f);
          st128(rW, vo1 + t4 * 40960, (u32x4){pk2(N[t4][0] * sa, N[t4][1] * sa), pk2(N[t4][2] * sa, N[t4][3] * sa), pk2(N[t4][4] * sa, N[t4][5] * sa), pk2(N[t4][6] * sa, N[t4][7] * sa)});
          st64(rW, vo2 + t4 * 40960, (u32x2){pk2(N[t4][8] * sa, N[t4][9] * sa), pk2(N[t4][10] * sa, N[t4][11] * sa)});
        }
        if (hw >= 2) G2(5);
        DRAIN();
        if (hw >= 2) G2(6);
        if (lds_arrive(lds + F_APUB) == 3) { st64(rW, lane == 0 ? WS_FLGA + (u32)(tt * 16 + hg) * 8u : OOB, tag); GSTAMP(5); }
      }
      if (hw == 0) { flags_wait<1>(rW, WS_FLGA + (u32)(t64 >> 1) * 128u, 8, tag); lds_arrive(lds + F_AOK); }
      HSTAMP(6);
    }
    f32x16 ac[5] = {zero16, zero16, zero16, zero16, zero16};
#pragma unroll 1
    for (int part = 0; part < 2; ++part) {
      if (part == 0) {
        if (w == 11) stream(F_AOK, 4u);
        lds_wait_ge<2>(lds + F_AOK, 4u); GSTAMP(6);
      } else {
        lds_arrive(lds + F_AFREE);
        if (w == 11) stream(F_AOK, 0xffffffffu);
        else { lds_wait_ge<2>(lds + F_AFREE, 4u); image(bwo); lds_arrive(lds + F_BIMG); GSTAMP(2); }
        lds_wait_ge<4>(lds + F_FIN, FINW);
        lds_wait_ge<2>(lds + F_BIMG, 3u);
        TSTAMP(3); FSTAMP(7);
      }
      const int ln = lane_id();
      const u32 aw = lds0 + L_IMG + (u32)(4 * (ln >> 4) + ((ln & 15) >> 2)) * 160u + (u32)(ln & 3) * 40u + (u32)kh * 25600u;
      const u64 tg = (u64)(u32)__builtin_amdgcn_readfirstlane(tag[0]) | ((u64)(u32)__builtin_amdgcn_readfirstlane(tag[1]) << 32);
      u32 t_, r_, want_, it_;
      asm volatile(TILE_GEMM_ASM
                   : "+{v[0:15]}"(ac[0]), "+{v[16:31]}"(ac[1]), "+{v[32:47]}"(ac[2]), "+{v[48:63]}"(ac[3]), "+{v[64:79]}"(ac[4]),
                     [t] "=&s"(t_), [r] "=&s"(r_), [want] "=&s"(want_), [it] "=&s"(it_)
                   : "{v140}"((u32)ln * 16u), "{v141}"(aw), "{v142}"(aw + 51200u), "{v143}"((u32)((ln >> 3) & 1) * 128u + (u32)(ln & 7) * 8u),
                     [rs] "s"(rsWS), [s0] "s"(WS_OFR + (u32)(part * 2621440 + (4 * t64 + 2 * (hw & 1)) * 20480 + kh * 5120)), [poll] "s"(part),
                     [fb] "s"(WS_FLGB + (u32)t64 * 512u + (u32)(hw & 1) * 256u + (u32)kh * 64u), [tag] "s"(tg)
                   : "memory", "scc", "vcc", TILE_GEMM_CLOB);
    }
    TSTAMP(5);
    {
      const int ln = lane_id();
      f32x4 acc[5][2];
#pragma unroll
      for (int T = 0; T < 5; ++T)
#pragma unroll
        for (int n = 0; n < 2; ++n)
#pragma unroll
          for (int i = 0; i < 4; ++i) { const int ra = 8 * T + 4 * n + i, rb = 40 + ra; acc[T][n][i] = ac[ra >> 4][ra & 15] + ac[rb >> 4][rb & 15]; }
      if (FINW < 8u) lds_wait_ge<1>(lds + F_FIN, 8u);
      ldsp xb = lds + L_RING + 3 * SL;
      ldsp xw = xb + (u32)hw * 5120u + ln * 16, xr = xb + (u32)(hw ^ 2) * 5120u + ln * 16;
#pragma unroll
      for (int T = 0; T < 5; ++T) *(LDSAS f32x4*)(xw + T * 1024) = kh ? acc[T][0] : acc[T][1];
      asm volatile("" ::: "memory");
      lds_st(lds + F_XF + hw * 4, 1u);
      f32x4 pv[5];
#pragma unroll 1
      for (u32 it = 0; it < SPIN_CAP; ++it) {
        asm volatile("" ::: "memory");
        const u32 f = lds_ld(lds + F_XF + (hw ^ 2) * 4);
        asm volatile("" ::: "memory");
#pragma unroll
        for (int T = 0; T < 5; ++T) pv[T] = *(LDSAS f32x4*)(xr + T * 1024);
        if (__builtin_amdgcn_readfirstlane(f)) break;
      }
      TSTAMP(6);
      u32 o[10];
#pragma unroll
      for (int T = 0; T < 5; ++T) {
        const f32x4 v = (kh ? acc[T][1] : acc[T][0]) + pv[T];
        o[2 * T] = pk2(v[0], v[1]); o[2 * T + 1] = pk2(v[2], v[3]);
      }
      const rsrc_t rOut = __builtin_amdgcn_make_buffer_rsrc(out, 0, SEQ * DM * 2, 0x00020000);
      const int dst = (64 * t64 + 32 * (hw & 1) + 16 * kh + (ln & 15)) * 1280 + cgp * 160 + (ln >> 4) * 40;
      __builtin_amdgcn_raw_buffer_store_b128((u32x4){o[0], o[1], o[2], o[3]}, rOut, dst, 0, OUT_AUX);
      __builtin_amdgcn_raw_buffer_store_b128((u32x4){o[4], o[5], o[6], o[7]}, rOut, dst + 16, 0, OUT_AUX);
      __builtin_amdgcn_raw_buffer_store_b64((u32x2){o[8], o[9]}, rOut, dst + 32, 0, OUT_AUX);
    }
    STAMP_FLUSH();
    return;
  }

  const int pr = w & 3, par = w >> 2, head = 4 * hg + pr, t64 = 2 * tt + type;
  u32x4 q1[2]; u32x2 q2[2];
  f32x16 O0, O1;
  const int lane = lane_id(), r32 = lane & 31, hh = lane >> 5;
  float og; u32x2 tag; u32 vo1, vo2, done;
  __builtin_amdgcn_s_setprio(3);
  {
    if (type) {
      lds_wait_ge<1>(lds + F_QOK, 1);
#pragma unroll
      for (int j = 0; j < 2; ++j) {
        const u32 qrow = WS_QB + (u32)(tt * 8 + hg) * 10240u + (u32)(2 * pr + (j ^ par)) * 1280u;
        q1[j] = gl128(rW, lane * 16, qrow);
        q2[j] = (u32x2){gl32(rW, r32 * 8 + hh * 4, qrow + 1024), 0u};
      }
    } else {
#pragma unroll
      for (int j = 0; j < 2; ++j) {
        ldsp qrow = lds + K1_QST + (u32)(2 * pr + (j ^ par)) * 1280u;
        q1[j] = ld128(qrow + lane * 16);
        q2[j] = (u32x2){*(LDSAS u32*)(qrow + 1024 + r32 * 8 + hh * 4), 0u};
      }
      PIN(q1[0]); PIN(q1[1]); PIN(q2[0]); PIN(q2[1]);
      lds_arrive(lds + F_QOK);
    }
    const u32x4 k1a = ld128(lds + L_K01 + lane * 16);
    const u32 k2a = *(LDSAS u32*)(lds + L_K01 + 1024 + lane * 4);
    u32x4 k1 = ld128(lds + L_K01 + par * 1280 + lane * 16);
    u32x2 k2 = {*(LDSAS u32*)(lds + L_K01 + par * 1280 + 1024 + lane * 4), hh ? 0u : 0x00003F80u};
    f32x16 S0, S1, S2, S3;
#pragma unroll
    for (int j = 0; j < 2; ++j) {
      f32x16 t = mfma32(k1a, q1[j], zero16);
      t = mfma32k8((u32x2){k2a, 0u}, q2[j], t);
      const float m = xhalf_max(max16(t));
      q2[j][1] = hh ? 0u : (pk2(-(m + 60.f), 0.f) & 0xffffu);
    }
    S0 = mfma32(k1, q1[0], zero16);
    S0 = mfma32k8(k2, q2[0], S0);
    u32x4 vlo, vhi;
    const u32 rb = lds0 + L_RING + (u32)par * REC;
    u32 aK1 = rb + (u32)lane * 16u, aK2 = rb + 1024u + (u32)lane * 4u;
    u32 aV = rb + 1280u + (u32)hh * 352u + (u32)(r32 <= 20 ? r32 : 21) * 16u;
    u32 aF = lds0 + L_FLG + (u32)w * 4u, tmp;
    og = 1.f - *(LDSAS float*)(lds + L_GT + (32 * par + r32) * 4);
    tag = ld64(lds + F_TAG);
    const u32 vo = WS_OFR + 2621440u + (u32)(4 * t64 + 2 * par + (r32 >> 4)) * 20480u + (r32 & 15) * 16 + (head >> 1) * 1280;
    vo1 = vo + hh * 256 + (head & 1) * 512; vo2 = hh ? OOB : vo + 1024 + (head & 1) * 8;
    done = lds0 + L_FLG + (u32)(lane & 7) * 4u;
    PIN(og); PIN(tag); PIN(vo1); PIN(vo2);
    HSTAMP(5); FSTAMP(5);
    u32 cnt, inc, slot, rdy;
    asm volatile(ATTN_LOOP_ASM
                 : "=&{v[0:15]}"(O0), "=&{v[16:31]}"(O1), "+{v[32:47]}"(S0), "=&{v[48:63]}"(S1), "=&{v[64:79]}"(S2), "=&{v[80:95]}"(S3),
                   "+{v[108:111]}"(k1), "+{v[112:113]}"(k2), "=&{v[116:119]}"(vlo), "=&{v[120:123]}"(vhi),
                   "+{v124}"(aK1), "+{v125}"(aK2), "+{v126}"(aV), "=&{v115}"(tmp), [dn] "+v"(done), [cnt] "=&s"(cnt), [inc] "=&s"(inc), [slot] "=&s"(slot), [rdy] "=&s"(rdy)
                 : "{v[96:99]}"(q1[0]), "{v[100:103]}"(q1[1]), "{v[104:105]}"(q2[0]), "{v[106:107]}"(q2[1]), "{v114}"(0xDDDD), "{v127}"(aF),
                   [wrap] "s"(0u - 4u * SL), [stg] "s"(SL), [par] "s"(par)
                 : "memory", "scc");
  }
  HSTAMP(6); TSTAMP(2); FSTAMP(6);
  if (__builtin_expect(!__all(done >= 13u), 0)) {
#pragma unroll 1
    for (u32 it = 0; it < SPIN_CAP; ++it) {
      if (__all(lds_ld(lds + L_FLG + (lane & 7) * 4) >= 13u)) break;
      __builtin_amdgcn_s_sleep(2);
    }
  }
  asm volatile("" ::: "memory");
  {
    ldsp mw = lds + L_MRG + (u32)w * REC;
    *(LDSAS f32x4*)(mw + lane * 16) = (f32x4){O1[0], O1[1], O1[2], O1[3]};
    *(LDSAS f32x4*)(mw + 1024 + lane * 16) = (f32x4){O1[4], O1[5], O1[6], O1[7]};
    if (!hh) *(LDSAS f32x4*)(mw + 2048 + r32 * 16) = (f32x4){O1[8], O1[9], O1[10], O1[11]};
    else *(LDSAS float*)(mw + 2560 + r32 * 4) = O1[8];
    asm volatile("" ::: "memory");
    lds_st(lds + L_FLG + 64 + w * 4, 1u);
  }
  TSTAMP(3);
  f32x16 O = O0;
  float l = from_hi(O[8]);
  {
    ldsp mr = lds + L_MRG + (u32)(w ^ 4) * REC;
    f32x4 a, b, c; float lp;
#pragma unroll 1
    for (u32 it = 0; it < SPIN_CAP; ++it) {
      asm volatile("" ::: "memory");
      const u32 f = lds_ld(lds + L_FLG + 64 + (w ^ 4) * 4);
      asm volatile("" ::: "memory");
      a = *(LDSAS f32x4*)(mr + lane * 16); b = *(LDSAS f32x4*)(mr + 1024 + lane * 16); c = *(LDSAS f32x4*)(mr + 2048 + r32 * 16);
      lp = *(LDSAS float*)(mr + 2560 + r32 * 4);
      if (__builtin_amdgcn_readfirstlane(f)) break;
      __builtin_amdgcn_s_sleep(FSLEEP);
    }
    TSTAMP(5);
    l += lp;
#pragma unroll
    for (int i = 0; i < 4; ++i) { O[i] += a[i]; O[4 + i] += b[i]; O[8 + i] += c[i]; }
  }
  float sb = og * __builtin_amdgcn_rcpf(l);
  u32x4 r1; u32x2 r2;
#pragma unroll 1
  for (int pass = 0; pass < 2; ++pass) {
    r1 = (u32x4){pk2(O[0] * sb, O[1] * sb), pk2(O[2] * sb, O[3] * sb), pk2(O[4] * sb, O[5] * sb), pk2(O[6] * sb, O[7] * sb)};
    r2 = (u32x2){pk2(O[8] * sb, O[9] * sb), pk2(O[10] * sb, O[11] * sb)};
    const u32 e = 0x7f807f80u;
    const u32 m0 = (r1[0] & e) + 0x00800080u, m1 = (r1[1] & e) + 0x00800080u, m2 = (r1[2] & e) + 0x00800080u, m3 = (r1[3] & e) + 0x00800080u;
    const u32 m4 = (r2[0] & e) + 0x00800080u, m5 = (r2[1] & e) + 0x00800080u;
    const bool bad = (((m0 | m1 | m2 | m3 | m4 | m5) & 0x80008000u) != 0u) || !(l < 1.0e37f);
    if (__builtin_expect(pass == 1 || !__any(bad), 1)) break;
    O = attn_exact(rW, q1[0], q2[0][0], lane);
    l = from_hi(O[8]);
    sb = og * __builtin_amdgcn_rcpf(l);
  }
  st128(rW, vo1, r1);
  st64(rW, vo2, r2);
  HSTAMP(7); TSTAMP(6);
  DRAIN();
  TSTAMP(7); FSTAMP(7);
  st64(rW, lane == 0 ? WS_FLGB + (u32)((t64 * 2 + par) * 32 + head) * 8u : OOB, tag);
  lds_arrive(lds + F_FIN);
  STAMP_FLUSH();
}

extern "C" void kernel_launch(void* const* d_in, const int* in_sizes, int n_in, void* d_out, int out_size, void* d_ws,
                              size_t ws_size, hipStream_t stream) {
  (void)in_sizes; (void)n_in; (void)out_size; (void)ws_size;
  (void)hipFuncSetAttribute((const void*)gm_fused, hipFuncAttributeMaxDynamicSharedMemorySize, LDS_BYTES);
  gm_fused<<<256, 768, LDS_BYTES, stream>>>((const char*)d_in[0], (const char*)d_in[1], (const char*)d_in[2], (const char*)d_in[5], (const char*)d_in[6],
      (const char*)d_in[7], (const char*)d_in[9], (const char*)d_in[3], (const char*)d_in[8], (char*)d_ws, (char*)d_out);
}
```

```cpp
#include <hip/hip_runtime.h>
#include <hip/hip_bf16.h>
#include <stdint.h>

typedef unsigned int u32;
typedef unsigned long long u64;
typedef u32 u32x2 __attribute__((ext_vector_type(2)));
typedef u32 u32x4 __attribute__((ext_vector_type(4)));
typedef u32 u32x16 __attribute__((ext_vector_type(16)));
typedef float f32x2 __attribute__((ext_vector_type(2)));
typedef float f32x4 __attribute__((ext_vector_type(4)));
typedef float f32x8 __attribute__((ext_vector_type(8)));
typedef float f32x16 __attribute__((ext_vector_type(16)));
typedef __bf16 bf16x2 __attribute__((ext_vector_type(2)));
typedef __bf16 bf16x8 __attribute__((ext_vector_type(8)));
typedef __bf16 bf16x16 __attribute__((ext_vector_type(16)));
typedef short s16x4 __attribute__((ext_vector_type(4)));
#define LDSAS __attribute__((address_space(3)))
typedef LDSAS char* ldsp;
typedef __attribute__((address_space(1))) u64 gu64;

constexpr int SEQ = 2048, DM = 640, NH = 32, DH = 20;
constexpr float LOG2E = 1.4426950408889634f;
constexpr float QSCALE = 1.4426950408889634f * 0.22360679774997896f;

constexpr u32 REC = 2688;
constexpr u32 WS_QB   = 0;
constexpr u32 WS_KVIM = WS_QB + 1310720;
constexpr u32 WS_KVP  = WS_KVIM + 64 * REC;
constexpr u32 WS_KVT  = WS_KVP + 32 * 16 * 3360;
constexpr u32 WS_GATE = WS_KVT + 32 * 1680;
constexpr u32 WS_OFR  = WS_GATE + 8192;
constexpr u32 WS_F1   = WS_OFR + 5242880;
constexpr u32 WS_F2   = WS_F1 + 16384;
constexpr u32 WS_F3   = WS_F2 + 1024;
constexpr u32 WS_FLGA = WS_F3 + 1024;
constexpr u32 WS_FLGB = WS_FLGA + 2048;
constexpr u32 WS_FQ   = WS_FLGB + 16384;
constexpr u32 WS_EPOCH = WS_FQ + 16384;
constexpr u32 WS_END  = WS_EPOCH + 256;
constexpr u32 OOB = 0x40000000u;
constexpr u32 SPIN_CAP = 1u << 20;

#define SBAR() __builtin_amdgcn_sched_barrier(0)
#define PIN(x) asm volatile("" : "+v"(x))
#define LBAR() asm volatile("s_waitcnt lgkmcnt(0)\n\ts_barrier" ::: "memory")
#define WAIT_BAR(N) asm volatile("s_waitcnt vmcnt(" #N ") lgkmcnt(0)\n\ts_barrier" ::: "memory")
#define DRAIN() asm volatile("s_waitcnt vmcnt(0)" ::: "memory")

__device__ __forceinline__ int lane_id() {
  int l;
  asm volatile("v_mbcnt_lo_u32_b32 %0, -1, 0\n\tv_mbcnt_hi_u32_b32 %0, -1, %0" : "=v"(l));
  return l;
}
__device__ __forceinline__ u32 pk2(float a, float b) {
  f32x2 v = {a, b};
  return __builtin_bit_cast(u32, __builtin_convertvector(v, bf16x2));
}
__device__ __forceinline__ f32x4 mfma16(u32x4 a, u32x4 b, f32x4 c) {
  return __builtin_amdgcn_mfma_f32_16x16x32_bf16(__builtin_bit_cast(bf16x8, a), __builtin_bit_cast(bf16x8, b), c, 0, 0, 0);
}
__device__ __forceinline__ f32x16 mfma32(u32x4 a, u32x4 b, f32x16 c) {
  return __builtin_amdgcn_mfma_f32_32x32x16_bf16(__builtin_bit_cast(bf16x8, a), __builtin_bit_cast(bf16x8, b), c, 0, 0, 0);
}
__device__ __forceinline__ f32x16 mfma32k8(u32x2 a, u32x2 b, f32x16 c) {
  return __builtin_amdgcn_mfma_f32_32x32x8bf16_1k(__builtin_bit_cast(s16x4, a), __builtin_bit_cast(s16x4, b), c, 0, 0, 0);
}
__device__ __forceinline__ f32x16 smf(u32x4 a, f32x8 b, f32x16 c, int idx) {
  return __builtin_amdgcn_smfmac_f32_32x32x32_bf16(__builtin_bit_cast(bf16x8, a), __builtin_bit_cast(bf16x16, b), c, idx, 0, 0);
}
__device__ __forceinline__ u32x2 trd(ldsp p) {
  return __builtin_bit_cast(u32x2, __builtin_amdgcn_ds_read_tr16_b64_v4i16((LDSAS s16x4*)p));
}
__device__ __forceinline__ u32x4 trd2(ldsp p0, ldsp p1) {
  u32x2 a = trd(p0), b = trd(p1);
  return (u32x4){a[0], a[1], b[0], b[1]};
}
__device__ __forceinline__ u32x4 ld128(ldsp p) { return *(LDSAS u32x4*)p; }
__device__ __forceinline__ u32x2 ld64(ldsp p) { return *(LDSAS u32x2*)p; }
__device__ __forceinline__ float bflo(u32 v) { return __uint_as_float(v << 16); }
__device__ __forceinline__ float bfhi(u32 v) { return __uint_as_float(v & 0xffff0000u); }

__device__ __forceinline__ u32x4 mkrsrc(const void* p, u32 bytes) {
  uint64_t a = (uint64_t)p;
  return (u32x4){(u32)__builtin_amdgcn_readfirstlane((u32)a), (u32)__builtin_amdgcn_readfirstlane((u32)(a >> 32) & 0xffffu), bytes, 0x00020000u};
}
__device__ __forceinline__ void dma1(u32x4 rs, u32 voff, u32 soff, u32 ldsaddr) {
  u32 keep;
  asm volatile("s_mov_b32 %0, m0\n\ts_mov_b32 m0, %4\n\ts_nop 2\n\tbuffer_load_dwordx4 %1, %2, %3 offen lds\n\ts_mov_b32 m0, %0"
               : "=&s"(keep) : "v"(voff), "s"(rs), "s"(__builtin_amdgcn_readfirstlane(soff)), "s"(__builtin_amdgcn_readfirstlane(ldsaddr)) : "memory");
}
__device__ __forceinline__ void dma1s(u32x4 rs, u32 voff, u32 soff, u32 ldsaddr) {
  u32 keep;
  asm volatile("s_mov_b32 %0, m0\n\ts_mov_b32 m0, %4\n\ts_nop 2\n\tbuffer_load_dwordx4 %1, %2, %3 offen sc1 lds\n\ts_mov_b32 m0, %0"
               : "=&s"(keep) : "v"(voff), "s"(rs), "s"(__builtin_amdgcn_readfirstlane(soff)), "s"(__builtin_amdgcn_readfirstlane(ldsaddr)) : "memory");
}
__device__ __forceinline__ void dma4(u32x4 rs, u32 voff, u32 soff, u32 ldsaddr) {
  u32 keep;
  asm volatile("s_mov_b32 %0, m0\n\ts_mov_b32 m0, %4\n\ts_nop 2\n\t"
               "buffer_load_dwordx4 %1, %2, %3 offen sc1 lds\n\tbuffer_load_dwordx4 %1, %2, %3 offen offset:1024 sc1 lds\n\t"
               "buffer_load_dwordx4 %1, %2, %3 offen offset:2048 sc1 lds\n\tbuffer_load_dwordx4 %1, %2, %3 offen offset:3072 sc1 lds\n\t"
               "s_mov_b32 m0, %0"
               : "=&s"(keep) : "v"(voff), "s"(rs), "s"(__builtin_amdgcn_readfirstlane(soff)), "s"(__builtin_amdgcn_readfirstlane(ldsaddr)) : "memory");
}
__device__ __forceinline__ void dma2h(u32x4 rs, u32 voff, u32 soff, u32 ldsaddr) {
  u32 keep;
  asm volatile("s_mov_b32 %0, m0\n\ts_mov_b32 m0, %4\n\ts_nop 2\n\t"
               "buffer_load_dwordx4 %1, %2, %3 offen sc1 lds\n\tbuffer_load_dwordx4 %1, %2, %3 offen offset:1024 sc1 lds\n\t"
               "s_mov_b32 exec_hi, 0\n\tbuffer_load_dwordx4 %1, %2, %3 offen offset:2048 sc1 lds\n\ts_mov_b32 exec_hi, -1\n\t"
               "s_mov_b32 m0, %0"
               : "=&s"(keep) : "v"(voff), "s"(rs), "s"(__builtin_amdgcn_readfirstlane(soff)), "s"(__builtin_amdgcn_readfirstlane(ldsaddr)) : "memory");
}
typedef __amdgpu_buffer_rsrc_t rsrc_t;
__device__ __forceinline__ void st128(rsrc_t r, u32 off, u32x4 v) { __builtin_amdgcn_raw_buffer_store_b128(v, r, (int)off, 0, 16); }
__device__ __forceinline__ void st64(rsrc_t r, u32 off, u32x2 v) { __builtin_amdgcn_raw_buffer_store_b64(v, r, (int)off, 0, 16); }
__device__ __forceinline__ void st32(rsrc_t r, u32 off, u32 v) { __builtin_amdgcn_raw_buffer_store_b32(v, r, (int)off, 0, 16); }
__device__ __forceinline__ void st16(rsrc_t r, u32 off, u32 v) { __builtin_amdgcn_raw_buffer_store_b16((unsigned short)v, r, (int)off, 0, 16); }
__device__ __forceinline__ u32x4 gl128(rsrc_t r, u32 voff, u32 soff) { return __builtin_amdgcn_raw_buffer_load_b128(r, (int)voff, (int)soff, 16); }
__device__ __forceinline__ u32x2 gl64(rsrc_t r, u32 voff, u32 soff) { return __builtin_amdgcn_raw_buffer_load_b64(r, (int)voff, (int)soff, 16); }
__device__ __forceinline__ u32 gl32(rsrc_t r, u32 voff, u32 soff) { return __builtin_amdgcn_raw_buffer_load_b32(r, (int)voff, (int)soff, 16); }

__device__ __forceinline__ f32x8 lo8(const f32x16& s) { return __builtin_shufflevector(s, s, 0, 1, 2, 3, 4, 5, 6, 7); }
__device__ __forceinline__ f32x8 hi8(const f32x16& s) { return __builtin_shufflevector(s, s, 8, 9, 10, 11, 12, 13, 14, 15); }
__device__ __forceinline__ float max16(const f32x16& s) {
  float a = fmaxf(fmaxf(s[0], s[1]), s[2]), b = fmaxf(fmaxf(s[3], s[4]), s[5]);
  a = fmaxf(fmaxf(a, s[6]), s[7]); b = fmaxf(fmaxf(b, s[8]), s[9]);
  a = fmaxf(fmaxf(a, s[10]), s[11]); b = fmaxf(fmaxf(b, s[12]), s[13]);
  a = fmaxf(fmaxf(a, s[14]), s[15]);
  return fmaxf(a, b);
}
__device__ __forceinline__ float xhalf_max(float m) {
  auto r = __builtin_amdgcn_permlane32_swap(__float_as_uint(m), __float_as_uint(m), false, false);
  return fmaxf(__uint_as_float(r[0]), __uint_as_float(r[1]));
}
__device__ __forceinline__ float from_hi(float v) {
  auto r = __builtin_amdgcn_permlane32_swap(__float_as_uint(v), __float_as_uint(v), false, false);
  return __uint_as_float(r[1]);
}
__device__ __forceinline__ u32 lds_ld(ldsp p) { return __hip_atomic_load((LDSAS u32*)p, __ATOMIC_RELAXED, __HIP_MEMORY_SCOPE_WORKGROUP); }
__device__ __forceinline__ void lds_st(ldsp p, u32 v) { __hip_atomic_store((LDSAS u32*)p, v, __ATOMIC_RELAXED, __HIP_MEMORY_SCOPE_WORKGROUP); }
template <int SLEEP>
__device__ __forceinline__ void lds_wait_ge(ldsp p, u32 want) {
#pragma unroll 1
  for (u32 it = 0; it < SPIN_CAP; ++it) {
    if ((u32)__builtin_amdgcn_readfirstlane(lds_ld(p)) >= want) break;
    __builtin_amdgcn_s_sleep(SLEEP);
  }
  asm volatile("" ::: "memory");
}
__device__ __forceinline__ u32 lds_arrive(ldsp p) {
  u32 old; u64 keep;
  asm volatile("v_mov_b32 %0, 1\n\ts_mov_b64 %1, exec\n\ts_mov_b64 exec, 1\n\ts_waitcnt lgkmcnt(0)\n\tds_add_rtn_u32 %0, %2, %0\n\t"
               "s_waitcnt lgkmcnt(0)\n\ts_mov_b64 exec, %1" : "=&v"(old), "=&s"(keep) : "v"((u32)(uintptr_t)p) : "memory");
  return (u32)__builtin_amdgcn_readfirstlane(old);
}
template <int SLEEP>
__device__ __forceinline__ void flags_wait(rsrc_t r, u32 off, int n, u32x2 tag) {
  const int lane = lane_id();
  const u32 vo = (u32)(lane < n ? lane : 0) * 8u;
#pragma unroll 1
  for (u32 it = 0; it < SPIN_CAP; ++it) {
    asm volatile("" ::: "memory");
    const u32x2 v = gl64(r, vo, off);
    if (__all(v[0] == tag[0] && v[1] == tag[1])) break;
    __builtin_amdgcn_s_sleep(SLEEP);
  }
  asm volatile("" ::: "memory");
}
template <int SLEEP>
__device__ __forceinline__ void flags_wait256(rsrc_t r, u32 off, u32x2 tag) {
  const u32 vo = (u32)lane_id() * 16u;
#pragma unroll 1
  for (u32 it = 0; it < SPIN_CAP; ++it) {
    asm volatile("" ::: "memory");
    const u32x4 a = gl128(r, vo, off), b = gl128(r, vo, off + 1024);
    const u32 d = (a[0] ^ tag[0]) | (a[1] ^ tag[1]) | (a[2] ^ tag[0]) | (a[3] ^ tag[1]) | (b[0] ^ tag[0]) | (b[1] ^ tag[1]) | (b[2] ^ tag[0]) | (b[3] ^ tag[1]);
    if (__all(d == 0u)) break;
    __builtin_amdgcn_s_sleep(SLEEP);
  }
  asm volatile("" ::: "memory");
}

#define STAMP(i) do {} while (0)
#define RSTAMP(i) do {} while (0)
#define STAMP_INIT()
#define STAMP_FLUSH()

#define ATTN_LOOP_ASM \
  "s_nop 11\n\t" \
  "v_mov_b64 v[0:1], 0\n\t" \
  "v_mov_b64 v[2:3], 0\n\t" \
  "v_mov_b64 v[4:5], 0\n\t" \
  "v_mov_b64 v[6:7], 0\n\t" \
  "v_mov_b64 v[8:9], 0\n\t" \
  "v_mov_b64 v[10:11], 0\n\t" \
  "v_mov_b64 v[12:13], 0\n\t" \
  "v_mov_b64 v[14:15], 0\n\t" \
  "v_mov_b64 v[16:17], 0\n\t" \
  "v_mov_b64 v[18:19], 0\n\t" \
  "v_mov_b64 v[20:21], 0\n\t" \
  "v_mov_b64 v[22:23], 0\n\t" \
  "v_mov_b64 v[24:25], 0\n\t" \
  "v_mov_b64 v[26:27], 0\n\t" \
  "v_mov_b64 v[28:29], 0\n\t" \
  "v_mov_b64 v[30:31], 0\n\t" \
  "v_mov_b64 v[80:81], 0\n\t" \
  "v_mov_b64 v[82:83], 0\n\t" \
  "v_mov_b64 v[84:85], 0\n\t" \
  "v_mov_b64 v[86:87], 0\n\t" \
  "v_mov_b64 v[88:89], 0\n\t" \
  "v_mov_b64 v[90:91], 0\n\t" \
  "v_mov_b64 v[92:93], 0\n\t" \
  "v_mov_b64 v[94:95], 0\n\t" \
  "v_mov_b64 v[116:117], 0\n\t" \
  "v_mov_b64 v[118:119], 0\n\t" \
  "v_mov_b64 v[120:121], 0\n\t" \
  "v_mov_b64 v[122:123], 0\n\t" \
  "s_mov_b32 %[cnt], 0\n\t" \
  "s_mov_b32 %[slot], 0\n\t" \
  "s_setprio 0\n\t" \
  "2:\n\t" \
  "s_mov_b32 %[inc], 0\n\t" \
  "3:\n\t" \
  "ds_read_b32 v115, v127 offset:32\n\t" \
  "s_add_u32 %[inc], %[inc], 1\n\t" \
  "s_waitcnt lgkmcnt(0)\n\t" \
  "v_readfirstlane_b32 %[rdy], v115\n\t" \
  "s_nop 3\n\t" \
  "s_cmp_gt_u32 %[rdy], %[cnt]\n\t" \
  "s_cbranch_scc1 4f\n\t" \
  "s_sleep 1\n\t" \
  "s_cmp_lt_u32 %[inc], 0x100000\n\t" \
  "s_cbranch_scc1 3b\n\t" \
  "s_branch 9f\n\t" \
  "4:\n\t" \
  "v_mfma_f32_32x32x16_bf16 v[48:63], v[108:111], v[100:103], 0\n\t" \
  "ds_read_b128 v[108:111], v124 offset:0\n\t" \
  "v_exp_f32 v32, v32\n\t" \
  "v_exp_f32 v33, v33\n\t" \
  "v_exp_f32 v34, v34\n\t" \
  "v_exp_f32 v35, v35\n\t" \
  "v_mfma_f32_32x32x8_bf16 v[48:63], v[112:113], v[106:107], v[48:63]\n\t" \
  "ds_read_b32 v112, v125 offset:0\n\t" \
  "v_exp_f32 v36, v36\n\t" \
  "v_exp_f32 v37, v37\n\t" \
  "v_exp_f32 v38, v38\n\t" \
  "v_exp_f32 v39, v39\n\t" \
  "v_smfmac_f32_32x32x32_bf16 v[16:31], v[116:119], v[80:87], v114\n\t" \
  "ds_read_b128 v[116:119], v126 offset:0\n\t" \
  "v_exp_f32 v40, v40\n\t" \
  "v_exp_f32 v41, v41\n\t" \
  "v_exp_f32 v42, v42\n\t" \
  "v_exp_f32 v43, v43\n\t" \
  "v_smfmac_f32_32x32x32_bf16 v[16:31], v[120:123], v[88:95], v114\n\t" \
  "ds_read_b128 v[120:123], v126 offset:704\n\t" \
  "v_exp_f32 v44, v44\n\t" \
  "v_exp_f32 v45, v45\n\t" \
  "v_exp_f32 v46, v46\n\t" \
  "v_exp_f32 v47, v47\n\t" \
  "s_waitcnt lgkmcnt(2)\n\t" \
  "v_mfma_f32_32x32x16_bf16 v[64:79], v[108:111], v[96:99], 0\n\t" \
  "v_exp_f32 v48, v48\n\t" \
  "v_exp_f32 v49, v49\n\t" \
  "v_exp_f32 v50, v50\n\t" \
  "v_exp_f32 v51, v51\n\t" \
  "v_mfma_f32_32x32x8_bf16 v[64:79], v[112:113], v[104:105], v[64:79]\n\t" \
  "v_exp_f32 v52, v52\n\t" \
  "v_exp_f32 v53, v53\n\t" \
  "v_exp_f32 v54, v54\n\t" \
  "v_exp_f32 v55, v55\n\t" \
  "s_waitcnt lgkmcnt(0)\n\t" \
  "v_smfmac_f32_32x32x32_bf16 v[0:15], v[116:119], v[32:39], v114\n\t" \
  "v_exp_f32 v56, v56\n\t" \
  "v_exp_f32 v57, v57\n\t" \
  "v_exp_f32 v58, v58\n\t" \
  "v_exp_f32 v59, v59\n\t" \
  "v_smfmac_f32_32x32x32_bf16 v[0:15], v[120:123], v[40:47], v114\n\t" \
  "v_exp_f32 v60, v60\n\t" \
  "v_exp_f32 v61, v61\n\t" \
  "v_exp_f32 v62, v62\n\t" \
  "v_exp_f32 v63, v63\n\t" \
  "v_mfma_f32_32x32x16_bf16 v[80:95], v[108:111], v[100:103], 0\n\t" \
  "ds_read_b128 v[108:111], v124 offset:2688\n\t" \
  "v_exp_f32 v64, v64\n\t" \
  "v_exp_f32 v65, v65\n\t" \
  "v_exp_f32 v66, v66\n\t" \
  "v_exp_f32 v67, v67\n\t" \
  "v_mfma_f32_32x32x8_bf16 v[80:95], v[112:113], v[106:107], v[80:95]\n\t" \
  "ds_read_b32 v112, v125 offset:2688\n\t" \
  "v_exp_f32 v68, v68\n\t" \
  "v_exp_f32 v69, v69\n\t" \
  "v_exp_f32 v70, v70\n\t" \
  "v_exp_f32 v71, v71\n\t" \
  "v_smfmac_f32_32x32x32_bf16 v[16:31], v[116:119], v[48:55], v114\n\t" \
  "ds_read_b128 v[116:119], v126 offset:2688\n\t" \
  "v_exp_f32 v72, v72\n\t" \
  "v_exp_f32 v73, v73\n\t" \
  "v_exp_f32 v74, v74\n\t" \
  "v_exp_f32 v75, v75\n\t" \
  "v_smfmac_f32_32x32x32_bf16 v[16:31], v[120:123], v[56:63], v114\n\t" \
  "ds_read_b128 v[120:123], v126 offset:3392\n\t" \
  "v_exp_f32 v76, v76\n\t" \
  "v_exp_f32 v77, v77\n\t" \
  "v_exp_f32 v78, v78\n\t" \
  "v_exp_f32 v79, v79\n\t" \
  "s_waitcnt lgkmcnt(2)\n\t" \
  "v_mfma_f32_32x32x16_bf16 v[32:47], v[108:111], v[96:99], 0\n\t" \
  "v_exp_f32 v80, v80\n\t" \
  "v_exp_f32 v81, v81\n\t" \
  "v_exp_f32 v82, v82\n\t" \
  "v_exp_f32 v83, v83\n\t" \
  "v_mfma_f32_32x32x8_bf16 v[32:47], v[112:113], v[104:105], v[32:47]\n\t" \
  "s_cmp_eq_u32 %[slot], 4\n\t" \
  "s_cselect_b32 %[inc], %[wrap], %[stg]\n\t" \
  "v_exp_f32 v84, v84\n\t" \
  "v_exp_f32 v85, v85\n\t" \
  "v_exp_f32 v86, v86\n\t" \
  "v_exp_f32 v87, v87\n\t" \
  "s_waitcnt lgkmcnt(0)\n\t" \
  "v_smfmac_f32_32x32x32_bf16 v[0:15], v[116:119], v[64:71], v114\n\t" \
  "s_cselect_b32 %[slot], -1, %[slot]\n\t" \
  "s_add_u32 %[slot], %[slot], 1\n\t" \
  "v_exp_f32 v88, v88\n\t" \
  "v_exp_f32 v89, v89\n\t" \
  "v_exp_f32 v90, v90\n\t" \
  "v_exp_f32 v91, v91\n\t" \
  "v_smfmac_f32_32x32x32_bf16 v[0:15], v[120:123], v[72:79], v114\n\t" \
  "v_exp_f32 v92, v92\n\t" \
  "v_exp_f32 v93, v93\n\t" \
  "v_exp_f32 v94, v94\n\t" \
  "v_exp_f32 v95, v95\n\t" \
  "v_mfma_f32_32x32x16_bf16 v[48:63], v[108:111], v[100:103], 0\n\t" \
  "ds_read_b128 v[108:111], v124 offset:5376\n\t" \
  "v_exp_f32 v32, v32\n\t" \
  "v_exp_f32 v33, v33\n\t" \
  "v_exp_f32 v34, v34\n\t" \
  "v_exp_f32 v35, v35\n\t" \
  "v_mfma_f32_32x32x8_bf16 v[48:63], v[112:113], v[106:107], v[48:63]\n\t" \
  "ds_read_b32 v112, v125 offset:5376\n\t" \
  "v_exp_f32 v36, v36\n\t" \
  "v_exp_f32 v37, v37\n\t" \
  "v_exp_f32 v38, v38\n\t" \
  "v_exp_f32 v39, v39\n\t" \
  "v_smfmac_f32_32x32x32_bf16 v[16:31], v[116:119], v[80:87], v114\n\t" \
  "ds_read_b128 v[116:119], v126 offset:5376\n\t" \
  "v_exp_f32 v40, v40\n\t" \
  "v_exp_f32 v41, v41\n\t" \
  "v_exp_f32 v42, v42\n\t" \
  "v_exp_f32 v43, v43\n\t" \
  "v_smfmac_f32_32x32x32_bf16 v[16:31], v[120:123], v[88:95], v114\n\t" \
  "ds_read_b128 v[120:123], v126 offset:6080\n\t" \
  "v_exp_f32 v44, v44\n\t" \
  "v_exp_f32 v45, v45\n\t" \
  "v_exp_f32 v46, v46\n\t" \
  "v_exp_f32 v47, v47\n\t" \
  "s_waitcnt lgkmcnt(2)\n\t" \
  "v_mfma_f32_32x32x16_bf16 v[64:79], v[108:111], v[96:99], 0\n\t" \
  "s_add_u32 %[cnt], %[cnt], 1\n\t" \
  "v_exp_f32 v48, v48\n\t" \
  "v_exp_f32 v49, v49\n\t" \
  "v_exp_f32 v50, v50\n\t" \
  "v_exp_f32 v51, v51\n\t" \
  "v_mfma_f32_32x32x8_bf16 v[64:79], v[112:113], v[104:105], v[64:79]\n\t" \
  "v_exp_f32 v52, v52\n\t" \
  "v_exp_f32 v53, v53\n\t" \
  "v_exp_f32 v54, v54\n\t" \
  "v_exp_f32 v55, v55\n\t" \
  "s_waitcnt lgkmcnt(0)\n\t" \
  "v_smfmac_f32_32x32x32_bf16 v[0:15], v[116:119], v[32:39], v114\n\t" \
  "v_exp_f32 v56, v56\n\t" \
  "v_exp_f32 v57, v57\n\t" \
  "v_exp_f32 v58, v58\n\t" \
  "v_exp_f32 v59, v59\n\t" \
  "v_smfmac_f32_32x32x32_bf16 v[0:15], v[120:123], v[40:47], v114\n\t" \
  "v_exp_f32 v60, v60\n\t" \
  "v_exp_f32 v61, v61\n\t" \
  "v_exp_f32 v62, v62\n\t" \
  "v_exp_f32 v63, v63\n\t" \
  "v_mfma_f32_32x32x16_bf16 v[80:95], v[108:111], v[100:103], 0\n\t" \
  "ds_read_b128 v[108:111], v124 offset:8064\n\t" \
  "v_exp_f32 v64, v64\n\t" \
  "v_exp_f32 v65, v65\n\t" \
  "v_exp_f32 v66, v66\n\t" \
  "v_exp_f32 v67, v67\n\t" \
  "v_mfma_f32_32x32x8_bf16 v[80:95], v[112:113], v[106:107], v[80:95]\n\t" \
  "ds_read_b32 v112, v125 offset:8064\n\t" \
  "v_exp_f32 v68, v68\n\t" \
  "v_exp_f32 v69, v69\n\t" \
  "v_exp_f32 v70, v70\n\t" \
  "v_exp_f32 v71, v71\n\t" \
  "v_smfmac_f32_32x32x32_bf16 v[16:31], v[116:119], v[48:55], v114\n\t" \
  "ds_read_b128 v[116:119], v126 offset:8064\n\t" \
  "v_exp_f32 v72, v72\n\t" \
  "v_exp_f32 v73, v73\n\t" \
  "v_exp_f32 v74, v74\n\t" \
  "v_exp_f32 v75, v75\n\t" \
  "v_smfmac_f32_32x32x32_bf16 v[16:31], v[120:123], v[56:63], v114\n\t" \
  "ds_read_b128 v[120:123], v126 offset:8768\n\t" \
  "v_exp_f32 v76, v76\n\t" \
  "v_exp_f32 v77, v77\n\t" \
  "v_exp_f32 v78, v78\n\t" \
  "v_exp_f32 v79, v79\n\t" \
  "s_waitcnt lgkmcnt(2)\n\t" \
  "v_mfma_f32_32x32x16_bf16 v[32:47], v[108:111], v[96:99], 0\n\t" \
  "ds_read_b32 v115, v127 offset:32\n\t" \
  "v_exp_f32 v80, v80\n\t" \
  "v_exp_f32 v81, v81\n\t" \
  "v_exp_f32 v82, v82\n\t" \
  "v_exp_f32 v83, v83\n\t" \
  "v_mfma_f32_32x32x8_bf16 v[32:47], v[112:113], v[104:105], v[32:47]\n\t" \
  "v_add_u32 v124, %[inc], v124\n\t" \
  "v_add_u32 v125, %[inc], v125\n\t" \
  "v_add_u32 v126, %[inc], v126\n\t" \
  "v_exp_f32 v84, v84\n\t" \
  "v_exp_f32 v85, v85\n\t" \
  "v_exp_f32 v86, v86\n\t" \
  "v_exp_f32 v87, v87\n\t" \
  "s_waitcnt lgkmcnt(1)\n\t" \
  "v_smfmac_f32_32x32x32_bf16 v[0:15], v[116:119], v[64:71], v114\n\t" \
  "v_exp_f32 v88, v88\n\t" \
  "v_exp_f32 v89, v89\n\t" \
  "v_exp_f32 v90, v90\n\t" \
  "v_exp_f32 v91, v91\n\t" \
  "s_waitcnt lgkmcnt(0)\n\t" \
  "v_readfirstlane_b32 %[rdy], v115\n\t" \
  "v_mov_b32 v115, %[cnt]\n\t" \
  "v_smfmac_f32_32x32x32_bf16 v[0:15], v[120:123], v[72:79], v114\n\t" \
  "ds_write_b32 v127, v115\n\t" \
  "s_cmp_gt_u32 %[rdy], %[cnt]\n\t" \
  "v_exp_f32 v92, v92\n\t" \
  "v_exp_f32 v93, v93\n\t" \
  "v_exp_f32 v94, v94\n\t" \
  "v_exp_f32 v95, v95\n\t" \
  "s_cbranch_scc1 4b\n\t" \
  "s_cmp_lt_u32 %[cnt], 16\n\t" \
  "s_cbranch_scc1 2b\n\t" \
  "9:\n\t" \
  "s_setprio 3\n\t" \
  "v_smfmac_f32_32x32x32_bf16 v[16:31], v[116:119], v[80:87], v114\n\t" \
  "v_smfmac_f32_32x32x32_bf16 v[16:31], v[120:123], v[88:95], v114\n\t" \
  "s_waitcnt lgkmcnt(0)\n\t" \
  "s_nop 12\n\t"
#define TILE_GEMM_ASM \
  "s_nop 4\n\t" \
  "buffer_load_dwordx4 v[60:63], v140, %[rs], %[s0] offen offset:0 sc1\n\t" \
  "buffer_load_dwordx4 v[64:67], v140, %[rs], %[s1] offen offset:0 sc1\n\t" \
  "buffer_load_dwordx4 v[68:71], v140, %[rs], %[s0] offen offset:1024 sc1\n\t" \
  "buffer_load_dwordx4 v[72:75], v140, %[rs], %[s1] offen offset:1024 sc1\n\t" \
  "buffer_load_dwordx4 v[76:79], v140, %[rs], %[s0] offen offset:2048 sc1\n\t" \
  "buffer_load_dwordx4 v[80:83], v140, %[rs], %[s1] offen offset:2048 sc1\n\t" \
  "buffer_load_dwordx4 v[84:87], v140, %[rs], %[s0] offen offset:3072 sc1\n\t" \
  "buffer_load_dwordx4 v[88:91], v140, %[rs], %[s1] offen offset:3072 sc1\n\t" \
  "s_add_u32 %[s0], %[s0], 4096\n\t" \
  "buffer_load_dwordx4 v[92:95], v140, %[rs], %[s0] offen offset:0 sc1\n\t" \
  "s_add_u32 %[s1], %[s1], 4096\n\t" \
  "buffer_load_dwordx4 v[96:99], v140, %[rs], %[s1] offen offset:0 sc1\n\t" \
  "buffer_load_dwordx4 v[100:103], v140, %[rs], %[s0] offen offset:1024 sc1\n\t" \
  "buffer_load_dwordx4 v[104:107], v140, %[rs], %[s1] offen offset:1024 sc1\n\t" \
  "buffer_load_dwordx4 v[108:111], v140, %[rs], %[s0] offen offset:2048 sc1\n\t" \
  "buffer_load_dwordx4 v[112:115], v140, %[rs], %[s1] offen offset:2048 sc1\n\t" \
  "buffer_load_dwordx4 v[116:119], v140, %[rs], %[s0] offen offset:3072 sc1\n\t" \
  "buffer_load_dwordx4 v[120:123], v140, %[rs], %[s1] offen offset:3072 sc1\n\t" \
  "s_add_u32 %[s0], %[s0], 4096\n\t" \
  "buffer_load_dwordx4 v[124:127], v140, %[rs], %[s0] offen offset:0 sc1\n\t" \
  "s_add_u32 %[s1], %[s1], 4096\n\t" \
  "buffer_load_dwordx4 v[128:131], v140, %[rs], %[s1] offen offset:0 sc1\n\t" \
  "buffer_load_dwordx4 v[132:135], v140, %[rs], %[s0] offen offset:1024 sc1\n\t" \
  "buffer_load_dwordx4 v[136:139], v140, %[rs], %[s1] offen offset:1024 sc1\n\t" \
  "ds_read_b64_tr_b16 v[40:41], v141 offset:0\n\t" \
  "ds_read_b64_tr_b16 v[42:43], v141 offset:2560\n\t" \
  "ds_read_b64_tr_b16 v[44:45], v141 offset:8\n\t" \
  "ds_read_b64_tr_b16 v[46:47], v141 offset:2568\n\t" \
  "ds_read_b64_tr_b16 v[48:49], v141 offset:16\n\t" \
  "ds_read_b64_tr_b16 v[50:51], v141 offset:2576\n\t" \
  "ds_read_b64_tr_b16 v[52:53], v141 offset:24\n\t" \
  "ds_read_b64_tr_b16 v[54:55], v141 offset:2584\n\t" \
  "ds_read_b64_tr_b16 v[56:57], v141 offset:32\n\t" \
  "ds_read_b64_tr_b16 v[58:59], v141 offset:2592\n\t" \
  "s_waitcnt vmcnt(18)\n\t" \
  "s_waitcnt lgkmcnt(8)\n\t" \
  "v_mfma_f32_16x16x32_bf16 v[0:3], v[40:43], v[60:63], v[0:3]\n\t" \
  "v_mfma_f32_16x16x32_bf16 v[4:7], v[40:43], v[64:67], v[4:7]\n\t" \
  "ds_read_b64_tr_b16 v[40:41], v141 offset:5120\n\t" \
  "ds_read_b64_tr_b16 v[42:43], v141 offset:7680\n\t" \
  "s_waitcnt lgkmcnt(8)\n\t" \
  "v_mfma_f32_16x16x32_bf16 v[8:11], v[44:47], v[60:63], v[8:11]\n\t" \
  "v_mfma_f32_16x16x32_bf16 v[12:15], v[44:47], v[64:67], v[12:15]\n\t" \
  "ds_read_b64_tr_b16 v[44:45], v141 offset:5128\n\t" \
  "ds_read_b64_tr_b16 v[46:47], v141 offset:7688\n\t" \
  "s_waitcnt lgkmcnt(8)\n\t" \
  "v_mfma_f32_16x16x32_bf16 v[16:19], v[48:51], v[60:63], v[16:19]\n\t" \
  "v_mfma_f32_16x16x32_bf16 v[20:23], v[48:51], v[64:67], v[20:23]\n\t" \
  "ds_read_b64_tr_b16 v[48:49], v141 offset:5136\n\t" \
  "ds_read_b64_tr_b16 v[50:51], v141 offset:7696\n\t" \
  "s_waitcnt lgkmcnt(8)\n\t" \
  "v_mfma_f32_16x16x32_bf16 v[24:27], v[52:55], v[60:63], v[24:27]\n\t" \
  "v_mfma_f32_16x16x32_bf16 v[28:31], v[52:55], v[64:67], v[28:31]\n\t" \
  "ds_read_b64_tr_b16 v[52:53], v141 offset:5144\n\t" \
  "ds_read_b64_tr_b16 v[54:55], v141 offset:7704\n\t" \
  "s_waitcnt lgkmcnt(8)\n\t" \
  "v_mfma_f32_16x16x32_bf16 v[32:35], v[56:59], v[60:63], v[32:35]\n\t" \
  "v_mfma_f32_16x16x32_bf16 v[36:39], v[56:59], v[64:67], v[36:39]\n\t" \
  "ds_read_b64_tr_b16 v[56:57], v141 offset:5152\n\t" \
  "ds_read_b64_tr_b16 v[58:59], v141 offset:7712\n\t" \
  "s_waitcnt vmcnt(16)\n\t" \
  "s_waitcnt lgkmcnt(8)\n\t" \
  "v_mfma_f32_16x16x32_bf16 v[0:3], v[40:43], v[68:71], v[0:3]\n\t" \
  "v_mfma_f32_16x16x32_bf16 v[4:7], v[40:43], v[72:75], v[4:7]\n\t" \
  "ds_read_b64_tr_b16 v[40:41], v141 offset:10240\n\t" \
  "ds_read_b64_tr_b16 v[42:43], v141 offset:12800\n\t" \
  "s_waitcnt lgkmcnt(8)\n\t" \
  "v_mfma_f32_16x16x32_bf16 v[8:11], v[44:47], v[68:71], v[8:11]\n\t" \
  "v_mfma_f32_16x16x32_bf16 v[12:15], v[44:47], v[72:75], v[12:15]\n\t" \
  "ds_read_b64_tr_b16 v[44:45], v141 offset:10248\n\t" \
  "ds_read_b64_tr_b16 v[46:47], v141 offset:12808\n\t" \
  "s_waitcnt lgkmcnt(8)\n\t" \
  "v_mfma_f32_16x16x32_bf16 v[16:19], v[48:51], v[68:71], v[16:19]\n\t" \
  "v_mfma_f32_16x16x32_bf16 v[20:23], v[48:51], v[72:75], v[20:23]\n\t" \
  "ds_read_b64_tr_b16 v[48:49], v141 offset:10256\n\t" \
  "ds_read_b64_tr_b16 v[50:51], v141 offset:12816\n\t" \
  "s_waitcnt lgkmcnt(8)\n\t" \
  "v_mfma_f32_16x16x32_bf16 v[24:27], v[52:55], v[68:71], v[24:27]\n\t" \
  "v_mfma_f32_16x16x32_bf16 v[28:31], v[52:55], v[72:75], v[28:31]\n\t" \
  "ds_read_b64_tr_b16 v[52:53], v141 offset:10264\n\t" \
  "ds_read_b64_tr_b16 v[54:55], v141 offset:12824\n\t" \
  "s_waitcnt lgkmcnt(8)\n\t" \
  "v_mfma_f32_16x16x32_bf16 v[32:35], v[56:59], v[68:71], v[32:35]\n\t" \
  "v_mfma_f32_16x16x32_bf16 v[36:39], v[56:59], v[72:75], v[36:39]\n\t" \
  "ds_read_b64_tr_b16 v[56:57], v141 offset:10272\n\t" \
  "ds_read_b64_tr_b16 v[58:59], v141 offset:12832\n\t" \
  "s_waitcnt vmcnt(14)\n\t" \
  "s_waitcnt lgkmcnt(8)\n\t" \
  "v_mfma_f32_16x16x32_bf16 v[0:3], v[40:43], v[76:79], v[0:3]\n\t" \
  "v_mfma_f32_16x16x32_bf16 v[4:7], v[40:43], v[80:83], v[4:7]\n\t" \
  "ds_read_b64_tr_b16 v[40:41], v141 offset:15360\n\t" \
  "ds_read_b64_tr_b16 v[42:43], v141 offset:17920\n\t" \
  "s_waitcnt lgkmcnt(8)\n\t" \
  "v_mfma_f32_16x16x32_bf16 v[8:11], v[44:47], v[76:79], v[8:11]\n\t" \
  "v_mfma_f32_16x16x32_bf16 v[12:15], v[44:47], v[80:83], v[12:15]\n\t" \
  "ds_read_b64_tr_b16 v[44:45], v141 offset:15368\n\t" \
  "ds_read_b64_tr_b16 v[46:47], v141 offset:17928\n\t" \
  "s_waitcnt lgkmcnt(8)\n\t" \
  "v_mfma_f32_16x16x32_bf16 v[16:19], v[48:51], v[76:79], v[16:19]\n\t" \
  "v_mfma_f32_16x16x32_bf16 v[20:23], v[48:51], v[80:83], v[20:23]\n\t" \
  "ds_read_b64_tr_b16 v[48:49], v141 offset:15376\n\t" \
  "ds_read_b64_tr_b16 v[50:51], v141 offset:17936\n\t" \
  "s_waitcnt lgkmcnt(8)\n\t" \
  "v_mfma_f32_16x16x32_bf16 v[24:27], v[52:55], v[76:79], v[24:27]\n\t" \
  "v_mfma_f32_16x16x32_bf16 v[28:31], v[52:55], v[80:83], v[28:31]\n\t" \
  "ds_read_b64_tr_b16 v[52:53], v141 offset:15384\n\t" \
  "ds_read_b64_tr_b16 v[54:55], v141 offset:17944\n\t" \
  "s_waitcnt lgkmcnt(8)\n\t" \
  "v_mfma_f32_16x16x32_bf16 v[32:35], v[56:59], v[76:79], v[32:35]\n\t" \
  "v_mfma_f32_16x16x32_bf16 v[36:39], v[56:59], v[80:83], v[36:39]\n\t" \
  "ds_read_b64_tr_b16 v[56:57], v141 offset:15392\n\t" \
  "ds_read_b64_tr_b16 v[58:59], v141 offset:17952\n\t" \
  "s_waitcnt vmcnt(12)\n\t" \
  "s_waitcnt lgkmcnt(8)\n\t" \
  "v_mfma_f32_16x16x32_bf16 v[0:3], v[40:43], v[84:87], v[0:3]\n\t" \
  "v_mfma_f32_16x16x32_bf16 v[4:7], v[40:43], v[88:91], v[4:7]\n\t" \
  "ds_read_b64_tr_b16 v[40:41], v141 offset:20480\n\t" \
  "ds_read_b64_tr_b16 v[42:43], v141 offset:23040\n\t" \
  "s_waitcnt lgkmcnt(8)\n\t" \
  "v_mfma_f32_16x16x32_bf16 v[8:11], v[44:47], v[84:87], v[8:11]\n\t" \
  "v_mfma_f32_16x16x32_bf16 v[12:15], v[44:47], v[88:91], v[12:15]\n\t" \
  "ds_read_b64_tr_b16 v[44:45], v141 offset:20488\n\t" \
  "ds_read_b64_tr_b16 v[46:47], v141 offset:23048\n\t" \
  "s_waitcnt lgkmcnt(8)\n\t" \
  "v_mfma_f32_16x16x32_bf16 v[16:19], v[48:51], v[84:87], v[16:19]\n\t" \
  "v_mfma_f32_16x16x32_bf16 v[20:23], v[48:51], v[88:91], v[20:23]\n\t" \
  "ds_read_b64_tr_b16 v[48:49], v141 offset:20496\n\t" \
  "ds_read_b64_tr_b16 v[50:51], v141 offset:23056\n\t" \
  "s_waitcnt lgkmcnt(8)\n\t" \
  "v_mfma_f32_16x16x32_bf16 v[24:27], v[52:55], v[84:87], v[24:27]\n\t" \
  "v_mfma_f32_16x16x32_bf16 v[28:31], v[52:55], v[88:91], v[28:31]\n\t" \
  "ds_read_b64_tr_b16 v[52:53], v141 offset:20504\n\t" \
  "ds_read_b64_tr_b16 v[54:55], v141 offset:23064\n\t" \
  "s_waitcnt lgkmcnt(8)\n\t" \
  "v_mfma_f32_16x16x32_bf16 v[32:35], v[56:59], v[84:87], v[32:35]\n\t" \
  "v_mfma_f32_16x16x32_bf16 v[36:39], v[56:59], v[88:91], v[36:39]\n\t" \
  "ds_read_b64_tr_b16 v[56:57], v141 offset:20512\n\t" \
  "ds_read_b64_tr_b16 v[58:59], v141 offset:23072\n\t" \
  "s_waitcnt vmcnt(10)\n\t" \
  "s_waitcnt lgkmcnt(8)\n\t" \
  "v_mfma_f32_16x16x32_bf16 v[0:3], v[40:43], v[92:95], v[0:3]\n\t" \
  "v_mfma_f32_16x16x32_bf16 v[4:7], v[40:43], v[96:99], v[4:7]\n\t" \
  "ds_read_b64_tr_b16 v[40:41], v141 offset:25600\n\t" \
  "ds_read_b64_tr_b16 v[42:43], v141 offset:28160\n\t" \
  "s_waitcnt lgkmcnt(8)\n\t" \
  "v_mfma_f32_16x16x32_bf16 v[8:11], v[44:47], v[92:95], v[8:11]\n\t" \
  "v_mfma_f32_16x16x32_bf16 v[12:15], v[44:47], v[96:99], v[12:15]\n\t" \
  "ds_read_b64_tr_b16 v[44:45], v141 offset:25608\n\t" \
  "ds_read_b64_tr_b16 v[46:47], v141 offset:28168\n\t" \
  "s_waitcnt lgkmcnt(8)\n\t" \
  "v_mfma_f32_16x16x32_bf16 v[16:19], v[48:51], v[92:95], v[16:19]\n\t" \
  "v_mfma_f32_16x16x32_bf16 v[20:23], v[48:51], v[96:99], v[20:23]\n\t" \
  "ds_read_b64_tr_b16 v[48:49], v141 offset:25616\n\t" \
  "ds_read_b64_tr_b16 v[50:51], v141 offset:28176\n\t" \
  "s_waitcnt lgkmcnt(8)\n\t" \
  "v_mfma_f32_16x16x32_bf16 v[24:27], v[52:55], v[92:95], v[24:27]\n\t" \
  "v_mfma_f32_16x16x32_bf16 v[28:31], v[52:55], v[96:99], v[28:31]\n\t" \
  "ds_read_b64_tr_b16 v[52:53], v141 offset:25624\n\t" \
  "ds_read_b64_tr_b16 v[54:55], v141 offset:28184\n\t" \
  "s_waitcnt lgkmcnt(8)\n\t" \
  "v_mfma_f32_16x16x32_bf16 v[32:35], v[56:59], v[92:95], v[32:35]\n\t" \
  "v_mfma_f32_16x16x32_bf16 v[36:39], v[56:59], v[96:99], v[36:39]\n\t" \
  "ds_read_b64_tr_b16 v[56:57], v141 offset:25632\n\t" \
  "ds_read_b64_tr_b16 v[58:59], v141 offset:28192\n\t" \
  "s_mov_b64 exec, 1\n\t" \
  "ds_add_u32 v142, v143\n\t" \
  "s_mov_b64 exec, -1\n\t" \
  "s_waitcnt vmcnt(8)\n\t" \
  "s_waitcnt lgkmcnt(8)\n\t" \
  "v_mfma_f32_16x16x32_bf16 v[0:3], v[40:43], v[100:103], v[0:3]\n\t" \
  "v_mfma_f32_16x16x32_bf16 v[4:7], v[40:43], v[104:107], v[4:7]\n\t" \
  "ds_read_b64_tr_b16 v[40:41], v141 offset:30720\n\t" \
  "ds_read_b64_tr_b16 v[42:43], v141 offset:33280\n\t" \
  "s_waitcnt lgkmcnt(8)\n\t" \
  "v_mfma_f32_16x16x32_bf16 v[8:11], v[44:47], v[100:103], v[8:11]\n\t" \
  "v_mfma_f32_16x16x32_bf16 v[12:15], v[44:47], v[104:107], v[12:15]\n\t" \
  "ds_read_b64_tr_b16 v[44:45], v141 offset:30728\n\t" \
  "ds_read_b64_tr_b16 v[46:47], v141 offset:33288\n\t" \
  "s_waitcnt lgkmcnt(8)\n\t" \
  "v_mfma_f32_16x16x32_bf16 v[16:19], v[48:51], v[100:103], v[16:19]\n\t" \
  "v_mfma_f32_16x16x32_bf16 v[20:23], v[48:51], v[104:107], v[20:23]\n\t" \
  "ds_read_b64_tr_b16 v[48:49], v141 offset:30736\n\t" \
  "ds_read_b64_tr_b16 v[50:51], v141 offset:33296\n\t" \
  "s_waitcnt lgkmcnt(8)\n\t" \
  "v_mfma_f32_16x16x32_bf16 v[24:27], v[52:55], v[100:103], v[24:27]\n\t" \
  "v_mfma_f32_16x16x32_bf16 v[28:31], v[52:55], v[104:107], v[28:31]\n\t" \
  "ds_read_b64_tr_b16 v[52:53], v141 offset:30744\n\t" \
  "ds_read_b64_tr_b16 v[54:55], v141 offset:33304\n\t" \
  "s_waitcnt lgkmcnt(8)\n\t" \
  "v_mfma_f32_16x16x32_bf16 v[32:35], v[56:59], v[100:103], v[32:35]\n\t" \
  "v_mfma_f32_16x16x32_bf16 v[36:39], v[56:59], v[104:107], v[36:39]\n\t" \
  "ds_read_b64_tr_b16 v[56:57], v141 offset:30752\n\t" \
  "ds_read_b64_tr_b16 v[58:59], v141 offset:33312\n\t" \
  "s_waitcnt vmcnt(6)\n\t" \
  "s_waitcnt lgkmcnt(8)\n\t" \
  "v_mfma_f32_16x16x32_bf16 v[0:3], v[40:43], v[108:111], v[0:3]\n\t" \
  "v_mfma_f32_16x16x32_bf16 v[4:7], v[40:43], v[112:115], v[4:7]\n\t" \
  "ds_read_b64_tr_b16 v[40:41], v141 offset:35840\n\t" \
  "ds_read_b64_tr_b16 v[42:43], v141 offset:38400\n\t" \
  "s_waitcnt lgkmcnt(8)\n\t" \
  "v_mfma_f32_16x16x32_bf16 v[8:11], v[44:47], v[108:111], v[8:11]\n\t" \
  "v_mfma_f32_16x16x32_bf16 v[12:15], v[44:47], v[112:115], v[12:15]\n\t" \
  "ds_read_b64_tr_b16 v[44:45], v141 offset:35848\n\t" \
  "ds_read_b64_tr_b16 v[46:47], v141 offset:38408\n\t" \
  "s_waitcnt lgkmcnt(8)\n\t" \
  "v_mfma_f32_16x16x32_bf16 v[16:19], v[48:51], v[108:111], v[16:19]\n\t" \
  "v_mfma_f32_16x16x32_bf16 v[20:23], v[48:51], v[112:115], v[20:23]\n\t" \
  "ds_read_b64_tr_b16 v[48:49], v141 offset:35856\n\t" \
  "ds_read_b64_tr_b16 v[50:51], v141 offset:38416\n\t" \
  "s_waitcnt lgkmcnt(8)\n\t" \
  "v_mfma_f32_16x16x32_bf16 v[24:27], v[52:55], v[108:111], v[24:27]\n\t" \
  "v_mfma_f32_16x16x32_bf16 v[28:31], v[52:55], v[112:115], v[28:31]\n\t" \
  "ds_read_b64_tr_b16 v[52:53], v141 offset:35864\n\t" \
  "ds_read_b64_tr_b16 v[54:55], v141 offset:38424\n\t" \
  "s_waitcnt lgkmcnt(8)\n\t" \
  "v_mfma_f32_16x16x32_bf16 v[32:35], v[56:59], v[108:111], v[32:35]\n\t" \
  "v_mfma_f32_16x16x32_bf16 v[36:39], v[56:59], v[112:115], v[36:39]\n\t" \
  "ds_read_b64_tr_b16 v[56:57], v141 offset:35872\n\t" \
  "ds_read_b64_tr_b16 v[58:59], v141 offset:38432\n\t" \
  "s_waitcnt vmcnt(4)\n\t" \
  "s_waitcnt lgkmcnt(8)\n\t" \
  "v_mfma_f32_16x16x32_bf16 v[0:3], v[40:43], v[116:119], v[0:3]\n\t" \
  "v_mfma_f32_16x16x32_bf16 v[4:7], v[40:43], v[120:123], v[4:7]\n\t" \
  "ds_read_b64_tr_b16 v[40:41], v141 offset:40960\n\t" \
  "ds_read_b64_tr_b16 v[42:43], v141 offset:43520\n\t" \
  "s_waitcnt lgkmcnt(8)\n\t" \
  "v_mfma_f32_16x16x32_bf16 v[8:11], v[44:47], v[116:119], v[8:11]\n\t" \
  "v_mfma_f32_16x16x32_bf16 v[12:15], v[44:47], v[120:123], v[12:15]\n\t" \
  "ds_read_b64_tr_b16 v[44:45], v141 offset:40968\n\t" \
  "ds_read_b64_tr_b16 v[46:47], v141 offset:43528\n\t" \
  "s_waitcnt lgkmcnt(8)\n\t" \
  "v_mfma_f32_16x16x32_bf16 v[16:19], v[48:51], v[116:119], v[16:19]\n\t" \
  "v_mfma_f32_16x16x32_bf16 v[20:23], v[48:51], v[120:123], v[20:23]\n\t" \
  "ds_read_b64_tr_b16 v[48:49], v141 offset:40976\n\t" \
  "ds_read_b64_tr_b16 v[50:51], v141 offset:43536\n\t" \
  "s_waitcnt lgkmcnt(8)\n\t" \
  "v_mfma_f32_16x16x32_bf16 v[24:27], v[52:55], v[116:119], v[24:27]\n\t" \
  "v_mfma_f32_16x16x32_bf16 v[28:31], v[52:55], v[120:123], v[28:31]\n\t" \
  "ds_read_b64_tr_b16 v[52:53], v141 offset:40984\n\t" \
  "ds_read_b64_tr_b16 v[54:55], v141 offset:43544\n\t" \
  "s_waitcnt lgkmcnt(8)\n\t" \
  "v_mfma_f32_16x16x32_bf16 v[32:35], v[56:59], v[116:119], v[32:35]\n\t" \
  "v_mfma_f32_16x16x32_bf16 v[36:39], v[56:59], v[120:123], v[36:39]\n\t" \
  "ds_read_b64_tr_b16 v[56:57], v141 offset:40992\n\t" \
  "ds_read_b64_tr_b16 v[58:59], v141 offset:43552\n\t" \
  "s_waitcnt vmcnt(2)\n\t" \
  "s_waitcnt lgkmcnt(8)\n\t" \
  "v_mfma_f32_16x16x32_bf16 v[0:3], v[40:43], v[124:127], v[0:3]\n\t" \
  "v_mfma_f32_16x16x32_bf16 v[4:7], v[40:43], v[128:131], v[4:7]\n\t" \
  "ds_read_b64_tr_b16 v[40:41], v141 offset:46080\n\t" \
  "ds_read_b64_tr_b16 v[42:43], v141 offset:48640\n\t" \
  "s_waitcnt lgkmcnt(8)\n\t" \
  "v_mfma_f32_16x16x32_bf16 v[8:11], v[44:47], v[124:127], v[8:11]\n\t" \
  "v_mfma_f32_16x16x32_bf16 v[12:15], v[44:47], v[128:131], v[12:15]\n\t" \
  "ds_read_b64_tr_b16 v[44:45], v141 offset:46088\n\t" \
  "ds_read_b64_tr_b16 v[46:47], v141 offset:48648\n\t" \
  "s_waitcnt lgkmcnt(8)\n\t" \
  "v_mfma_f32_16x16x32_bf16 v[16:19], v[48:51], v[124:127], v[16:19]\n\t" \
  "v_mfma_f32_16x16x32_bf16 v[20:23], v[48:51], v[128:131], v[20:23]\n\t" \
  "ds_read_b64_tr_b16 v[48:49], v141 offset:46096\n\t" \
  "ds_read_b64_tr_b16 v[50:51], v141 offset:48656\n\t" \
  "s_waitcnt lgkmcnt(8)\n\t" \
  "v_mfma_f32_16x16x32_bf16 v[24:27], v[52:55], v[124:127], v[24:27]\n\t" \
  "v_mfma_f32_16x16x32_bf16 v[28:31], v[52:55], v[128:131], v[28:31]\n\t" \
  "ds_read_b64_tr_b16 v[52:53], v141 offset:46104\n\t" \
  "ds_read_b64_tr_b16 v[54:55], v141 offset:48664\n\t" \
  "s_waitcnt lgkmcnt(8)\n\t" \
  "v_mfma_f32_16x16x32_bf16 v[32:35], v[56:59], v[124:127], v[32:35]\n\t" \
  "v_mfma_f32_16x16x32_bf16 v[36:39], v[56:59], v[128:131], v[36:39]\n\t" \
  "ds_read_b64_tr_b16 v[56:57], v141 offset:46112\n\t" \
  "ds_read_b64_tr_b16 v[58:59], v141 offset:48672\n\t" \
  "s_waitcnt vmcnt(0)\n\t" \
  "s_waitcnt lgkmcnt(8)\n\t" \
  "v_mfma_f32_16x16x32_bf16 v[0:3], v[40:43], v[132:135], v[0:3]\n\t" \
  "v_mfma_f32_16x16x32_bf16 v[4:7], v[40:43], v[136:139], v[4:7]\n\t" \
  "s_waitcnt lgkmcnt(6)\n\t" \
  "v_mfma_f32_16x16x32_bf16 v[8:11], v[44:47], v[132:135], v[8:11]\n\t" \
  "v_mfma_f32_16x16x32_bf16 v[12:15], v[44:47], v[136:139], v[12:15]\n\t" \
  "s_waitcnt lgkmcnt(4)\n\t" \
  "v_mfma_f32_16x16x32_bf16 v[16:19], v[48:51], v[132:135], v[16:19]\n\t" \
  "v_mfma_f32_16x16x32_bf16 v[20:23], v[48:51], v[136:139], v[20:23]\n\t" \
  "s_waitcnt lgkmcnt(2)\n\t" \
  "v_mfma_f32_16x16x32_bf16 v[24:27], v[52:55], v[132:135], v[24:27]\n\t" \
  "v_mfma_f32_16x16x32_bf16 v[28:31], v[52:55], v[136:139], v[28:31]\n\t" \
  "s_waitcnt lgkmcnt(0)\n\t" \
  "v_mfma_f32_16x16x32_bf16 v[32:35], v[56:59], v[132:135], v[32:35]\n\t" \
  "v_mfma_f32_16x16x32_bf16 v[36:39], v[56:59], v[136:139], v[36:39]\n\t" \
  "s_nop 7\n\t"
#define TILE_GEMM_CLOB "v40", "v41", "v42", "v43", "v44", "v45", "v46", "v47", "v48", "v49", "v50", "v51", "v52", "v53", "v54", "v55", "v56", "v57", "v58", "v59", "v60", "v61", "v62", "v63", "v64", "v65", "v66", "v67", "v68", "v69", "v70", "v71", "v72", "v73", "v74", "v75", "v76", "v77", "v78", "v79", "v80", "v81", "v82", "v83", "v84", "v85", "v86", "v87", "v88", "v89", "v90", "v91", "v92", "v93", "v94", "v95", "v96", "v97", "v98", "v99", "v100", "v101", "v102", "v103", "v104", "v105", "v106", "v107", "v108", "v109", "v110", "v111", "v112", "v113", "v114", "v115", "v116", "v117", "v118", "v119", "v120", "v121", "v122", "v123", "v124", "v125", "v126", "v127", "v128", "v129", "v130", "v131", "v132", "v133", "v134", "v135", "v136", "v137", "v138", "v139"
#define K1_LOOP_ASM \
  "v_mov_b64 v[0:1], 0\n\t" \
  "v_mov_b64 v[2:3], 0\n\t" \
  "v_mov_b64 v[4:5], 0\n\t" \
  "v_mov_b64 v[6:7], 0\n\t" \
  "v_mov_b64 v[8:9], 0\n\t" \
  "v_mov_b64 v[10:11], 0\n\t" \
  "v_mov_b64 v[12:13], 0\n\t" \
  "v_mov_b64 v[14:15], 0\n\t" \
  "v_mov_b64 v[16:17], 0\n\t" \
  "v_mov_b64 v[18:19], 0\n\t" \
  "v_mov_b64 v[20:21], 0\n\t" \
  "v_mov_b64 v[22:23], 0\n\t" \
  "v_mov_b64 v[24:25], 0\n\t" \
  "v_mov_b64 v[26:27], 0\n\t" \
  "v_mov_b64 v[28:29], 0\n\t" \
  "v_mov_b64 v[30:31], 0\n\t" \
  "v_mov_b64 v[32:33], 0\n\t" \
  "v_mov_b64 v[34:35], 0\n\t" \
  "v_mov_b64 v[36:37], 0\n\t" \
  "v_mov_b64 v[38:39], 0\n\t" \
  "v_mov_b64 v[40:41], 0\n\t" \
  "v_mov_b64 v[42:43], 0\n\t" \
  "v_mov_b64 v[44:45], 0\n\t" \
  "v_mov_b64 v[46:47], 0\n\t" \
  "v_mov_b64 v[48:49], 0\n\t" \
  "v_mov_b64 v[50:51], 0\n\t" \
  "v_mov_b64 v[52:53], 0\n\t" \
  "v_mov_b64 v[54:55], 0\n\t" \
  "v_mov_b64 v[56:57], 0\n\t" \
  "v_mov_b64 v[58:59], 0\n\t" \
  "v_mov_b64 v[60:61], 0\n\t" \
  "v_mov_b64 v[62:63], 0\n\t" \
  "v_mov_b64 v[64:65], 0\n\t" \
  "v_mov_b64 v[66:67], 0\n\t" \
  "v_mov_b64 v[68:69], 0\n\t" \
  "v_mov_b64 v[70:71], 0\n\t" \
  "v_mov_b64 v[72:73], 0\n\t" \
  "v_mov_b64 v[74:75], 0\n\t" \
  "v_mov_b64 v[76:77], 0\n\t" \
  "v_mov_b64 v[78:79], 0\n\t" \
  "v_mov_b64 v[116:117], 0\n\t" \
  "v_mov_b64 v[118:119], 0\n\t" \
  "v_mov_b64 v[120:121], 0\n\t" \
  "v_mov_b64 v[122:123], 0\n\t" \
  "v_mov_b64 v[124:125], 0\n\t" \
  "v_mov_b64 v[126:127], 0\n\t" \
  "v_mov_b64 v[128:129], 0\n\t" \
  "v_mov_b64 v[130:131], 0\n\t" \
  "v_mov_b64 v[132:133], 0\n\t" \
  "v_mov_b64 v[134:135], 0\n\t" \
  "v_mov_b64 v[136:137], 0\n\t" \
  "v_mov_b64 v[138:139], 0\n\t" \
  "v_mov_b64 v[140:141], 0\n\t" \
  "v_mov_b64 v[142:143], 0\n\t" \
  "v_mov_b64 v[144:145], 0\n\t" \
  "v_mov_b64 v[146:147], 0\n\t" \
  "v_mov_b64 v[148:149], 0\n\t" \
  "v_mov_b64 v[150:151], 0\n\t" \
  "s_mov_b32 %[cnt], 0\n\t" \
  "1:\n\t" \
  "s_barrier\n\t" \
  "v_mfma_f32_16x16x32_bf16 v[0:3], v[132:135], v[116:119], v[0:3]\n\t" \
  "ds_read_b128 v[80:83], v152\n\t" \
  "v_mfma_f32_16x16x32_bf16 v[4:7], v[132:135], v[120:123], v[4:7]\n\t" \
  "ds_read_b64_tr_b16 v[96:97], v154 offset:0\n\t" \
  "v_mfma_f32_16x16x32_bf16 v[8:11], v[132:135], v[124:127], v[8:11]\n\t" \
  "ds_read_b64_tr_b16 v[98:99], v154 offset:2560\n\t" \
  "v_mfma_f32_16x16x32_bf16 v[12:15], v[132:135], v[128:131], v[12:15]\n\t" \
  "ds_read_b128 v[84:87], v152 offset:2048\n\t" \
  "v_mfma_f32_16x16x32_bf16 v[16:19], v[136:139], v[116:119], v[16:19]\n\t" \
  "ds_read_b128 v[88:91], v152 offset:4096\n\t" \
  "v_mfma_f32_16x16x32_bf16 v[20:23], v[136:139], v[120:123], v[20:23]\n\t" \
  "ds_read_b128 v[92:95], v152 offset:6144\n\t" \
  "v_mfma_f32_16x16x32_bf16 v[24:27], v[136:139], v[124:127], v[24:27]\n\t" \
  "ds_read_b64_tr_b16 v[100:101], v154 offset:8\n\t" \
  "v_mfma_f32_16x16x32_bf16 v[28:31], v[136:139], v[128:131], v[28:31]\n\t" \
  "ds_read_b64_tr_b16 v[102:103], v154 offset:2568\n\t" \
  "v_mfma_f32_16x16x32_bf16 v[32:35], v[140:143], v[116:119], v[32:35]\n\t" \
  "ds_read_b64_tr_b16 v[104:105], v154 offset:16\n\t" \
  "v_mfma_f32_16x16x32_bf16 v[36:39], v[140:143], v[120:123], v[36:39]\n\t" \
  "ds_read_b64_tr_b16 v[106:107], v154 offset:2576\n\t" \
  "v_mfma_f32_16x16x32_bf16 v[40:43], v[140:143], v[124:127], v[40:43]\n\t" \
  "ds_read_b64_tr_b16 v[108:109], v154 offset:24\n\t" \
  "v_mfma_f32_16x16x32_bf16 v[44:47], v[140:143], v[128:131], v[44:47]\n\t" \
  "ds_read_b64_tr_b16 v[110:111], v154 offset:2584\n\t" \
  "v_mfma_f32_16x16x32_bf16 v[48:51], v[144:147], v[116:119], v[48:51]\n\t" \
  "ds_read_b64_tr_b16 v[112:113], v154 offset:32\n\t" \
  "v_mfma_f32_16x16x32_bf16 v[52:55], v[144:147], v[120:123], v[52:55]\n\t" \
  "ds_read_b64_tr_b16 v[114:115], v154 offset:2592\n\t" \
  "v_mfma_f32_16x16x32_bf16 v[56:59], v[144:147], v[124:127], v[56:59]\n\t" \
  "s_and_b32 %[inc], %[cnt], 3\n\t" \
  "v_mfma_f32_16x16x32_bf16 v[60:63], v[144:147], v[128:131], v[60:63]\n\t" \
  "s_cmp_eq_u32 %[inc], 3\n\t" \
  "v_mfma_f32_16x16x32_bf16 v[64:67], v[148:151], v[116:119], v[64:67]\n\t" \
  "s_cselect_b32 %[inc], %[wrap], %[stp]\n\t" \
  "v_mfma_f32_16x16x32_bf16 v[68:71], v[148:151], v[120:123], v[68:71]\n\t" \
  "v_add_u32 v152, %[inc], v152\n\t" \
  "v_mfma_f32_16x16x32_bf16 v[72:75], v[148:151], v[124:127], v[72:75]\n\t" \
  "v_mfma_f32_16x16x32_bf16 v[76:79], v[148:151], v[128:131], v[76:79]\n\t" \
  "s_waitcnt lgkmcnt(0)\n\t" \
  "v_mfma_f32_16x16x32_bf16 v[0:3], v[96:99], v[80:83], v[0:3]\n\t" \
  "ds_read_b128 v[116:119], v153\n\t" \
  "v_mfma_f32_16x16x32_bf16 v[4:7], v[96:99], v[84:87], v[4:7]\n\t" \
  "ds_read_b64_tr_b16 v[132:133], v154 offset:5120\n\t" \
  "v_mfma_f32_16x16x32_bf16 v[8:11], v[96:99], v[88:91], v[8:11]\n\t" \
  "ds_read_b64_tr_b16 v[134:135], v154 offset:7680\n\t" \
  "v_mfma_f32_16x16x32_bf16 v[12:15], v[96:99], v[92:95], v[12:15]\n\t" \
  "ds_read_b128 v[120:123], v153 offset:2048\n\t" \
  "v_mfma_f32_16x16x32_bf16 v[16:19], v[100:103], v[80:83], v[16:19]\n\t" \
  "ds_read_b128 v[124:127], v153 offset:4096\n\t" \
  "v_mfma_f32_16x16x32_bf16 v[20:23], v[100:103], v[84:87], v[20:23]\n\t" \
  "ds_read_b128 v[128:131], v153 offset:6144\n\t" \
  "v_mfma_f32_16x16x32_bf16 v[24:27], v[100:103], v[88:91], v[24:27]\n\t" \
  "ds_read_b64_tr_b16 v[136:137], v154 offset:5128\n\t" \
  "v_mfma_f32_16x16x32_bf16 v[28:31], v[100:103], v[92:95], v[28:31]\n\t" \
  "ds_read_b64_tr_b16 v[138:139], v154 offset:7688\n\t" \
  "v_mfma_f32_16x16x32_bf16 v[32:35], v[104:107], v[80:83], v[32:35]\n\t" \
  "ds_read_b64_tr_b16 v[140:141], v154 offset:5136\n\t" \
  "v_mfma_f32_16x16x32_bf16 v[36:39], v[104:107], v[84:87], v[36:39]\n\t" \
  "ds_read_b64_tr_b16 v[142:143], v154 offset:7696\n\t" \
  "v_mfma_f32_16x16x32_bf16 v[40:43], v[104:107], v[88:91], v[40:43]\n\t" \
  "ds_read_b64_tr_b16 v[144:145], v154 offset:5144\n\t" \
  "v_mfma_f32_16x16x32_bf16 v[44:47], v[104:107], v[92:95], v[44:47]\n\t" \
  "ds_read_b64_tr_b16 v[146:147], v154 offset:7704\n\t" \
  "v_mfma_f32_16x16x32_bf16 v[48:51], v[108:111], v[80:83], v[48:51]\n\t" \
  "ds_read_b64_tr_b16 v[148:149], v154 offset:5152\n\t" \
  "v_mfma_f32_16x16x32_bf16 v[52:55], v[108:111], v[84:87], v[52:55]\n\t" \
  "ds_read_b64_tr_b16 v[150:151], v154 offset:7712\n\t" \
  "v_mfma_f32_16x16x32_bf16 v[56:59], v[108:111], v[88:91], v[56:59]\n\t" \
  "v_add_u32 v153, %[inc], v153\n\t" \
  "v_mfma_f32_16x16x32_bf16 v[60:63], v[108:111], v[92:95], v[60:63]\n\t" \
  "v_add_u32 v154, %[inc], v154\n\t" \
  "v_mfma_f32_16x16x32_bf16 v[64:67], v[112:115], v[80:83], v[64:67]\n\t" \
  "s_add_u32 %[cnt], %[cnt], 1\n\t" \
  "v_mfma_f32_16x16x32_bf16 v[68:71], v[112:115], v[84:87], v[68:71]\n\t" \
  "s_cmp_lt_u32 %[cnt], 10\n\t" \
  "v_mfma_f32_16x16x32_bf16 v[72:75], v[112:115], v[88:91], v[72:75]\n\t" \
  "v_mfma_f32_16x16x32_bf16 v[76:79], v[112:115], v[92:95], v[76:79]\n\t" \
  "s_waitcnt lgkmcnt(0)\n\t" \
  "s_cbranch_scc1 1b\n\t" \
  "v_mfma_f32_16x16x32_bf16 v[0:3], v[132:135], v[116:119], v[0:3]\n\t" \
  "v_mfma_f32_16x16x32_bf16 v[16:19], v[136:139], v[116:119], v[16:19]\n\t" \
  "v_mfma_f32_16x16x32_bf16 v[32:35], v[140:143], v[116:119], v[32:35]\n\t" \
  "v_mfma_f32_16x16x32_bf16 v[48:51], v[144:147], v[116:119], v[48:51]\n\t" \
  "v_mfma_f32_16x16x32_bf16 v[64:67], v[148:151], v[116:119], v[64:67]\n\t" \
  "v_mfma_f32_16x16x32_bf16 v[4:7], v[132:135], v[120:123], v[4:7]\n\t" \
  "v_mfma_f32_16x16x32_bf16 v[20:23], v[136:139], v[120:123], v[20:23]\n\t" \
  "v_mfma_f32_16x16x32_bf16 v[36:39], v[140:143], v[120:123], v[36:39]\n\t" \
  "v_mfma_f32_16x16x32_bf16 v[52:55], v[144:147], v[120:123], v[52:55]\n\t" \
  "v_mfma_f32_16x16x32_bf16 v[68:71], v[148:151], v[120:123], v[68:71]\n\t" \
  "v_mfma_f32_16x16x32_bf16 v[8:11], v[132:135], v[124:127], v[8:11]\n\t" \
  "v_cvt_pk_bf16_f32 v80, v0, v1\n\t" \
  "v_cvt_pk_bf16_f32 v81, v2, v3\n\t" \
  "v_mfma_f32_16x16x32_bf16 v[24:27], v[136:139], v[124:127], v[24:27]\n\t" \
  "v_cvt_pk_bf16_f32 v82, v16, v17\n\t" \
  "v_cvt_pk_bf16_f32 v83, v18, v19\n\t" \
  "v_mfma_f32_16x16x32_bf16 v[40:43], v[140:143], v[124:127], v[40:43]\n\t" \
  "v_cvt_pk_bf16_f32 v84, v32, v33\n\t" \
  "v_cvt_pk_bf16_f32 v85, v34, v35\n\t" \
  "v_mfma_f32_16x16x32_bf16 v[56:59], v[144:147], v[124:127], v[56:59]\n\t" \
  "v_cvt_pk_bf16_f32 v86, v48, v49\n\t" \
  "v_cvt_pk_bf16_f32 v87, v50, v51\n\t" \
  "v_mfma_f32_16x16x32_bf16 v[72:75], v[148:151], v[124:127], v[72:75]\n\t" \
  "v_cvt_pk_bf16_f32 v112, v64, v65\n\t" \
  "v_cvt_pk_bf16_f32 v113, v66, v67\n\t" \
  "v_mfma_f32_16x16x32_bf16 v[12:15], v[132:135], v[128:131], v[12:15]\n\t" \
  "v_cvt_pk_bf16_f32 v88, v4, v5\n\t" \
  "v_cvt_pk_bf16_f32 v89, v6, v7\n\t" \
  "v_mfma_f32_16x16x32_bf16 v[28:31], v[136:139], v[128:131], v[28:31]\n\t" \
  "v_cvt_pk_bf16_f32 v90, v20, v21\n\t" \
  "v_cvt_pk_bf16_f32 v91, v22, v23\n\t" \
  "v_mfma_f32_16x16x32_bf16 v[44:47], v[140:143], v[128:131], v[44:47]\n\t" \
  "v_cvt_pk_bf16_f32 v92, v36, v37\n\t" \
  "v_cvt_pk_bf16_f32 v93, v38, v39\n\t" \
  "v_mfma_f32_16x16x32_bf16 v[60:63], v[144:147], v[128:131], v[60:63]\n\t" \
  "v_cvt_pk_bf16_f32 v94, v52, v53\n\t" \
  "v_cvt_pk_bf16_f32 v95, v54, v55\n\t" \
  "v_mfma_f32_16x16x32_bf16 v[76:79], v[148:151], v[128:131], v[76:79]\n\t" \
  "v_cvt_pk_bf16_f32 v114, v68, v69\n\t" \
  "v_cvt_pk_bf16_f32 v115, v70, v71\n\t" \
  "v_cvt_pk_bf16_f32 v96, v8, v9\n\t" \
  "v_cvt_pk_bf16_f32 v97, v10, v11\n\t" \
  "v_cvt_pk_bf16_f32 v98, v24, v25\n\t" \
  "v_cvt_pk_bf16_f32 v99, v26, v27\n\t" \
  "v_cvt_pk_bf16_f32 v100, v40, v41\n\t" \
  "v_cvt_pk_bf16_f32 v101, v42, v43\n\t" \
  "v_cvt_pk_bf16_f32 v102, v56, v57\n\t" \
  "v_cvt_pk_bf16_f32 v103, v58, v59\n\t" \
  "v_cvt_pk_bf16_f32 v156, v72, v73\n\t" \
  "v_cvt_pk_bf16_f32 v157, v74, v75\n\t" \
  "v_cvt_pk_bf16_f32 v104, v12, v13\n\t" \
  "v_cvt_pk_bf16_f32 v105, v14, v15\n\t" \
  "v_cvt_pk_bf16_f32 v106, v28, v29\n\t" \
  "v_cvt_pk_bf16_f32 v107, v30, v31\n\t" \
  "v_cvt_pk_bf16_f32 v108, v44, v45\n\t" \
  "v_cvt_pk_bf16_f32 v109, v46, v47\n\t" \
  "v_cvt_pk_bf16_f32 v110, v60, v61\n\t" \
  "v_cvt_pk_bf16_f32 v111, v62, v63\n\t" \
  "v_cvt_pk_bf16_f32 v158, v76, v77\n\t" \
  "v_cvt_pk_bf16_f32 v159, v78, v79\n\t" \
  "s_nop 1\n\t"
#define K1_LOOP_CLOB "v0", "v1", "v2", "v3", "v4", "v5", "v6", "v7", "v8", "v9", "v10", "v11", "v12", "v13", "v14", "v15", "v16", "v17", "v18", "v19", "v20", "v21", "v22", "v23", "v24", "v25", "v26", "v27", "v28", "v29", "v30", "v31", "v32", "v33", "v34", "v35", "v36", "v37", "v38", "v39", "v40", "v41", "v42", "v43", "v44", "v45", "v46", "v47", "v48", "v49", "v50", "v51", "v52", "v53", "v54", "v55", "v56", "v57", "v58", "v59", "v60", "v61", "v62", "v63", "v64", "v65", "v66", "v67", "v68", "v69", "v70", "v71", "v72", "v73", "v74", "v75", "v76", "v77", "v78", "v79", "v116", "v117", "v118", "v119", "v120", "v121", "v122", "v123", "v124", "v125", "v126", "v127", "v128", "v129", "v130", "v131", "v132", "v133", "v134", "v135", "v136", "v137", "v138", "v139", "v140", "v141", "v142", "v143", "v144", "v145", "v146", "v147", "v148", "v149", "v150", "v151"
#define PRE 3
#define SWAVE 7
#define TPRIO 3
#define XORD -1
#define GS 1
#define LS 2
#define SS 14
#define BWARM 1
#define FS2 4
#define FINWORD F_FIN
#define LAG 1
#define VPOS 1
#define TPAR 1
#define OUT_AUX 2
#define LDELAY 2
#define QOKW 9u
#define FINW 4u
#define PHIPRIO 2
#define GPRIO 3
#define G2(i) do {} while (0)
#define GSTAMP(i) do {} while (0)
#define FSTAMP(i) do {} while (0)
#define HSTAMP(i) STAMP(i)
#define TSTAMP(i) do {} while (0)
#define FSLEEP 1
#define BSLEEP 1
#define K1_ROT ((tt + 3 * (jb >> 3)) % 10)
constexpr u32 K1_SLOT = 36864, K1_W = 16384;
constexpr u32 K1_EX = 3 * K1_SLOT, K1_EXP = 4 * K1_SLOT;
constexpr u32 K1_LGT = 0;
constexpr u32 K1_PHK = 27648, K1_PHK_P = 336;
constexpr u32 K1_PHQ = 20480;
constexpr u32 K1_XCH = 73728;
constexpr u32 K1_WPHI = 156160;
constexpr u32 L_K01 = 160256;
constexpr u32 SL = 4 * REC;
constexpr u32 L_IMG = 0;
constexpr u32 L_RING = 102400;
constexpr u32 L_MRG = L_RING + SL;
constexpr u32 L_FLG = 162816;
constexpr u32 F_GBAR = L_FLG + 96, F_APUB = L_FLG + 100, F_AFREE = L_FLG + 104  , F_P1 = L_FLG + 112, F_PHB = L_FLG + 116,
              F_P2 = L_FLG + 120, F_KVOK = L_FLG + 124, F_BIMG = L_FLG + 128, F_QOK = L_FLG + 132, F_TAG = L_FLG + 136, F_FIN = L_FLG + 152, F_K01 = L_FLG + 156, F_AOK = L_FLG + 160, F_P3 = L_FLG + 164, F_XF = L_FLG + 176, L_GT = L_FLG + 256;
constexpr u32 LDS_BYTES = 163840;

__device__ __forceinline__ f32x16 attn_exact(rsrc_t rW, u32x4 qx, u32 q2lo, int lane) {
  const f32x16 zero16 = {0.f, 0.f, 0.f, 0.f, 0.f, 0.f, 0.f, 0.f, 0.f, 0.f, 0.f, 0.f, 0.f, 0.f, 0.f, 0.f};
  const int r32 = lane & 31, hh = lane >> 5;
  const u32x2 qz = {q2lo, 0u};
  float m = -INFINITY;
#pragma unroll 1
  for (u32 rec = 0; rec < 64; ++rec) {
    const u32x4 a1 = gl128(rW, lane * 16, WS_KVIM + rec * REC);
    const u32 a2 = gl32(rW, lane * 4, WS_KVIM + rec * REC + 1024);
    f32x16 s = mfma32(a1, qx, zero16);
    s = mfma32k8((u32x2){a2, 0u}, qz, s);
    m = fmaxf(m, max16(s));
  }
  m = xhalf_max(m);
  f32x16 O = zero16;
  const u32 av = 1280u + (u32)hh * 352u + (u32)(r32 <= 20 ? r32 : 21) * 16u;
#pragma unroll 1
  for (u32 t2 = 0; t2 < 64; ++t2) {
    const u32 rec = (t2 + 63) & 63;
    const u32x4 a1 = gl128(rW, lane * 16, WS_KVIM + rec * REC);
    const u32 a2 = gl32(rW, lane * 4, WS_KVIM + rec * REC + 1024);
    const u32x4 b1 = gl128(rW, av, WS_KVIM + t2 * REC), b2 = gl128(rW, av, WS_KVIM + t2 * REC + 704);
    f32x16 s = mfma32(a1, qx, zero16);
    s = mfma32k8((u32x2){a2, 0u}, qz, s);
#pragma unroll
    for (int r = 0; r < 16; ++r) s[r] = __builtin_amdgcn_exp2f(s[r] - m);
    O = smf(b1, lo8(s), O, 0xDDDD);
    O = smf(b2, hi8(s), O, 0xDDDD);
  }
  return O;
}

extern "C" __global__ void __launch_bounds__(768, 3)
gm_fused(const char* __restrict__ x, const char* __restrict__ wqkv, const char* __restrict__ wphi, const char* __restrict__ wq,
         const char* __restrict__ wk, const char* __restrict__ wv, const char* __restrict__ gw, const char* __restrict__ awo,
         const char* __restrict__ bwo, char* ws, char* __restrict__ out) {
  extern __shared__ __attribute__((aligned(16))) char smem[];
  ldsp lds = (ldsp)smem;
  const u32 lds0 = (u32)(uintptr_t)lds;
  const int w = __builtin_amdgcn_readfirstlane(threadIdx.x >> 6);
  const int bid = blockIdx.x, xcd = bid & 7, jb = bid >> 3;
  const rsrc_t rW = __builtin_amdgcn_make_buffer_rsrc(ws, 0, WS_END, 0x00020000);
  const f32x16 zero16 = {0.f, 0.f, 0.f, 0.f, 0.f, 0.f, 0.f, 0.f, 0.f, 0.f, 0.f, 0.f, 0.f, 0.f, 0.f, 0.f};
  asm volatile("" :: "s"(x), "s"(wqkv), "s"(wphi), "s"(wq), "s"(wk), "s"(wv), "s"(gw), "s"(awo), "s"(bwo), "s"(ws), "s"(out));
  STAMP_INIT();
  const int type = (xcd & 1) ^ TPAR, tt = ((xcd >> 1) & 1) * 8 + (jb & 7), hg = (xcd >> 2) * 4 + (jb >> 3);
  const u32x4 rsWS = mkrsrc(ws, WS_END);
  u32 vim[9];
  {
    if (w < 4) {
      const int cg = w >> 1, th = w & 1;
      u32x16 pa, pb; u32x4 pc, pd;
      {
        const int lane = lane_id(), g = lane >> 4, i16 = lane & 15;
        u32 aW = lds0 + K1_W + (u32)cg * 10240u + (u32)(4 * g + (i16 >> 2)) * 160u + (u32)(i16 & 3) * 40u;
        u32 aX0 = lds0 + (u32)(64 * th + i16) * 128u + (u32)((g ^ (i16 >> 1)) * 16), aX1 = aX0 ^ 64u;
        u32 cnt, inc;
        asm volatile(K1_LOOP_ASM
                     : "=&{v[80:95]}"(pa), "=&{v[96:111]}"(pb), "=&{v[112:115]}"(pc), "=&{v[156:159]}"(pd),
                       "+{v152}"(aX0), "+{v153}"(aX1), "+{v154}"(aW), [cnt] "=&s"(cnt), [inc] "=&s"(inc)
                     : [wrap] "s"(0u - 3u * K1_SLOT), [stp] "s"(K1_SLOT)
                     : "memory", "scc", K1_LOOP_CLOB);
      }
      HSTAMP(2); FSTAMP(2);
      const int lane = lane_id();
      const u32x4 qa[4] = {{pa[0], pa[1], pa[2], pa[3]}, {pa[8], pa[9], pa[10], pa[11]}, {pb[0], pb[1], pb[2], pb[3]}, {pb[8], pb[9], pb[10], pb[11]}};
      const u32x4 qb[4] = {{pa[4], pa[5], pa[6], pa[7]}, {pa[12], pa[13], pa[14], pa[15]}, {pb[4], pb[5], pb[6], pb[7]}, {pb[12], pb[13], pb[14], pb[15]}};
      const u32x2 qc[4] = {{pc[0], pc[1]}, {pc[2], pc[3]}, {pd[0], pd[1]}, {pd[2], pd[3]}};
      if (w == 3 && type == 0) {
        const u32 dst = WS_QB + (u32)(tt * 8 + hg) * 10240u;
#pragma unroll
        for (int n = 0; n < 4; ++n) {
          st128(rW, dst + n * 2560 + lane * 16, qa[n]); st128(rW, dst + n * 2560 + 1024 + lane * 16, qb[n]); st64(rW, dst + n * 2560 + 2048 + lane * 8, qc[n]);
        }
      } else {
        ldsp xw = lds + (cg ? K1_LGT : K1_XCH) + (u32)th * 10240u;
#pragma unroll
        for (int n = 0; n < 4; ++n) {
          *(LDSAS u32x4*)(xw + n * 2560 + lane * 16) = qa[n]; *(LDSAS u32x4*)(xw + n * 2560 + 1024 + lane * 16) = qb[n];
          *(LDSAS u32x2*)(xw + n * 2560 + 2048 + lane * 8) = qc[n];
        }
      }
      LBAR();
      HSTAMP(3);
      if (w == 3) {
        const u32x2 tag = ld64(lds + F_TAG);
        const u32 fq = WS_FQ + (u32)(tt * 8 + hg) * 128u;
        if (type == 0) { DRAIN(); st64(rW, lane == 0 ? fq : OOB, tag); FSTAMP(3); }
        else { flags_wait<1>(rW, fq, 1, tag); lds_st(lds + F_QOK, 1u); FSTAMP(4); }
      }
    } else {
      const int lane = lane_id(), g = lane >> 4, i16 = lane & 15;
      if (w >= 8) {
        __builtin_amdgcn_s_setprio(1);
        const int lw = w - 8, g1 = lw >> 1, hb = lw & 1;
        const u32x4 rsX = mkrsrc(x - 4096, SEQ * DM * 2 + 4096);
        const bool useq = g1 && !type;
        const u32x4 rsW = useq ? mkrsrc(wq - 4096, DM * DM * 2 + 4096) : mkrsrc(wqkv - 4096, DM * 3 * DM * 2 + 4096);
        const u32 pitch = useq ? 1280u : 3840u;
        const u32 col = (type ? (g1 ? 1280u : 640u) : 0u) + 80u * hg;
        u32 vx[4], vw[5];
        const u32 vx0 = 4096u + (u32)(lane >> 3) * 1280u + (u32)(((lane & 7) ^ (lane >> 4)) * 16);
#pragma unroll
        for (int i = 0; i < 4; ++i) vx[i] = (vx0 ^ (u32)((i & 1) * 64)) + (u32)i * 9216u;
#pragma unroll
        for (int j = 0; j < 5; ++j) {
          const u32 c = 64u * j + lane, rho = (c * 205u) >> 11, cc = c - rho * 10u;
          const u32 k = 8u * ((rho >> 2) & 3u) + 4u * (rho >> 4) + (rho & 3u);
          vw[j] = 4096u + k * pitch + cc * 16u - (u32)(j & 3) * 1024u;
        }
        const u32 sx0 = (u32)(128 * tt + 32 * lw) * 1280u, sw0 = 32u * hb * pitch + col * 2u;
        int ks = K1_ROT;
        auto issue = [&](int s) {
          const u32 base = lds0 + (u32)(s & 3) * K1_SLOT;
          u32 keep;
          asm volatile(
              "s_mov_b32 %0, m0\n\ts_mov_b32 m0, %[mx]\n\ts_nop 2\n\t"
              "buffer_load_dwordx4 %[x0], %[rx], %[sx] offen lds\n\t"
              "buffer_load_dwordx4 %[x1], %[rx], %[sx] offen offset:1024 lds\n\t"
              "buffer_load_dwordx4 %[x2], %[rx], %[sx] offen offset:2048 lds\n\t"
              "buffer_load_dwordx4 %[x3], %[rx], %[sx] offen offset:3072 lds\n\t"
              "s_mov_b32 m0, %[mw]\n\ts_nop 0\n\t"
              "buffer_load_dwordx4 %[w0], %[rw], %[sw] offen lds\n\t"
              "buffer_load_dwordx4 %[w1], %[rw], %[sw] offen offset:1024 lds\n\t"
              "buffer_load_dwordx4 %[w2], %[rw], %[sw] offen offset:2048 lds\n\t"
              "buffer_load_dwordx4 %[w3], %[rw], %[sw] offen offset:3072 lds\n\t"
              "s_add_u32 m0, %[mw], 4096\n\ts_nop 0\n\t"
              "buffer_load_dwordx4 %[w4], %[rw], %[sw] offen lds\n\t"
              "s_mov_b32 m0, %0"
              : "=&s"(keep)
              : [mx] "s"(base + (u32)lw * 4096u), [mw] "s"(base + K1_W + (u32)lw * 5120u),
                [rx] "s"(rsX), [rw] "s"(rsW), [sx] "s"(sx0 + (u32)ks * 128u), [sw] "s"(sw0 + (u32)ks * 64u * pitch),
                [x0] "v"(vx[0]), [x1] "v"(vx[1]), [x2] "v"(vx[2]), [x3] "v"(vx[3]), [w0] "v"(vw[0]), [w1] "v"(vw[1]), [w2] "v"(vw[2]), [w3] "v"(vw[3]),
                [w4] "v"(vw[4])
              : "memory", "scc");
          ks = ks == 9 ? 0 : ks + 1;
        };
        __builtin_amdgcn_s_sleep(LDELAY);
        issue(0);
        issue(1);
        WAIT_BAR(9);
        HSTAMP(2); FSTAMP(2);
#pragma unroll 1
        for (int s = 1; s < 9; ++s) {
          issue(s + 1);
          WAIT_BAR(9);
        }
        auto prefill = [&]() {
          const u32 o = (u32)lw * 8192u, vl = (u32)lane * 16u;
          dma4(rsWS, vl, WS_KVIM + o, lds0 + L_RING + o); dma4(rsWS, vl, WS_KVIM + o + 4096u, lds0 + L_RING + o + 4096u);
        };
        WAIT_BAR(0);
        lds_wait_ge<1>(lds + F_KVOK, 2);
        prefill();
      } else {
        const int v4 = w - 4, tok0 = 128 * tt + 16 * hg;
        __builtin_amdgcn_s_setprio(3);
        {
          const u32x4 rsE = mkrsrc((type ? wv : wk) + v4 * 6400, 6400);
          const u32 la = lds0 + K1_EX + (u32)v4 * 7168u;
#pragma unroll 1
          for (u32 c = 0; c < 7; ++c) dma1(rsE, (u32)lane * 16u, c * 1024u, la + c * 1024u);
          u32x4 xf[5], gf[5];
#pragma unroll
          for (int s = 0; s < 5; ++s) {
            xf[s] = *(const u32x4*)(x + (size_t)(tok0 + i16) * 1280 + v4 * 320 + s * 64 + g * 16);
            gf[s] = *(const u32x4*)(gw + v4 * 320 + s * 64 + g * 16);
          }
          u64 e = 0;
          if (w == 7) e = __hip_atomic_load((const gu64*)(ws + WS_EPOCH), __ATOMIC_RELAXED, __HIP_MEMORY_SCOPE_AGENT) + 0x9E3779B97F4A7C15ull;
          __builtin_amdgcn_s_setprio(0);
#pragma unroll
          for (int i = 0; i < 9; ++i) {
            const u32 c = 64u * (u32)(v4 + 3 * i) + lane, rho = (c * 6554u) >> 16, cc = c - rho * 10u;
            const u32 U = 4u * (rho >> 5) + ((rho >> 2) & 3u), u = (rho >> 4) & 1u, q = rho & 3u;
            const u32 hp = (U * 205u) >> 10, un = U - hp * 5u;
            const u32 row = 40u * hp + q + ((un < 4u) ? (un >> 1) * 20u + 4u * (un & 1u) + 8u * u : u * 20u + 16u);
            vim[i] = row * 1280u + cc * 16u;
            PIN(vim[i]);
          }
          __builtin_amdgcn_s_setprio(3);
          DRAIN();
          if (w == 7) lds_st(lds + L_FLG + lane * 4, lane == 34 ? (u32)e : (lane == 35 ? (u32)(e >> 32) : 0u));
          ldsp pe = lds + K1_EX + (u32)v4 * 7168u + (u32)(8 * g + (i16 >> 2)) * 40u + (u32)(i16 & 3) * 8u;
          f32x4 c0 = {0.f, 0.f, 0.f, 0.f}, c1 = c0, c2 = c0;
#pragma unroll
          for (int s = 0; s < 5; ++s) {
            const u32x4 w0 = trd2(pe + s * 1280, pe + s * 1280 + 160), w1 = trd2(pe + s * 1280 + 32, pe + s * 1280 + 192);
            if (type) { c0 = mfma16(xf[s], w0, c0); c1 = mfma16(xf[s], w1, c1); }
            else { c0 = mfma16(w0, xf[s], c0); c1 = mfma16(w1, xf[s], c1); }
            c2 = mfma16(gf[s], xf[s], c2);
          }
          ldsp pp = lds + K1_EXP + (u32)v4 * 1024u + lane * 16;
          *(LDSAS f32x4*)(pp) = c0; *(LDSAS f32x4*)(pp + 4096) = c1; *(LDSAS f32x4*)(pp + 8192) = c2;
        }
        LBAR();
        if (v4 < 3) {
          ldsp pp = lds + K1_EXP + (u32)v4 * 4096u + lane * 16;
          const f32x4 e0 = (*(LDSAS f32x4*)(pp) + *(LDSAS f32x4*)(pp + 1024)) + (*(LDSAS f32x4*)(pp + 2048) + *(LDSAS f32x4*)(pp + 3072));
          const float ks_ = type ? 1.f : QSCALE;
          const u32x2 ev = {pk2(e0[0] * ks_, e0[1] * ks_), pk2(e0[2] * ks_, e0[3] * ks_)};
          const int tile = tok0 >> 5, k0 = tok0 & 31;
          if (v4 == 2) {
            st32(rW, (type == 0 && g == 0) ? WS_GATE + (u32)(tok0 + i16) * 4u : OOB, __float_as_uint(__builtin_amdgcn_rcpf(1.f + __builtin_amdgcn_exp2f(-e0[0] * LOG2E))));
          } else if (type == 0) {
            const u32 rec = WS_KVIM + (u32)((tile + 63) & 63) * REC;
            if (v4 == 0) st64(rW, rec + (g >> 1) * 512 + (k0 + i16) * 16 + (g & 1) * 8, ev);
            else { const u32 o = g ? OOB : rec + 1024 + (k0 + i16) * 4; st32(rW, o, ev[0]); st32(rW, o + 128, ev[1]); }
          } else {
            const int d = 16 * v4 + i16, k32 = k0 + 4 * g;
            const u32x2 val = d == 20 ? (u32x2){0x3F803F80u, 0x3F803F80u} : (d == 21 ? (u32x2){0u, 0u} : ev);
            st64(rW, d <= 21 ? WS_KVIM + (u32)tile * REC + 1280 + (k32 >> 3) * 352 + d * 16 + (k32 & 7) * 2 : OOB, val);
          }
        }
        LBAR();
        {
          const u32 be = (u32)(v4 * 64 + lane) * 16u;
          dma1(mkrsrc(wphi, NH * 400 * 2), be < 3200u ? be : OOB, (u32)hg * 3200u, lds0 + K1_WPHI + (u32)v4 * 1024u);
        }
        LBAR();
        WAIT_BAR(0);
        if (w == 7) {
          const u32x2 tag = ld64(lds + F_TAG);
          st64(rW, lane < 8 ? WS_F1 + (u32)lane * 2048u + (u32)bid * 8u : OOB, tag);
          FSTAMP(3);
          LBAR(); LBAR(); LBAR(); LBAR(); LBAR();
          const u32 fo = WS_F1 + (u32)xcd * 2048u;
          u32x4 fa = gl128(rW, lane * 16, fo), fb = gl128(rW, lane * 16, fo + 1024);
          LBAR();
          PIN(fa); PIN(fb);
          const u32 dd = (fa[0] ^ tag[0]) | (fa[1] ^ tag[1]) | (fa[2] ^ tag[0]) | (fa[3] ^ tag[1]) | (fb[0] ^ tag[0]) | (fb[1] ^ tag[1]) | (fb[2] ^ tag[0]) | (fb[3] ^ tag[1]);
          if (!__all(dd == 0u)) flags_wait256<2>(rW, fo, tag);
          asm volatile("" ::: "memory");
          FSTAMP(4);
          const u32x4 ka = gl128(rW, lane * 16, WS_KVIM + 63 * REC);
          const u32 kc = gl32(rW, lane * 4, WS_KVIM + 63 * REC + 1024);
          const u32 gtv = gl32(rW, lane * 4, WS_GATE + (u32)(2 * tt + type) * 256u);
          u32 wrm = gl32(rW, lane < 29 ? (u32)(jb * 29 + lane) * 128u : OOB, WS_KVIM + 5 * SL);
          asm volatile("" ::: "memory");
          lds_st(lds + F_KVOK, 2u);
          if (bid == 0 && lane == 0)
            __hip_atomic_store((gu64*)(ws + WS_EPOCH), (u64)tag[0] | ((u64)tag[1] << 32), __ATOMIC_RELAXED, __HIP_MEMORY_SCOPE_AGENT);
          *(LDSAS u32x4*)(lds + L_K01 + lane * 16) = ka; *(LDSAS u32*)(lds + L_K01 + 1024 + lane * 4) = kc;
          *(LDSAS u32*)(lds + L_GT + lane * 4) = gtv;
          PIN(wrm);
        } else {
          LBAR(); LBAR(); LBAR(); LBAR();
          LBAR(); LBAR();
        }
      }
      __builtin_amdgcn_s_setprio(PHIPRIO);
      const int j8 = w - 4;
      const u32 aP = K1_WPHI + (((i16 & 3) == g) ? (u32)g * 800u + (u32)(i16 >> 2) * 40u : 3200u);
      u32x4 a0[5], a1[5]; u32x2 a2[5];
#pragma unroll
      for (int T = 0; T < 5; ++T) {
        a0[T] = trd2(lds + aP + T * 8, lds + aP + T * 8 + 160);
        a1[T] = trd2(lds + aP + T * 8 + 320, lds + aP + T * 8 + 480);
        a2[T] = trd(lds + aP + T * 8 + 640);
      }
      LBAR();
      HSTAMP(3);
      ldsp xr = lds + K1_XCH + (u32)j8 * 2560u;
      const u32x4 bq0 = ld128(xr + lane * 16), bq1 = ld128(xr + 1024 + lane * 16);
      const u32x2 bq2 = ld64(xr + 2048 + lane * 8);
      if (w >= 8) {
        DRAIN();
        if (lds_arrive(lds + F_K01) == 3 && lane < 8) lds_st(lds + L_FLG + 32 + lane * 4, PRE);
      }
      f32x4 pq[5];
#pragma unroll
      for (int T = 0; T < 5; ++T) pq[T] = mfma16(a0[T], bq0, (f32x4){0.f, 0.f, 0.f, 0.f});
#pragma unroll
      for (int T = 0; T < 5; ++T) pq[T] = mfma16(a1[T], bq1, pq[T]);
#pragma unroll
      for (int T = 0; T < 5; ++T) pq[T] = mfma16((u32x4){a2[T][0], a2[T][1], 0u, 0u}, (u32x4){bq2[0], bq2[1], 0u, 0u}, pq[T]);
      float mx = fmaxf(pq[0][0], pq[0][1]), mn = fminf(pq[0][0], pq[0][1]);
      mx = fmaxf(fmaxf(mx, pq[0][2]), pq[0][3]); mn = fminf(fminf(mn, pq[0][2]), pq[0][3]);
#pragma unroll
      for (int T = 1; T < 5; ++T) {
        mx = fmaxf(fmaxf(mx, pq[T][0]), pq[T][1]); mx = fmaxf(fmaxf(mx, pq[T][2]), pq[T][3]);
        mn = fminf(fminf(mn, pq[T][0]), pq[T][1]); mn = fminf(fminf(mn, pq[T][2]), pq[T][3]);
      }
      const float cp = -mx * LOG2E, cn = mn * LOG2E;
      f32x4 ep[5], en[5];
#pragma unroll
      for (int T = 0; T < 5; ++T) { ep[T] = pq[T] * LOG2E + cp; en[T] = pq[T] * (-LOG2E) + cn; PIN(ep[T]); PIN(en[T]); }
#pragma unroll
      for (int T = 0; T < 5; ++T)
#pragma unroll
        for (int i = 0; i < 4; ++i) { ep[T][i] = __builtin_amdgcn_exp2f(ep[T][i]); en[T][i] = __builtin_amdgcn_exp2f(en[T][i]); }
      const f32x4 sp = (ep[0] + ep[1]) + (ep[2] + ep[3]) + ep[4], sn = (en[0] + en[1]) + (en[2] + en[3]) + en[4];
      const float rp = __builtin_amdgcn_rcpf((sp[0] + sp[1]) + (sp[2] + sp[3])), rn = __builtin_amdgcn_rcpf((sn[0] + sn[1]) + (sn[2] + sn[3]));
      u32 ph[20];
#pragma unroll
      for (int T = 0; T < 5; ++T) {
        const f32x4 a = ep[T] * rp, b = en[T] * rn;
        ph[2 * T] = pk2(a[0], a[1]); ph[2 * T + 1] = pk2(a[2], a[3]);
        ph[10 + 2 * T] = pk2(b[0], b[1]); ph[11 + 2 * T] = pk2(b[2], b[3]);
      }
      const int tl = 16 * j8 + i16;
      if (type == 0) {
        ldsp dst = lds + K1_PHQ + (u32)(4 * g + (tl >> 5)) * 2560u + (tl & 31) * 16;
#pragma unroll
        for (int c = 0; c < 5; ++c) *(LDSAS u32x4*)(dst + c * 512) = (u32x4){ph[4 * c], ph[4 * c + 1], ph[4 * c + 2], ph[4 * c + 3]};
      } else {
        ldsp dst = lds + K1_PHK + (u32)tl * K1_PHK_P + (u32)g * 80u;
#pragma unroll
        for (int c = 0; c < 5; ++c) *(LDSAS u32x4*)(dst + 16 * c) = (u32x4){ph[4 * c], ph[4 * c + 1], ph[4 * c + 2], ph[4 * c + 3]};
        lds_arrive(lds + F_PHB);
        lds_wait_ge<1>(lds + F_PHB, 8);
        const int hl = j8 >> 1, dt = j8 & 1;
        const u32 trow = (u32)(8 * g + (i16 >> 2));
        ldsp pa = lds + K1_PHK + trow * K1_PHK_P + (u32)hl * 80u + (u32)(i16 & 3) * 8u;
        const u32 vrow = (u32)(hl * 16) + (trow & 15u), vq = (u32)(i16 & 3);
        ldsp pb = lds + K1_LGT + (u32)(g >> 1) * 2560u + (dt ? 2048u + vrow * 8u : (vq >> 1) * 1024u + vrow * 16u + (vq & 1u) * 8u);
        const u32 vstep = dt ? 32u : 64u;
        f32x4 kv[3];
#pragma unroll
        for (int ft = 0; ft < 3; ++ft) kv[ft] = (f32x4){0.f, 0.f, 0.f, 0.f};
#pragma unroll
        for (int ks = 0; ks < 4; ++ks) {
          u32x4 bv = trd2(pb + ks * 5120, pb + ks * 5120 + vstep);
          if (dt && i16 == 4) bv = (u32x4){0x3F803F80u, 0x3F803F80u, 0x3F803F80u, 0x3F803F80u};
#pragma unroll
          for (int ft = 0; ft < 3; ++ft) {
            u32x4 av = trd2(pa + ks * 32 * K1_PHK_P + ft * 32, pa + (ks * 32 + 4) * K1_PHK_P + ft * 32);
            kv[ft] = mfma16(av, bv, kv[ft]);
          }
        }
        const u32 kb = WS_KVP + (u32)((hg * 16 + tt) * 4 + hl) * 3360u;
        const u32 ko = dt ? (i16 < 5 ? kb + 2560u + (u32)(g * 5 + i16) * 16u : OOB) : kb + (u32)lane * 16u, kst = dt ? 320u : 1024u;
        st128(rW, ko, __builtin_bit_cast(u32x4, kv[0])); st128(rW, ko + kst, __builtin_bit_cast(u32x4, kv[1]));
        st128(rW, g < 2 ? ko + 2 * kst : OOB, __builtin_bit_cast(u32x4, kv[2]));
      }
      lds_arrive(lds + F_P2);
      if (type) {
        const u32x2 tag = ld64(lds + F_TAG);
        DRAIN();
        if (lds_arrive(lds + F_P1) == 7) { st64(rW, lane == 0 ? WS_F2 + (u32)(hg * 16 + tt) * 8u : OOB, tag); GSTAMP(3); }
      }
      HSTAMP(4);
    }
  }

  if (w >= 8) {
    __builtin_amdgcn_s_setprio(GPRIO);
    const int hw = w - 8, kh = hw >> 1, t64 = xcd * 4 + (jb & 3), cgp = jb >> 2;
    const u32x2 tag = ld64(lds + F_TAG);
    lds_wait_ge<2>(lds + F_P2, 8);
    {
      const int lane = lane_id(), g = lane >> 4, i16 = lane & 15, r32 = lane & 31, hh = lane >> 5;
      u32x4 p0[4], p1[4], p2[4]; u32 gt[4];
      if (type) {
        const u32 it = (u32)(tt * 53 + hw * 14 + i16);
        const bool ok = i16 < 14 && hw * 14 + i16 < 53 && it < 840u;
        const u32 hl = (it * 4994u) >> 20, e = it - hl * 210u;
        const u32 e1 = e - 160u, ft1 = (e1 * 205u) >> 12, r1 = e1 - ft1 * 20u, g1 = (r1 * 205u) >> 10;
        const u32 d = e < 160u ? (e & 15u) : 16u + r1 - g1 * 5u, f = e < 160u ? (e >> 6) * 16u + ((e >> 4) & 3u) * 4u : ft1 * 16u + g1 * 4u;
        const u32 vo = ok ? it * 16u + (u32)g * 4u * 13440u : OOB, pb = WS_KVP + (u32)hg * 16u * 13440u;
        u32 dst = (ok && g == 0) ? WS_KVT + (u32)(4 * hg + hl) * 1680u + d * 80u + f * 2u : OOB;
        PIN(dst);
        G2(2);
        if (hw == 0) { flags_wait<GS>(rW, WS_F2 + (u32)hg * 128u, 16, tag); lds_st(lds + F_GBAR, 1u); }
        else lds_wait_ge<LS>(lds + F_GBAR, 1);
        G2(3);
        f32x4 a = (__builtin_bit_cast(f32x4, gl128(rW, vo, pb)) + __builtin_bit_cast(f32x4, gl128(rW, vo, pb + 13440))) +
                  (__builtin_bit_cast(f32x4, gl128(rW, vo, pb + 2 * 13440)) + __builtin_bit_cast(f32x4, gl128(rW, vo, pb + 3 * 13440)));
#pragma unroll
        for (int i = 0; i < 4; ++i) { a[i] += __shfl_xor(a[i], 16); a[i] += __shfl_xor(a[i], 32); }
        st64(rW, dst, (u32x2){pk2(a[0], a[1]), pk2(a[2], a[3])});
        G2(4);
        DRAIN();
        G2(5);
        if (lds_arrive(lds + F_APUB) == 3) { st64(rW, lane == 0 ? WS_F3 + (u32)(hg * 16 + tt) * 8u : OOB, tag); GSTAMP(4); }
      } else {
#pragma unroll
        for (int t4 = 0; t4 < 4; ++t4) {
          ldsp pk = lds + K1_PHQ + (u32)(4 * hw + t4) * 2560u;
          p0[t4] = ld128(pk + lane * 16); p1[t4] = ld128(pk + 1024 + lane * 16); p2[t4] = ld128(pk + 2048 + r32 * 16);
          gt[t4] = gl32(rW, r32 * 4, WS_GATE + (u32)(128 * tt + 32 * t4) * 4u);
          PIN(p0[t4]); PIN(p1[t4]); PIN(p2[t4]);
        }
        lds_arrive(lds + F_P3);
        if (hw == 0) { lds_wait_ge<4>(lds + F_QOK, 4); lds_arrive(lds + F_P3); }
      }
      if (type == 0) {
        if (hw == 0) { flags_wait<GS>(rW, WS_F3 + (u32)hg * 128u, 16, tag); lds_st(lds + F_GBAR, 1u); }
        if (hw >= 2) G2(2);
        lds_wait_ge<LS>(lds + F_GBAR, 1);
        if (hw >= 2) G2(3);
        const u32 ao = (u32)(r32 < 20 ? r32 : 20) * 80u + (u32)hh * 16u, ab = WS_KVT + (u32)(4 * hg + hw) * 1680u;
        const u32x4 a0 = gl128(rW, ao, ab), a1 = gl128(rW, ao, ab + 32), a2 = gl128(rW, hh ? OOB : ao, ab + 64);
        const u32 vo0 = WS_OFR + (u32)(8 * tt + (r32 >> 4)) * 20480u + (r32 & 15) * 16 + (u32)(2 * hg + (hw >> 1)) * 1280u;
        const u32 vo1 = vo0 + hh * 256 + (hw & 1) * 512, vo2 = hh ? OOB : vo0 + 1024 + (hw & 1) * 8;
        f32x16 N[4];
#pragma unroll
        for (int t4 = 0; t4 < 4; ++t4) N[t4] = mfma32(a0, p0[t4], zero16);
#pragma unroll
        for (int t4 = 0; t4 < 4; ++t4) N[t4] = mfma32(a1, p1[t4], N[t4]);
#pragma unroll
        for (int t4 = 0; t4 < 4; ++t4) N[t4] = mfma32(a2, p2[t4], N[t4]);
#pragma unroll
        for (int t4 = 0; t4 < 4; ++t4) {
          const float sa = __uint_as_float(gt[t4]) * __builtin_amdgcn_rcpf(N[t4][12] + 1e-6f);
          st128(rW, vo1 + t4 * 40960, (u32x4){pk2(N[t4][0] * sa, N[t4][1] * sa), pk2(N[t4][2] * sa, N[t4][3] * sa), pk2(N[t4][4] * sa, N[t4][5] * sa), pk2(N[t4][6] * sa, N[t4][7] * sa)});
          st64(rW, vo2 + t4 * 40960, (u32x2){pk2(N[t4][8] * sa, N[t4][9] * sa), pk2(N[t4][10] * sa, N[t4][11] * sa)});
        }
        if (hw >= 2) G2(5);
        DRAIN();
        if (hw >= 2) G2(6);
        if (lds_arrive(lds + F_APUB) == 3) { st64(rW, lane == 0 ? WS_FLGA + (u32)(tt * 16 + hg) * 8u : OOB, tag); GSTAMP(5); }
      }
      if (hw == 0) { flags_wait<GS>(rW, WS_FLGA + (u32)(t64 >> 1) * 128u, 8, tag); lds_arrive(lds + F_AOK); }
      HSTAMP(6);
    }
    f32x16 ac0 = zero16, ac1 = zero16; f32x8 ac2 = {0.f, 0.f, 0.f, 0.f, 0.f, 0.f, 0.f, 0.f};
#pragma unroll 1
    for (int part = 0; part < 2; ++part) {
      if (part == 0) {
        lds_wait_ge<LS>(lds + F_AOK, 4u); GSTAMP(6); HSTAMP(5);
      } else {
        HSTAMP(7);
        lds_arrive(lds + F_AFREE + 4);
        lds_wait_ge<8>(lds + F_BIMG, 3u); GSTAMP(2);
        lds_wait_ge<FS2>(lds + F_FIN, 4u);
        TSTAMP(3);
        if (hw == 0) { flags_wait<BSLEEP>(rW, WS_FLGB + (u32)t64 * 256u, 32, tag); lds_st(lds + F_FIN, 5u); }
        else lds_wait_ge<0>(lds + F_FIN, 5u);
        TSTAMP(4); FSTAMP(7);
      }
      const int ln = lane_id();
      u32 s0 = WS_OFR + (u32)(part * 2621440 + (4 * t64 + 2 * (hw & 1) + kh) * 20480 + kh * 10240), s1 = s0 + (kh ? 0u - 20480u : 20480u);
      asm volatile(TILE_GEMM_ASM
                   : "+{v[0:15]}"(ac0), "+{v[16:31]}"(ac1), "+{v[32:39]}"(ac2), [s0] "+s"(s0), [s1] "+s"(s1)
                   : "{v140}"((u32)ln * 16u), "{v141}"(lds0 + L_IMG + (u32)(4 * (ln >> 4) + ((ln & 15) >> 2)) * 160u + (u32)(ln & 3) * 40u + (u32)kh * 51200u), "{v142}"(lds0 + F_AFREE), "{v143}"(1u), [rs] "s"(rsWS)
                   : "memory", "scc", TILE_GEMM_CLOB);
    }
    TSTAMP(5);
    {
      const int ln = lane_id();
      f32x4 acc[5][2];
#pragma unroll
      for (int n = 0; n < 2; ++n) {
        acc[0][n] = (f32x4){ac0[4 * n], ac0[4 * n + 1], ac0[4 * n + 2], ac0[4 * n + 3]}; acc[1][n] = (f32x4){ac0[8 + 4 * n], ac0[9 + 4 * n], ac0[10 + 4 * n], ac0[11 + 4 * n]};
        acc[2][n] = (f32x4){ac1[4 * n], ac1[4 * n + 1], ac1[4 * n + 2], ac1[4 * n + 3]}; acc[3][n] = (f32x4){ac1[8 + 4 * n], ac1[9 + 4 * n], ac1[10 + 4 * n], ac1[11 + 4 * n]};
        acc[4][n] = (f32x4){ac2[4 * n], ac2[4 * n + 1], ac2[4 * n + 2], ac2[4 * n + 3]};
      }
      ldsp xb = lds + L_RING + 3 * SL;
      ldsp xw = xb + (u32)hw * 5120u + ln * 16, xr = xb + (u32)(hw ^ 2) * 5120u + ln * 16;
#pragma unroll
      for (int T = 0; T < 5; ++T) *(LDSAS f32x4*)(xw + T * 1024) = acc[T][1];
      asm volatile("" ::: "memory");
      lds_st(lds + F_XF + hw * 4, 1u);
      f32x4 pv[5];
#pragma unroll 1
      for (u32 it = 0; it < SPIN_CAP; ++it) {
        asm volatile("" ::: "memory");
        const u32 f = lds_ld(lds + F_XF + (hw ^ 2) * 4);
        asm volatile("" ::: "memory");
#pragma unroll
        for (int T = 0; T < 5; ++T) pv[T] = *(LDSAS f32x4*)(xr + T * 1024);
        if (__builtin_amdgcn_readfirstlane(f)) break;
      }
      TSTAMP(6);
      u32 o[10];
#pragma unroll
      for (int T = 0; T < 5; ++T) {
        const f32x4 v = acc[T][0] + pv[T];
        o[2 * T] = pk2(v[0], v[1]); o[2 * T + 1] = pk2(v[2], v[3]);
      }
      const rsrc_t rOut = __builtin_amdgcn_make_buffer_rsrc(out, 0, SEQ * DM * 2, 0x00020000);
      const int dst = (64 * t64 + 32 * (hw & 1) + 16 * kh + (ln & 15)) * 1280 + cgp * 160 + (ln >> 4) * 40;
      __builtin_amdgcn_raw_buffer_store_b128((u32x4){o[0], o[1], o[2], o[3]}, rOut, dst, 0, OUT_AUX);
      __builtin_amdgcn_raw_buffer_store_b128((u32x4){o[4], o[5], o[6], o[7]}, rOut, dst + 16, 0, OUT_AUX);
      __builtin_amdgcn_raw_buffer_store_b64((u32x2){o[8], o[9]}, rOut, dst + 32, 0, OUT_AUX);
    }
    STAMP_FLUSH();
    return;
  }

  if (w >= 4) {
    __builtin_amdgcn_s_setprio(3);
    if (w < 7) {
      const int v4 = w - 4, cgp = jb >> 2;
#pragma unroll 1
      for (int part = 0; part < 2; ++part) {
        if (part == 0) lds_wait_ge<4>(lds + (type ? F_P2 : F_P3), type ? 8u : 5u);
        if (part) HSTAMP(7); else HSTAMP(5);
        const u32x4 rsO = mkrsrc(part ? bwo : awo, DM * DM * 2);
#pragma unroll 1
        for (u32 pi = 0; pi < 4; ++pi) {
          const u32 per = (pi >> 1) | ((pi & 1) << 1);
          if (part && !(pi & 1)) lds_wait_ge<2>(lds + F_AFREE + (pi >> 1) * 4, 4u);
          const u32 so = per * 204800u + (u32)cgp * 160u, la = lds0 + L_IMG + (per * 25u + (u32)v4) * 1024u;
#pragma unroll
          for (int i = 0; i < 8; ++i) dma1(rsO, vim[i], so, la + i * 3072);
          if (v4 == 0) dma1(rsO, vim[8], so, la + 24576);
        }
        DRAIN();
        lds_arrive(lds + (part ? F_BIMG : F_AOK));
        if (part == 0) HSTAMP(6);
        if (part == 0) {
          const int ln = lane_id();
          const u32 l0 = (u32)jb * 200u + (u32)v4 * 64u + ln;
          u32 dummy;
          asm volatile("s_nop 4\n\tbuffer_load_dword %0, %1, %2, 0 offen\n\tbuffer_load_dword %0, %3, %2, 0 offen\n\ts_waitcnt vmcnt(0)"
                       : "=&v"(dummy) : "v"(l0 * 128u), "s"(mkrsrc(bwo, DM * DM * 2)), "v"((v4 == 0 && ln < 8) ? (l0 + 192u) * 128u : OOB) : "memory");
        }
      }
    } else {
      const int ln = lane_id();
      const u32 vl = (u32)ln * 16u;
      u32 s = PRE, slot = PRE % 5, ps = PRE;
      lds_wait_ge<2>(lds + L_FLG + 32, PRE);
#pragma unroll 1
      for (u32 it = 0; it < SPIN_CAP && ps < 16; ++it) {
        const u32 fl = lds_ld(lds + L_FLG + (ln & 3) * 4);
        const u32 ns = s;
        if (s < 16 && __all(fl + 4 >= s)) {
          const u32 so = WS_KVIM + s * SL, la = lds0 + L_RING + slot * SL;
          dma4(rsWS, vl, so, la); dma4(rsWS, vl, so + 4096u, la + 4096u); dma2h(rsWS, vl, so + 8192u, la + 8192u);
          asm volatile("s_waitcnt vmcnt(11)" ::: "memory");
          ++s; slot = slot == 4 ? 0 : slot + 1;
        } else {
          DRAIN();
          if (ps == ns) __builtin_amdgcn_s_sleep(SS);
        }
        if (ps != ns) { ps = ns; if (ln < 4) lds_st(lds + L_FLG + 32 + ln * 4, ps); }
      }
    }
    STAMP_FLUSH();
    return;
  }
  const int head = 4 * hg + w, t64 = 2 * tt + type;
  u32x4 q1[2]; u32x2 q2[2];
  f32x16 O0, O1;
  const int lane = lane_id(), r32 = lane & 31, hh = lane >> 5;
  float og[2]; u32x2 tag; u32 vo1, vo2;
  __builtin_amdgcn_s_setprio(3);
  {
    const u32 qo1 = (u32)(r32 >> 4) * 2560u + (u32)hh * 1024u + (u32)(w * 16 + (r32 & 15)) * 16u;
    const u32 qo2 = (u32)(r32 >> 4) * 2560u + 2048u + (u32)(w * 16 + (r32 & 15)) * 8u + (u32)hh * 4u;
    if (type) {
      lds_wait_ge<1>(lds + F_QOK, 1);
#pragma unroll
      for (int j = 0; j < 2; ++j) {
        const u32 qrow = WS_QB + (u32)(tt * 8 + hg) * 10240u + (u32)j * 5120u;
        q1[j] = gl128(rW, qo1, qrow);
        q2[j] = (u32x2){gl32(rW, qo2, qrow), 0u};
      }
    } else {
#pragma unroll
      for (int j = 0; j < 2; ++j) {
        q1[j] = ld128(lds + K1_LGT + j * 5120 + qo1);
        q2[j] = (u32x2){*(LDSAS u32*)(lds + K1_LGT + j * 5120 + qo2), 0u};
      }
      PIN(q1[0]); PIN(q1[1]); PIN(q2[0]); PIN(q2[1]);
      lds_arrive(lds + F_QOK);
    }
    u32x4 k1 = ld128(lds + L_K01 + lane * 16);
    u32x2 k2 = {*(LDSAS u32*)(lds + L_K01 + 1024 + lane * 4), hh ? 0u : 0x00003F80u};
    f32x16 S0, S1, S2, S3;
#pragma unroll
    for (int j = 0; j < 2; ++j) {
      f32x16 t = mfma32(k1, q1[j], zero16);
      t = mfma32k8(k2, q2[j], t);
      const float m = xhalf_max(max16(t));
      q2[j][1] = hh ? 0u : (pk2(-(m + 60.f), 0.f) & 0xffffu);
    }
    S0 = mfma32(k1, q1[0], zero16);
    S0 = mfma32k8(k2, q2[0], S0);
    u32x4 vlo, vhi;
    const u32 rb = lds0 + L_RING;
    u32 aK1 = rb + (u32)lane * 16u, aK2 = rb + 1024u + (u32)lane * 4u;
    u32 aV = rb + 1280u + (u32)hh * 352u + (u32)(r32 <= 20 ? r32 : 21) * 16u;
    u32 aF = lds0 + L_FLG + (u32)w * 4u, tmp;
    og[0] = 1.f - *(LDSAS float*)(lds + L_GT + r32 * 4); og[1] = 1.f - *(LDSAS float*)(lds + L_GT + 128 + r32 * 4);
    tag = ld64(lds + F_TAG);
    const u32 vo = WS_OFR + 2621440u + (u32)(4 * t64 + (r32 >> 4)) * 20480u + (r32 & 15) * 16 + (head >> 1) * 1280;
    vo1 = vo + hh * 256 + (head & 1) * 512; vo2 = hh ? OOB : vo + 1024 + (head & 1) * 8;
    PIN(og[0]); PIN(og[1]); PIN(tag); PIN(vo1); PIN(vo2);
    HSTAMP(5); FSTAMP(5);
    u32 cnt, inc, slot, rdy;
    asm volatile(ATTN_LOOP_ASM
                 : "=&{v[0:15]}"(O0), "=&{v[16:31]}"(O1), "+{v[32:47]}"(S0), "=&{v[48:63]}"(S1), "=&{v[64:79]}"(S2), "=&{v[80:95]}"(S3),
                   "+{v[108:111]}"(k1), "+{v[112:113]}"(k2), "=&{v[116:119]}"(vlo), "=&{v[120:123]}"(vhi),
                   "+{v124}"(aK1), "+{v125}"(aK2), "+{v126}"(aV), "=&{v115}"(tmp), [cnt] "=&s"(cnt), [inc] "=&s"(inc), [slot] "=&s"(slot), [rdy] "=&s"(rdy)
                 : "{v[96:99]}"(q1[0]), "{v[100:103]}"(q1[1]), "{v[104:105]}"(q2[0]), "{v[106:107]}"(q2[1]), "{v114}"(0xDDDD), "{v127}"(aF),
                   [wrap] "s"(0u - 4u * SL), [stg] "s"(SL)
                 : "memory", "scc");
  }
  HSTAMP(6); TSTAMP(2); FSTAMP(6);
#pragma unroll
  for (int j = 0; j < 2; ++j) {
    f32x16 O = j ? O1 : O0;
    u32x4 r1; u32x2 r2;
#pragma unroll 1
    for (int pass = 0; pass < 2; ++pass) {
      const float l = from_hi(O[8]), sb = og[j] * __builtin_amdgcn_rcpf(l);
      const f32x4 y0 = (f32x4){O[0], O[1], O[2], O[3]} * sb, y1 = (f32x4){O[4], O[5], O[6], O[7]} * sb, y2 = (f32x4){O[8], O[9], O[10], O[11]} * sb;
      r1 = (u32x4){pk2(y0[0], y0[1]), pk2(y0[2], y0[3]), pk2(y1[0], y1[1]), pk2(y1[2], y1[3])};
      r2 = (u32x2){pk2(y2[0], y2[1]), pk2(y2[2], y2[3])};
      const f32x4 ys = (y0 + y1) + y2;
      const float yy = (ys[0] + ys[1]) + (ys[2] + ys[3]);
      const bool bad = !(__builtin_fabsf(yy) < 3.0e38f) || !(l < 1.0e37f) || !(l > 1.0e-25f);
      if (__builtin_expect(pass == 1 || !__any(bad), 1)) break;
      O = attn_exact(rW, q1[j], q2[j][0], lane);
    }
    st128(rW, vo1 + j * 40960, r1);
    st64(rW, vo2 + j * 40960, r2);
  }
  HSTAMP(7); TSTAMP(6);
  DRAIN();
  TSTAMP(7); FSTAMP(7);
  st64(rW, lane == 0 ? WS_FLGB + (u32)(t64 * 32 + head) * 8u : OOB, tag);
  lds_arrive(lds + F_FIN);
  STAMP_FLUSH();
}

extern "C" void kernel_launch(void* const* d_in, const int* in_sizes, int n_in, void* d_out, int out_size, void* d_ws,
                              size_t ws_size, hipStream_t stream) {
  (void)in_sizes; (void)n_in; (void)out_size; (void)ws_size;
  (void)hipFuncSetAttribute((const void*)gm_fused, hipFuncAttributeMaxDynamicSharedMemorySize, LDS_BYTES);
  gm_fused<<<256, 768, LDS_BYTES, stream>>>((const char*)d_in[0], (const char*)d_in[1], (const char*)d_in[2], (const char*)d_in[5], (const char*)d_in[6],
      (const char*)d_in[7], (const char*)d_in[9], (const char*)d_in[3], (const char*)d_in[8], (char*)d_ws, (char*)d_out);
}
```

```cpp
#include <hip/hip_runtime.h>
#include <hip/hip_bf16.h>
#include <stdint.h>

typedef unsigned int u32;
typedef unsigned long long u64;
typedef u32 u32x2 __attribute__((ext_vector_type(2)));
typedef u32 u32x4 __attribute__((ext_vector_type(4)));
typedef u32 u32x16 __attribute__((ext_vector_type(16)));
typedef float f32x2 __attribute__((ext_vector_type(2)));
typedef float f32x4 __attribute__((ext_vector_type(4)));
typedef float f32x8 __attribute__((ext_vector_type(8)));
typedef float f32x16 __attribute__((ext_vector_type(16)));
typedef __bf16 bf16x2 __attribute__((ext_vector_type(2)));
typedef __bf16 bf16x8 __attribute__((ext_vector_type(8)));
typedef __bf16 bf16x16 __attribute__((ext_vector_type(16)));
typedef short s16x4 __attribute__((ext_vector_type(4)));
#define LDSAS __attribute__((address_space(3)))
typedef LDSAS char* ldsp;
typedef __attribute__((address_space(1))) u64 gu64;

constexpr int SEQ = 2048, DM = 640, NH = 32, DH = 20;
constexpr float LOG2E = 1.4426950408889634f;
constexpr float QSCALE = 1.4426950408889634f * 0.22360679774997896f;

constexpr u32 REC = 2688;
constexpr u32 WS_QB   = 0;
constexpr u32 WS_KVIM = WS_QB + 1310720;
constexpr u32 WS_KVP  = WS_KVIM + 64 * REC;
constexpr u32 WS_KVT  = WS_KVP + 32 * 16 * 3360;
constexpr u32 WS_GATE = WS_KVT + 32 * 1680;
constexpr u32 WS_OFR  = WS_GATE + 8192;
constexpr u32 WS_F1   = WS_OFR + 5242880;
constexpr u32 WS_F2   = WS_F1 + 16384;
constexpr u32 WS_F3   = WS_F2 + 1024;
constexpr u32 WS_FLGA = WS_F3 + 1024;
constexpr u32 WS_FLGB = WS_FLGA + 2048;
constexpr u32 WS_FQ   = WS_FLGB + 16384;
constexpr u32 WS_EPOCH = WS_FQ + 16384;
constexpr u32 WS_END  = WS_EPOCH + 256;
constexpr u32 OOB = 0x40000000u;
constexpr u32 SPIN_CAP = 1u << 20;

#define SBAR() __builtin_amdgcn_sched_barrier(0)
#define PIN(x) asm volatile("" : "+v"(x))
#define LBAR() asm volatile("s_waitcnt lgkmcnt(0)\n\ts_barrier" ::: "memory")
#define WAIT_BAR(N) asm volatile("s_waitcnt vmcnt(" #N ") lgkmcnt(0)\n\ts_barrier" ::: "memory")
#define DRAIN() asm volatile("s_waitcnt vmcnt(0)" ::: "memory")

__device__ __forceinline__ int lane_id() {
  int l;
  asm volatile("v_mbcnt_lo_u32_b32 %0, -1, 0\n\tv_mbcnt_hi_u32_b32 %0, -1, %0" : "=v"(l));
  return l;
}
__device__ __forceinline__ u32 pk2(float a, float b) {
  f32x2 v = {a, b};
  return __builtin_bit_cast(u32, __builtin_convertvector(v, bf16x2));
}
__device__ __forceinline__ f32x4 mfma16(u32x4 a, u32x4 b, f32x4 c) {
  return __builtin_amdgcn_mfma_f32_16x16x32_bf16(__builtin_bit_cast(bf16x8, a), __builtin_bit_cast(bf16x8, b), c, 0, 0, 0);
}
__device__ __forceinline__ f32x16 mfma32(u32x4 a, u32x4 b, f32x16 c) {
  return __builtin_amdgcn_mfma_f32_32x32x16_bf16(__builtin_bit_cast(bf16x8, a), __builtin_bit_cast(bf16x8, b), c, 0, 0, 0);
}
__device__ __forceinline__ f32x16 mfma32k8(u32x2 a, u32x2 b, f32x16 c) {
  return __builtin_amdgcn_mfma_f32_32x32x8bf16_1k(__builtin_bit_cast(s16x4, a), __builtin_bit_cast(s16x4, b), c, 0, 0, 0);
}
__device__ __forceinline__ f32x16 smf(u32x4 a, f32x8 b, f32x16 c, int idx) {
  return __builtin_amdgcn_smfmac_f32_32x32x32_bf16(__builtin_bit_cast(bf16x8, a), __builtin_bit_cast(bf16x16, b), c, idx, 0, 0);
}
__device__ __forceinline__ u32x2 trd(ldsp p) {
  return __builtin_bit_cast(u32x2, __builtin_amdgcn_ds_read_tr16_b64_v4i16((LDSAS s16x4*)p));
}
__device__ __forceinline__ u32x4 trd2(ldsp p0, ldsp p1) {
  u32x2 a = trd(p0), b = trd(p1);
  return (u32x4){a[0], a[1], b[0], b[1]};
}
__device__ __forceinline__ u32x4 ld128(ldsp p) { return *(LDSAS u32x4*)p; }
__device__ __forceinline__ u32x2 ld64(ldsp p) { return *(LDSAS u32x2*)p; }
__device__ __forceinline__ float bflo(u32 v) { return __uint_as_float(v << 16); }
__device__ __forceinline__ float bfhi(u32 v) { return __uint_as_float(v & 0xffff0000u); }

__device__ __forceinline__ u32x4 mkrsrc(const void* p, u32 bytes) {
  uint64_t a = (uint64_t)p;
  return (u32x4){(u32)__builtin_amdgcn_readfirstlane((u32)a), (u32)__builtin_amdgcn_readfirstlane((u32)(a >> 32) & 0xffffu), bytes, 0x00020000u};
}
__device__ __forceinline__ void dma1(u32x4 rs, u32 voff, u32 soff, u32 ldsaddr) {
  u32 keep;
  asm volatile("s_mov_b32 %0, m0\n\ts_mov_b32 m0, %4\n\ts_nop 2\n\tbuffer_load_dwordx4 %1, %2, %3 offen lds\n\ts_mov_b32 m0, %0"
               : "=&s"(keep) : "v"(voff), "s"(rs), "s"(__builtin_amdgcn_readfirstlane(soff)), "s"(__builtin_amdgcn_readfirstlane(ldsaddr)) : "memory");
}
__device__ __forceinline__ void dma1s(u32x4 rs, u32 voff, u32 soff, u32 ldsaddr) {
  u32 keep;
  asm volatile("s_mov_b32 %0, m0\n\ts_mov_b32 m0, %4\n\ts_nop 2\n\tbuffer_load_dwordx4 %1, %2, %3 offen sc1 lds\n\ts_mov_b32 m0, %0"
               : "=&s"(keep) : "v"(voff), "s"(rs), "s"(__builtin_amdgcn_readfirstlane(soff)), "s"(__builtin_amdgcn_readfirstlane(ldsaddr)) : "memory");
}
__device__ __forceinline__ void dma4(u32x4 rs, u32 voff, u32 soff, u32 ldsaddr) {
  u32 keep;
  asm volatile("s_mov_b32 %0, m0\n\ts_mov_b32 m0, %4\n\ts_nop 2\n\t"
               "buffer_load_dwordx4 %1, %2, %3 offen sc1 lds\n\tbuffer_load_dwordx4 %1, %2, %3 offen offset:1024 sc1 lds\n\t"
               "buffer_load_dwordx4 %1, %2, %3 offen offset:2048 sc1 lds\n\tbuffer_load_dwordx4 %1, %2, %3 offen offset:3072 sc1 lds\n\t"
               "s_mov_b32 m0, %0"
               : "=&s"(keep) : "v"(voff), "s"(rs), "s"(__builtin_amdgcn_readfirstlane(soff)), "s"(__builtin_amdgcn_readfirstlane(ldsaddr)) : "memory");
}
__device__ __forceinline__ void dma2h(u32x4 rs, u32 voff, u32 soff, u32 ldsaddr) {
  u32 keep;
  asm volatile("s_mov_b32 %0, m0\n\ts_mov_b32 m0, %4\n\ts_nop 2\n\t"
               "buffer_load_dwordx4 %1, %2, %3 offen sc1 lds\n\tbuffer_load_dwordx4 %1, %2, %3 offen offset:1024 sc1 lds\n\t"
               "s_mov_b32 exec_hi, 0\n\tbuffer_load_dwordx4 %1, %2, %3 offen offset:2048 sc1 lds\n\ts_mov_b32 exec_hi, -1\n\t"
               "s_mov_b32 m0, %0"
               : "=&s"(keep) : "v"(voff), "s"(rs), "s"(__builtin_amdgcn_readfirstlane(soff)), "s"(__builtin_amdgcn_readfirstlane(ldsaddr)) : "memory");
}
typedef __amdgpu_buffer_rsrc_t rsrc_t;
__device__ __forceinline__ void st128(rsrc_t r, u32 off, u32x4 v) { __builtin_amdgcn_raw_buffer_store_b128(v, r, (int)off, 0, 16); }
__device__ __forceinline__ void st64(rsrc_t r, u32 off, u32x2 v) { __builtin_amdgcn_raw_buffer_store_b64(v, r, (int)off, 0, 16); }
__device__ __forceinline__ void st32(rsrc_t r, u32 off, u32 v) { __builtin_amdgcn_raw_buffer_store_b32(v, r, (int)off, 0, 16); }
__device__ __forceinline__ void st16(rsrc_t r, u32 off, u32 v) { __builtin_amdgcn_raw_buffer_store_b16((unsigned short)v, r, (int)off, 0, 16); }
__device__ __forceinline__ u32x4 gl128(rsrc_t r, u32 voff, u32 soff) { return __builtin_amdgcn_raw_buffer_load_b128(r, (int)voff, (int)soff, 16); }
__device__ __forceinline__ u32x2 gl64(rsrc_t r, u32 voff, u32 soff) { return __builtin_amdgcn_raw_buffer_load_b64(r, (int)voff, (int)soff, 16); }
__device__ __forceinline__ u32 gl32(rsrc_t r, u32 voff, u32 soff) { return __builtin_amdgcn_raw_buffer_load_b32(r, (int)voff, (int)soff, 16); }

__device__ __forceinline__ f32x8 lo8(const f32x16& s) { return __builtin_shufflevector(s, s, 0, 1, 2, 3, 4, 5, 6, 7); }
__device__ __forceinline__ f32x8 hi8(const f32x16& s) { return __builtin_shufflevector(s, s, 8, 9, 10, 11, 12, 13, 14, 15); }
__device__ __forceinline__ float max16(const f32x16& s) {
  float a = fmaxf(fmaxf(s[0], s[1]), s[2]), b = fmaxf(fmaxf(s[3], s[4]), s[5]);
  a = fmaxf(fmaxf(a, s[6]), s[7]); b = fmaxf(fmaxf(b, s[8]), s[9]);
  a = fmaxf(fmaxf(a, s[10]), s[11]); b = fmaxf(fmaxf(b, s[12]), s[13]);
  a = fmaxf(fmaxf(a, s[14]), s[15]);
  return fmaxf(a, b);
}
__device__ __forceinline__ float xhalf_max(float m) {
  auto r = __builtin_amdgcn_permlane32_swap(__float_as_uint(m), __float_as_uint(m), false, false);
  return fmaxf(__uint_as_float(r[0]), __uint_as_float(r[1]));
}
__device__ __forceinline__ float from_hi(float v) {
  auto r = __builtin_amdgcn_permlane32_swap(__float_as_uint(v), __float_as_uint(v), false, false);
  return __uint_as_float(r[1]);
}
__device__ __forceinline__ u32 lds_ld(ldsp p) { return __hip_atomic_load((LDSAS u32*)p, __ATOMIC_RELAXED, __HIP_MEMORY_SCOPE_WORKGROUP); }
__device__ __forceinline__ void lds_st(ldsp p, u32 v) { __hip_atomic_store((LDSAS u32*)p, v, __ATOMIC_RELAXED, __HIP_MEMORY_SCOPE_WORKGROUP); }
template <int SLEEP>
__device__ __forceinline__ void lds_wait_ge(ldsp p, u32 want) {
#pragma unroll 1
  for (u32 it = 0; it < SPIN_CAP; ++it) {
    if ((u32)__builtin_amdgcn_readfirstlane(lds_ld(p)) >= want) break;
    __builtin_amdgcn_s_sleep(SLEEP);
  }
  asm volatile("" ::: "memory");
}
__device__ __forceinline__ u32 lds_arrive(ldsp p) {
  u32 old; u64 keep;
  asm volatile("v_mov_b32 %0, 1\n\ts_mov_b64 %1, exec\n\ts_mov_b64 exec, 1\n\ts_waitcnt lgkmcnt(0)\n\tds_add_rtn_u32 %0, %2, %0\n\t"
               "s_waitcnt lgkmcnt(0)\n\ts_mov_b64 exec, %1" : "=&v"(old), "=&s"(keep) : "v"((u32)(uintptr_t)p) : "memory");
  return (u32)__builtin_amdgcn_readfirstlane(old);
}
template <int SLEEP>
__device__ __forceinline__ void flags_wait(rsrc_t r, u32 off, int n, u32x2 tag) {
  const int lane = lane_id();
  const u32 vo = (u32)(lane < n ? lane : 0) * 8u;
#pragma unroll 1
  for (u32 it = 0; it < SPIN_CAP; ++it) {
    asm volatile("" ::: "memory");
    const u32x2 v = gl64(r, vo, off);
    if (__all(v[0] == tag[0] && v[1] == tag[1])) break;
    __builtin_amdgcn_s_sleep(SLEEP);
  }
  asm volatile("" ::: "memory");
}
template <int SLEEP>
__device__ __forceinline__ void flags_wait256(rsrc_t r, u32 off, u32x2 tag) {
  const u32 vo = (u32)lane_id() * 16u;
#pragma unroll 1
  for (u32 it = 0; it < SPIN_CAP; ++it) {
    asm volatile("" ::: "memory");
    const u32x4 a = gl128(r, vo, off), b = gl128(r, vo, off + 1024);
    const u32 d = (a[0] ^ tag[0]) | (a[1] ^ tag[1]) | (a[2] ^ tag[0]) | (a[3] ^ tag[1]) | (b[0] ^ tag[0]) | (b[1] ^ tag[1]) | (b[2] ^ tag[0]) | (b[3] ^ tag[1]);
    if (__all(d == 0u)) break;
    __builtin_amdgcn_s_sleep(SLEEP);
  }
  asm volatile("" ::: "memory");
}

#define ATTN_LOOP_ASM \
  "s_nop 11\n\t" \
  "v_mov_b64 v[0:1], 0\n\t" \
  "v_mov_b64 v[2:3], 0\n\t" \
  "v_mov_b64 v[4:5], 0\n\t" \
  "v_mov_b64 v[6:7], 0\n\t" \
  "v_mov_b64 v[8:9], 0\n\t" \
  "v_mov_b64 v[10:11], 0\n\t" \
  "v_mov_b64 v[12:13], 0\n\t" \
  "v_mov_b64 v[14:15], 0\n\t" \
  "v_mov_b64 v[16:17], 0\n\t" \
  "v_mov_b64 v[18:19], 0\n\t" \
  "v_mov_b64 v[20:21], 0\n\t" \
  "v_mov_b64 v[22:23], 0\n\t" \
  "v_mov_b64 v[24:25], 0\n\t" \
  "v_mov_b64 v[26:27], 0\n\t" \
  "v_mov_b64 v[28:29], 0\n\t" \
  "v_mov_b64 v[30:31], 0\n\t" \
  "v_mov_b64 v[80:81], 0\n\t" \
  "v_mov_b64 v[82:83], 0\n\t" \
  "v_mov_b64 v[84:85], 0\n\t" \
  "v_mov_b64 v[86:87], 0\n\t" \
  "v_mov_b64 v[88:89], 0\n\t" \
  "v_mov_b64 v[90:91], 0\n\t" \
  "v_mov_b64 v[92:93], 0\n\t" \
  "v_mov_b64 v[94:95], 0\n\t" \
  "v_mov_b64 v[116:117], 0\n\t" \
  "v_mov_b64 v[118:119], 0\n\t" \
  "v_mov_b64 v[120:121], 0\n\t" \
  "v_mov_b64 v[122:123], 0\n\t" \
  "s_mov_b32 %[cnt], 0\n\t" \
  "s_mov_b32 %[slot], 0\n\t" \
  "s_setprio 0\n\t" \
  "2:\n\t" \
  "s_mov_b32 %[inc], 0\n\t" \
  "3:\n\t" \
  "ds_read_b32 v115, v127 offset:32\n\t" \
  "s_add_u32 %[inc], %[inc], 1\n\t" \
  "s_waitcnt lgkmcnt(0)\n\t" \
  "v_readfirstlane_b32 %[rdy], v115\n\t" \
  "s_nop 3\n\t" \
  "s_cmp_gt_u32 %[rdy], %[cnt]\n\t" \
  "s_cbranch_scc1 4f\n\t" \
  "s_sleep 1\n\t" \
  "s_cmp_lt_u32 %[inc], 0x100000\n\t" \
  "s_cbranch_scc1 3b\n\t" \
  "s_branch 9f\n\t" \
  "4:\n\t" \
  "v_mfma_f32_32x32x16_bf16 v[48:63], v[108:111], v[100:103], 0\n\t" \
  "ds_read_b128 v[108:111], v124 offset:0\n\t" \
  "v_exp_f32 v32, v32\n\t" \
  "v_exp_f32 v33, v33\n\t" \
  "v_exp_f32 v34, v34\n\t" \
  "v_exp_f32 v35, v35\n\t" \
  "v_mfma_f32_32x32x8_bf16 v[48:63], v[112:113], v[106:107], v[48:63]\n\t" \
  "ds_read_b32 v112, v125 offset:0\n\t" \
  "v_exp_f32 v36, v36\n\t" \
  "v_exp_f32 v37, v37\n\t" \
  "v_exp_f32 v38, v38\n\t" \
  "v_exp_f32 v39, v39\n\t" \
  "v_smfmac_f32_32x32x32_bf16 v[16:31], v[116:119], v[80:87], v114\n\t" \
  "ds_read_b128 v[116:119], v126 offset:0\n\t" \
  "v_exp_f32 v40, v40\n\t" \
  "v_exp_f32 v41, v41\n\t" \
  "v_exp_f32 v42, v42\n\t" \
  "v_exp_f32 v43, v43\n\t" \
  "v_smfmac_f32_32x32x32_bf16 v[16:31], v[120:123], v[88:95], v114\n\t" \
  "ds_read_b128 v[120:123], v126 offset:704\n\t" \
  "v_exp_f32 v44, v44\n\t" \
  "v_exp_f32 v45, v45\n\t" \
  "v_exp_f32 v46, v46\n\t" \
  "v_exp_f32 v47, v47\n\t" \
  "s_waitcnt lgkmcnt(2)\n\t" \
  "v_mfma_f32_32x32x16_bf16 v[64:79], v[108:111], v[96:99], 0\n\t" \
  "v_exp_f32 v48, v48\n\t" \
  "v_exp_f32 v49, v49\n\t" \
  "v_exp_f32 v50, v50\n\t" \
  "v_exp_f32 v51, v51\n\t" \
  "v_mfma_f32_32x32x8_bf16 v[64:79], v[112:113], v[104:105], v[64:79]\n\t" \
  "v_exp_f32 v52, v52\n\t" \
  "v_exp_f32 v53, v53\n\t" \
  "v_exp_f32 v54, v54\n\t" \
  "v_exp_f32 v55, v55\n\t" \
  "s_waitcnt lgkmcnt(0)\n\t" \
  "v_smfmac_f32_32x32x32_bf16 v[0:15], v[116:119], v[32:39], v114\n\t" \
  "v_exp_f32 v56, v56\n\t" \
  "v_exp_f32 v57, v57\n\t" \
  "v_exp_f32 v58, v58\n\t" \
  "v_exp_f32 v59, v59\n\t" \
  "v_smfmac_f32_32x32x32_bf16 v[0:15], v[120:123], v[40:47], v114\n\t" \
  "v_exp_f32 v60, v60\n\t" \
  "v_exp_f32 v61, v61\n\t" \
  "v_exp_f32 v62, v62\n\t" \
  "v_exp_f32 v63, v63\n\t" \
  "v_mfma_f32_32x32x16_bf16 v[80:95], v[108:111], v[100:103], 0\n\t" \
  "ds_read_b128 v[108:111], v124 offset:2688\n\t" \
  "v_exp_f32 v64, v64\n\t" \
  "v_exp_f32 v65, v65\n\t" \
  "v_exp_f32 v66, v66\n\t" \
  "v_exp_f32 v67, v67\n\t" \
  "v_mfma_f32_32x32x8_bf16 v[80:95], v[112:113], v[106:107], v[80:95]\n\t" \
  "ds_read_b32 v112, v125 offset:2688\n\t" \
  "v_exp_f32 v68, v68\n\t" \
  "v_exp_f32 v69, v69\n\t" \
  "v_exp_f32 v70, v70\n\t" \
  "v_exp_f32 v71, v71\n\t" \
  "v_smfmac_f32_32x32x32_bf16 v[16:31], v[116:119], v[48:55], v114\n\t" \
  "ds_read_b128 v[116:119], v126 offset:2688\n\t" \
  "v_exp_f32 v72, v72\n\t" \
  "v_exp_f32 v73, v73\n\t" \
  "v_exp_f32 v74, v74\n\t" \
  "v_exp_f32 v75, v75\n\t" \
  "v_smfmac_f32_32x32x32_bf16 v[16:31], v[120:123], v[56:63], v114\n\t" \
  "ds_read_b128 v[120:123], v126 offset:3392\n\t" \
  "v_exp_f32 v76, v76\n\t" \
  "v_exp_f32 v77, v77\n\t" \
  "v_exp_f32 v78, v78\n\t" \
  "v_exp_f32 v79, v79\n\t" \
  "s_waitcnt lgkmcnt(2)\n\t" \
  "v_mfma_f32_32x32x16_bf16 v[32:47], v[108:111], v[96:99], 0\n\t" \
  "v_exp_f32 v80, v80\n\t" \
  "v_exp_f32 v81, v81\n\t" \
  "v_exp_f32 v82, v82\n\t" \
  "v_exp_f32 v83, v83\n\t" \
  "v_mfma_f32_32x32x8_bf16 v[32:47], v[112:113], v[104:105], v[32:47]\n\t" \
  "s_cmp_eq_u32 %[slot], 4\n\t" \
  "s_cselect_b32 %[inc], %[wrap], %[stg]\n\t" \
  "v_exp_f32 v84, v84\n\t" \
  "v_exp_f32 v85, v85\n\t" \
  "v_exp_f32 v86, v86\n\t" \
  "v_exp_f32 v87, v87\n\t" \
  "s_waitcnt lgkmcnt(0)\n\t" \
  "v_smfmac_f32_32x32x32_bf16 v[0:15], v[116:119], v[64:71], v114\n\t" \
  "s_cselect_b32 %[slot], -1, %[slot]\n\t" \
  "s_add_u32 %[slot], %[slot], 1\n\t" \
  "v_exp_f32 v88, v88\n\t" \
  "v_exp_f32 v89, v89\n\t" \
  "v_exp_f32 v90, v90\n\t" \
  "v_exp_f32 v91, v91\n\t" \
  "v_smfmac_f32_32x32x32_bf16 v[0:15], v[120:123], v[72:79], v114\n\t" \
  "v_exp_f32 v92, v92\n\t" \
  "v_exp_f32 v93, v93\n\t" \
  "v_exp_f32 v94, v94\n\t" \
  "v_exp_f32 v95, v95\n\t" \
  "v_mfma_f32_32x32x16_bf16 v[48:63], v[108:111], v[100:103], 0\n\t" \
  "ds_read_b128 v[108:111], v124 offset:5376\n\t" \
  "v_exp_f32 v32, v32\n\t" \
  "v_exp_f32 v33, v33\n\t" \
  "v_exp_f32 v34, v34\n\t" \
  "v_exp_f32 v35, v35\n\t" \
  "v_mfma_f32_32x32x8_bf16 v[48:63], v[112:113], v[106:107], v[48:63]\n\t" \
  "ds_read_b32 v112, v125 offset:5376\n\t" \
  "v_exp_f32 v36, v36\n\t" \
  "v_exp_f32 v37, v37\n\t" \
  "v_exp_f32 v38, v38\n\t" \
  "v_exp_f32 v39, v39\n\t" \
  "v_smfmac_f32_32x32x32_bf16 v[16:31], v[116:119], v[80:87], v114\n\t" \
  "ds_read_b128 v[116:119], v126 offset:5376\n\t" \
  "v_exp_f32 v40, v40\n\t" \
  "v_exp_f32 v41, v41\n\t" \
  "v_exp_f32 v42, v42\n\t" \
  "v_exp_f32 v43, v43\n\t" \
  "v_smfmac_f32_32x32x32_bf16 v[16:31], v[120:123], v[88:95], v114\n\t" \
  "ds_read_b128 v[120:123], v126 offset:6080\n\t" \
  "v_exp_f32 v44, v44\n\t" \
  "v_exp_f32 v45, v45\n\t" \
  "v_exp_f32 v46, v46\n\t" \
  "v_exp_f32 v47, v47\n\t" \
  "s_waitcnt lgkmcnt(2)\n\t" \
  "v_mfma_f32_32x32x16_bf16 v[64:79], v[108:111], v[96:99], 0\n\t" \
  "s_add_u32 %[cnt], %[cnt], 1\n\t" \
  "v_exp_f32 v48, v48\n\t" \
  "v_exp_f32 v49, v49\n\t" \
  "v_exp_f32 v50, v50\n\t" \
  "v_exp_f32 v51, v51\n\t" \
  "v_mfma_f32_32x32x8_bf16 v[64:79], v[112:113], v[104:105], v[64:79]\n\t" \
  "v_exp_f32 v52, v52\n\t" \
  "v_exp_f32 v53, v53\n\t" \
  "v_exp_f32 v54, v54\n\t" \
  "v_exp_f32 v55, v55\n\t" \
  "s_waitcnt lgkmcnt(0)\n\t" \
  "v_smfmac_f32_32x32x32_bf16 v[0:15], v[116:119], v[32:39], v114\n\t" \
  "v_exp_f32 v56, v56\n\t" \
  "v_exp_f32 v57, v57\n\t" \
  "v_exp_f32 v58, v58\n\t" \
  "v_exp_f32 v59, v59\n\t" \
  "v_smfmac_f32_32x32x32_bf16 v[0:15], v[120:123], v[40:47], v114\n\t" \
  "v_exp_f32 v60, v60\n\t" \
  "v_exp_f32 v61, v61\n\t" \
  "v_exp_f32 v62, v62\n\t" \
  "v_exp_f32 v63, v63\n\t" \
  "v_mfma_f32_32x32x16_bf16 v[80:95], v[108:111], v[100:103], 0\n\t" \
  "ds_read_b128 v[108:111], v124 offset:8064\n\t" \
  "v_exp_f32 v64, v64\n\t" \
  "v_exp_f32 v65, v65\n\t" \
  "v_exp_f32 v66, v66\n\t" \
  "v_exp_f32 v67, v67\n\t" \
  "v_mfma_f32_32x32x8_bf16 v[80:95], v[112:113], v[106:107], v[80:95]\n\t" \
  "ds_read_b32 v112, v125 offset:8064\n\t" \
  "v_exp_f32 v68, v68\n\t" \
  "v_exp_f32 v69, v69\n\t" \
  "v_exp_f32 v70, v70\n\t" \
  "v_exp_f32 v71, v71\n\t" \
  "v_smfmac_f32_32x32x32_bf16 v[16:31], v[116:119], v[48:55], v114\n\t" \
  "ds_read_b128 v[116:119], v126 offset:8064\n\t" \
  "v_exp_f32 v72, v72\n\t" \
  "v_exp_f32 v73, v73\n\t" \
  "v_exp_f32 v74, v74\n\t" \
  "v_exp_f32 v75, v75\n\t" \
  "v_smfmac_f32_32x32x32_bf16 v[16:31], v[120:123], v[56:63], v114\n\t" \
  "ds_read_b128 v[120:123], v126 offset:8768\n\t" \
  "v_exp_f32 v76, v76\n\t" \
  "v_exp_f32 v77, v77\n\t" \
  "v_exp_f32 v78, v78\n\t" \
  "v_exp_f32 v79, v79\n\t" \
  "s_waitcnt lgkmcnt(2)\n\t" \
  "v_mfma_f32_32x32x16_bf16 v[32:47], v[108:111], v[96:99], 0\n\t" \
  "ds_read_b32 v115, v127 offset:32\n\t" \
  "v_exp_f32 v80, v80\n\t" \
  "v_exp_f32 v81, v81\n\t" \
  "v_exp_f32 v82, v82\n\t" \
  "v_exp_f32 v83, v83\n\t" \
  "v_mfma_f32_32x32x8_bf16 v[32:47], v[112:113], v[104:105], v[32:47]\n\t" \
  "v_add_u32 v124, %[inc], v124\n\t" \
  "v_add_u32 v125, %[inc], v125\n\t" \
  "v_add_u32 v126, %[inc], v126\n\t" \
  "v_exp_f32 v84, v84\n\t" \
  "v_exp_f32 v85, v85\n\t" \
  "v_exp_f32 v86, v86\n\t" \
  "v_exp_f32 v87, v87\n\t" \
  "s_waitcnt lgkmcnt(1)\n\t" \
  "v_smfmac_f32_32x32x32_bf16 v[0:15], v[116:119], v[64:71], v114\n\t" \
  "v_exp_f32 v88, v88\n\t" \
  "v_exp_f32 v89, v89\n\t" \
  "v_exp_f32 v90, v90\n\t" \
  "v_exp_f32 v91, v91\n\t" \
  "s_waitcnt lgkmcnt(0)\n\t" \
  "v_readfirstlane_b32 %[rdy], v115\n\t" \
  "v_mov_b32 v115, %[cnt]\n\t" \
  "v_smfmac_f32_32x32x32_bf16 v[0:15], v[120:123], v[72:79], v114\n\t" \
  "ds_write_b32 v127, v115\n\t" \
  "s_cmp_gt_u32 %[rdy], %[cnt]\n\t" \
  "v_exp_f32 v92, v92\n\t" \
  "v_exp_f32 v93, v93\n\t" \
  "v_exp_f32 v94, v94\n\t" \
  "v_exp_f32 v95, v95\n\t" \
  "s_cbranch_scc1 4b\n\t" \
  "s_cmp_lt_u32 %[cnt], 16\n\t" \
  "s_cbranch_scc1 2b\n\t" \
  "9:\n\t" \
  "s_setprio 3\n\t" \
  "v_smfmac_f32_32x32x32_bf16 v[16:31], v[116:119], v[80:87], v114\n\t" \
  "v_smfmac_f32_32x32x32_bf16 v[16:31], v[120:123], v[88:95], v114\n\t" \
  "s_waitcnt lgkmcnt(0)\n\t" \
  "s_nop 12\n\t"
#define TILE_GEMM_ASM \
  "s_nop 4\n\t" \
  "buffer_load_dwordx4 v[60:63], v140, %[rs], %[s0] offen offset:0 sc1\n\t" \
  "buffer_load_dwordx4 v[64:67], v140, %[rs], %[s1] offen offset:0 sc1\n\t" \
  "buffer_load_dwordx4 v[68:71], v140, %[rs], %[s0] offen offset:1024 sc1\n\t" \
  "buffer_load_dwordx4 v[72:75], v140, %[rs], %[s1] offen offset:1024 sc1\n\t" \
  "buffer_load_dwordx4 v[76:79], v140, %[rs], %[s0] offen offset:2048 sc1\n\t" \
  "buffer_load_dwordx4 v[80:83], v140, %[rs], %[s1] offen offset:2048 sc1\n\t" \
  "buffer_load_dwordx4 v[84:87], v140, %[rs], %[s0] offen offset:3072 sc1\n\t" \
  "buffer_load_dwordx4 v[88:91], v140, %[rs], %[s1] offen offset:3072 sc1\n\t" \
  "s_add_u32 %[s0], %[s0], 4096\n\t" \
  "buffer_load_dwordx4 v[92:95], v140, %[rs], %[s0] offen offset:0 sc1\n\t" \
  "s_add_u32 %[s1], %[s1], 4096\n\t" \
  "buffer_load_dwordx4 v[96:99], v140, %[rs], %[s1] offen offset:0 sc1\n\t" \
  "buffer_load_dwordx4 v[100:103], v140, %[rs], %[s0] offen offset:1024 sc1\n\t" \
  "buffer_load_dwordx4 v[104:107], v140, %[rs], %[s1] offen offset:1024 sc1\n\t" \
  "buffer_load_dwordx4 v[108:111], v140, %[rs], %[s0] offen offset:2048 sc1\n\t" \
  "buffer_load_dwordx4 v[112:115], v140, %[rs], %[s1] offen offset:2048 sc1\n\t" \
  "buffer_load_dwordx4 v[116:119], v140, %[rs], %[s0] offen offset:3072 sc1\n\t" \
  "buffer_load_dwordx4 v[120:123], v140, %[rs], %[s1] offen offset:3072 sc1\n\t" \
  "s_add_u32 %[s0], %[s0], 4096\n\t" \
  "buffer_load_dwordx4 v[124:127], v140, %[rs], %[s0] offen offset:0 sc1\n\t" \
  "s_add_u32 %[s1], %[s1], 4096\n\t" \
  "buffer_load_dwordx4 v[128:131], v140, %[rs], %[s1] offen offset:0 sc1\n\t" \
  "buffer_load_dwordx4 v[132:135], v140, %[rs], %[s0] offen offset:1024 sc1\n\t" \
  "buffer_load_dwordx4 v[136:139], v140, %[rs], %[s1] offen offset:1024 sc1\n\t" \
  "ds_read_b64_tr_b16 v[40:41], v141 offset:0\n\t" \
  "ds_read_b64_tr_b16 v[42:43], v141 offset:2560\n\t" \
  "ds_read_b64_tr_b16 v[44:45], v141 offset:8\n\t" \
  "ds_read_b64_tr_b16 v[46:47], v141 offset:2568\n\t" \
  "ds_read_b64_tr_b16 v[48:49], v141 offset:16\n\t" \
  "ds_read_b64_tr_b16 v[50:51], v141 offset:2576\n\t" \
  "ds_read_b64_tr_b16 v[52:53], v141 offset:24\n\t" \
  "ds_read_b64_tr_b16 v[54:55], v141 offset:2584\n\t" \
  "ds_read_b64_tr_b16 v[56:57], v141 offset:32\n\t" \
  "ds_read_b64_tr_b16 v[58:59], v141 offset:2592\n\t" \
  "s_waitcnt vmcnt(18)\n\t" \
  "s_waitcnt lgkmcnt(8)\n\t" \
  "v_mfma_f32_16x16x32_bf16 v[0:3], v[40:43], v[60:63], v[0:3]\n\t" \
  "v_mfma_f32_16x16x32_bf16 v[4:7], v[40:43], v[64:67], v[4:7]\n\t" \
  "ds_read_b64_tr_b16 v[40:41], v141 offset:5120\n\t" \
  "ds_read_b64_tr_b16 v[42:43], v141 offset:7680\n\t" \
  "s_waitcnt lgkmcnt(8)\n\t" \
  "v_mfma_f32_16x16x32_bf16 v[8:11], v[44:47], v[60:63], v[8:11]\n\t" \
  "v_mfma_f32_16x16x32_bf16 v[12:15], v[44:47], v[64:67], v[12:15]\n\t" \
  "ds_read_b64_tr_b16 v[44:45], v141 offset:5128\n\t" \
  "ds_read_b64_tr_b16 v[46:47], v141 offset:7688\n\t" \
  "s_waitcnt lgkmcnt(8)\n\t" \
  "v_mfma_f32_16x16x32_bf16 v[16:19], v[48:51], v[60:63], v[16:19]\n\t" \
  "v_mfma_f32_16x16x32_bf16 v[20:23], v[48:51], v[64:67], v[20:23]\n\t" \
  "ds_read_b64_tr_b16 v[48:49], v141 offset:5136\n\t" \
  "ds_read_b64_tr_b16 v[50:51], v141 offset:7696\n\t" \
  "s_waitcnt lgkmcnt(8)\n\t" \
  "v_mfma_f32_16x16x32_bf16 v[24:27], v[52:55], v[60:63], v[24:27]\n\t" \
  "v_mfma_f32_16x16x32_bf16 v[28:31], v[52:55], v[64:67], v[28:31]\n\t" \
  "ds_read_b64_tr_b16 v[52:53], v141 offset:5144\n\t" \
  "ds_read_b64_tr_b16 v[54:55], v141 offset:7704\n\t" \
  "s_waitcnt lgkmcnt(8)\n\t" \
  "v_mfma_f32_16x16x32_bf16 v[32:35], v[56:59], v[60:63], v[32:35]\n\t" \
  "v_mfma_f32_16x16x32_bf16 v[36:39], v[56:59], v[64:67], v[36:39]\n\t" \
  "ds_read_b64_tr_b16 v[56:57], v141 offset:5152\n\t" \
  "ds_read_b64_tr_b16 v[58:59], v141 offset:7712\n\t" \
  "s_waitcnt vmcnt(16)\n\t" \
  "s_waitcnt lgkmcnt(8)\n\t" \
  "v_mfma_f32_16x16x32_bf16 v[0:3], v[40:43], v[68:71], v[0:3]\n\t" \
  "v_mfma_f32_16x16x32_bf16 v[4:7], v[40:43], v[72:75], v[4:7]\n\t" \
  "ds_read_b64_tr_b16 v[40:41], v141 offset:10240\n\t" \
  "ds_read_b64_tr_b16 v[42:43], v141 offset:12800\n\t" \
  "s_waitcnt lgkmcnt(8)\n\t" \
  "v_mfma_f32_16x16x32_bf16 v[8:11], v[44:47], v[68:71], v[8:11]\n\t" \
  "v_mfma_f32_16x16x32_bf16 v[12:15], v[44:47], v[72:75], v[12:15]\n\t" \
  "ds_read_b64_tr_b16 v[44:45], v141 offset:10248\n\t" \
  "ds_read_b64_tr_b16 v[46:47], v141 offset:12808\n\t" \
  "s_waitcnt lgkmcnt(8)\n\t" \
  "v_mfma_f32_16x16x32_bf16 v[16:19], v[48:51], v[68:71], v[16:19]\n\t" \
  "v_mfma_f32_16x16x32_bf16 v[20:23], v[48:51], v[72:75], v[20:23]\n\t" \
  "ds_read_b64_tr_b16 v[48:49], v141 offset:10256\n\t" \
  "ds_read_b64_tr_b16 v[50:51], v141 offset:12816\n\t" \
  "s_waitcnt lgkmcnt(8)\n\t" \
  "v_mfma_f32_16x16x32_bf16 v[24:27], v[52:55], v[68:71], v[24:27]\n\t" \
  "v_mfma_f32_16x16x32_bf16 v[28:31], v[52:55], v[72:75], v[28:31]\n\t" \
  "ds_read_b64_tr_b16 v[52:53], v141 offset:10264\n\t" \
  "ds_read_b64_tr_b16 v[54:55], v141 offset:12824\n\t" \
  "s_waitcnt lgkmcnt(8)\n\t" \
  "v_mfma_f32_16x16x32_bf16 v[32:35], v[56:59], v[68:71], v[32:35]\n\t" \
  "v_mfma_f32_16x16x32_bf16 v[36:39], v[56:59], v[72:75], v[36:39]\n\t" \
  "ds_read_b64_tr_b16 v[56:57], v141 offset:10272\n\t" \
  "ds_read_b64_tr_b16 v[58:59], v141 offset:12832\n\t" \
  "s_waitcnt vmcnt(14)\n\t" \
  "s_waitcnt lgkmcnt(8)\n\t" \
  "v_mfma_f32_16x16x32_bf16 v[0:3], v[40:43], v[76:79], v[0:3]\n\t" \
  "v_mfma_f32_16x16x32_bf16 v[4:7], v[40:43], v[80:83], v[4:7]\n\t" \
  "ds_read_b64_tr_b16 v[40:41], v141 offset:15360\n\t" \
  "ds_read_b64_tr_b16 v[42:43], v141 offset:17920\n\t" \
  "s_waitcnt lgkmcnt(8)\n\t" \
  "v_mfma_f32_16x16x32_bf16 v[8:11], v[44:47], v[76:79], v[8:11]\n\t" \
  "v_mfma_f32_16x16x32_bf16 v[12:15], v[44:47], v[80:83], v[12:15]\n\t" \
  "ds_read_b64_tr_b16 v[44:45], v141 offset:15368\n\t" \
  "ds_read_b64_tr_b16 v[46:47], v141 offset:17928\n\t" \
  "s_waitcnt lgkmcnt(8)\n\t" \
  "v_mfma_f32_16x16x32_bf16 v[16:19], v[48:51], v[76:79], v[16:19]\n\t" \
  "v_mfma_f32_16x16x32_bf16 v[20:23], v[48:51], v[80:83], v[20:23]\n\t" \
  "ds_read_b64_tr_b16 v[48:49], v141 offset:15376\n\t" \
  "ds_read_b64_tr_b16 v[50:51], v141 offset:17936\n\t" \
  "s_waitcnt lgkmcnt(8)\n\t" \
  "v_mfma_f32_16x16x32_bf16 v[24:27], v[52:55], v[76:79], v[24:27]\n\t" \
  "v_mfma_f32_16x16x32_bf16 v[28:31], v[52:55], v[80:83], v[28:31]\n\t" \
  "ds_read_b64_tr_b16 v[52:53], v141 offset:15384\n\t" \
  "ds_read_b64_tr_b16 v[54:55], v141 offset:17944\n\t" \
  "s_waitcnt lgkmcnt(8)\n\t" \
  "v_mfma_f32_16x16x32_bf16 v[32:35], v[56:59], v[76:79], v[32:35]\n\t" \
  "v_mfma_f32_16x16x32_bf16 v[36:39], v[56:59], v[80:83], v[36:39]\n\t" \
  "ds_read_b64_tr_b16 v[56:57], v141 offset:15392\n\t" \
  "ds_read_b64_tr_b16 v[58:59], v141 offset:17952\n\t" \
  "s_waitcnt vmcnt(12)\n\t" \
  "s_waitcnt lgkmcnt(8)\n\t" \
  "v_mfma_f32_16x16x32_bf16 v[0:3], v[40:43], v[84:87], v[0:3]\n\t" \
  "v_mfma_f32_16x16x32_bf16 v[4:7], v[40:43], v[88:91], v[4:7]\n\t" \
  "ds_read_b64_tr_b16 v[40:41], v141 offset:20480\n\t" \
  "ds_read_b64_tr_b16 v[42:43], v141 offset:23040\n\t" \
  "s_waitcnt lgkmcnt(8)\n\t" \
  "v_mfma_f32_16x16x32_bf16 v[8:11], v[44:47], v[84:87], v[8:11]\n\t" \
  "v_mfma_f32_16x16x32_bf16 v[12:15], v[44:47], v[88:91], v[12:15]\n\t" \
  "ds_read_b64_tr_b16 v[44:45], v141 offset:20488\n\t" \
  "ds_read_b64_tr_b16 v[46:47], v141 offset:23048\n\t" \
  "s_waitcnt lgkmcnt(8)\n\t" \
  "v_mfma_f32_16x16x32_bf16 v[16:19], v[48:51], v[84:87], v[16:19]\n\t" \
  "v_mfma_f32_16x16x32_bf16 v[20:23], v[48:51], v[88:91], v[20:23]\n\t" \
  "ds_read_b64_tr_b16 v[48:49], v141 offset:20496\n\t" \
  "ds_read_b64_tr_b16 v[50:51], v141 offset:23056\n\t" \
  "s_waitcnt lgkmcnt(8)\n\t" \
  "v_mfma_f32_16x16x32_bf16 v[24:27], v[52:55], v[84:87], v[24:27]\n\t" \
  "v_mfma_f32_16x16x32_bf16 v[28:31], v[52:55], v[88:91], v[28:31]\n\t" \
  "ds_read_b64_tr_b16 v[52:53], v141 offset:20504\n\t" \
  "ds_read_b64_tr_b16 v[54:55], v141 offset:23064\n\t" \
  "s_waitcnt lgkmcnt(8)\n\t" \
  "v_mfma_f32_16x16x32_bf16 v[32:35], v[56:59], v[84:87], v[32:35]\n\t" \
  "v_mfma_f32_16x16x32_bf16 v[36:39], v[56:59], v[88:91], v[36:39]\n\t" \
  "ds_read_b64_tr_b16 v[56:57], v141 offset:20512\n\t" \
  "ds_read_b64_tr_b16 v[58:59], v141 offset:23072\n\t" \
  "s_waitcnt vmcnt(10)\n\t" \
  "s_waitcnt lgkmcnt(8)\n\t" \
  "v_mfma_f32_16x16x32_bf16 v[0:3], v[40:43], v[92:95], v[0:3]\n\t" \
  "v_mfma_f32_16x16x32_bf16 v[4:7], v[40:43], v[96:99], v[4:7]\n\t" \
  "ds_read_b64_tr_b16 v[40:41], v141 offset:25600\n\t" \
  "ds_read_b64_tr_b16 v[42:43], v141 offset:28160\n\t" \
  "s_waitcnt lgkmcnt(8)\n\t" \
  "v_mfma_f32_16x16x32_bf16 v[8:11], v[44:47], v[92:95], v[8:11]\n\t" \
  "v_mfma_f32_16x16x32_bf16 v[12:15], v[44:47], v[96:99], v[12:15]\n\t" \
  "ds_read_b64_tr_b16 v[44:45], v141 offset:25608\n\t" \
  "ds_read_b64_tr_b16 v[46:47], v141 offset:28168\n\t" \
  "s_waitcnt lgkmcnt(8)\n\t" \
  "v_mfma_f32_16x16x32_bf16 v[16:19], v[48:51], v[92:95], v[16:19]\n\t" \
  "v_mfma_f32_16x16x32_bf16 v[20:23], v[48:51], v[96:99], v[20:23]\n\t" \
  "ds_read_b64_tr_b16 v[48:49], v141 offset:25616\n\t" \
  "ds_read_b64_tr_b16 v[50:51], v141 offset:28176\n\t" \
  "s_waitcnt lgkmcnt(8)\n\t" \
  "v_mfma_f32_16x16x32_bf16 v[24:27], v[52:55], v[92:95], v[24:27]\n\t" \
  "v_mfma_f32_16x16x32_bf16 v[28:31], v[52:55], v[96:99], v[28:31]\n\t" \
  "ds_read_b64_tr_b16 v[52:53], v141 offset:25624\n\t" \
  "ds_read_b64_tr_b16 v[54:55], v141 offset:28184\n\t" \
  "s_waitcnt lgkmcnt(8)\n\t" \
  "v_mfma_f32_16x16x32_bf16 v[32:35], v[56:59], v[92:95], v[32:35]\n\t" \
  "v_mfma_f32_16x16x32_bf16 v[36:39], v[56:59], v[96:99], v[36:39]\n\t" \
  "ds_read_b64_tr_b16 v[56:57], v141 offset:25632\n\t" \
  "ds_read_b64_tr_b16 v[58:59], v141 offset:28192\n\t" \
  "s_mov_b64 exec, 1\n\t" \
  "ds_add_u32 v142, v143\n\t" \
  "s_mov_b64 exec, -1\n\t" \
  "s_waitcnt vmcnt(8)\n\t" \
  "s_waitcnt lgkmcnt(8)\n\t" \
  "v_mfma_f32_16x16x32_bf16 v[0:3], v[40:43], v[100:103], v[0:3]\n\t" \
  "v_mfma_f32_16x16x32_bf16 v[4:7], v[40:43], v[104:107], v[4:7]\n\t" \
  "ds_read_b64_tr_b16 v[40:41], v141 offset:30720\n\t" \
  "ds_read_b64_tr_b16 v[42:43], v141 offset:33280\n\t" \
  "s_waitcnt lgkmcnt(8)\n\t" \
  "v_mfma_f32_16x16x32_bf16 v[8:11], v[44:47], v[100:103], v[8:11]\n\t" \
  "v_mfma_f32_16x16x32_bf16 v[12:15], v[44:47], v[104:107], v[12:15]\n\t" \
  "ds_read_b64_tr_b16 v[44:45], v141 offset:30728\n\t" \
  "ds_read_b64_tr_b16 v[46:47], v141 offset:33288\n\t" \
  "s_waitcnt lgkmcnt(8)\n\t" \
  "v_mfma_f32_16x16x32_bf16 v[16:19], v[48:51], v[100:103], v[16:19]\n\t" \
  "v_mfma_f32_16x16x32_bf16 v[20:23], v[48:51], v[104:107], v[20:23]\n\t" \
  "ds_read_b64_tr_b16 v[48:49], v141 offset:30736\n\t" \
  "ds_read_b64_tr_b16 v[50:51], v141 offset:33296\n\t" \
  "s_waitcnt lgkmcnt(8)\n\t" \
  "v_mfma_f32_16x16x32_bf16 v[24:27], v[52:55], v[100:103], v[24:27]\n\t" \
  "v_mfma_f32_16x16x32_bf16 v[28:31], v[52:55], v[104:107], v[28:31]\n\t" \
  "ds_read_b64_tr_b16 v[52:53], v141 offset:30744\n\t" \
  "ds_read_b64_tr_b16 v[54:55], v141 offset:33304\n\t" \
  "s_waitcnt lgkmcnt(8)\n\t" \
  "v_mfma_f32_16x16x32_bf16 v[32:35], v[56:59], v[100:103], v[32:35]\n\t" \
  "v_mfma_f32_16x16x32_bf16 v[36:39], v[56:59], v[104:107], v[36:39]\n\t" \
  "ds_read_b64_tr_b16 v[56:57], v141 offset:30752\n\t" \
  "ds_read_b64_tr_b16 v[58:59], v141 offset:33312\n\t" \
  "s_waitcnt vmcnt(6)\n\t" \
  "s_waitcnt lgkmcnt(8)\n\t" \
  "v_mfma_f32_16x16x32_bf16 v[0:3], v[40:43], v[108:111], v[0:3]\n\t" \
  "v_mfma_f32_16x16x32_bf16 v[4:7], v[40:43], v[112:115], v[4:7]\n\t" \
  "ds_read_b64_tr_b16 v[40:41], v141 offset:35840\n\t" \
  "ds_read_b64_tr_b16 v[42:43], v141 offset:38400\n\t" \
  "s_waitcnt lgkmcnt(8)\n\t" \
  "v_mfma_f32_16x16x32_bf16 v[8:11], v[44:47], v[108:111], v[8:11]\n\t" \
  "v_mfma_f32_16x16x32_bf16 v[12:15], v[44:47], v[112:115], v[12:15]\n\t" \
  "ds_read_b64_tr_b16 v[44:45], v141 offset:35848\n\t" \
  "ds_read_b64_tr_b16 v[46:47], v141 offset:38408\n\t" \
  "s_waitcnt lgkmcnt(8)\n\t" \
  "v_mfma_f32_16x16x32_bf16 v[16:19], v[48:51], v[108:111], v[16:19]\n\t" \
  "v_mfma_f32_16x16x32_bf16 v[20:23], v[48:51], v[112:115], v[20:23]\n\t" \
  "ds_read_b64_tr_b16 v[48:49], v141 offset:35856\n\t" \
  "ds_read_b64_tr_b16 v[50:51], v141 offset:38416\n\t" \
  "s_waitcnt lgkmcnt(8)\n\t" \
  "v_mfma_f32_16x16x32_bf16 v[24:27], v[52:55], v[108:111], v[24:27]\n\t" \
  "v_mfma_f32_16x16x32_bf16 v[28:31], v[52:55], v[112:115], v[28:31]\n\t" \
  "ds_read_b64_tr_b16 v[52:53], v141 offset:35864\n\t" \
  "ds_read_b64_tr_b16 v[54:55], v141 offset:38424\n\t" \
  "s_waitcnt lgkmcnt(8)\n\t" \
  "v_mfma_f32_16x16x32_bf16 v[32:35], v[56:59], v[108:111], v[32:35]\n\t" \
  "v_mfma_f32_16x16x32_bf16 v[36:39], v[56:59], v[112:115], v[36:39]\n\t" \
  "ds_read_b64_tr_b16 v[56:57], v141 offset:35872\n\t" \
  "ds_read_b64_tr_b16 v[58:59], v141 offset:38432\n\t" \
  "s_waitcnt vmcnt(4)\n\t" \
  "s_waitcnt lgkmcnt(8)\n\t" \
  "v_mfma_f32_16x16x32_bf16 v[0:3], v[40:43], v[116:119], v[0:3]\n\t" \
  "v_mfma_f32_16x16x32_bf16 v[4:7], v[40:43], v[120:123], v[4:7]\n\t" \
  "ds_read_b64_tr_b16 v[40:41], v141 offset:40960\n\t" \
  "ds_read_b64_tr_b16 v[42:43], v141 offset:43520\n\t" \
  "s_waitcnt lgkmcnt(8)\n\t" \
  "v_mfma_f32_16x16x32_bf16 v[8:11], v[44:47], v[116:119], v[8:11]\n\t" \
  "v_mfma_f32_16x16x32_bf16 v[12:15], v[44:47], v[120:123], v[12:15]\n\t" \
  "ds_read_b64_tr_b16 v[44:45], v141 offset:40968\n\t" \
  "ds_read_b64_tr_b16 v[46:47], v141 offset:43528\n\t" \
  "s_waitcnt lgkmcnt(8)\n\t" \
  "v_mfma_f32_16x16x32_bf16 v[16:19], v[48:51], v[116:119], v[16:19]\n\t" \
  "v_mfma_f32_16x16x32_bf16 v[20:23], v[48:51], v[120:123], v[20:23]\n\t" \
  "ds_read_b64_tr_b16 v[48:49], v141 offset:40976\n\t" \
  "ds_read_b64_tr_b16 v[50:51], v141 offset:43536\n\t" \
  "s_waitcnt lgkmcnt(8)\n\t" \
  "v_mfma_f32_16x16x32_bf16 v[24:27], v[52:55], v[116:119], v[24:27]\n\t" \
  "v_mfma_f32_16x16x32_bf16 v[28:31], v[52:55], v[120:123], v[28:31]\n\t" \
  "ds_read_b64_tr_b16 v[52:53], v141 offset:40984\n\t" \
  "ds_read_b64_tr_b16 v[54:55], v141 offset:43544\n\t" \
  "s_waitcnt lgkmcnt(8)\n\t" \
  "v_mfma_f32_16x16x32_bf16 v[32:35], v[56:59], v[116:119], v[32:35]\n\t" \
  "v_mfma_f32_16x16x32_bf16 v[36:39], v[56:59], v[120:123], v[36:39]\n\t" \
  "ds_read_b64_tr_b16 v[56:57], v141 offset:40992\n\t" \
  "ds_read_b64_tr_b16 v[58:59], v141 offset:43552\n\t" \
  "s_waitcnt vmcnt(2)\n\t" \
  "s_waitcnt lgkmcnt(8)\n\t" \
  "v_mfma_f32_16x16x32_bf16 v[0:3], v[40:43], v[124:127], v[0:3]\n\t" \
  "v_mfma_f32_16x16x32_bf16 v[4:7], v[40:43], v[128:131], v[4:7]\n\t" \
  "ds_read_b64_tr_b16 v[40:41], v141 offset:46080\n\t" \
  "ds_read_b64_tr_b16 v[42:43], v141 offset:48640\n\t" \
  "s_waitcnt lgkmcnt(8)\n\t" \
  "v_mfma_f32_16x16x32_bf16 v[8:11], v[44:47], v[124:127], v[8:11]\n\t" \
  "v_mfma_f32_16x16x32_bf16 v[12:15], v[44:47], v[128:131], v[12:15]\n\t" \
  "ds_read_b64_tr_b16 v[44:45], v141 offset:46088\n\t" \
  "ds_read_b64_tr_b16 v[46:47], v141 offset:48648\n\t" \
  "s_waitcnt lgkmcnt(8)\n\t" \
  "v_mfma_f32_16x16x32_bf16 v[16:19], v[48:51], v[124:127], v[16:19]\n\t" \
  "v_mfma_f32_16x16x32_bf16 v[20:23], v[48:51], v[128:131], v[20:23]\n\t" \
  "ds_read_b64_tr_b16 v[48:49], v141 offset:46096\n\t" \
  "ds_read_b64_tr_b16 v[50:51], v141 offset:48656\n\t" \
  "s_waitcnt lgkmcnt(8)\n\t" \
  "v_mfma_f32_16x16x32_bf16 v[24:27], v[52:55], v[124:127], v[24:27]\n\t" \
  "v_mfma_f32_16x16x32_bf16 v[28:31], v[52:55], v[128:131], v[28:31]\n\t" \
  "ds_read_b64_tr_b16 v[52:53], v141 offset:46104\n\t" \
  "ds_read_b64_tr_b16 v[54:55], v141 offset:48664\n\t" \
  "s_waitcnt lgkmcnt(8)\n\t" \
  "v_mfma_f32_16x16x32_bf16 v[32:35], v[56:59], v[124:127], v[32:35]\n\t" \
  "v_mfma_f32_16x16x32_bf16 v[36:39], v[56:59], v[128:131], v[36:39]\n\t" \
  "ds_read_b64_tr_b16 v[56:57], v141 offset:46112\n\t" \
  "ds_read_b64_tr_b16 v[58:59], v141 offset:48672\n\t" \
  "s_waitcnt vmcnt(0)\n\t" \
  "s_waitcnt lgkmcnt(8)\n\t" \
  "v_mfma_f32_16x16x32_bf16 v[0:3], v[40:43], v[132:135], v[0:3]\n\t" \
  "v_mfma_f32_16x16x32_bf16 v[4:7], v[40:43], v[136:139], v[4:7]\n\t" \
  "s_waitcnt lgkmcnt(6)\n\t" \
  "v_mfma_f32_16x16x32_bf16 v[8:11], v[44:47], v[132:135], v[8:11]\n\t" \
  "v_mfma_f32_16x16x32_bf16 v[12:15], v[44:47], v[136:139], v[12:15]\n\t" \
  "s_waitcnt lgkmcnt(4)\n\t" \
  "v_mfma_f32_16x16x32_bf16 v[16:19], v[48:51], v[132:135], v[16:19]\n\t" \
  "v_mfma_f32_16x16x32_bf16 v[20:23], v[48:51], v[136:139], v[20:23]\n\t" \
  "s_waitcnt lgkmcnt(2)\n\t" \
  "v_mfma_f32_16x16x32_bf16 v[24:27], v[52:55], v[132:135], v[24:27]\n\t" \
  "v_mfma_f32_16x16x32_bf16 v[28:31], v[52:55], v[136:139], v[28:31]\n\t" \
  "s_waitcnt lgkmcnt(0)\n\t" \
  "v_mfma_f32_16x16x32_bf16 v[32:35], v[56:59], v[132:135], v[32:35]\n\t" \
  "v_mfma_f32_16x16x32_bf16 v[36:39], v[56:59], v[136:139], v[36:39]\n\t" \
  "s_nop 7\n\t"
#define TILE_GEMM_CLOB "v40", "v41", "v42", "v43", "v44", "v45", "v46", "v47", "v48", "v49", "v50", "v51", "v52", "v53", "v54", "v55", "v56", "v57", "v58", "v59", "v60", "v61", "v62", "v63", "v64", "v65", "v66", "v67", "v68", "v69", "v70", "v71", "v72", "v73", "v74", "v75", "v76", "v77", "v78", "v79", "v80", "v81", "v82", "v83", "v84", "v85", "v86", "v87", "v88", "v89", "v90", "v91", "v92", "v93", "v94", "v95", "v96", "v97", "v98", "v99", "v100", "v101", "v102", "v103", "v104", "v105", "v106", "v107", "v108", "v109", "v110", "v111", "v112", "v113", "v114", "v115", "v116", "v117", "v118", "v119", "v120", "v121", "v122", "v123", "v124", "v125", "v126", "v127", "v128", "v129", "v130", "v131", "v132", "v133", "v134", "v135", "v136", "v137", "v138", "v139"
#define K1_LOOP_ASM \
  "v_mov_b64 v[0:1], 0\n\t" \
  "v_mov_b64 v[2:3], 0\n\t" \
  "v_mov_b64 v[4:5], 0\n\t" \
  "v_mov_b64 v[6:7], 0\n\t" \
  "v_mov_b64 v[8:9], 0\n\t" \
  "v_mov_b64 v[10:11], 0\n\t" \
  "v_mov_b64 v[12:13], 0\n\t" \
  "v_mov_b64 v[14:15], 0\n\t" \
  "v_mov_b64 v[16:17], 0\n\t" \
  "v_mov_b64 v[18:19], 0\n\t" \
  "v_mov_b64 v[20:21], 0\n\t" \
  "v_mov_b64 v[22:23], 0\n\t" \
  "v_mov_b64 v[24:25], 0\n\t" \
  "v_mov_b64 v[26:27], 0\n\t" \
  "v_mov_b64 v[28:29], 0\n\t" \
  "v_mov_b64 v[30:31], 0\n\t" \
  "v_mov_b64 v[32:33], 0\n\t" \
  "v_mov_b64 v[34:35], 0\n\t" \
  "v_mov_b64 v[36:37], 0\n\t" \
  "v_mov_b64 v[38:39], 0\n\t" \
  "v_mov_b64 v[40:41], 0\n\t" \
  "v_mov_b64 v[42:43], 0\n\t" \
  "v_mov_b64 v[44:45], 0\n\t" \
  "v_mov_b64 v[46:47], 0\n\t" \
  "v_mov_b64 v[48:49], 0\n\t" \
  "v_mov_b64 v[50:51], 0\n\t" \
  "v_mov_b64 v[52:53], 0\n\t" \
  "v_mov_b64 v[54:55], 0\n\t" \
  "v_mov_b64 v[56:57], 0\n\t" \
  "v_mov_b64 v[58:59], 0\n\t" \
  "v_mov_b64 v[60:61], 0\n\t" \
  "v_mov_b64 v[62:63], 0\n\t" \
  "v_mov_b64 v[64:65], 0\n\t" \
  "v_mov_b64 v[66:67], 0\n\t" \
  "v_mov_b64 v[68:69], 0\n\t" \
  "v_mov_b64 v[70:71], 0\n\t" \
  "v_mov_b64 v[72:73], 0\n\t" \
  "v_mov_b64 v[74:75], 0\n\t" \
  "v_mov_b64 v[76:77], 0\n\t" \
  "v_mov_b64 v[78:79], 0\n\t" \
  "v_mov_b64 v[116:117], 0\n\t" \
  "v_mov_b64 v[118:119], 0\n\t" \
  "v_mov_b64 v[120:121], 0\n\t" \
  "v_mov_b64 v[122:123], 0\n\t" \
  "v_mov_b64 v[124:125], 0\n\t" \
  "v_mov_b64 v[126:127], 0\n\t" \
  "v_mov_b64 v[128:129], 0\n\t" \
  "v_mov_b64 v[130:131], 0\n\t" \
  "v_mov_b64 v[132:133], 0\n\t" \
  "v_mov_b64 v[134:135], 0\n\t" \
  "v_mov_b64 v[136:137], 0\n\t" \
  "v_mov_b64 v[138:139], 0\n\t" \
  "v_mov_b64 v[140:141], 0\n\t" \
  "v_mov_b64 v[142:143], 0\n\t" \
  "v_mov_b64 v[144:145], 0\n\t" \
  "v_mov_b64 v[146:147], 0\n\t" \
  "v_mov_b64 v[148:149], 0\n\t" \
  "v_mov_b64 v[150:151], 0\n\t" \
  "s_mov_b32 %[cnt], 0\n\t" \
  "1:\n\t" \
  "s_barrier\n\t" \
  "v_mfma_f32_16x16x32_bf16 v[0:3], v[132:135], v[116:119], v[0:3]\n\t" \
  "ds_read_b128 v[80:83], v152\n\t" \
  "v_mfma_f32_16x16x32_bf16 v[4:7], v[132:135], v[120:123], v[4:7]\n\t" \
  "ds_read_b64_tr_b16 v[96:97], v154 offset:0\n\t" \
  "v_mfma_f32_16x16x32_bf16 v[8:11], v[132:135], v[124:127], v[8:11]\n\t" \
  "ds_read_b64_tr_b16 v[98:99], v154 offset:2560\n\t" \
  "v_mfma_f32_16x16x32_bf16 v[12:15], v[132:135], v[128:131], v[12:15]\n\t" \
  "ds_read_b128 v[84:87], v152 offset:2048\n\t" \
  "v_mfma_f32_16x16x32_bf16 v[16:19], v[136:139], v[116:119], v[16:19]\n\t" \
  "ds_read_b128 v[88:91], v152 offset:4096\n\t" \
  "v_mfma_f32_16x16x32_bf16 v[20:23], v[136:139], v[120:123], v[20:23]\n\t" \
  "ds_read_b128 v[92:95], v152 offset:6144\n\t" \
  "v_mfma_f32_16x16x32_bf16 v[24:27], v[136:139], v[124:127], v[24:27]\n\t" \
  "ds_read_b64_tr_b16 v[100:101], v154 offset:8\n\t" \
  "v_mfma_f32_16x16x32_bf16 v[28:31], v[136:139], v[128:131], v[28:31]\n\t" \
  "ds_read_b64_tr_b16 v[102:103], v154 offset:2568\n\t" \
  "v_mfma_f32_16x16x32_bf16 v[32:35], v[140:143], v[116:119], v[32:35]\n\t" \
  "ds_read_b64_tr_b16 v[104:105], v154 offset:16\n\t" \
  "v_mfma_f32_16x16x32_bf16 v[36:39], v[140:143], v[120:123], v[36:39]\n\t" \
  "ds_read_b64_tr_b16 v[106:107], v154 offset:2576\n\t" \
  "v_mfma_f32_16x16x32_bf16 v[40:43], v[140:143], v[124:127], v[40:43]\n\t" \
  "ds_read_b64_tr_b16 v[108:109], v154 offset:24\n\t" \
  "v_mfma_f32_16x16x32_bf16 v[44:47], v[140:143], v[128:131], v[44:47]\n\t" \
  "ds_read_b64_tr_b16 v[110:111], v154 offset:2584\n\t" \
  "v_mfma_f32_16x16x32_bf16 v[48:51], v[144:147], v[116:119], v[48:51]\n\t" \
  "ds_read_b64_tr_b16 v[112:113], v154 offset:32\n\t" \
  "v_mfma_f32_16x16x32_bf16 v[52:55], v[144:147], v[120:123], v[52:55]\n\t" \
  "ds_read_b64_tr_b16 v[114:115], v154 offset:2592\n\t" \
  "v_mfma_f32_16x16x32_bf16 v[56:59], v[144:147], v[124:127], v[56:59]\n\t" \
  "s_and_b32 %[inc], %[cnt], 3\n\t" \
  "v_mfma_f32_16x16x32_bf16 v[60:63], v[144:147], v[128:131], v[60:63]\n\t" \
  "s_cmp_eq_u32 %[inc], 3\n\t" \
  "v_mfma_f32_16x16x32_bf16 v[64:67], v[148:151], v[116:119], v[64:67]\n\t" \
  "s_cselect_b32 %[inc], %[wrap], %[stp]\n\t" \
  "v_mfma_f32_16x16x32_bf16 v[68:71], v[148:151], v[120:123], v[68:71]\n\t" \
  "v_add_u32 v152, %[inc], v152\n\t" \
  "v_mfma_f32_16x16x32_bf16 v[72:75], v[148:151], v[124:127], v[72:75]\n\t" \
  "v_mfma_f32_16x16x32_bf16 v[76:79], v[148:151], v[128:131], v[76:79]\n\t" \
  "s_waitcnt lgkmcnt(0)\n\t" \
  "v_mfma_f32_16x16x32_bf16 v[0:3], v[96:99], v[80:83], v[0:3]\n\t" \
  "ds_read_b128 v[116:119], v153\n\t" \
  "v_mfma_f32_16x16x32_bf16 v[4:7], v[96:99], v[84:87], v[4:7]\n\t" \
  "ds_read_b64_tr_b16 v[132:133], v154 offset:5120\n\t" \
  "v_mfma_f32_16x16x32_bf16 v[8:11], v[96:99], v[88:91], v[8:11]\n\t" \
  "ds_read_b64_tr_b16 v[134:135], v154 offset:7680\n\t" \
  "v_mfma_f32_16x16x32_bf16 v[12:15], v[96:99], v[92:95], v[12:15]\n\t" \
  "ds_read_b128 v[120:123], v153 offset:2048\n\t" \
  "v_mfma_f32_16x16x32_bf16 v[16:19], v[100:103], v[80:83], v[16:19]\n\t" \
  "ds_read_b128 v[124:127], v153 offset:4096\n\t" \
  "v_mfma_f32_16x16x32_bf16 v[20:23], v[100:103], v[84:87], v[20:23]\n\t" \
  "ds_read_b128 v[128:131], v153 offset:6144\n\t" \
  "v_mfma_f32_16x16x32_bf16 v[24:27], v[100:103], v[88:91], v[24:27]\n\t" \
  "ds_read_b64_tr_b16 v[136:137], v154 offset:5128\n\t" \
  "v_mfma_f32_16x16x32_bf16 v[28:31], v[100:103], v[92:95], v[28:31]\n\t" \
  "ds_read_b64_tr_b16 v[138:139], v154 offset:7688\n\t" \
  "v_mfma_f32_16x16x32_bf16 v[32:35], v[104:107], v[80:83], v[32:35]\n\t" \
  "ds_read_b64_tr_b16 v[140:141], v154 offset:5136\n\t" \
  "v_mfma_f32_16x16x32_bf16 v[36:39], v[104:107], v[84:87], v[36:39]\n\t" \
  "ds_read_b64_tr_b16 v[142:143], v154 offset:7696\n\t" \
  "v_mfma_f32_16x16x32_bf16 v[40:43], v[104:107], v[88:91], v[40:43]\n\t" \
  "ds_read_b64_tr_b16 v[144:145], v154 offset:5144\n\t" \
  "v_mfma_f32_16x16x32_bf16 v[44:47], v[104:107], v[92:95], v[44:47]\n\t" \
  "ds_read_b64_tr_b16 v[146:147], v154 offset:7704\n\t" \
  "v_mfma_f32_16x16x32_bf16 v[48:51], v[108:111], v[80:83], v[48:51]\n\t" \
  "ds_read_b64_tr_b16 v[148:149], v154 offset:5152\n\t" \
  "v_mfma_f32_16x16x32_bf16 v[52:55], v[108:111], v[84:87], v[52:55]\n\t" \
  "ds_read_b64_tr_b16 v[150:151], v154 offset:7712\n\t" \
  "v_mfma_f32_16x16x32_bf16 v[56:59], v[108:111], v[88:91], v[56:59]\n\t" \
  "v_add_u32 v153, %[inc], v153\n\t" \
  "v_mfma_f32_16x16x32_bf16 v[60:63], v[108:111], v[92:95], v[60:63]\n\t" \
  "v_add_u32 v154, %[inc], v154\n\t" \
  "v_mfma_f32_16x16x32_bf16 v[64:67], v[112:115], v[80:83], v[64:67]\n\t" \
  "s_add_u32 %[cnt], %[cnt], 1\n\t" \
  "v_mfma_f32_16x16x32_bf16 v[68:71], v[112:115], v[84:87], v[68:71]\n\t" \
  "s_cmp_lt_u32 %[cnt], 10\n\t" \
  "v_mfma_f32_16x16x32_bf16 v[72:75], v[112:115], v[88:91], v[72:75]\n\t" \
  "v_mfma_f32_16x16x32_bf16 v[76:79], v[112:115], v[92:95], v[76:79]\n\t" \
  "s_waitcnt lgkmcnt(0)\n\t" \
  "s_cbranch_scc1 1b\n\t" \
  "v_mfma_f32_16x16x32_bf16 v[0:3], v[132:135], v[116:119], v[0:3]\n\t" \
  "v_mfma_f32_16x16x32_bf16 v[16:19], v[136:139], v[116:119], v[16:19]\n\t" \
  "v_mfma_f32_16x16x32_bf16 v[32:35], v[140:143], v[116:119], v[32:35]\n\t" \
  "v_mfma_f32_16x16x32_bf16 v[48:51], v[144:147], v[116:119], v[48:51]\n\t" \
  "v_mfma_f32_16x16x32_bf16 v[64:67], v[148:151], v[116:119], v[64:67]\n\t" \
  "v_mfma_f32_16x16x32_bf16 v[4:7], v[132:135], v[120:123], v[4:7]\n\t" \
  "v_mfma_f32_16x16x32_bf16 v[20:23], v[136:139], v[120:123], v[20:23]\n\t" \
  "v_mfma_f32_16x16x32_bf16 v[36:39], v[140:143], v[120:123], v[36:39]\n\t" \
  "v_mfma_f32_16x16x32_bf16 v[52:55], v[144:147], v[120:123], v[52:55]\n\t" \
  "v_mfma_f32_16x16x32_bf16 v[68:71], v[148:151], v[120:123], v[68:71]\n\t" \
  "v_mfma_f32_16x16x32_bf16 v[8:11], v[132:135], v[124:127], v[8:11]\n\t" \
  "v_cvt_pk_bf16_f32 v80, v0, v1\n\t" \
  "v_cvt_pk_bf16_f32 v81, v2, v3\n\t" \
  "v_mfma_f32_16x16x32_bf16 v[24:27], v[136:139], v[124:127], v[24:27]\n\t" \
  "v_cvt_pk_bf16_f32 v82, v16, v17\n\t" \
  "v_cvt_pk_bf16_f32 v83, v18, v19\n\t" \
  "v_mfma_f32_16x16x32_bf16 v[40:43], v[140:143], v[124:127], v[40:43]\n\t" \
  "v_cvt_pk_bf16_f32 v84, v32, v33\n\t" \
  "v_cvt_pk_bf16_f32 v85, v34, v35\n\t" \
  "v_mfma_f32_16x16x32_bf16 v[56:59], v[144:147], v[124:127], v[56:59]\n\t" \
  "v_cvt_pk_bf16_f32 v86, v48, v49\n\t" \
  "v_cvt_pk_bf16_f32 v87, v50, v51\n\t" \
  "v_mfma_f32_16x16x32_bf16 v[72:75], v[148:151], v[124:127], v[72:75]\n\t" \
  "v_cvt_pk_bf16_f32 v112, v64, v65\n\t" \
  "v_cvt_pk_bf16_f32 v113, v66, v67\n\t" \
  "v_mfma_f32_16x16x32_bf16 v[12:15], v[132:135], v[128:131], v[12:15]\n\t" \
  "v_cvt_pk_bf16_f32 v88, v4, v5\n\t" \
  "v_cvt_pk_bf16_f32 v89, v6, v7\n\t" \
  "v_mfma_f32_16x16x32_bf16 v[28:31], v[136:139], v[128:131], v[28:31]\n\t" \
  "v_cvt_pk_bf16_f32 v90, v20, v21\n\t" \
  "v_cvt_pk_bf16_f32 v91, v22, v23\n\t" \
  "v_mfma_f32_16x16x32_bf16 v[44:47], v[140:143], v[128:131], v[44:47]\n\t" \
  "v_cvt_pk_bf16_f32 v92, v36, v37\n\t" \
  "v_cvt_pk_bf16_f32 v93, v38, v39\n\t" \
  "v_mfma_f32_16x16x32_bf16 v[60:63], v[144:147], v[128:131], v[60:63]\n\t" \
  "v_cvt_pk_bf16_f32 v94, v52, v53\n\t" \
  "v_cvt_pk_bf16_f32 v95, v54, v55\n\t" \
  "v_mfma_f32_16x16x32_bf16 v[76:79], v[148:151], v[128:131], v[76:79]\n\t" \
  "v_cvt_pk_bf16_f32 v114, v68, v69\n\t" \
  "v_cvt_pk_bf16_f32 v115, v70, v71\n\t" \
  "v_cvt_pk_bf16_f32 v96, v8, v9\n\t" \
  "v_cvt_pk_bf16_f32 v97, v10, v11\n\t" \
  "v_cvt_pk_bf16_f32 v98, v24, v25\n\t" \
  "v_cvt_pk_bf16_f32 v99, v26, v27\n\t" \
  "v_cvt_pk_bf16_f32 v100, v40, v41\n\t" \
  "v_cvt_pk_bf16_f32 v101, v42, v43\n\t" \
  "v_cvt_pk_bf16_f32 v102, v56, v57\n\t" \
  "v_cvt_pk_bf16_f32 v103, v58, v59\n\t" \
  "v_cvt_pk_bf16_f32 v156, v72, v73\n\t" \
  "v_cvt_pk_bf16_f32 v157, v74, v75\n\t" \
  "v_cvt_pk_bf16_f32 v104, v12, v13\n\t" \
  "v_cvt_pk_bf16_f32 v105, v14, v15\n\t" \
  "v_cvt_pk_bf16_f32 v106, v28, v29\n\t" \
  "v_cvt_pk_bf16_f32 v107, v30, v31\n\t" \
  "v_cvt_pk_bf16_f32 v108, v44, v45\n\t" \
  "v_cvt_pk_bf16_f32 v109, v46, v47\n\t" \
  "v_cvt_pk_bf16_f32 v110, v60, v61\n\t" \
  "v_cvt_pk_bf16_f32 v111, v62, v63\n\t" \
  "v_cvt_pk_bf16_f32 v158, v76, v77\n\t" \
  "v_cvt_pk_bf16_f32 v159, v78, v79\n\t" \
  "s_nop 1\n\t"
#define K1_LOOP_CLOB "v0", "v1", "v2", "v3", "v4", "v5", "v6", "v7", "v8", "v9", "v10", "v11", "v12", "v13", "v14", "v15", "v16", "v17", "v18", "v19", "v20", "v21", "v22", "v23", "v24", "v25", "v26", "v27", "v28", "v29", "v30", "v31", "v32", "v33", "v34", "v35", "v36", "v37", "v38", "v39", "v40", "v41", "v42", "v43", "v44", "v45", "v46", "v47", "v48", "v49", "v50", "v51", "v52", "v53", "v54", "v55", "v56", "v57", "v58", "v59", "v60", "v61", "v62", "v63", "v64", "v65", "v66", "v67", "v68", "v69", "v70", "v71", "v72", "v73", "v74", "v75", "v76", "v77", "v78", "v79", "v116", "v117", "v118", "v119", "v120", "v121", "v122", "v123", "v124", "v125", "v126", "v127", "v128", "v129", "v130", "v131", "v132", "v133", "v134", "v135", "v136", "v137", "v138", "v139", "v140", "v141", "v142", "v143", "v144", "v145", "v146", "v147", "v148", "v149", "v150", "v151"

#define K1_ROT ((tt + 3 * (jb >> 3)) % 10)
constexpr u32 K1_SLOT = 36864, K1_W = 16384;
constexpr u32 K1_EX = 3 * K1_SLOT, K1_EXP = 4 * K1_SLOT;
constexpr u32 K1_LGT = 0;
constexpr u32 K1_PHK = 27648, K1_PHK_P = 336;
constexpr u32 K1_PHQ = 20480;
constexpr u32 K1_XCH = 73728;
constexpr u32 K1_WPHI = 156160;
constexpr u32 L_K01 = 160256;
constexpr u32 SL = 4 * REC;
constexpr u32 L_IMG = 0;
constexpr u32 L_RING = 102400;
constexpr u32 L_FLG = 162816;
constexpr u32 F_GBAR = L_FLG + 96, F_APUB = L_FLG + 100, F_AFREE = L_FLG + 104  , F_P1 = L_FLG + 112, F_PHB = L_FLG + 116,
              F_P2 = L_FLG + 120, F_KVOK = L_FLG + 124, F_BIMG = L_FLG + 128, F_QOK = L_FLG + 132, F_TAG = L_FLG + 136, F_FIN = L_FLG + 152, F_K01 = L_FLG + 156, F_AOK = L_FLG + 160, F_P3 = L_FLG + 164, F_XF = L_FLG + 176, L_GT = L_FLG + 256;
constexpr u32 LDS_BYTES = 163840;

__device__ __forceinline__ f32x16 attn_exact(rsrc_t rW, u32x4 qx, u32 q2lo, int lane) {
  const f32x16 zero16 = {0.f, 0.f, 0.f, 0.f, 0.f, 0.f, 0.f, 0.f, 0.f, 0.f, 0.f, 0.f, 0.f, 0.f, 0.f, 0.f};
  const int r32 = lane & 31, hh = lane >> 5;
  const u32x2 qz = {q2lo, 0u};
  float m = -INFINITY;
#pragma unroll 1
  for (u32 rec = 0; rec < 64; ++rec) {
    const u32x4 a1 = gl128(rW, lane * 16, WS_KVIM + rec * REC);
    const u32 a2 = gl32(rW, lane * 4, WS_KVIM + rec * REC + 1024);
    f32x16 s = mfma32(a1, qx, zero16);
    s = mfma32k8((u32x2){a2, 0u}, qz, s);
    m = fmaxf(m, max16(s));
  }
  m = xhalf_max(m);
  f32x16 O = zero16;
  const u32 av = 1280u + (u32)hh * 352u + (u32)(r32 <= 20 ? r32 : 21) * 16u;
#pragma unroll 1
  for (u32 t2 = 0; t2 < 64; ++t2) {
    const u32 rec = (t2 + 63) & 63;
    const u32x4 a1 = gl128(rW, lane * 16, WS_KVIM + rec * REC);
    const u32 a2 = gl32(rW, lane * 4, WS_KVIM + rec * REC + 1024);
    const u32x4 b1 = gl128(rW, av, WS_KVIM + t2 * REC), b2 = gl128(rW, av, WS_KVIM + t2 * REC + 704);
    f32x16 s = mfma32(a1, qx, zero16);
    s = mfma32k8((u32x2){a2, 0u}, qz, s);
#pragma unroll
    for (int r = 0; r < 16; ++r) s[r] = __builtin_amdgcn_exp2f(s[r] - m);
    O = smf(b1, lo8(s), O, 0xDDDD);
    O = smf(b2, hi8(s), O, 0xDDDD);
  }
  return O;
}

extern "C" __global__ void __launch_bounds__(768, 3)
gm_fused(const char* __restrict__ x, const char* __restrict__ wqkv, const char* __restrict__ wphi, const char* __restrict__ wq,
         const char* __restrict__ wk, const char* __restrict__ wv, const char* __restrict__ gw, const char* __restrict__ awo,
         const char* __restrict__ bwo, char* ws, char* __restrict__ out) {
  extern __shared__ __attribute__((aligned(16))) char smem[];
  ldsp lds = (ldsp)smem;
  const u32 lds0 = (u32)(uintptr_t)lds;
  const int w = __builtin_amdgcn_readfirstlane(threadIdx.x >> 6);
  const int bid = blockIdx.x, xcd = bid & 7, jb = bid >> 3;
  const rsrc_t rW = __builtin_amdgcn_make_buffer_rsrc(ws, 0, WS_END, 0x00020000);
  const f32x16 zero16 = {0.f, 0.f, 0.f, 0.f, 0.f, 0.f, 0.f, 0.f, 0.f, 0.f, 0.f, 0.f, 0.f, 0.f, 0.f, 0.f};
  asm volatile("" :: "s"(x), "s"(wqkv), "s"(wphi), "s"(wq), "s"(wk), "s"(wv), "s"(gw), "s"(awo), "s"(bwo), "s"(ws), "s"(out));
  const int type = (xcd & 1) ^ 1, tt = ((xcd >> 1) & 1) * 8 + (jb & 7), hg = (xcd >> 2) * 4 + (jb >> 3);
  const u32x4 rsWS = mkrsrc(ws, WS_END);
  u32 vim[9];
  {
    if (w < 4) {
      const int cg = w >> 1, th = w & 1;
      u32x16 pa, pb; u32x4 pc, pd;
      {
        const int lane = lane_id(), g = lane >> 4, i16 = lane & 15;
        u32 aW = lds0 + K1_W + (u32)cg * 10240u + (u32)(4 * g + (i16 >> 2)) * 160u + (u32)(i16 & 3) * 40u;
        u32 aX0 = lds0 + (u32)(64 * th + i16) * 128u + (u32)((g ^ (i16 >> 1)) * 16), aX1 = aX0 ^ 64u;
        u32 cnt, inc;
        asm volatile(K1_LOOP_ASM
                     : "=&{v[80:95]}"(pa), "=&{v[96:111]}"(pb), "=&{v[112:115]}"(pc), "=&{v[156:159]}"(pd),
                       "+{v152}"(aX0), "+{v153}"(aX1), "+{v154}"(aW), [cnt] "=&s"(cnt), [inc] "=&s"(inc)
                     : [wrap] "s"(0u - 3u * K1_SLOT), [stp] "s"(K1_SLOT)
                     : "memory", "scc", K1_LOOP_CLOB);
      }
      const int lane = lane_id();
      const u32x4 qa[4] = {{pa[0], pa[1], pa[2], pa[3]}, {pa[8], pa[9], pa[10], pa[11]}, {pb[0], pb[1], pb[2], pb[3]}, {pb[8], pb[9], pb[10], pb[11]}};
      const u32x4 qb[4] = {{pa[4], pa[5], pa[6], pa[7]}, {pa[12], pa[13], pa[14], pa[15]}, {pb[4], pb[5], pb[6], pb[7]}, {pb[12], pb[13], pb[14], pb[15]}};
      const u32x2 qc[4] = {{pc[0], pc[1]}, {pc[2], pc[3]}, {pd[0], pd[1]}, {pd[2], pd[3]}};
      if (w == 3 && type == 0) {
        const u32 dst = WS_QB + (u32)(tt * 8 + hg) * 10240u;
#pragma unroll
        for (int n = 0; n < 4; ++n) {
          st128(rW, dst + n * 2560 + lane * 16, qa[n]); st128(rW, dst + n * 2560 + 1024 + lane * 16, qb[n]); st64(rW, dst + n * 2560 + 2048 + lane * 8, qc[n]);
        }
      } else {
        ldsp xw = lds + (cg ? K1_LGT : K1_XCH) + (u32)th * 10240u;
#pragma unroll
        for (int n = 0; n < 4; ++n) {
          *(LDSAS u32x4*)(xw + n * 2560 + lane * 16) = qa[n]; *(LDSAS u32x4*)(xw + n * 2560 + 1024 + lane * 16) = qb[n];
          *(LDSAS u32x2*)(xw + n * 2560 + 2048 + lane * 8) = qc[n];
        }
      }
      LBAR();
      if (w == 3) {
        const u32x2 tag = ld64(lds + F_TAG);
        const u32 fq = WS_FQ + (u32)(tt * 8 + hg) * 128u;
        if (type == 0) { DRAIN(); st64(rW, lane == 0 ? fq : OOB, tag); }
        else { flags_wait<1>(rW, fq, 1, tag); lds_st(lds + F_QOK, 1u); }
      }
    } else {
      const int lane = lane_id(), g = lane >> 4, i16 = lane & 15;
      if (w >= 8) {
        __builtin_amdgcn_s_setprio(1);
        const int lw = w - 8, g1 = lw >> 1, hb = lw & 1;
        const u32x4 rsX = mkrsrc(x - 4096, SEQ * DM * 2 + 4096);
        const bool useq = g1 && !type;
        const u32x4 rsW = useq ? mkrsrc(wq - 4096, DM * DM * 2 + 4096) : mkrsrc(wqkv - 4096, DM * 3 * DM * 2 + 4096);
        const u32 pitch = useq ? 1280u : 3840u;
        const u32 col = (type ? (g1 ? 1280u : 640u) : 0u) + 80u * hg;
        u32 vx[4], vw[5];
        const u32 vx0 = 4096u + (u32)(lane >> 3) * 1280u + (u32)(((lane & 7) ^ (lane >> 4)) * 16);
#pragma unroll
        for (int i = 0; i < 4; ++i) vx[i] = (vx0 ^ (u32)((i & 1) * 64)) + (u32)i * 9216u;
#pragma unroll
        for (int j = 0; j < 5; ++j) {
          const u32 c = 64u * j + lane, rho = (c * 205u) >> 11, cc = c - rho * 10u;
          const u32 k = 8u * ((rho >> 2) & 3u) + 4u * (rho >> 4) + (rho & 3u);
          vw[j] = 4096u + k * pitch + cc * 16u - (u32)(j & 3) * 1024u;
        }
        const u32 sx0 = (u32)(128 * tt + 32 * lw) * 1280u, sw0 = 32u * hb * pitch + col * 2u;
        int ks = K1_ROT;
        auto issue = [&](int s) {
          const u32 base = lds0 + (u32)(s & 3) * K1_SLOT;
          u32 keep;
          asm volatile(
              "s_mov_b32 %0, m0\n\ts_mov_b32 m0, %[mx]\n\ts_nop 2\n\t"
              "buffer_load_dwordx4 %[x0], %[rx], %[sx] offen lds\n\t"
              "buffer_load_dwordx4 %[x1], %[rx], %[sx] offen offset:1024 lds\n\t"
              "buffer_load_dwordx4 %[x2], %[rx], %[sx] offen offset:2048 lds\n\t"
              "buffer_load_dwordx4 %[x3], %[rx], %[sx] offen offset:3072 lds\n\t"
              "s_mov_b32 m0, %[mw]\n\ts_nop 0\n\t"
              "buffer_load_dwordx4 %[w0], %[rw], %[sw] offen lds\n\t"
              "buffer_load_dwordx4 %[w1], %[rw], %[sw] offen offset:1024 lds\n\t"
              "buffer_load_dwordx4 %[w2], %[rw], %[sw] offen offset:2048 lds\n\t"
              "buffer_load_dwordx4 %[w3], %[rw], %[sw] offen offset:3072 lds\n\t"
              "s_add_u32 m0, %[mw], 4096\n\ts_nop 0\n\t"
              "buffer_load_dwordx4 %[w4], %[rw], %[sw] offen lds\n\t"
              "s_mov_b32 m0, %0"
              : "=&s"(keep)
              : [mx] "s"(base + (u32)lw * 4096u), [mw] "s"(base + K1_W + (u32)lw * 5120u),
                [rx] "s"(rsX), [rw] "s"(rsW), [sx] "s"(sx0 + (u32)ks * 128u), [sw] "s"(sw0 + (u32)ks * 64u * pitch),
                [x0] "v"(vx[0]), [x1] "v"(vx[1]), [x2] "v"(vx[2]), [x3] "v"(vx[3]), [w0] "v"(vw[0]), [w1] "v"(vw[1]), [w2] "v"(vw[2]), [w3] "v"(vw[3]),
                [w4] "v"(vw[4])
              : "memory", "scc");
          ks = ks == 9 ? 0 : ks + 1;
        };
        __builtin_amdgcn_s_sleep(2);
        issue(0);
        issue(1);
        WAIT_BAR(9);
#pragma unroll 1
        for (int s = 1; s < 9; ++s) {
          issue(s + 1);
          WAIT_BAR(9);
        }
        auto prefill = [&]() {
          const u32 o = (u32)lw * 8192u, vl = (u32)lane * 16u;
          dma4(rsWS, vl, WS_KVIM + o, lds0 + L_RING + o); dma4(rsWS, vl, WS_KVIM + o + 4096u, lds0 + L_RING + o + 4096u);
        };
        WAIT_BAR(0);
        lds_wait_ge<1>(lds + F_KVOK, 2);
        prefill();
      } else {
        const int v4 = w - 4, tok0 = 128 * tt + 16 * hg;
        __builtin_amdgcn_s_setprio(3);
        {
          const u32x4 rsE = mkrsrc((type ? wv : wk) + v4 * 6400, 6400);
          const u32 la = lds0 + K1_EX + (u32)v4 * 7168u;
#pragma unroll 1
          for (u32 c = 0; c < 7; ++c) dma1(rsE, (u32)lane * 16u, c * 1024u, la + c * 1024u);
          u32x4 xf[5], gf[5];
#pragma unroll
          for (int s = 0; s < 5; ++s) {
            xf[s] = *(const u32x4*)(x + (size_t)(tok0 + i16) * 1280 + v4 * 320 + s * 64 + g * 16);
            gf[s] = *(const u32x4*)(gw + v4 * 320 + s * 64 + g * 16);
          }
          u64 e = 0;
          if (w == 7) e = __hip_atomic_load((const gu64*)(ws + WS_EPOCH), __ATOMIC_RELAXED, __HIP_MEMORY_SCOPE_AGENT) + 0x9E3779B97F4A7C15ull;
          __builtin_amdgcn_s_setprio(0);
#pragma unroll
          for (int i = 0; i < 9; ++i) {
            const u32 c = 64u * (u32)(v4 + 3 * i) + lane, rho = (c * 6554u) >> 16, cc = c - rho * 10u;
            const u32 U = 4u * (rho >> 5) + ((rho >> 2) & 3u), u = (rho >> 4) & 1u, q = rho & 3u;
            const u32 hp = (U * 205u) >> 10, un = U - hp * 5u;
            const u32 row = 40u * hp + q + ((un < 4u) ? (un >> 1) * 20u + 4u * (un & 1u) + 8u * u : u * 20u + 16u);
            vim[i] = row * 1280u + cc * 16u;
            PIN(vim[i]);
          }
          __builtin_amdgcn_s_setprio(3);
          DRAIN();
          if (w == 7) lds_st(lds + L_FLG + lane * 4, lane == 34 ? (u32)e : (lane == 35 ? (u32)(e >> 32) : 0u));
          ldsp pe = lds + K1_EX + (u32)v4 * 7168u + (u32)(8 * g + (i16 >> 2)) * 40u + (u32)(i16 & 3) * 8u;
          f32x4 c0 = {0.f, 0.f, 0.f, 0.f}, c1 = c0, c2 = c0;
#pragma unroll
          for (int s = 0; s < 5; ++s) {
            const u32x4 w0 = trd2(pe + s * 1280, pe + s * 1280 + 160), w1 = trd2(pe + s * 1280 + 32, pe + s * 1280 + 192);
            if (type) { c0 = mfma16(xf[s], w0, c0); c1 = mfma16(xf[s], w1, c1); }
            else { c0 = mfma16(w0, xf[s], c0); c1 = mfma16(w1, xf[s], c1); }
            c2 = mfma16(gf[s], xf[s], c2);
          }
          ldsp pp = lds + K1_EXP + (u32)v4 * 1024u + lane * 16;
          *(LDSAS f32x4*)(pp) = c0; *(LDSAS f32x4*)(pp + 4096) = c1; *(LDSAS f32x4*)(pp + 8192) = c2;
        }
        LBAR();
        if (v4 < 3) {
          ldsp pp = lds + K1_EXP + (u32)v4 * 4096u + lane * 16;
          const f32x4 e0 = (*(LDSAS f32x4*)(pp) + *(LDSAS f32x4*)(pp + 1024)) + (*(LDSAS f32x4*)(pp + 2048) + *(LDSAS f32x4*)(pp + 3072));
          const float ks_ = type ? 1.f : QSCALE;
          const u32x2 ev = {pk2(e0[0] * ks_, e0[1] * ks_), pk2(e0[2] * ks_, e0[3] * ks_)};
          const int tile = tok0 >> 5, k0 = tok0 & 31;
          if (v4 == 2) {
            st32(rW, (type == 0 && g == 0) ? WS_GATE + (u32)(tok0 + i16) * 4u : OOB, __float_as_uint(__builtin_amdgcn_rcpf(1.f + __builtin_amdgcn_exp2f(-e0[0] * LOG2E))));
          } else if (type == 0) {
            const u32 rec = WS_KVIM + (u32)((tile + 63) & 63) * REC;
            if (v4 == 0) st64(rW, rec + (g >> 1) * 512 + (k0 + i16) * 16 + (g & 1) * 8, ev);
            else { const u32 o = g ? OOB : rec + 1024 + (k0 + i16) * 4; st32(rW, o, ev[0]); st32(rW, o + 128, ev[1]); }
          } else {
            const int d = 16 * v4 + i16, k32 = k0 + 4 * g;
            const u32x2 val = d == 20 ? (u32x2){0x3F803F80u, 0x3F803F80u} : (d == 21 ? (u32x2){0u, 0u} : ev);
            st64(rW, d <= 21 ? WS_KVIM + (u32)tile * REC + 1280 + (k32 >> 3) * 352 + d * 16 + (k32 & 7) * 2 : OOB, val);
          }
        }
        LBAR();
        {
          const u32 be = (u32)(v4 * 64 + lane) * 16u;
          dma1(mkrsrc(wphi, NH * 400 * 2), be < 3200u ? be : OOB, (u32)hg * 3200u, lds0 + K1_WPHI + (u32)v4 * 1024u);
        }
        LBAR();
        WAIT_BAR(0);
        if (w == 7) {
          const u32x2 tag = ld64(lds + F_TAG);
          st64(rW, lane < 8 ? WS_F1 + (u32)lane * 2048u + (u32)bid * 8u : OOB, tag);
          LBAR(); LBAR(); LBAR(); LBAR(); LBAR();
          const u32 fo = WS_F1 + (u32)xcd * 2048u;
          u32x4 fa = gl128(rW, lane * 16, fo), fb = gl128(rW, lane * 16, fo + 1024);
          LBAR();
          PIN(fa); PIN(fb);
          const u32 dd = (fa[0] ^ tag[0]) | (fa[1] ^ tag[1]) | (fa[2] ^ tag[0]) | (fa[3] ^ tag[1]) | (fb[0] ^ tag[0]) | (fb[1] ^ tag[1]) | (fb[2] ^ tag[0]) | (fb[3] ^ tag[1]);
          if (!__all(dd == 0u)) flags_wait256<2>(rW, fo, tag);
          asm volatile("" ::: "memory");
          const u32x4 ka = gl128(rW, lane * 16, WS_KVIM + 63 * REC);
          const u32 kc = gl32(rW, lane * 4, WS_KVIM + 63 * REC + 1024);
          const u32 gtv = gl32(rW, lane * 4, WS_GATE + (u32)(2 * tt + type) * 256u);
          u32 wrm = gl32(rW, lane < 34 ? (u32)(jb * 34 + lane) * 128u : OOB, WS_KVIM + 32768);
          asm volatile("" ::: "memory");
          lds_st(lds + F_KVOK, 2u);
          if (bid == 0 && lane == 0)
            __hip_atomic_store((gu64*)(ws + WS_EPOCH), (u64)tag[0] | ((u64)tag[1] << 32), __ATOMIC_RELAXED, __HIP_MEMORY_SCOPE_AGENT);
          *(LDSAS u32x4*)(lds + L_K01 + lane * 16) = ka; *(LDSAS u32*)(lds + L_K01 + 1024 + lane * 4) = kc;
          *(LDSAS u32*)(lds + L_GT + lane * 4) = gtv;
          PIN(wrm);
        } else {
          LBAR(); LBAR(); LBAR(); LBAR();
          LBAR(); LBAR();
        }
      }
      __builtin_amdgcn_s_setprio(2);
      const int j8 = w - 4;
      const u32 aP = K1_WPHI + (((i16 & 3) == g) ? (u32)g * 800u + (u32)(i16 >> 2) * 40u : 3200u);
      u32x4 a0[5], a1[5]; u32x2 a2[5];
#pragma unroll
      for (int T = 0; T < 5; ++T) {
        a0[T] = trd2(lds + aP + T * 8, lds + aP + T * 8 + 160);
        a1[T] = trd2(lds + aP + T * 8 + 320, lds + aP + T * 8 + 480);
        a2[T] = trd(lds + aP + T * 8 + 640);
      }
      LBAR();
      ldsp xr = lds + K1_XCH + (u32)j8 * 2560u;
      const u32x4 bq0 = ld128(xr + lane * 16), bq1 = ld128(xr + 1024 + lane * 16);
      const u32x2 bq2 = ld64(xr + 2048 + lane * 8);
      if (w >= 8) {
        DRAIN();
        if (lds_arrive(lds + F_K01) == 3 && lane < 8) lds_st(lds + L_FLG + 32 + lane * 4, 3);
      }
      f32x4 pq[5];
#pragma unroll
      for (int T = 0; T < 5; ++T) pq[T] = mfma16(a0[T], bq0, (f32x4){0.f, 0.f, 0.f, 0.f});
#pragma unroll
      for (int T = 0; T < 5; ++T) pq[T] = mfma16(a1[T], bq1, pq[T]);
#pragma unroll
      for (int T = 0; T < 5; ++T) pq[T] = mfma16((u32x4){a2[T][0], a2[T][1], 0u, 0u}, (u32x4){bq2[0], bq2[1], 0u, 0u}, pq[T]);
      float mx = fmaxf(pq[0][0], pq[0][1]), mn = fminf(pq[0][0], pq[0][1]);
      mx = fmaxf(fmaxf(mx, pq[0][2]), pq[0][3]); mn = fminf(fminf(mn, pq[0][2]), pq[0][3]);
#pragma unroll
      for (int T = 1; T < 5; ++T) {
        mx = fmaxf(fmaxf(mx, pq[T][0]), pq[T][1]); mx = fmaxf(fmaxf(mx, pq[T][2]), pq[T][3]);
        mn = fminf(fminf(mn, pq[T][0]), pq[T][1]); mn = fminf(fminf(mn, pq[T][2]), pq[T][3]);
      }
      const float cp = -mx * LOG2E, cn = mn * LOG2E;
      f32x4 ep[5], en[5];
#pragma unroll
      for (int T = 0; T < 5; ++T) { ep[T] = pq[T] * LOG2E + cp; en[T] = pq[T] * (-LOG2E) + cn; PIN(ep[T]); PIN(en[T]); }
#pragma unroll
      for (int T = 0; T < 5; ++T)
#pragma unroll
        for (int i = 0; i < 4; ++i) { ep[T][i] = __builtin_amdgcn_exp2f(ep[T][i]); en[T][i] = __builtin_amdgcn_exp2f(en[T][i]); }
      const f32x4 sp = (ep[0] + ep[1]) + (ep[2] + ep[3]) + ep[4], sn = (en[0] + en[1]) + (en[2] + en[3]) + en[4];
      const float rp = __builtin_amdgcn_rcpf((sp[0] + sp[1]) + (sp[2] + sp[3])), rn = __builtin_amdgcn_rcpf((sn[0] + sn[1]) + (sn[2] + sn[3]));
      u32 ph[20];
#pragma unroll
      for (int T = 0; T < 5; ++T) {
        const f32x4 a = ep[T] * rp, b = en[T] * rn;
        ph[2 * T] = pk2(a[0], a[1]); ph[2 * T + 1] = pk2(a[2], a[3]);
        ph[10 + 2 * T] = pk2(b[0], b[1]); ph[11 + 2 * T] = pk2(b[2], b[3]);
      }
      const int tl = 16 * j8 + i16;
      if (type == 0) {
        ldsp dst = lds + K1_PHQ + (u32)(4 * g + (tl >> 5)) * 2560u + (tl & 31) * 16;
#pragma unroll
        for (int c = 0; c < 5; ++c) *(LDSAS u32x4*)(dst + c * 512) = (u32x4){ph[4 * c], ph[4 * c + 1], ph[4 * c + 2], ph[4 * c + 3]};
      } else {
        ldsp dst = lds + K1_PHK + (u32)tl * K1_PHK_P + (u32)g * 80u;
#pragma unroll
        for (int c = 0; c < 5; ++c) *(LDSAS u32x4*)(dst + 16 * c) = (u32x4){ph[4 * c], ph[4 * c + 1], ph[4 * c + 2], ph[4 * c + 3]};
        lds_arrive(lds + F_PHB);
        lds_wait_ge<1>(lds + F_PHB, 8);
        const int hl = j8 >> 1, dt = j8 & 1;
        const u32 trow = (u32)(8 * g + (i16 >> 2));
        ldsp pa = lds + K1_PHK + trow * K1_PHK_P + (u32)hl * 80u + (u32)(i16 & 3) * 8u;
        const u32 vrow = (u32)(hl * 16) + (trow & 15u), vq = (u32)(i16 & 3);
        ldsp pb = lds + K1_LGT + (u32)(g >> 1) * 2560u + (dt ? 2048u + vrow * 8u : (vq >> 1) * 1024u + vrow * 16u + (vq & 1u) * 8u);
        const u32 vstep = dt ? 32u : 64u;
        f32x4 kv[3];
#pragma unroll
        for (int ft = 0; ft < 3; ++ft) kv[ft] = (f32x4){0.f, 0.f, 0.f, 0.f};
#pragma unroll
        for (int ks = 0; ks < 4; ++ks) {
          u32x4 bv = trd2(pb + ks * 5120, pb + ks * 5120 + vstep);
          if (dt && i16 == 4) bv = (u32x4){0x3F803F80u, 0x3F803F80u, 0x3F803F80u, 0x3F803F80u};
#pragma unroll
          for (int ft = 0; ft < 3; ++ft) {
            u32x4 av = trd2(pa + ks * 32 * K1_PHK_P + ft * 32, pa + (ks * 32 + 4) * K1_PHK_P + ft * 32);
            kv[ft] = mfma16(av, bv, kv[ft]);
          }
        }
        const u32 kb = WS_KVP + (u32)((hg * 16 + tt) * 4 + hl) * 3360u;
        const u32 ko = dt ? (i16 < 5 ? kb + 2560u + (u32)(g * 5 + i16) * 16u : OOB) : kb + (u32)lane * 16u, kst = dt ? 320u : 1024u;
        st128(rW, ko, __builtin_bit_cast(u32x4, kv[0])); st128(rW, ko + kst, __builtin_bit_cast(u32x4, kv[1]));
        st128(rW, g < 2 ? ko + 2 * kst : OOB, __builtin_bit_cast(u32x4, kv[2]));
      }
      lds_arrive(lds + F_P2);
      if (type) {
        const u32x2 tag = ld64(lds + F_TAG);
        DRAIN();
        if (lds_arrive(lds + F_P1) == 7) { st64(rW, lane == 0 ? WS_F2 + (u32)(hg * 16 + tt) * 8u : OOB, tag); }
      }
    }
  }

  if (w >= 8) {
    __builtin_amdgcn_s_setprio(3);
    const int hw = w - 8, kh = hw >> 1, t64 = xcd * 4 + (jb & 3), cgp = jb >> 2;
    const u32x2 tag = ld64(lds + F_TAG);
    lds_wait_ge<2>(lds + F_P2, 8);
    {
      const int lane = lane_id(), g = lane >> 4, i16 = lane & 15, r32 = lane & 31, hh = lane >> 5;
      u32x4 p0[4], p1[4], p2[4]; u32 gt[4];
      if (type) {
        const u32 it = (u32)(tt * 53 + hw * 14 + i16);
        const bool ok = i16 < 14 && hw * 14 + i16 < 53 && it < 840u;
        const u32 hl = (it * 4994u) >> 20, e = it - hl * 210u;
        const u32 e1 = e - 160u, ft1 = (e1 * 205u) >> 12, r1 = e1 - ft1 * 20u, g1 = (r1 * 205u) >> 10;
        const u32 d = e < 160u ? (e & 15u) : 16u + r1 - g1 * 5u, f = e < 160u ? (e >> 6) * 16u + ((e >> 4) & 3u) * 4u : ft1 * 16u + g1 * 4u;
        const u32 vo = ok ? it * 16u + (u32)g * 4u * 13440u : OOB, pb = WS_KVP + (u32)hg * 16u * 13440u;
        u32 dst = (ok && g == 0) ? WS_KVT + (u32)(4 * hg + hl) * 1680u + d * 80u + f * 2u : OOB;
        PIN(dst);
        if (hw == 0) { flags_wait<1>(rW, WS_F2 + (u32)hg * 128u, 16, tag); lds_st(lds + F_GBAR, 1u); }
        else lds_wait_ge<2>(lds + F_GBAR, 1);
        f32x4 a = (__builtin_bit_cast(f32x4, gl128(rW, vo, pb)) + __builtin_bit_cast(f32x4, gl128(rW, vo, pb + 13440))) +
                  (__builtin_bit_cast(f32x4, gl128(rW, vo, pb + 2 * 13440)) + __builtin_bit_cast(f32x4, gl128(rW, vo, pb + 3 * 13440)));
#pragma unroll
        for (int i = 0; i < 4; ++i) { a[i] += __shfl_xor(a[i], 16); a[i] += __shfl_xor(a[i], 32); }
        st64(rW, dst, (u32x2){pk2(a[0], a[1]), pk2(a[2], a[3])});
        DRAIN();
        if (lds_arrive(lds + F_APUB) == 3) { st64(rW, lane == 0 ? WS_F3 + (u32)(hg * 16 + tt) * 8u : OOB, tag); }
      } else {
#pragma unroll
        for (int t4 = 0; t4 < 4; ++t4) {
          ldsp pk = lds + K1_PHQ + (u32)(4 * hw + t4) * 2560u;
          p0[t4] = ld128(pk + lane * 16); p1[t4] = ld128(pk + 1024 + lane * 16); p2[t4] = ld128(pk + 2048 + r32 * 16);
          gt[t4] = gl32(rW, r32 * 4, WS_GATE + (u32)(128 * tt + 32 * t4) * 4u);
          PIN(p0[t4]); PIN(p1[t4]); PIN(p2[t4]);
        }
        lds_arrive(lds + F_P3);
        if (hw == 0) { lds_wait_ge<4>(lds + F_QOK, 4); lds_arrive(lds + F_P3); }
      }
      if (type == 0) {
        if (hw == 0) { flags_wait<1>(rW, WS_F3 + (u32)hg * 128u, 16, tag); lds_st(lds + F_GBAR, 1u); }
        lds_wait_ge<2>(lds + F_GBAR, 1);
        const u32 ao = (u32)(r32 < 20 ? r32 : 20) * 80u + (u32)hh * 16u, ab = WS_KVT + (u32)(4 * hg + hw) * 1680u;
        const u32x4 a0 = gl128(rW, ao, ab), a1 = gl128(rW, ao, ab + 32), a2 = gl128(rW, hh ? OOB : ao, ab + 64);
        const u32 vo0 = WS_OFR + (u32)(8 * tt + (r32 >> 4)) * 20480u + (r32 & 15) * 16 + (u32)(2 * hg + (hw >> 1)) * 1280u;
        const u32 vo1 = vo0 + hh * 256 + (hw & 1) * 512, vo2 = hh ? OOB : vo0 + 1024 + (hw & 1) * 8;
        f32x16 N[4];
#pragma unroll
        for (int t4 = 0; t4 < 4; ++t4) N[t4] = mfma32(a0, p0[t4], zero16);
#pragma unroll
        for (int t4 = 0; t4 < 4; ++t4) N[t4] = mfma32(a1, p1[t4], N[t4]);
#pragma unroll
        for (int t4 = 0; t4 < 4; ++t4) N[t4] = mfma32(a2, p2[t4], N[t4]);
#pragma unroll
        for (int t4 = 0; t4 < 4; ++t4) {
          const float sa = __uint_as_float(gt[t4]) * __builtin_amdgcn_rcpf(N[t4][12] + 1e-6f);
          st128(rW, vo1 + t4 * 40960, (u32x4){pk2(N[t4][0] * sa, N[t4][1] * sa), pk2(N[t4][2] * sa, N[t4][3] * sa), pk2(N[t4][4] * sa, N[t4][5] * sa), pk2(N[t4][6] * sa, N[t4][7] * sa)});
          st64(rW, vo2 + t4 * 40960, (u32x2){pk2(N[t4][8] * sa, N[t4][9] * sa), pk2(N[t4][10] * sa, N[t4][11] * sa)});
        }
        DRAIN();
        if (lds_arrive(lds + F_APUB) == 3) { st64(rW, lane == 0 ? WS_FLGA + (u32)(tt * 16 + hg) * 8u : OOB, tag); }
      }
      if (hw == 0) { flags_wait<1>(rW, WS_FLGA + (u32)(t64 >> 1) * 128u, 8, tag); lds_arrive(lds + F_AOK); }
    }
    f32x16 ac0 = zero16, ac1 = zero16; f32x8 ac2 = {0.f, 0.f, 0.f, 0.f, 0.f, 0.f, 0.f, 0.f};
#pragma unroll 1
    for (int part = 0; part < 2; ++part) {
      if (part == 0) {
        lds_wait_ge<2>(lds + F_AOK, 4u);
      } else {
        lds_arrive(lds + F_AFREE + 4);
        lds_wait_ge<8>(lds + F_BIMG, 3u);
        lds_wait_ge<4>(lds + F_FIN, 4u);
        if (hw == 0) { flags_wait<1>(rW, WS_FLGB + (u32)t64 * 256u, 32, tag); lds_st(lds + F_FIN, 5u); }
        else lds_wait_ge<0>(lds + F_FIN, 5u);
      }
      const int ln = lane_id();
      u32 s0 = WS_OFR + (u32)(part * 2621440 + (4 * t64 + 2 * (hw & 1) + kh) * 20480 + kh * 10240), s1 = s0 + (kh ? 0u - 20480u : 20480u);
      asm volatile(TILE_GEMM_ASM
                   : "+{v[0:15]}"(ac0), "+{v[16:31]}"(ac1), "+{v[32:39]}"(ac2), [s0] "+s"(s0), [s1] "+s"(s1)
                   : "{v140}"((u32)ln * 16u), "{v141}"(lds0 + L_IMG + (u32)(4 * (ln >> 4) + ((ln & 15) >> 2)) * 160u + (u32)(ln & 3) * 40u + (u32)kh * 51200u), "{v142}"(lds0 + F_AFREE), "{v143}"(1u), [rs] "s"(rsWS)
                   : "memory", "scc", TILE_GEMM_CLOB);
    }
    {
      const int ln = lane_id();
      f32x4 acc[5][2];
#pragma unroll
      for (int n = 0; n < 2; ++n) {
        acc[0][n] = (f32x4){ac0[4 * n], ac0[4 * n + 1], ac0[4 * n + 2], ac0[4 * n + 3]}; acc[1][n] = (f32x4){ac0[8 + 4 * n], ac0[9 + 4 * n], ac0[10 + 4 * n], ac0[11 + 4 * n]};
        acc[2][n] = (f32x4){ac1[4 * n], ac1[4 * n + 1], ac1[4 * n + 2], ac1[4 * n + 3]}; acc[3][n] = (f32x4){ac1[8 + 4 * n], ac1[9 + 4 * n], ac1[10 + 4 * n], ac1[11 + 4 * n]};
        acc[4][n] = (f32x4){ac2[4 * n], ac2[4 * n + 1], ac2[4 * n + 2], ac2[4 * n + 3]};
      }
      ldsp xb = lds + L_RING + 3 * SL;
      ldsp xw = xb + (u32)hw * 5120u + ln * 16, xr = xb + (u32)(hw ^ 2) * 5120u + ln * 16;
#pragma unroll
      for (int T = 0; T < 5; ++T) *(LDSAS f32x4*)(xw + T * 1024) = acc[T][1];
      asm volatile("" ::: "memory");
      lds_st(lds + F_XF + hw * 4, 1u);
      f32x4 pv[5];
#pragma unroll 1
      for (u32 it = 0; it < SPIN_CAP; ++it) {
        asm volatile("" ::: "memory");
        const u32 f = lds_ld(lds + F_XF + (hw ^ 2) * 4);
        asm volatile("" ::: "memory");
#pragma unroll
        for (int T = 0; T < 5; ++T) pv[T] = *(LDSAS f32x4*)(xr + T * 1024);
        if (__builtin_amdgcn_readfirstlane(f)) break;
      }
      u32 o[10];
#pragma unroll
      for (int T = 0; T < 5; ++T) {
        const f32x4 v = acc[T][0] + pv[T];
        o[2 * T] = pk2(v[0], v[1]); o[2 * T + 1] = pk2(v[2], v[3]);
      }
      const rsrc_t rOut = __builtin_amdgcn_make_buffer_rsrc(out, 0, SEQ * DM * 2, 0x00020000);
      const int dst = (64 * t64 + 32 * (hw & 1) + 16 * kh + (ln & 15)) * 1280 + cgp * 160 + (ln >> 4) * 40;
      __builtin_amdgcn_raw_buffer_store_b128((u32x4){o[0], o[1], o[2], o[3]}, rOut, dst, 0, 2);
      __builtin_amdgcn_raw_buffer_store_b128((u32x4){o[4], o[5], o[6], o[7]}, rOut, dst + 16, 0, 2);
      __builtin_amdgcn_raw_buffer_store_b64((u32x2){o[8], o[9]}, rOut, dst + 32, 0, 2);
    }
    return;
  }

  if (w >= 4) {
    __builtin_amdgcn_s_setprio(3);
    if (w < 7) {
      const int v4 = w - 4, cgp = jb >> 2;
#pragma unroll 1
      for (int part = 0; part < 2; ++part) {
        if (part == 0) lds_wait_ge<4>(lds + (type ? F_P2 : F_P3), type ? 8u : 5u);
        const u32x4 rsO = mkrsrc(part ? bwo : awo, DM * DM * 2);
#pragma unroll 1
        for (u32 pi = 0; pi < 4; ++pi) {
          const u32 per = (pi >> 1) | ((pi & 1) << 1);
          if (part && !(pi & 1)) lds_wait_ge<2>(lds + F_AFREE + (pi >> 1) * 4, 4u);
          const u32 so = per * 204800u + (u32)cgp * 160u, la = lds0 + L_IMG + (per * 25u + (u32)v4) * 1024u;
#pragma unroll
          for (int i = 0; i < 8; ++i) dma1(rsO, vim[i], so, la + i * 3072);
          if (v4 == 0) dma1(rsO, vim[8], so, la + 24576);
        }
        DRAIN();
        lds_arrive(lds + (part ? F_BIMG : F_AOK));
        if (part == 0) {
          const int ln = lane_id();
          const u32 l0 = (u32)jb * 200u + (u32)v4 * 64u + ln;
          u32 dummy;
          asm volatile("s_nop 4\n\tbuffer_load_dword %0, %1, %2, 0 offen\n\tbuffer_load_dword %0, %3, %2, 0 offen\n\ts_waitcnt vmcnt(0)"
                       : "=&v"(dummy) : "v"(l0 * 128u), "s"(mkrsrc(bwo, DM * DM * 2)), "v"((v4 == 0 && ln < 8) ? (l0 + 192u) * 128u : OOB) : "memory");
        }
      }
    } else {
      const int ln = lane_id();
      const u32 vl = (u32)ln * 16u;
      u32 s = 3, slot = 3, ps = 3;
      lds_wait_ge<2>(lds + L_FLG + 32, 3);
#pragma unroll 1
      for (u32 it = 0; it < SPIN_CAP && ps < 16; ++it) {
        const u32 fl = lds_ld(lds + L_FLG + (ln & 3) * 4);
        const u32 ns = s;
        if (s < 16 && __all(fl + 4 >= s)) {
          const u32 so = WS_KVIM + s * SL, la = lds0 + L_RING + slot * SL;
          dma4(rsWS, vl, so, la); dma4(rsWS, vl, so + 4096u, la + 4096u); dma2h(rsWS, vl, so + 8192u, la + 8192u);
          asm volatile("s_waitcnt vmcnt(11)" ::: "memory");
          ++s; slot = slot == 4 ? 0 : slot + 1;
        } else {
          DRAIN();
          if (ps == ns) __builtin_amdgcn_s_sleep(14);
        }
        if (ps != ns) { ps = ns; if (ln < 4) lds_st(lds + L_FLG + 32 + ln * 4, ps); }
      }
    }
    return;
  }
  const int head = 4 * hg + w, t64 = 2 * tt + type;
  u32x4 q1[2]; u32x2 q2[2];
  f32x16 O0, O1;
  const int lane = lane_id(), r32 = lane & 31, hh = lane >> 5;
  float og[2]; u32x2 tag; u32 vo1, vo2;
  __builtin_amdgcn_s_setprio(3);
  {
    const u32 qo1 = (u32)(r32 >> 4) * 2560u + (u32)hh * 1024u + (u32)(w * 16 + (r32 & 15)) * 16u;
    const u32 qo2 = (u32)(r32 >> 4) * 2560u + 2048u + (u32)(w * 16 + (r32 & 15)) * 8u + (u32)hh * 4u;
    if (type) {
      lds_wait_ge<1>(lds + F_QOK, 1);
#pragma unroll
      for (int j = 0; j < 2; ++j) {
        const u32 qrow = WS_QB + (u32)(tt * 8 + hg) * 10240u + (u32)j * 5120u;
        q1[j] = gl128(rW, qo1, qrow);
        q2[j] = (u32x2){gl32(rW, qo2, qrow), 0u};
      }
    } else {
#pragma unroll
      for (int j = 0; j < 2; ++j) {
        q1[j] = ld128(lds + K1_LGT + j * 5120 + qo1);
        q2[j] = (u32x2){*(LDSAS u32*)(lds + K1_LGT + j * 5120 + qo2), 0u};
      }
      PIN(q1[0]); PIN(q1[1]); PIN(q2[0]); PIN(q2[1]);
      lds_arrive(lds + F_QOK);
    }
    u32x4 k1 = ld128(lds + L_K01 + lane * 16);
    u32x2 k2 = {*(LDSAS u32*)(lds + L_K01 + 1024 + lane * 4), hh ? 0u : 0x00003F80u};
    f32x16 S0, S1, S2, S3;
#pragma unroll
    for (int j = 0; j < 2; ++j) {
      f32x16 t = mfma32(k1, q1[j], zero16);
      t = mfma32k8(k2, q2[j], t);
      const float m = xhalf_max(max16(t));
      q2[j][1] = hh ? 0u : (pk2(-(m + 48.f), 0.f) & 0xffffu);
    }
    S0 = mfma32(k1, q1[0], zero16);
    S0 = mfma32k8(k2, q2[0], S0);
    u32x4 vlo, vhi;
    const u32 rb = lds0 + L_RING;
    u32 aK1 = rb + (u32)lane * 16u, aK2 = rb + 1024u + (u32)lane * 4u;
    u32 aV = rb + 1280u + (u32)hh * 352u + (u32)(r32 <= 20 ? r32 : 21) * 16u;
    u32 aF = lds0 + L_FLG + (u32)w * 4u, tmp;
    og[0] = 1.f - *(LDSAS float*)(lds + L_GT + r32 * 4); og[1] = 1.f - *(LDSAS float*)(lds + L_GT + 128 + r32 * 4);
    tag = ld64(lds + F_TAG);
    const u32 vo = WS_OFR + 2621440u + (u32)(4 * t64 + (r32 >> 4)) * 20480u + (r32 & 15) * 16 + (head >> 1) * 1280;
    vo1 = vo + hh * 256 + (head & 1) * 512; vo2 = hh ? OOB : vo + 1024 + (head & 1) * 8;
    PIN(og[0]); PIN(og[1]); PIN(tag); PIN(vo1); PIN(vo2);
    u32 cnt, inc, slot, rdy;
    asm volatile(ATTN_LOOP_ASM
                 : "=&{v[0:15]}"(O0), "=&{v[16:31]}"(O1), "+{v[32:47]}"(S0), "=&{v[48:63]}"(S1), "=&{v[64:79]}"(S2), "=&{v[80:95]}"(S3),
                   "+{v[108:111]}"(k1), "+{v[112:113]}"(k2), "=&{v[116:119]}"(vlo), "=&{v[120:123]}"(vhi),
                   "+{v124}"(aK1), "+{v125}"(aK2), "+{v126}"(aV), "=&{v115}"(tmp), [cnt] "=&s"(cnt), [inc] "=&s"(inc), [slot] "=&s"(slot), [rdy] "=&s"(rdy)
                 : "{v[96:99]}"(q1[0]), "{v[100:103]}"(q1[1]), "{v[104:105]}"(q2[0]), "{v[106:107]}"(q2[1]), "{v114}"(0xDDDD), "{v127}"(aF),
                   [wrap] "s"(0u - 4u * SL), [stg] "s"(SL)
                 : "memory", "scc");
  }
#pragma unroll
  for (int j = 0; j < 2; ++j) {
    f32x16 O = j ? O1 : O0;
    u32x4 r1; u32x2 r2;
#pragma unroll 1
    for (int pass = 0; pass < 2; ++pass) {
      const float l = from_hi(O[8]), sb = og[j] * __builtin_amdgcn_rcpf(l);
      const f32x4 y0 = (f32x4){O[0], O[1], O[2], O[3]} * sb, y1 = (f32x4){O[4], O[5], O[6], O[7]} * sb, y2 = (f32x4){O[8], O[9], O[10], O[11]} * sb;
      r1 = (u32x4){pk2(y0[0], y0[1]), pk2(y0[2], y0[3]), pk2(y1[0], y1[1]), pk2(y1[2], y1[3])};
      r2 = (u32x2){pk2(y2[0], y2[1]), pk2(y2[2], y2[3])};
      const f32x4 ys = (y0 + y1) + y2;
      const float yy = (ys[0] + ys[1]) + (ys[2] + ys[3]);
      const bool bad = !(__builtin_fabsf(yy) < 3.0e38f) || !(l < 1.0e37f) || !(l > 1.0e-25f);
      if (__builtin_expect(pass == 1 || !__any(bad), 1)) break;
      O = attn_exact(rW, q1[j], q2[j][0], lane);
    }
    st128(rW, vo1 + j * 40960, r1);
    st64(rW, vo2 + j * 40960, r2);
  }
  DRAIN();
  st64(rW, lane == 0 ? WS_FLGB + (u32)(t64 * 32 + head) * 8u : OOB, tag);
  lds_arrive(lds + F_FIN);
}

extern "C" void kernel_launch(void* const* d_in, const int* in_sizes, int n_in, void* d_out, int out_size, void* d_ws,
                              size_t ws_size, hipStream_t stream) {
  (void)in_sizes; (void)n_in; (void)out_size; (void)ws_size;
  (void)hipFuncSetAttribute((const void*)gm_fused, hipFuncAttributeMaxDynamicSharedMemorySize, LDS_BYTES);
  gm_fused<<<256, 768, LDS_BYTES, stream>>>((const char*)d_in[0], (const char*)d_in[1], (const char*)d_in[2], (const char*)d_in[5], (const char*)d_in[6],
      (const char*)d_in[7], (const char*)d_in[9], (const char*)d_in[3], (const char*)d_in[8], (char*)d_ws, (char*)d_out);
}
```

```cpp
#include <hip/hip_runtime.h>
#include <hip/hip_bf16.h>
#include <stdint.h>

typedef unsigned int u32;
typedef unsigned long long u64;
typedef u32 u32x2 __attribute__((ext_vector_type(2)));
typedef u32 u32x4 __attribute__((ext_vector_type(4)));
typedef u32 u32x16 __attribute__((ext_vector_type(16)));
typedef float f32x2 __attribute__((ext_vector_type(2)));
typedef float f32x4 __attribute__((ext_vector_type(4)));
typedef float f32x8 __attribute__((ext_vector_type(8)));
typedef float f32x16 __attribute__((ext_vector_type(16)));
typedef __bf16 bf16x2 __attribute__((ext_vector_type(2)));
typedef __bf16 bf16x8 __attribute__((ext_vector_type(8)));
typedef __bf16 bf16x16 __attribute__((ext_vector_type(16)));
typedef short s16x4 __attribute__((ext_vector_type(4)));
#define LDSAS __attribute__((address_space(3)))
typedef LDSAS char* ldsp;
typedef __attribute__((address_space(1))) u64 gu64;

constexpr int SEQ = 2048, DM = 640, NH = 32, DH = 20;
constexpr float LOG2E = 1.4426950408889634f;
constexpr float QSCALE = 1.4426950408889634f * 0.22360679774997896f;

constexpr u32 REC = 2688;
constexpr u32 WS_QB   = 0;
constexpr u32 WS_KVIM = WS_QB + 1310720;
constexpr u32 WS_KVP  = WS_KVIM + 64 * REC;
constexpr u32 WS_KVT  = WS_KVP + 32 * 16 * 3360;
constexpr u32 WS_GATE = WS_KVT + 32 * 1680;
constexpr u32 WS_OFR  = WS_GATE + 8192;
constexpr u32 WS_F1   = WS_OFR + 5242880;
constexpr u32 WS_F2   = WS_F1 + 16384;
constexpr u32 WS_F3   = WS_F2 + 1024;
constexpr u32 WS_FLGA = WS_F3 + 1024;
constexpr u32 WS_FLGB = WS_FLGA + 2048;
constexpr u32 WS_FQ   = WS_FLGB + 16384;
constexpr u32 WS_EPOCH = WS_FQ + 16384;
constexpr u32 WS_END  = WS_EPOCH + 256;
constexpr u32 OOB = 0x40000000u;
constexpr u32 SPIN_CAP = 1u << 20;

#define SBAR() __builtin_amdgcn_sched_barrier(0)
#define PIN(x) asm volatile("" : "+v"(x))
#define LBAR() asm volatile("s_waitcnt lgkmcnt(0)\n\ts_barrier" ::: "memory")
#define WAIT_BAR(N) asm volatile("s_waitcnt vmcnt(" #N ") lgkmcnt(0)\n\ts_barrier" ::: "memory")
#define DRAIN() asm volatile("s_waitcnt vmcnt(0)" ::: "memory")

__device__ __forceinline__ int lane_id() {
  int l;
  asm volatile("v_mbcnt_lo_u32_b32 %0, -1, 0\n\tv_mbcnt_hi_u32_b32 %0, -1, %0" : "=v"(l));
  return l;
}
__device__ __forceinline__ u32 pk2(float a, float b) {
  f32x2 v = {a, b};
  return __builtin_bit_cast(u32, __builtin_convertvector(v, bf16x2));
}
__device__ __forceinline__ f32x4 mfma16(u32x4 a, u32x4 b, f32x4 c) {
  return __builtin_amdgcn_mfma_f32_16x16x32_bf16(__builtin_bit_cast(bf16x8, a), __builtin_bit_cast(bf16x8, b), c, 0, 0, 0);
}
__device__ __forceinline__ f32x16 mfma32(u32x4 a, u32x4 b, f32x16 c) {
  return __builtin_amdgcn_mfma_f32_32x32x16_bf16(__builtin_bit_cast(bf16x8, a), __builtin_bit_cast(bf16x8, b), c, 0, 0, 0);
}
__device__ __forceinline__ f32x16 mfma32k8(u32x2 a, u32x2 b, f32x16 c) {
  return __builtin_amdgcn_mfma_f32_32x32x8bf16_1k(__builtin_bit_cast(s16x4, a), __builtin_bit_cast(s16x4, b), c, 0, 0, 0);
}
__device__ __forceinline__ f32x16 smf(u32x4 a, f32x8 b, f32x16 c, int idx) {
  return __builtin_amdgcn_smfmac_f32_32x32x32_bf16(__builtin_bit_cast(bf16x8, a), __builtin_bit_cast(bf16x16, b), c, idx, 0, 0);
}
__device__ __forceinline__ u32x2 trd(ldsp p) {
  return __builtin_bit_cast(u32x2, __builtin_amdgcn_ds_read_tr16_b64_v4i16((LDSAS s16x4*)p));
}
__device__ __forceinline__ u32x4 trd2(ldsp p0, ldsp p1) {
  u32x2 a = trd(p0), b = trd(p1);
  return (u32x4){a[0], a[1], b[0], b[1]};
}
__device__ __forceinline__ u32x4 ld128(ldsp p) { return *(LDSAS u32x4*)p; }
__device__ __forceinline__ u32x2 ld64(ldsp p) { return *(LDSAS u32x2*)p; }
__device__ __forceinline__ float bflo(u32 v) { return __uint_as_float(v << 16); }
__device__ __forceinline__ float bfhi(u32 v) { return __uint_as_float(v & 0xffff0000u); }

__device__ __forceinline__ u32x4 mkrsrc(const void* p, u32 bytes) {
  uint64_t a = (uint64_t)p;
  return (u32x4){(u32)__builtin_amdgcn_readfirstlane((u32)a), (u32)__builtin_amdgcn_readfirstlane((u32)(a >> 32) & 0xffffu), bytes, 0x00020000u};
}
__device__ __forceinline__ void dma1(u32x4 rs, u32 voff, u32 soff, u32 ldsaddr) {
  u32 keep;
  asm volatile("s_mov_b32 %0, m0\n\ts_mov_b32 m0, %4\n\ts_nop 2\n\tbuffer_load_dwordx4 %1, %2, %3 offen lds\n\ts_mov_b32 m0, %0"
               : "=&s"(keep) : "v"(voff), "s"(rs), "s"(__builtin_amdgcn_readfirstlane(soff)), "s"(__builtin_amdgcn_readfirstlane(ldsaddr)) : "memory");
}
__device__ __forceinline__ void dma1s(u32x4 rs, u32 voff, u32 soff, u32 ldsaddr) {
  u32 keep;
  asm volatile("s_mov_b32 %0, m0\n\ts_mov_b32 m0, %4\n\ts_nop 2\n\tbuffer_load_dwordx4 %1, %2, %3 offen sc1 lds\n\ts_mov_b32 m0, %0"
               : "=&s"(keep) : "v"(voff), "s"(rs), "s"(__builtin_amdgcn_readfirstlane(soff)), "s"(__builtin_amdgcn_readfirstlane(ldsaddr)) : "memory");
}
__device__ __forceinline__ void dma4(u32x4 rs, u32 voff, u32 soff, u32 ldsaddr) {
  u32 keep;
  asm volatile("s_mov_b32 %0, m0\n\ts_mov_b32 m0, %4\n\ts_nop 2\n\t"
               "buffer_load_dwordx4 %1, %2, %3 offen sc1 lds\n\tbuffer_load_dwordx4 %1, %2, %3 offen offset:1024 sc1 lds\n\t"
               "buffer_load_dwordx4 %1, %2, %3 offen offset:2048 sc1 lds\n\tbuffer_load_dwordx4 %1, %2, %3 offen offset:3072 sc1 lds\n\t"
               "s_mov_b32 m0, %0"
               : "=&s"(keep) : "v"(voff), "s"(rs), "s"(__builtin_amdgcn_readfirstlane(soff)), "s"(__builtin_amdgcn_readfirstlane(ldsaddr)) : "memory");
}
__device__ __forceinline__ void dma2h(u32x4 rs, u32 voff, u32 soff, u32 ldsaddr) {
  u32 keep;
  asm volatile("s_mov_b32 %0, m0\n\ts_mov_b32 m0, %4\n\ts_nop 2\n\t"
               "buffer_load_dwordx4 %1, %2, %3 offen sc1 lds\n\tbuffer_load_dwordx4 %1, %2, %3 offen offset:1024 sc1 lds\n\t"
               "s_mov_b32 exec_hi, 0\n\tbuffer_load_dwordx4 %1, %2, %3 offen offset:2048 sc1 lds\n\ts_mov_b32 exec_hi, -1\n\t"
               "s_mov_b32 m0, %0"
               : "=&s"(keep) : "v"(voff), "s"(rs), "s"(__builtin_amdgcn_readfirstlane(soff)), "s"(__builtin_amdgcn_readfirstlane(ldsaddr)) : "memory");
}
typedef __amdgpu_buffer_rsrc_t rsrc_t;
__device__ __forceinline__ void st128(rsrc_t r, u32 off, u32x4 v) { __builtin_amdgcn_raw_buffer_store_b128(v, r, (int)off, 0, 16); }
__device__ __forceinline__ void st64(rsrc_t r, u32 off, u32x2 v) { __builtin_amdgcn_raw_buffer_store_b64(v, r, (int)off, 0, 16); }
__device__ __forceinline__ void st32(rsrc_t r, u32 off, u32 v) { __builtin_amdgcn_raw_buffer_store_b32(v, r, (int)off, 0, 16); }
__device__ __forceinline__ void st16(rsrc_t r, u32 off, u32 v) { __builtin_amdgcn_raw_buffer_store_b16((unsigned short)v, r, (int)off, 0, 16); }
__device__ __forceinline__ u32x4 gl128(rsrc_t r, u32 voff, u32 soff) { return __builtin_amdgcn_raw_buffer_load_b128(r, (int)voff, (int)soff, 16); }
__device__ __forceinline__ u32x2 gl64(rsrc_t r, u32 voff, u32 soff) { return __builtin_amdgcn_raw_buffer_load_b64(r, (int)voff, (int)soff, 16); }
__device__ __forceinline__ u32 gl32(rsrc_t r, u32 voff, u32 soff) { return __builtin_amdgcn_raw_buffer_load_b32(r, (int)voff, (int)soff, 16); }

__device__ __forceinline__ f32x8 lo8(const f32x16& s) { return __builtin_shufflevector(s, s, 0, 1, 2, 3, 4, 5, 6, 7); }
__device__ __forceinline__ f32x8 hi8(const f32x16& s) { return __builtin_shufflevector(s, s, 8, 9, 10, 11, 12, 13, 14, 15); }
__device__ __forceinline__ float max16(const f32x16& s) {
  float a = fmaxf(fmaxf(s[0], s[1]), s[2]), b = fmaxf(fmaxf(s[3], s[4]), s[5]);
  a = fmaxf(fmaxf(a, s[6]), s[7]); b = fmaxf(fmaxf(b, s[8]), s[9]);
  a = fmaxf(fmaxf(a, s[10]), s[11]); b = fmaxf(fmaxf(b, s[12]), s[13]);
  a = fmaxf(fmaxf(a, s[14]), s[15]);
  return fmaxf(a, b);
}
__device__ __forceinline__ float xhalf_max(float m) {
  auto r = __builtin_amdgcn_permlane32_swap(__float_as_uint(m), __float_as_uint(m), false, false);
  return fmaxf(__uint_as_float(r[0]), __uint_as_float(r[1]));
}
__device__ __forceinline__ float from_hi(float v) {
  auto r = __builtin_amdgcn_permlane32_swap(__float_as_uint(v), __float_as_uint(v), false, false);
  return __uint_as_float(r[1]);
}
__device__ __forceinline__ u32 lds_ld(ldsp p) { return __hip_atomic_load((LDSAS u32*)p, __ATOMIC_RELAXED, __HIP_MEMORY_SCOPE_WORKGROUP); }
__device__ __forceinline__ void lds_st(ldsp p, u32 v) { __hip_atomic_store((LDSAS u32*)p, v, __ATOMIC_RELAXED, __HIP_MEMORY_SCOPE_WORKGROUP); }
template <int SLEEP>
__device__ __forceinline__ void lds_wait_ge(ldsp p, u32 want) {
#pragma unroll 1
  for (u32 it = 0; it < SPIN_CAP; ++it) {
    if ((u32)__builtin_amdgcn_readfirstlane(lds_ld(p)) >= want) break;
    __builtin_amdgcn_s_sleep(SLEEP);
  }
  asm volatile("" ::: "memory");
}
__device__ __forceinline__ u32 lds_arrive(ldsp p) {
  u32 old; u64 keep;
  asm volatile("v_mov_b32 %0, 1\n\ts_mov_b64 %1, exec\n\ts_mov_b64 exec, 1\n\ts_waitcnt lgkmcnt(0)\n\tds_add_rtn_u32 %0, %2, %0\n\t"
               "s_waitcnt lgkmcnt(0)\n\ts_mov_b64 exec, %1" : "=&v"(old), "=&s"(keep) : "v"((u32)(uintptr_t)p) : "memory");
  return (u32)__builtin_amdgcn_readfirstlane(old);
}
template <int SLEEP>
__device__ __forceinline__ void flags_wait(rsrc_t r, u32 off, int n, u32x2 tag) {
  const int lane = lane_id();
  const u32 vo = (u32)(lane < n ? lane : 0) * 8u;
#pragma unroll 1
  for (u32 it = 0; it < SPIN_CAP; ++it) {
    asm volatile("" ::: "memory");
    const u32x2 v = gl64(r, vo, off);
    if (__all(v[0] == tag[0] && v[1] == tag[1])) break;
    __builtin_amdgcn_s_sleep(SLEEP);
  }
  asm volatile("" ::: "memory");
}
template <int SLEEP>
__device__ __forceinline__ void flags_wait256(rsrc_t r, u32 off, u32x2 tag) {
  const u32 vo = (u32)lane_id() * 16u;
#pragma unroll 1
  for (u32 it = 0; it < SPIN_CAP; ++it) {
    asm volatile("" ::: "memory");
    const u32x4 a = gl128(r, vo, off), b = gl128(r, vo, off + 1024);
    const u32 d = (a[0] ^ tag[0]) | (a[1] ^ tag[1]) | (a[2] ^ tag[0]) | (a[3] ^ tag[1]) | (b[0] ^ tag[0]) | (b[1] ^ tag[1]) | (b[2] ^ tag[0]) | (b[3] ^ tag[1]);
    if (__all(d == 0u)) break;
    __builtin_amdgcn_s_sleep(SLEEP);
  }
  asm volatile("" ::: "memory");
}

#define ATTN_LOOP_ASM \
  "s_nop 11\n\t" \
  "v_mov_b64 v[0:1], 0\n\t" \
  "v_mov_b64 v[2:3], 0\n\t" \
  "v_mov_b64 v[4:5], 0\n\t" \
  "v_mov_b64 v[6:7], 0\n\t" \
  "v_mov_b64 v[8:9], 0\n\t" \
  "v_mov_b64 v[10:11], 0\n\t" \
  "v_mov_b64 v[12:13], 0\n\t" \
  "v_mov_b64 v[14:15], 0\n\t" \
  "v_mov_b64 v[16:17], 0\n\t" \
  "v_mov_b64 v[18:19], 0\n\t" \
  "v_mov_b64 v[20:21], 0\n\t" \
  "v_mov_b64 v[22:23], 0\n\t" \
  "v_mov_b64 v[24:25], 0\n\t" \
  "v_mov_b64 v[26:27], 0\n\t" \
  "v_mov_b64 v[28:29], 0\n\t" \
  "v_mov_b64 v[30:31], 0\n\t" \
  "v_mov_b64 v[80:81], 0\n\t" \
  "v_mov_b64 v[82:83], 0\n\t" \
  "v_mov_b64 v[84:85], 0\n\t" \
  "v_mov_b64 v[86:87], 0\n\t" \
  "v_mov_b64 v[88:89], 0\n\t" \
  "v_mov_b64 v[90:91], 0\n\t" \
  "v_mov_b64 v[92:93], 0\n\t" \
  "v_mov_b64 v[94:95], 0\n\t" \
  "v_mov_b64 v[116:117], 0\n\t" \
  "v_mov_b64 v[118:119], 0\n\t" \
  "v_mov_b64 v[120:121], 0\n\t" \
  "v_mov_b64 v[122:123], 0\n\t" \
  "s_mov_b32 %[cnt], 0\n\t" \
  "s_mov_b32 %[slot], 0\n\t" \
  "s_setprio 0\n\t" \
  "2:\n\t" \
  "s_mov_b32 %[inc], 0\n\t" \
  "3:\n\t" \
  "ds_read_b32 v115, v127 offset:32\n\t" \
  "s_add_u32 %[inc], %[inc], 1\n\t" \
  "s_waitcnt lgkmcnt(0)\n\t" \
  "v_readfirstlane_b32 %[rdy], v115\n\t" \
  "s_nop 3\n\t" \
  "s_cmp_gt_u32 %[rdy], %[cnt]\n\t" \
  "s_cbranch_scc1 4f\n\t" \
  "s_sleep 1\n\t" \
  "s_cmp_lt_u32 %[inc], 0x100000\n\t" \
  "s_cbranch_scc1 3b\n\t" \
  "s_branch 9f\n\t" \
  "4:\n\t" \
  "v_mfma_f32_32x32x16_bf16 v[48:63], v[108:111], v[100:103], 0\n\t" \
  "ds_read_b128 v[108:111], v124 offset:0\n\t" \
  "v_exp_f32 v32, v32\n\t" \
  "v_exp_f32 v33, v33\n\t" \
  "v_exp_f32 v34, v34\n\t" \
  "v_exp_f32 v35, v35\n\t" \
  "v_mfma_f32_32x32x8_bf16 v[48:63], v[112:113], v[106:107], v[48:63]\n\t" \
  "ds_read_b32 v112, v125 offset:0\n\t" \
  "v_exp_f32 v36, v36\n\t" \
  "v_exp_f32 v37, v37\n\t" \
  "v_exp_f32 v38, v38\n\t" \
  "v_exp_f32 v39, v39\n\t" \
  "v_smfmac_f32_32x32x32_bf16 v[16:31], v[116:119], v[80:87], v114\n\t" \
  "ds_read_b128 v[116:119], v126 offset:0\n\t" \
  "v_exp_f32 v40, v40\n\t" \
  "v_exp_f32 v41, v41\n\t" \
  "v_exp_f32 v42, v42\n\t" \
  "v_exp_f32 v43, v43\n\t" \
  "v_smfmac_f32_32x32x32_bf16 v[16:31], v[120:123], v[88:95], v114\n\t" \
  "ds_read_b128 v[120:123], v126 offset:704\n\t" \
  "v_exp_f32 v44, v44\n\t" \
  "v_exp_f32 v45, v45\n\t" \
  "v_exp_f32 v46, v46\n\t" \
  "v_exp_f32 v47, v47\n\t" \
  "s_waitcnt lgkmcnt(2)\n\t" \
  "v_mfma_f32_32x32x16_bf16 v[64:79], v[108:111], v[96:99], 0\n\t" \
  "v_exp_f32 v48, v48\n\t" \
  "v_exp_f32 v49, v49\n\t" \
  "v_exp_f32 v50, v50\n\t" \
  "v_exp_f32 v51, v51\n\t" \
  "v_mfma_f32_32x32x8_bf16 v[64:79], v[112:113], v[104:105], v[64:79]\n\t" \
  "v_exp_f32 v52, v52\n\t" \
  "v_exp_f32 v53, v53\n\t" \
  "v_exp_f32 v54, v54\n\t" \
  "v_exp_f32 v55, v55\n\t" \
  "s_waitcnt lgkmcnt(0)\n\t" \
  "v_smfmac_f32_32x32x32_bf16 v[0:15], v[116:119], v[32:39], v114\n\t" \
  "v_exp_f32 v56, v56\n\t" \
  "v_exp_f32 v57, v57\n\t" \
  "v_exp_f32 v58, v58\n\t" \
  "v_exp_f32 v59, v59\n\t" \
  "v_smfmac_f32_32x32x32_bf16 v[0:15], v[120:123], v[40:47], v114\n\t" \
  "v_exp_f32 v60, v60\n\t" \
  "v_exp_f32 v61, v61\n\t" \
  "v_exp_f32 v62, v62\n\t" \
  "v_exp_f32 v63, v63\n\t" \
  "v_mfma_f32_32x32x16_bf16 v[80:95], v[108:111], v[100:103], 0\n\t" \
  "ds_read_b128 v[108:111], v124 offset:2688\n\t" \
  "v_exp_f32 v64, v64\n\t" \
  "v_exp_f32 v65, v65\n\t" \
  "v_exp_f32 v66, v66\n\t" \
  "v_exp_f32 v67, v67\n\t" \
  "v_mfma_f32_32x32x8_bf16 v[80:95], v[112:113], v[106:107], v[80:95]\n\t" \
  "ds_read_b32 v112, v125 offset:2688\n\t" \
  "v_exp_f32 v68, v68\n\t" \
  "v_exp_f32 v69, v69\n\t" \
  "v_exp_f32 v70, v70\n\t" \
  "v_exp_f32 v71, v71\n\t" \
  "v_smfmac_f32_32x32x32_bf16 v[16:31], v[116:119], v[48:55], v114\n\t" \
  "ds_read_b128 v[116:119], v126 offset:2688\n\t" \
  "v_exp_f32 v72, v72\n\t" \
  "v_exp_f32 v73, v73\n\t" \
  "v_exp_f32 v74, v74\n\t" \
  "v_exp_f32 v75, v75\n\t" \
  "v_smfmac_f32_32x32x32_bf16 v[16:31], v[120:123], v[56:63], v114\n\t" \
  "ds_read_b128 v[120:123], v126 offset:3392\n\t" \
  "v_exp_f32 v76, v76\n\t" \
  "v_exp_f32 v77, v77\n\t" \
  "v_exp_f32 v78, v78\n\t" \
  "v_exp_f32 v79, v79\n\t" \
  "s_waitcnt lgkmcnt(2)\n\t" \
  "v_mfma_f32_32x32x16_bf16 v[32:47], v[108:111], v[96:99], 0\n\t" \
  "v_exp_f32 v80, v80\n\t" \
  "v_exp_f32 v81, v81\n\t" \
  "v_exp_f32 v82, v82\n\t" \
  "v_exp_f32 v83, v83\n\t" \
  "v_mfma_f32_32x32x8_bf16 v[32:47], v[112:113], v[104:105], v[32:47]\n\t" \
  "s_cmp_eq_u32 %[slot], 4\n\t" \
  "s_cselect_b32 %[inc], %[wrap], %[stg]\n\t" \
  "v_exp_f32 v84, v84\n\t" \
  "v_exp_f32 v85, v85\n\t" \
  "v_exp_f32 v86, v86\n\t" \
  "v_exp_f32 v87, v87\n\t" \
  "s_waitcnt lgkmcnt(0)\n\t" \
  "v_smfmac_f32_32x32x32_bf16 v[0:15], v[116:119], v[64:71], v114\n\t" \
  "s_cselect_b32 %[slot], -1, %[slot]\n\t" \
  "s_add_u32 %[slot], %[slot], 1\n\t" \
  "v_exp_f32 v88, v88\n\t" \
  "v_exp_f32 v89, v89\n\t" \
  "v_exp_f32 v90, v90\n\t" \
  "v_exp_f32 v91, v91\n\t" \
  "v_smfmac_f32_32x32x32_bf16 v[0:15], v[120:123], v[72:79], v114\n\t" \
  "v_exp_f32 v92, v92\n\t" \
  "v_exp_f32 v93, v93\n\t" \
  "v_exp_f32 v94, v94\n\t" \
  "v_exp_f32 v95, v95\n\t" \
  "v_mfma_f32_32x32x16_bf16 v[48:63], v[108:111], v[100:103], 0\n\t" \
  "ds_read_b128 v[108:111], v124 offset:5376\n\t" \
  "v_exp_f32 v32, v32\n\t" \
  "v_exp_f32 v33, v33\n\t" \
  "v_exp_f32 v34, v34\n\t" \
  "v_exp_f32 v35, v35\n\t" \
  "v_mfma_f32_32x32x8_bf16 v[48:63], v[112:113], v[106:107], v[48:63]\n\t" \
  "ds_read_b32 v112, v125 offset:5376\n\t" \
  "v_exp_f32 v36, v36\n\t" \
  "v_exp_f32 v37, v37\n\t" \
  "v_exp_f32 v38, v38\n\t" \
  "v_exp_f32 v39, v39\n\t" \
  "v_smfmac_f32_32x32x32_bf16 v[16:31], v[116:119], v[80:87], v114\n\t" \
  "ds_read_b128 v[116:119], v126 offset:5376\n\t" \
  "v_exp_f32 v40, v40\n\t" \
  "v_exp_f32 v41, v41\n\t" \
  "v_exp_f32 v42, v42\n\t" \
  "v_exp_f32 v43, v43\n\t" \
  "v_smfmac_f32_32x32x32_bf16 v[16:31], v[120:123], v[88:95], v114\n\t" \
  "ds_read_b128 v[120:123], v126 offset:6080\n\t" \
  "v_exp_f32 v44, v44\n\t" \
  "v_exp_f32 v45, v45\n\t" \
  "v_exp_f32 v46, v46\n\t" \
  "v_exp_f32 v47, v47\n\t" \
  "s_waitcnt lgkmcnt(2)\n\t" \
  "v_mfma_f32_32x32x16_bf16 v[64:79], v[108:111], v[96:99], 0\n\t" \
  "s_add_u32 %[cnt], %[cnt], 1\n\t" \
  "v_exp_f32 v48, v48\n\t" \
  "v_exp_f32 v49, v49\n\t" \
  "v_exp_f32 v50, v50\n\t" \
  "v_exp_f32 v51, v51\n\t" \
  "v_mfma_f32_32x32x8_bf16 v[64:79], v[112:113], v[104:105], v[64:79]\n\t" \
  "v_exp_f32 v52, v52\n\t" \
  "v_exp_f32 v53, v53\n\t" \
  "v_exp_f32 v54, v54\n\t" \
  "v_exp_f32 v55, v55\n\t" \
  "s_waitcnt lgkmcnt(0)\n\t" \
  "v_smfmac_f32_32x32x32_bf16 v[0:15], v[116:119], v[32:39], v114\n\t" \
  "v_exp_f32 v56, v56\n\t" \
  "v_exp_f32 v57, v57\n\t" \
  "v_exp_f32 v58, v58\n\t" \
  "v_exp_f32 v59, v59\n\t" \
  "v_smfmac_f32_32x32x32_bf16 v[0:15], v[120:123], v[40:47], v114\n\t" \
  "v_exp_f32 v60, v60\n\t" \
  "v_exp_f32 v61, v61\n\t" \
  "v_exp_f32 v62, v62\n\t" \
  "v_exp_f32 v63, v63\n\t" \
  "v_mfma_f32_32x32x16_bf16 v[80:95], v[108:111], v[100:103], 0\n\t" \
  "ds_read_b128 v[108:111], v124 offset:8064\n\t" \
  "v_exp_f32 v64, v64\n\t" \
  "v_exp_f32 v65, v65\n\t" \
  "v_exp_f32 v66, v66\n\t" \
  "v_exp_f32 v67, v67\n\t" \
  "v_mfma_f32_32x32x8_bf16 v[80:95], v[112:113], v[106:107], v[80:95]\n\t" \
  "ds_read_b32 v112, v125 offset:8064\n\t" \
  "v_exp_f32 v68, v68\n\t" \
  "v_exp_f32 v69, v69\n\t" \
  "v_exp_f32 v70, v70\n\t" \
  "v_exp_f32 v71, v71\n\t" \
  "v_smfmac_f32_32x32x32_bf16 v[16:31], v[116:119], v[48:55], v114\n\t" \
  "ds_read_b128 v[116:119], v126 offset:8064\n\t" \
  "v_exp_f32 v72, v72\n\t" \
  "v_exp_f32 v73, v73\n\t" \
  "v_exp_f32 v74, v74\n\t" \
  "v_exp_f32 v75, v75\n\t" \
  "v_smfmac_f32_32x32x32_bf16 v[16:31], v[120:123], v[56:63], v114\n\t" \
  "ds_read_b128 v[120:123], v126 offset:8768\n\t" \
  "v_exp_f32 v76, v76\n\t" \
  "v_exp_f32 v77, v77\n\t" \
  "v_exp_f32 v78, v78\n\t" \
  "v_exp_f32 v79, v79\n\t" \
  "s_waitcnt lgkmcnt(2)\n\t" \
  "v_mfma_f32_32x32x16_bf16 v[32:47], v[108:111], v[96:99], 0\n\t" \
  "ds_read_b32 v115, v127 offset:32\n\t" \
  "v_exp_f32 v80, v80\n\t" \
  "v_exp_f32 v81, v81\n\t" \
  "v_exp_f32 v82, v82\n\t" \
  "v_exp_f32 v83, v83\n\t" \
  "v_mfma_f32_32x32x8_bf16 v[32:47], v[112:113], v[104:105], v[32:47]\n\t" \
  "v_add_u32 v124, %[inc], v124\n\t" \
  "v_add_u32 v125, %[inc], v125\n\t" \
  "v_add_u32 v126, %[inc], v126\n\t" \
  "v_exp_f32 v84, v84\n\t" \
  "v_exp_f32 v85, v85\n\t" \
  "v_exp_f32 v86, v86\n\t" \
  "v_exp_f32 v87, v87\n\t" \
  "s_waitcnt lgkmcnt(1)\n\t" \
  "v_smfmac_f32_32x32x32_bf16 v[0:15], v[116:119], v[64:71], v114\n\t" \
  "v_exp_f32 v88, v88\n\t" \
  "v_exp_f32 v89, v89\n\t" \
  "v_exp_f32 v90, v90\n\t" \
  "v_exp_f32 v91, v91\n\t" \
  "s_waitcnt lgkmcnt(0)\n\t" \
  "v_readfirstlane_b32 %[rdy], v115\n\t" \
  "v_mov_b32 v115, %[cnt]\n\t" \
  "v_smfmac_f32_32x32x32_bf16 v[0:15], v[120:123], v[72:79], v114\n\t" \
  "ds_write_b32 v127, v115\n\t" \
  "s_cmp_gt_u32 %[rdy], %[cnt]\n\t" \
  "v_exp_f32 v92, v92\n\t" \
  "v_exp_f32 v93, v93\n\t" \
  "v_exp_f32 v94, v94\n\t" \
  "v_exp_f32 v95, v95\n\t" \
  "s_cbranch_scc1 4b\n\t" \
  "s_cmp_lt_u32 %[cnt], 16\n\t" \
  "s_cbranch_scc1 2b\n\t" \
  "9:\n\t" \
  "s_setprio 3\n\t" \
  "v_smfmac_f32_32x32x32_bf16 v[16:31], v[116:119], v[80:87], v114\n\t" \
  "v_smfmac_f32_32x32x32_bf16 v[16:31], v[120:123], v[88:95], v114\n\t" \
  "s_waitcnt lgkmcnt(0)\n\t" \
  "s_nop 12\n\t"
#define TILE_GEMM_ASM \
  "s_nop 4\n\t" \
  "buffer_load_dwordx4 v[60:63], v140, %[rs], %[s0] offen offset:0 sc1\n\t" \
  "buffer_load_dwordx4 v[64:67], v140, %[rs], %[s1] offen offset:0 sc1\n\t" \
  "buffer_load_dwordx4 v[68:71], v140, %[rs], %[s0] offen offset:1024 sc1\n\t" \
  "buffer_load_dwordx4 v[72:75], v140, %[rs], %[s1] offen offset:1024 sc1\n\t" \
  "buffer_load_dwordx4 v[76:79], v140, %[rs], %[s0] offen offset:2048 sc1\n\t" \
  "buffer_load_dwordx4 v[80:83], v140, %[rs], %[s1] offen offset:2048 sc1\n\t" \
  "buffer_load_dwordx4 v[84:87], v140, %[rs], %[s0] offen offset:3072 sc1\n\t" \
  "buffer_load_dwordx4 v[88:91], v140, %[rs], %[s1] offen offset:3072 sc1\n\t" \
  "s_add_u32 %[s0], %[s0], 4096\n\t" \
  "buffer_load_dwordx4 v[92:95], v140, %[rs], %[s0] offen offset:0 sc1\n\t" \
  "s_add_u32 %[s1], %[s1], 4096\n\t" \
  "buffer_load_dwordx4 v[96:99], v140, %[rs], %[s1] offen offset:0 sc1\n\t" \
  "buffer_load_dwordx4 v[100:103], v140, %[rs], %[s0] offen offset:1024 sc1\n\t" \
  "buffer_load_dwordx4 v[104:107], v140, %[rs], %[s1] offen offset:1024 sc1\n\t" \
  "buffer_load_dwordx4 v[108:111], v140, %[rs], %[s0] offen offset:2048 sc1\n\t" \
  "buffer_load_dwordx4 v[112:115], v140, %[rs], %[s1] offen offset:2048 sc1\n\t" \
  "buffer_load_dwordx4 v[116:119], v140, %[rs], %[s0] offen offset:3072 sc1\n\t" \
  "buffer_load_dwordx4 v[120:123], v140, %[rs], %[s1] offen offset:3072 sc1\n\t" \
  "s_add_u32 %[s0], %[s0], 4096\n\t" \
  "buffer_load_dwordx4 v[124:127], v140, %[rs], %[s0] offen offset:0 sc1\n\t" \
  "s_add_u32 %[s1], %[s1], 4096\n\t" \
  "buffer_load_dwordx4 v[128:131], v140, %[rs], %[s1] offen offset:0 sc1\n\t" \
  "buffer_load_dwordx4 v[132:135], v140, %[rs], %[s0] offen offset:1024 sc1\n\t" \
  "buffer_load_dwordx4 v[136:139], v140, %[rs], %[s1] offen offset:1024 sc1\n\t" \
  "ds_read_b64_tr_b16 v[40:41], v141 offset:0\n\t" \
  "ds_read_b64_tr_b16 v[42:43], v141 offset:2560\n\t" \
  "ds_read_b64_tr_b16 v[44:45], v141 offset:8\n\t" \
  "ds_read_b64_tr_b16 v[46:47], v141 offset:2568\n\t" \
  "ds_read_b64_tr_b16 v[48:49], v141 offset:16\n\t" \
  "ds_read_b64_tr_b16 v[50:51], v141 offset:2576\n\t" \
  "ds_read_b64_tr_b16 v[52:53], v141 offset:24\n\t" \
  "ds_read_b64_tr_b16 v[54:55], v141 offset:2584\n\t" \
  "ds_read_b64_tr_b16 v[56:57], v141 offset:32\n\t" \
  "ds_read_b64_tr_b16 v[58:59], v141 offset:2592\n\t" \
  "s_waitcnt vmcnt(18)\n\t" \
  "s_waitcnt lgkmcnt(8)\n\t" \
  "v_mfma_f32_16x16x32_bf16 v[0:3], v[40:43], v[60:63], v[0:3]\n\t" \
  "v_mfma_f32_16x16x32_bf16 v[4:7], v[40:43], v[64:67], v[4:7]\n\t" \
  "ds_read_b64_tr_b16 v[40:41], v141 offset:5120\n\t" \
  "ds_read_b64_tr_b16 v[42:43], v141 offset:7680\n\t" \
  "s_waitcnt lgkmcnt(8)\n\t" \
  "v_mfma_f32_16x16x32_bf16 v[8:11], v[44:47], v[60:63], v[8:11]\n\t" \
  "v_mfma_f32_16x16x32_bf16 v[12:15], v[44:47], v[64:67], v[12:15]\n\t" \
  "ds_read_b64_tr_b16 v[44:45], v141 offset:5128\n\t" \
  "ds_read_b64_tr_b16 v[46:47], v141 offset:7688\n\t" \
  "s_waitcnt lgkmcnt(8)\n\t" \
  "v_mfma_f32_16x16x32_bf16 v[16:19], v[48:51], v[60:63], v[16:19]\n\t" \
  "v_mfma_f32_16x16x32_bf16 v[20:23], v[48:51], v[64:67], v[20:23]\n\t" \
  "ds_read_b64_tr_b16 v[48:49], v141 offset:5136\n\t" \
  "ds_read_b64_tr_b16 v[50:51], v141 offset:7696\n\t" \
  "s_waitcnt lgkmcnt(8)\n\t" \
  "v_mfma_f32_16x16x32_bf16 v[24:27], v[52:55], v[60:63], v[24:27]\n\t" \
  "v_mfma_f32_16x16x32_bf16 v[28:31], v[52:55], v[64:67], v[28:31]\n\t" \
  "ds_read_b64_tr_b16 v[52:53], v141 offset:5144\n\t" \
  "ds_read_b64_tr_b16 v[54:55], v141 offset:7704\n\t" \
  "s_waitcnt lgkmcnt(8)\n\t" \
  "v_mfma_f32_16x16x32_bf16 v[32:35], v[56:59], v[60:63], v[32:35]\n\t" \
  "v_mfma_f32_16x16x32_bf16 v[36:39], v[56:59], v[64:67], v[36:39]\n\t" \
  "ds_read_b64_tr_b16 v[56:57], v141 offset:5152\n\t" \
  "ds_read_b64_tr_b16 v[58:59], v141 offset:7712\n\t" \
  "s_waitcnt vmcnt(16)\n\t" \
  "s_waitcnt lgkmcnt(8)\n\t" \
  "v_mfma_f32_16x16x32_bf16 v[0:3], v[40:43], v[68:71], v[0:3]\n\t" \
  "v_mfma_f32_16x16x32_bf16 v[4:7], v[40:43], v[72:75], v[4:7]\n\t" \
  "ds_read_b64_tr_b16 v[40:41], v141 offset:10240\n\t" \
  "ds_read_b64_tr_b16 v[42:43], v141 offset:12800\n\t" \
  "s_waitcnt lgkmcnt(8)\n\t" \
  "v_mfma_f32_16x16x32_bf16 v[8:11], v[44:47], v[68:71], v[8:11]\n\t" \
  "v_mfma_f32_16x16x32_bf16 v[12:15], v[44:47], v[72:75], v[12:15]\n\t" \
  "ds_read_b64_tr_b16 v[44:45], v141 offset:10248\n\t" \
  "ds_read_b64_tr_b16 v[46:47], v141 offset:12808\n\t" \
  "s_waitcnt lgkmcnt(8)\n\t" \
  "v_mfma_f32_16x16x32_bf16 v[16:19], v[48:51], v[68:71], v[16:19]\n\t" \
  "v_mfma_f32_16x16x32_bf16 v[20:23], v[48:51], v[72:75], v[20:23]\n\t" \
  "ds_read_b64_tr_b16 v[48:49], v141 offset:10256\n\t" \
  "ds_read_b64_tr_b16 v[50:51], v141 offset:12816\n\t" \
  "s_waitcnt lgkmcnt(8)\n\t" \
  "v_mfma_f32_16x16x32_bf16 v[24:27], v[52:55], v[68:71], v[24:27]\n\t" \
  "v_mfma_f32_16x16x32_bf16 v[28:31], v[52:55], v[72:75], v[28:31]\n\t" \
  "ds_read_b64_tr_b16 v[52:53], v141 offset:10264\n\t" \
  "ds_read_b64_tr_b16 v[54:55], v141 offset:12824\n\t" \
  "s_waitcnt lgkmcnt(8)\n\t" \
  "v_mfma_f32_16x16x32_bf16 v[32:35], v[56:59], v[68:71], v[32:35]\n\t" \
  "v_mfma_f32_16x16x32_bf16 v[36:39], v[56:59], v[72:75], v[36:39]\n\t" \
  "ds_read_b64_tr_b16 v[56:57], v141 offset:10272\n\t" \
  "ds_read_b64_tr_b16 v[58:59], v141 offset:12832\n\t" \
  "s_waitcnt vmcnt(14)\n\t" \
  "s_waitcnt lgkmcnt(8)\n\t" \
  "v_mfma_f32_16x16x32_bf16 v[0:3], v[40:43], v[76:79], v[0:3]\n\t" \
  "v_mfma_f32_16x16x32_bf16 v[4:7], v[40:43], v[80:83], v[4:7]\n\t" \
  "ds_read_b64_tr_b16 v[40:41], v141 offset:15360\n\t" \
  "ds_read_b64_tr_b16 v[42:43], v141 offset:17920\n\t" \
  "s_waitcnt lgkmcnt(8)\n\t" \
  "v_mfma_f32_16x16x32_bf16 v[8:11], v[44:47], v[76:79], v[8:11]\n\t" \
  "v_mfma_f32_16x16x32_bf16 v[12:15], v[44:47], v[80:83], v[12:15]\n\t" \
  "ds_read_b64_tr_b16 v[44:45], v141 offset:15368\n\t" \
  "ds_read_b64_tr_b16 v[46:47], v141 offset:17928\n\t" \
  "s_waitcnt lgkmcnt(8)\n\t" \
  "v_mfma_f32_16x16x32_bf16 v[16:19], v[48:51], v[76:79], v[16:19]\n\t" \
  "v_mfma_f32_16x16x32_bf16 v[20:23], v[48:51], v[80:83], v[20:23]\n\t" \
  "ds_read_b64_tr_b16 v[48:49], v141 offset:15376\n\t" \
  "ds_read_b64_tr_b16 v[50:51], v141 offset:17936\n\t" \
  "s_waitcnt lgkmcnt(8)\n\t" \
  "v_mfma_f32_16x16x32_bf16 v[24:27], v[52:55], v[76:79], v[24:27]\n\t" \
  "v_mfma_f32_16x16x32_bf16 v[28:31], v[52:55], v[80:83], v[28:31]\n\t" \
  "ds_read_b64_tr_b16 v[52:53], v141 offset:15384\n\t" \
  "ds_read_b64_tr_b16 v[54:55], v141 offset:17944\n\t" \
  "s_waitcnt lgkmcnt(8)\n\t" \
  "v_mfma_f32_16x16x32_bf16 v[32:35], v[56:59], v[76:79], v[32:35]\n\t" \
  "v_mfma_f32_16x16x32_bf16 v[36:39], v[56:59], v[80:83], v[36:39]\n\t" \
  "ds_read_b64_tr_b16 v[56:57], v141 offset:15392\n\t" \
  "ds_read_b64_tr_b16 v[58:59], v141 offset:17952\n\t" \
  "s_waitcnt vmcnt(12)\n\t" \
  "s_waitcnt lgkmcnt(8)\n\t" \
  "v_mfma_f32_16x16x32_bf16 v[0:3], v[40:43], v[84:87], v[0:3]\n\t" \
  "v_mfma_f32_16x16x32_bf16 v[4:7], v[40:43], v[88:91], v[4:7]\n\t" \
  "ds_read_b64_tr_b16 v[40:41], v141 offset:20480\n\t" \
  "ds_read_b64_tr_b16 v[42:43], v141 offset:23040\n\t" \
  "s_waitcnt lgkmcnt(8)\n\t" \
  "v_mfma_f32_16x16x32_bf16 v[8:11], v[44:47], v[84:87], v[8:11]\n\t" \
  "v_mfma_f32_16x16x32_bf16 v[12:15], v[44:47], v[88:91], v[12:15]\n\t" \
  "ds_read_b64_tr_b16 v[44:45], v141 offset:20488\n\t" \
  "ds_read_b64_tr_b16 v[46:47], v141 offset:23048\n\t" \
  "s_waitcnt lgkmcnt(8)\n\t" \
  "v_mfma_f32_16x16x32_bf16 v[16:19], v[48:51], v[84:87], v[16:19]\n\t" \
  "v_mfma_f32_16x16x32_bf16 v[20:23], v[48:51], v[88:91], v[20:23]\n\t" \
  "ds_read_b64_tr_b16 v[48:49], v141 offset:20496\n\t" \
  "ds_read_b64_tr_b16 v[50:51], v141 offset:23056\n\t" \
  "s_waitcnt lgkmcnt(8)\n\t" \
  "v_mfma_f32_16x16x32_bf16 v[24:27], v[52:55], v[84:87], v[24:27]\n\t" \
  "v_mfma_f32_16x16x32_bf16 v[28:31], v[52:55], v[88:91], v[28:31]\n\t" \
  "ds_read_b64_tr_b16 v[52:53], v141 offset:20504\n\t" \
  "ds_read_b64_tr_b16 v[54:55], v141 offset:23064\n\t" \
  "s_waitcnt lgkmcnt(8)\n\t" \
  "v_mfma_f32_16x16x32_bf16 v[32:35], v[56:59], v[84:87], v[32:35]\n\t" \
  "v_mfma_f32_16x16x32_bf16 v[36:39], v[56:59], v[88:91], v[36:39]\n\t" \
  "ds_read_b64_tr_b16 v[56:57], v141 offset:20512\n\t" \
  "ds_read_b64_tr_b16 v[58:59], v141 offset:23072\n\t" \
  "s_waitcnt vmcnt(10)\n\t" \
  "s_waitcnt lgkmcnt(8)\n\t" \
  "v_mfma_f32_16x16x32_bf16 v[0:3], v[40:43], v[92:95], v[0:3]\n\t" \
  "v_mfma_f32_16x16x32_bf16 v[4:7], v[40:43], v[96:99], v[4:7]\n\t" \
  "ds_read_b64_tr_b16 v[40:41], v141 offset:25600\n\t" \
  "ds_read_b64_tr_b16 v[42:43], v141 offset:28160\n\t" \
  "s_waitcnt lgkmcnt(8)\n\t" \
  "v_mfma_f32_16x16x32_bf16 v[8:11], v[44:47], v[92:95], v[8:11]\n\t" \
  "v_mfma_f32_16x16x32_bf16 v[12:15], v[44:47], v[96:99], v[12:15]\n\t" \
  "ds_read_b64_tr_b16 v[44:45], v141 offset:25608\n\t" \
  "ds_read_b64_tr_b16 v[46:47], v141 offset:28168\n\t" \
  "s_waitcnt lgkmcnt(8)\n\t" \
  "v_mfma_f32_16x16x32_bf16 v[16:19], v[48:51], v[92:95], v[16:19]\n\t" \
  "v_mfma_f32_16x16x32_bf16 v[20:23], v[48:51], v[96:99], v[20:23]\n\t" \
  "ds_read_b64_tr_b16 v[48:49], v141 offset:25616\n\t" \
  "ds_read_b64_tr_b16 v[50:51], v141 offset:28176\n\t" \
  "s_waitcnt lgkmcnt(8)\n\t" \
  "v_mfma_f32_16x16x32_bf16 v[24:27], v[52:55], v[92:95], v[24:27]\n\t" \
  "v_mfma_f32_16x16x32_bf16 v[28:31], v[52:55], v[96:99], v[28:31]\n\t" \
  "ds_read_b64_tr_b16 v[52:53], v141 offset:25624\n\t" \
  "ds_read_b64_tr_b16 v[54:55], v141 offset:28184\n\t" \
  "s_waitcnt lgkmcnt(8)\n\t" \
  "v_mfma_f32_16x16x32_bf16 v[32:35], v[56:59], v[92:95], v[32:35]\n\t" \
  "v_mfma_f32_16x16x32_bf16 v[36:39], v[56:59], v[96:99], v[36:39]\n\t" \
  "ds_read_b64_tr_b16 v[56:57], v141 offset:25632\n\t" \
  "ds_read_b64_tr_b16 v[58:59], v141 offset:28192\n\t" \
  "s_mov_b64 exec, 1\n\t" \
  "ds_add_u32 v142, v143\n\t" \
  "s_mov_b64 exec, -1\n\t" \
  "s_waitcnt vmcnt(8)\n\t" \
  "s_waitcnt lgkmcnt(8)\n\t" \
  "v_mfma_f32_16x16x32_bf16 v[0:3], v[40:43], v[100:103], v[0:3]\n\t" \
  "v_mfma_f32_16x16x32_bf16 v[4:7], v[40:43], v[104:107], v[4:7]\n\t" \
  "ds_read_b64_tr_b16 v[40:41], v141 offset:30720\n\t" \
  "ds_read_b64_tr_b16 v[42:43], v141 offset:33280\n\t" \
  "s_waitcnt lgkmcnt(8)\n\t" \
  "v_mfma_f32_16x16x32_bf16 v[8:11], v[44:47], v[100:103], v[8:11]\n\t" \
  "v_mfma_f32_16x16x32_bf16 v[12:15], v[44:47], v[104:107], v[12:15]\n\t" \
  "ds_read_b64_tr_b16 v[44:45], v141 offset:30728\n\t" \
  "ds_read_b64_tr_b16 v[46:47], v141 offset:33288\n\t" \
  "s_waitcnt lgkmcnt(8)\n\t" \
  "v_mfma_f32_16x16x32_bf16 v[16:19], v[48:51], v[100:103], v[16:19]\n\t" \
  "v_mfma_f32_16x16x32_bf16 v[20:23], v[48:51], v[104:107], v[20:23]\n\t" \
  "ds_read_b64_tr_b16 v[48:49], v141 offset:30736\n\t" \
  "ds_read_b64_tr_b16 v[50:51], v141 offset:33296\n\t" \
  "s_waitcnt lgkmcnt(8)\n\t" \
  "v_mfma_f32_16x16x32_bf16 v[24:27], v[52:55], v[100:103], v[24:27]\n\t" \
  "v_mfma_f32_16x16x32_bf16 v[28:31], v[52:55], v[104:107], v[28:31]\n\t" \
  "ds_read_b64_tr_b16 v[52:53], v141 offset:30744\n\t" \
  "ds_read_b64_tr_b16 v[54:55], v141 offset:33304\n\t" \
  "s_waitcnt lgkmcnt(8)\n\t" \
  "v_mfma_f32_16x16x32_bf16 v[32:35], v[56:59], v[100:103], v[32:35]\n\t" \
  "v_mfma_f32_16x16x32_bf16 v[36:39], v[56:59], v[104:107], v[36:39]\n\t" \
  "ds_read_b64_tr_b16 v[56:57], v141 offset:30752\n\t" \
  "ds_read_b64_tr_b16 v[58:59], v141 offset:33312\n\t" \
  "s_waitcnt vmcnt(6)\n\t" \
  "s_waitcnt lgkmcnt(8)\n\t" \
  "v_mfma_f32_16x16x32_bf16 v[0:3], v[40:43], v[108:111], v[0:3]\n\t" \
  "v_mfma_f32_16x16x32_bf16 v[4:7], v[40:43], v[112:115], v[4:7]\n\t" \
  "ds_read_b64_tr_b16 v[40:41], v141 offset:35840\n\t" \
  "ds_read_b64_tr_b16 v[42:43], v141 offset:38400\n\t" \
  "s_waitcnt lgkmcnt(8)\n\t" \
  "v_mfma_f32_16x16x32_bf16 v[8:11], v[44:47], v[108:111], v[8:11]\n\t" \
  "v_mfma_f32_16x16x32_bf16 v[12:15], v[44:47], v[112:115], v[12:15]\n\t" \
  "ds_read_b64_tr_b16 v[44:45], v141 offset:35848\n\t" \
  "ds_read_b64_tr_b16 v[46:47], v141 offset:38408\n\t" \
  "s_waitcnt lgkmcnt(8)\n\t" \
  "v_mfma_f32_16x16x32_bf16 v[16:19], v[48:51], v[108:111], v[16:19]\n\t" \
  "v_mfma_f32_16x16x32_bf16 v[20:23], v[48:51], v[112:115], v[20:23]\n\t" \
  "ds_read_b64_tr_b16 v[48:49], v141 offset:35856\n\t" \
  "ds_read_b64_tr_b16 v[50:51], v141 offset:38416\n\t" \
  "s_waitcnt lgkmcnt(8)\n\t" \
  "v_mfma_f32_16x16x32_bf16 v[24:27], v[52:55], v[108:111], v[24:27]\n\t" \
  "v_mfma_f32_16x16x32_bf16 v[28:31], v[52:55], v[112:115], v[28:31]\n\t" \
  "ds_read_b64_tr_b16 v[52:53], v141 offset:35864\n\t" \
  "ds_read_b64_tr_b16 v[54:55], v141 offset:38424\n\t" \
  "s_waitcnt lgkmcnt(8)\n\t" \
  "v_mfma_f32_16x16x32_bf16 v[32:35], v[56:59], v[108:111], v[32:35]\n\t" \
  "v_mfma_f32_16x16x32_bf16 v[36:39], v[56:59], v[112:115], v[36:39]\n\t" \
  "ds_read_b64_tr_b16 v[56:57], v141 offset:35872\n\t" \
  "ds_read_b64_tr_b16 v[58:59], v141 offset:38432\n\t" \
  "s_waitcnt vmcnt(4)\n\t" \
  "s_waitcnt lgkmcnt(8)\n\t" \
  "v_mfma_f32_16x16x32_bf16 v[0:3], v[40:43], v[116:119], v[0:3]\n\t" \
  "v_mfma_f32_16x16x32_bf16 v[4:7], v[40:43], v[120:123], v[4:7]\n\t" \
  "ds_read_b64_tr_b16 v[40:41], v141 offset:40960\n\t" \
  "ds_read_b64_tr_b16 v[42:43], v141 offset:43520\n\t" \
  "s_waitcnt lgkmcnt(8)\n\t" \
  "v_mfma_f32_16x16x32_bf16 v[8:11], v[44:47], v[116:119], v[8:11]\n\t" \
  "v_mfma_f32_16x16x32_bf16 v[12:15], v[44:47], v[120:123], v[12:15]\n\t" \
  "ds_read_b64_tr_b16 v[44:45], v141 offset:40968\n\t" \
  "ds_read_b64_tr_b16 v[46:47], v141 offset:43528\n\t" \
  "s_waitcnt lgkmcnt(8)\n\t" \
  "v_mfma_f32_16x16x32_bf16 v[16:19], v[48:51], v[116:119], v[16:19]\n\t" \
  "v_mfma_f32_16x16x32_bf16 v[20:23], v[48:51], v[120:123], v[20:23]\n\t" \
  "ds_read_b64_tr_b16 v[48:49], v141 offset:40976\n\t" \
  "ds_read_b64_tr_b16 v[50:51], v141 offset:43536\n\t" \
  "s_waitcnt lgkmcnt(8)\n\t" \
  "v_mfma_f32_16x16x32_bf16 v[24:27], v[52:55], v[116:119], v[24:27]\n\t" \
  "v_mfma_f32_16x16x32_bf16 v[28:31], v[52:55], v[120:123], v[28:31]\n\t" \
  "ds_read_b64_tr_b16 v[52:53], v141 offset:40984\n\t" \
  "ds_read_b64_tr_b16 v[54:55], v141 offset:43544\n\t" \
  "s_waitcnt lgkmcnt(8)\n\t" \
  "v_mfma_f32_16x16x32_bf16 v[32:35], v[56:59], v[116:119], v[32:35]\n\t" \
  "v_mfma_f32_16x16x32_bf16 v[36:39], v[56:59], v[120:123], v[36:39]\n\t" \
  "ds_read_b64_tr_b16 v[56:57], v141 offset:40992\n\t" \
  "ds_read_b64_tr_b16 v[58:59], v141 offset:43552\n\t" \
  "s_waitcnt vmcnt(2)\n\t" \
  "s_waitcnt lgkmcnt(8)\n\t" \
  "v_mfma_f32_16x16x32_bf16 v[0:3], v[40:43], v[124:127], v[0:3]\n\t" \
  "v_mfma_f32_16x16x32_bf16 v[4:7], v[40:43], v[128:131], v[4:7]\n\t" \
  "ds_read_b64_tr_b16 v[40:41], v141 offset:46080\n\t" \
  "ds_read_b64_tr_b16 v[42:43], v141 offset:48640\n\t" \
  "s_waitcnt lgkmcnt(8)\n\t" \
  "v_mfma_f32_16x16x32_bf16 v[8:11], v[44:47], v[124:127], v[8:11]\n\t" \
  "v_mfma_f32_16x16x32_bf16 v[12:15], v[44:47], v[128:131], v[12:15]\n\t" \
  "ds_read_b64_tr_b16 v[44:45], v141 offset:46088\n\t" \
  "ds_read_b64_tr_b16 v[46:47], v141 offset:48648\n\t" \
  "s_waitcnt lgkmcnt(8)\n\t" \
  "v_mfma_f32_16x16x32_bf16 v[16:19], v[48:51], v[124:127], v[16:19]\n\t" \
  "v_mfma_f32_16x16x32_bf16 v[20:23], v[48:51], v[128:131], v[20:23]\n\t" \
  "ds_read_b64_tr_b16 v[48:49], v141 offset:46096\n\t" \
  "ds_read_b64_tr_b16 v[50:51], v141 offset:48656\n\t" \
  "s_waitcnt lgkmcnt(8)\n\t" \
  "v_mfma_f32_16x16x32_bf16 v[24:27], v[52:55], v[124:127], v[24:27]\n\t" \
  "v_mfma_f32_16x16x32_bf16 v[28:31], v[52:55], v[128:131], v[28:31]\n\t" \
  "ds_read_b64_tr_b16 v[52:53], v141 offset:46104\n\t" \
  "ds_read_b64_tr_b16 v[54:55], v141 offset:48664\n\t" \
  "s_waitcnt lgkmcnt(8)\n\t" \
  "v_mfma_f32_16x16x32_bf16 v[32:35], v[56:59], v[124:127], v[32:35]\n\t" \
  "v_mfma_f32_16x16x32_bf16 v[36:39], v[56:59], v[128:131], v[36:39]\n\t" \
  "ds_read_b64_tr_b16 v[56:57], v141 offset:46112\n\t" \
  "ds_read_b64_tr_b16 v[58:59], v141 offset:48672\n\t" \
  "s_waitcnt vmcnt(0)\n\t" \
  "s_waitcnt lgkmcnt(8)\n\t" \
  "v_mfma_f32_16x16x32_bf16 v[0:3], v[40:43], v[132:135], v[0:3]\n\t" \
  "v_mfma_f32_16x16x32_bf16 v[4:7], v[40:43], v[136:139], v[4:7]\n\t" \
  "s_waitcnt lgkmcnt(6)\n\t" \
  "v_mfma_f32_16x16x32_bf16 v[8:11], v[44:47], v[132:135], v[8:11]\n\t" \
  "v_mfma_f32_16x16x32_bf16 v[12:15], v[44:47], v[136:139], v[12:15]\n\t" \
  "s_waitcnt lgkmcnt(4)\n\t" \
  "v_mfma_f32_16x16x32_bf16 v[16:19], v[48:51], v[132:135], v[16:19]\n\t" \
  "v_mfma_f32_16x16x32_bf16 v[20:23], v[48:51], v[136:139], v[20:23]\n\t" \
  "s_waitcnt lgkmcnt(2)\n\t" \
  "v_mfma_f32_16x16x32_bf16 v[24:27], v[52:55], v[132:135], v[24:27]\n\t" \
  "v_mfma_f32_16x16x32_bf16 v[28:31], v[52:55], v[136:139], v[28:31]\n\t" \
  "s_waitcnt lgkmcnt(0)\n\t" \
  "v_mfma_f32_16x16x32_bf16 v[32:35], v[56:59], v[132:135], v[32:35]\n\t" \
  "v_mfma_f32_16x16x32_bf16 v[36:39], v[56:59], v[136:139], v[36:39]\n\t" \
  "s_nop 7\n\t"
#define TILE_GEMM_CLOB "v40", "v41", "v42", "v43", "v44", "v45", "v46", "v47", "v48", "v49", "v50", "v51", "v52", "v53", "v54", "v55", "v56", "v57", "v58", "v59", "v60", "v61", "v62", "v63", "v64", "v65", "v66", "v67", "v68", "v69", "v70", "v71", "v72", "v73", "v74", "v75", "v76", "v77", "v78", "v79", "v80", "v81", "v82", "v83", "v84", "v85", "v86", "v87", "v88", "v89", "v90", "v91", "v92", "v93", "v94", "v95", "v96", "v97", "v98", "v99", "v100", "v101", "v102", "v103", "v104", "v105", "v106", "v107", "v108", "v109", "v110", "v111", "v112", "v113", "v114", "v115", "v116", "v117", "v118", "v119", "v120", "v121", "v122", "v123", "v124", "v125", "v126", "v127", "v128", "v129", "v130", "v131", "v132", "v133", "v134", "v135", "v136", "v137", "v138", "v139"
#define K1_LOOP_ASM \
  "v_mov_b64 v[0:1], 0\n\t" \
  "v_mov_b64 v[2:3], 0\n\t" \
  "v_mov_b64 v[4:5], 0\n\t" \
  "v_mov_b64 v[6:7], 0\n\t" \
  "v_mov_b64 v[8:9], 0\n\t" \
  "v_mov_b64 v[10:11], 0\n\t" \
  "v_mov_b64 v[12:13], 0\n\t" \
  "v_mov_b64 v[14:15], 0\n\t" \
  "v_mov_b64 v[16:17], 0\n\t" \
  "v_mov_b64 v[18:19], 0\n\t" \
  "v_mov_b64 v[20:21], 0\n\t" \
  "v_mov_b64 v[22:23], 0\n\t" \
  "v_mov_b64 v[24:25], 0\n\t" \
  "v_mov_b64 v[26:27], 0\n\t" \
  "v_mov_b64 v[28:29], 0\n\t" \
  "v_mov_b64 v[30:31], 0\n\t" \
  "v_mov_b64 v[32:33], 0\n\t" \
  "v_mov_b64 v[34:35], 0\n\t" \
  "v_mov_b64 v[36:37], 0\n\t" \
  "v_mov_b64 v[38:39], 0\n\t" \
  "v_mov_b64 v[40:41], 0\n\t" \
  "v_mov_b64 v[42:43], 0\n\t" \
  "v_mov_b64 v[44:45], 0\n\t" \
  "v_mov_b64 v[46:47], 0\n\t" \
  "v_mov_b64 v[48:49], 0\n\t" \
  "v_mov_b64 v[50:51], 0\n\t" \
  "v_mov_b64 v[52:53], 0\n\t" \
  "v_mov_b64 v[54:55], 0\n\t" \
  "v_mov_b64 v[56:57], 0\n\t" \
  "v_mov_b64 v[58:59], 0\n\t" \
  "v_mov_b64 v[60:61], 0\n\t" \
  "v_mov_b64 v[62:63], 0\n\t" \
  "v_mov_b64 v[64:65], 0\n\t" \
  "v_mov_b64 v[66:67], 0\n\t" \
  "v_mov_b64 v[68:69], 0\n\t" \
  "v_mov_b64 v[70:71], 0\n\t" \
  "v_mov_b64 v[72:73], 0\n\t" \
  "v_mov_b64 v[74:75], 0\n\t" \
  "v_mov_b64 v[76:77], 0\n\t" \
  "v_mov_b64 v[78:79], 0\n\t" \
  "v_mov_b64 v[116:117], 0\n\t" \
  "v_mov_b64 v[118:119], 0\n\t" \
  "v_mov_b64 v[120:121], 0\n\t" \
  "v_mov_b64 v[122:123], 0\n\t" \
  "v_mov_b64 v[124:125], 0\n\t" \
  "v_mov_b64 v[126:127], 0\n\t" \
  "v_mov_b64 v[128:129], 0\n\t" \
  "v_mov_b64 v[130:131], 0\n\t" \
  "v_mov_b64 v[132:133], 0\n\t" \
  "v_mov_b64 v[134:135], 0\n\t" \
  "v_mov_b64 v[136:137], 0\n\t" \
  "v_mov_b64 v[138:139], 0\n\t" \
  "v_mov_b64 v[140:141], 0\n\t" \
  "v_mov_b64 v[142:143], 0\n\t" \
  "v_mov_b64 v[144:145], 0\n\t" \
  "v_mov_b64 v[146:147], 0\n\t" \
  "v_mov_b64 v[148:149], 0\n\t" \
  "v_mov_b64 v[150:151], 0\n\t" \
  "s_mov_b32 %[cnt], 0\n\t" \
  "1:\n\t" \
  "s_barrier\n\t" \
  "v_mfma_f32_16x16x32_bf16 v[0:3], v[132:135], v[116:119], v[0:3]\n\t" \
  "ds_read_b128 v[80:83], v152\n\t" \
  "v_mfma_f32_16x16x32_bf16 v[4:7], v[132:135], v[120:123], v[4:7]\n\t" \
  "ds_read_b64_tr_b16 v[96:97], v154 offset:0\n\t" \
  "v_mfma_f32_16x16x32_bf16 v[8:11], v[132:135], v[124:127], v[8:11]\n\t" \
  "ds_read_b64_tr_b16 v[98:99], v154 offset:2560\n\t" \
  "v_mfma_f32_16x16x32_bf16 v[12:15], v[132:135], v[128:131], v[12:15]\n\t" \
  "ds_read_b128 v[84:87], v152 offset:2048\n\t" \
  "v_mfma_f32_16x16x32_bf16 v[16:19], v[136:139], v[116:119], v[16:19]\n\t" \
  "ds_read_b128 v[88:91], v152 offset:4096\n\t" \
  "v_mfma_f32_16x16x32_bf16 v[20:23], v[136:139], v[120:123], v[20:23]\n\t" \
  "ds_read_b128 v[92:95], v152 offset:6144\n\t" \
  "v_mfma_f32_16x16x32_bf16 v[24:27], v[136:139], v[124:127], v[24:27]\n\t" \
  "ds_read_b64_tr_b16 v[100:101], v154 offset:8\n\t" \
  "v_mfma_f32_16x16x32_bf16 v[28:31], v[136:139], v[128:131], v[28:31]\n\t" \
  "ds_read_b64_tr_b16 v[102:103], v154 offset:2568\n\t" \
  "v_mfma_f32_16x16x32_bf16 v[32:35], v[140:143], v[116:119], v[32:35]\n\t" \
  "ds_read_b64_tr_b16 v[104:105], v154 offset:16\n\t" \
  "v_mfma_f32_16x16x32_bf16 v[36:39], v[140:143], v[120:123], v[36:39]\n\t" \
  "ds_read_b64_tr_b16 v[106:107], v154 offset:2576\n\t" \
  "v_mfma_f32_16x16x32_bf16 v[40:43], v[140:143], v[124:127], v[40:43]\n\t" \
  "ds_read_b64_tr_b16 v[108:109], v154 offset:24\n\t" \
  "v_mfma_f32_16x16x32_bf16 v[44:47], v[140:143], v[128:131], v[44:47]\n\t" \
  "ds_read_b64_tr_b16 v[110:111], v154 offset:2584\n\t" \
  "v_mfma_f32_16x16x32_bf16 v[48:51], v[144:147], v[116:119], v[48:51]\n\t" \
  "ds_read_b64_tr_b16 v[112:113], v154 offset:32\n\t" \
  "v_mfma_f32_16x16x32_bf16 v[52:55], v[144:147], v[120:123], v[52:55]\n\t" \
  "ds_read_b64_tr_b16 v[114:115], v154 offset:2592\n\t" \
  "v_mfma_f32_16x16x32_bf16 v[56:59], v[144:147], v[124:127], v[56:59]\n\t" \
  "s_and_b32 %[inc], %[cnt], 3\n\t" \
  "v_mfma_f32_16x16x32_bf16 v[60:63], v[144:147], v[128:131], v[60:63]\n\t" \
  "s_cmp_eq_u32 %[inc], 3\n\t" \
  "v_mfma_f32_16x16x32_bf16 v[64:67], v[148:151], v[116:119], v[64:67]\n\t" \
  "s_cselect_b32 %[inc], %[wrap], %[stp]\n\t" \
  "v_mfma_f32_16x16x32_bf16 v[68:71], v[148:151], v[120:123], v[68:71]\n\t" \
  "v_add_u32 v152, %[inc], v152\n\t" \
  "v_mfma_f32_16x16x32_bf16 v[72:75], v[148:151], v[124:127], v[72:75]\n\t" \
  "v_mfma_f32_16x16x32_bf16 v[76:79], v[148:151], v[128:131], v[76:79]\n\t" \
  "s_waitcnt lgkmcnt(0)\n\t" \
  "v_mfma_f32_16x16x32_bf16 v[0:3], v[96:99], v[80:83], v[0:3]\n\t" \
  "ds_read_b128 v[116:119], v153\n\t" \
  "v_mfma_f32_16x16x32_bf16 v[4:7], v[96:99], v[84:87], v[4:7]\n\t" \
  "ds_read_b64_tr_b16 v[132:133], v154 offset:5120\n\t" \
  "v_mfma_f32_16x16x32_bf16 v[8:11], v[96:99], v[88:91], v[8:11]\n\t" \
  "ds_read_b64_tr_b16 v[134:135], v154 offset:7680\n\t" \
  "v_mfma_f32_16x16x32_bf16 v[12:15], v[96:99], v[92:95], v[12:15]\n\t" \
  "ds_read_b128 v[120:123], v153 offset:2048\n\t" \
  "v_mfma_f32_16x16x32_bf16 v[16:19], v[100:103], v[80:83], v[16:19]\n\t" \
  "ds_read_b128 v[124:127], v153 offset:4096\n\t" \
  "v_mfma_f32_16x16x32_bf16 v[20:23], v[100:103], v[84:87], v[20:23]\n\t" \
  "ds_read_b128 v[128:131], v153 offset:6144\n\t" \
  "v_mfma_f32_16x16x32_bf16 v[24:27], v[100:103], v[88:91], v[24:27]\n\t" \
  "ds_read_b64_tr_b16 v[136:137], v154 offset:5128\n\t" \
  "v_mfma_f32_16x16x32_bf16 v[28:31], v[100:103], v[92:95], v[28:31]\n\t" \
  "ds_read_b64_tr_b16 v[138:139], v154 offset:7688\n\t" \
  "v_mfma_f32_16x16x32_bf16 v[32:35], v[104:107], v[80:83], v[32:35]\n\t" \
  "ds_read_b64_tr_b16 v[140:141], v154 offset:5136\n\t" \
  "v_mfma_f32_16x16x32_bf16 v[36:39], v[104:107], v[84:87], v[36:39]\n\t" \
  "ds_read_b64_tr_b16 v[142:143], v154 offset:7696\n\t" \
  "v_mfma_f32_16x16x32_bf16 v[40:43], v[104:107], v[88:91], v[40:43]\n\t" \
  "ds_read_b64_tr_b16 v[144:145], v154 offset:5144\n\t" \
  "v_mfma_f32_16x16x32_bf16 v[44:47], v[104:107], v[92:95], v[44:47]\n\t" \
  "ds_read_b64_tr_b16 v[146:147], v154 offset:7704\n\t" \
  "v_mfma_f32_16x16x32_bf16 v[48:51], v[108:111], v[80:83], v[48:51]\n\t" \
  "ds_read_b64_tr_b16 v[148:149], v154 offset:5152\n\t" \
  "v_mfma_f32_16x16x32_bf16 v[52:55], v[108:111], v[84:87], v[52:55]\n\t" \
  "ds_read_b64_tr_b16 v[150:151], v154 offset:7712\n\t" \
  "v_mfma_f32_16x16x32_bf16 v[56:59], v[108:111], v[88:91], v[56:59]\n\t" \
  "v_add_u32 v153, %[inc], v153\n\t" \
  "v_mfma_f32_16x16x32_bf16 v[60:63], v[108:111], v[92:95], v[60:63]\n\t" \
  "v_add_u32 v154, %[inc], v154\n\t" \
  "v_mfma_f32_16x16x32_bf16 v[64:67], v[112:115], v[80:83], v[64:67]\n\t" \
  "s_add_u32 %[cnt], %[cnt], 1\n\t" \
  "v_mfma_f32_16x16x32_bf16 v[68:71], v[112:115], v[84:87], v[68:71]\n\t" \
  "s_cmp_lt_u32 %[cnt], 10\n\t" \
  "v_mfma_f32_16x16x32_bf16 v[72:75], v[112:115], v[88:91], v[72:75]\n\t" \
  "v_mfma_f32_16x16x32_bf16 v[76:79], v[112:115], v[92:95], v[76:79]\n\t" \
  "s_waitcnt lgkmcnt(0)\n\t" \
  "s_cbranch_scc1 1b\n\t" \
  "v_mfma_f32_16x16x32_bf16 v[0:3], v[132:135], v[116:119], v[0:3]\n\t" \
  "v_mfma_f32_16x16x32_bf16 v[16:19], v[136:139], v[116:119], v[16:19]\n\t" \
  "v_mfma_f32_16x16x32_bf16 v[32:35], v[140:143], v[116:119], v[32:35]\n\t" \
  "v_mfma_f32_16x16x32_bf16 v[48:51], v[144:147], v[116:119], v[48:51]\n\t" \
  "v_mfma_f32_16x16x32_bf16 v[64:67], v[148:151], v[116:119], v[64:67]\n\t" \
  "v_mfma_f32_16x16x32_bf16 v[4:7], v[132:135], v[120:123], v[4:7]\n\t" \
  "v_mfma_f32_16x16x32_bf16 v[20:23], v[136:139], v[120:123], v[20:23]\n\t" \
  "v_mfma_f32_16x16x32_bf16 v[36:39], v[140:143], v[120:123], v[36:39]\n\t" \
  "v_mfma_f32_16x16x32_bf16 v[52:55], v[144:147], v[120:123], v[52:55]\n\t" \
  "v_mfma_f32_16x16x32_bf16 v[68:71], v[148:151], v[120:123], v[68:71]\n\t" \
  "v_mfma_f32_16x16x32_bf16 v[8:11], v[132:135], v[124:127], v[8:11]\n\t" \
  "v_cvt_pk_bf16_f32 v80, v0, v1\n\t" \
  "v_cvt_pk_bf16_f32 v81, v2, v3\n\t" \
  "v_mfma_f32_16x16x32_bf16 v[24:27], v[136:139], v[124:127], v[24:27]\n\t" \
  "v_cvt_pk_bf16_f32 v82, v16, v17\n\t" \
  "v_cvt_pk_bf16_f32 v83, v18, v19\n\t" \
  "v_mfma_f32_16x16x32_bf16 v[40:43], v[140:143], v[124:127], v[40:43]\n\t" \
  "v_cvt_pk_bf16_f32 v84, v32, v33\n\t" \
  "v_cvt_pk_bf16_f32 v85, v34, v35\n\t" \
  "v_mfma_f32_16x16x32_bf16 v[56:59], v[144:147], v[124:127], v[56:59]\n\t" \
  "v_cvt_pk_bf16_f32 v86, v48, v49\n\t" \
  "v_cvt_pk_bf16_f32 v87, v50, v51\n\t" \
  "v_mfma_f32_16x16x32_bf16 v[72:75], v[148:151], v[124:127], v[72:75]\n\t" \
  "v_cvt_pk_bf16_f32 v112, v64, v65\n\t" \
  "v_cvt_pk_bf16_f32 v113, v66, v67\n\t" \
  "v_mfma_f32_16x16x32_bf16 v[12:15], v[132:135], v[128:131], v[12:15]\n\t" \
  "v_cvt_pk_bf16_f32 v88, v4, v5\n\t" \
  "v_cvt_pk_bf16_f32 v89, v6, v7\n\t" \
  "v_mfma_f32_16x16x32_bf16 v[28:31], v[136:139], v[128:131], v[28:31]\n\t" \
  "v_cvt_pk_bf16_f32 v90, v20, v21\n\t" \
  "v_cvt_pk_bf16_f32 v91, v22, v23\n\t" \
  "v_mfma_f32_16x16x32_bf16 v[44:47], v[140:143], v[128:131], v[44:47]\n\t" \
  "v_cvt_pk_bf16_f32 v92, v36, v37\n\t" \
  "v_cvt_pk_bf16_f32 v93, v38, v39\n\t" \
  "v_mfma_f32_16x16x32_bf16 v[60:63], v[144:147], v[128:131], v[60:63]\n\t" \
  "v_cvt_pk_bf16_f32 v94, v52, v53\n\t" \
  "v_cvt_pk_bf16_f32 v95, v54, v55\n\t" \
  "v_mfma_f32_16x16x32_bf16 v[76:79], v[148:151], v[128:131], v[76:79]\n\t" \
  "v_cvt_pk_bf16_f32 v114, v68, v69\n\t" \
  "v_cvt_pk_bf16_f32 v115, v70, v71\n\t" \
  "v_cvt_pk_bf16_f32 v96, v8, v9\n\t" \
  "v_cvt_pk_bf16_f32 v97, v10, v11\n\t" \
  "v_cvt_pk_bf16_f32 v98, v24, v25\n\t" \
  "v_cvt_pk_bf16_f32 v99, v26, v27\n\t" \
  "v_cvt_pk_bf16_f32 v100, v40, v41\n\t" \
  "v_cvt_pk_bf16_f32 v101, v42, v43\n\t" \
  "v_cvt_pk_bf16_f32 v102, v56, v57\n\t" \
  "v_cvt_pk_bf16_f32 v103, v58, v59\n\t" \
  "v_cvt_pk_bf16_f32 v156, v72, v73\n\t" \
  "v_cvt_pk_bf16_f32 v157, v74, v75\n\t" \
  "v_cvt_pk_bf16_f32 v104, v12, v13\n\t" \
  "v_cvt_pk_bf16_f32 v105, v14, v15\n\t" \
  "v_cvt_pk_bf16_f32 v106, v28, v29\n\t" \
  "v_cvt_pk_bf16_f32 v107, v30, v31\n\t" \
  "v_cvt_pk_bf16_f32 v108, v44, v45\n\t" \
  "v_cvt_pk_bf16_f32 v109, v46, v47\n\t" \
  "v_cvt_pk_bf16_f32 v110, v60, v61\n\t" \
  "v_cvt_pk_bf16_f32 v111, v62, v63\n\t" \
  "v_cvt_pk_bf16_f32 v158, v76, v77\n\t" \
  "v_cvt_pk_bf16_f32 v159, v78, v79\n\t" \
  "s_nop 1\n\t"
#define K1_LOOP_CLOB "v0", "v1", "v2", "v3", "v4", "v5", "v6", "v7", "v8", "v9", "v10", "v11", "v12", "v13", "v14", "v15", "v16", "v17", "v18", "v19", "v20", "v21", "v22", "v23", "v24", "v25", "v26", "v27", "v28", "v29", "v30", "v31", "v32", "v33", "v34", "v35", "v36", "v37", "v38", "v39", "v40", "v41", "v42", "v43", "v44", "v45", "v46", "v47", "v48", "v49", "v50", "v51", "v52", "v53", "v54", "v55", "v56", "v57", "v58", "v59", "v60", "v61", "v62", "v63", "v64", "v65", "v66", "v67", "v68", "v69", "v70", "v71", "v72", "v73", "v74", "v75", "v76", "v77", "v78", "v79", "v116", "v117", "v118", "v119", "v120", "v121", "v122", "v123", "v124", "v125", "v126", "v127", "v128", "v129", "v130", "v131", "v132", "v133", "v134", "v135", "v136", "v137", "v138", "v139", "v140", "v141", "v142", "v143", "v144", "v145", "v146", "v147", "v148", "v149", "v150", "v151"

#define K1_ROT ((tt + 3 * (jb >> 3)) % 10)
constexpr u32 K1_SLOT = 36864, K1_W = 16384;
constexpr u32 K1_EX = 3 * K1_SLOT, K1_EXP = 4 * K1_SLOT;
constexpr u32 K1_LGT = 0;
constexpr u32 K1_PHK = 27648, K1_PHK_P = 336;
constexpr u32 K1_PHQ = 20480;
constexpr u32 K1_XCH = 73728;
constexpr u32 K1_WPHI = 156160;
constexpr u32 L_K01 = 160256;
constexpr u32 SL = 4 * REC;
constexpr u32 L_IMG = 0;
constexpr u32 L_RING = 102400;
constexpr u32 L_FLG = 162816;
constexpr u32 F_GBAR = L_FLG + 96, F_APUB = L_FLG + 100, F_AFREE = L_FLG + 104  , F_P1 = L_FLG + 112, F_PHB = L_FLG + 116,
              F_P2 = L_FLG + 120, F_KVOK = L_FLG + 124, F_BIMG = L_FLG + 128, F_QOK = L_FLG + 132, F_TAG = L_FLG + 136, F_FIN = L_FLG + 152, F_K01 = L_FLG + 156, F_AOK = L_FLG + 160, F_P3 = L_FLG + 164, F_XF = L_FLG + 176, L_GT = L_FLG + 256;
constexpr u32 LDS_BYTES = 163840;

__device__ __forceinline__ f32x16 attn_exact(rsrc_t rW, u32x4 qx, u32 q2lo, int lane) {
  const f32x16 zero16 = {0.f, 0.f, 0.f, 0.f, 0.f, 0.f, 0.f, 0.f, 0.f, 0.f, 0.f, 0.f, 0.f, 0.f, 0.f, 0.f};
  const int r32 = lane & 31, hh = lane >> 5;
  const u32x2 qz = {q2lo, 0u};
  float m = -INFINITY;
#pragma unroll 1
  for (u32 rec = 0; rec < 64; ++rec) {
    const u32x4 a1 = gl128(rW, lane * 16, WS_KVIM + rec * REC);
    const u32 a2 = gl32(rW, lane * 4, WS_KVIM + rec * REC + 1024);
    f32x16 s = mfma32(a1, qx, zero16);
    s = mfma32k8((u32x2){a2, 0u}, qz, s);
    m = fmaxf(m, max16(s));
  }
  m = xhalf_max(m);
  f32x16 O = zero16;
  const u32 av = 1280u + (u32)hh * 352u + (u32)(r32 <= 20 ? r32 : 21) * 16u;
#pragma unroll 1
  for (u32 t2 = 0; t2 < 64; ++t2) {
    const u32 rec = (t2 + 63) & 63;
    const u32x4 a1 = gl128(rW, lane * 16, WS_KVIM + rec * REC);
    const u32 a2 = gl32(rW, lane * 4, WS_KVIM + rec * REC + 1024);
    const u32x4 b1 = gl128(rW, av, WS_KVIM + t2 * REC), b2 = gl128(rW, av, WS_KVIM + t2 * REC + 704);
    f32x16 s = mfma32(a1, qx, zero16);
    s = mfma32k8((u32x2){a2, 0u}, qz, s);
#pragma unroll
    for (int r = 0; r < 16; ++r) s[r] = __builtin_amdgcn_exp2f(s[r] - m);
    O = smf(b1, lo8(s), O, 0xDDDD);
    O = smf(b2, hi8(s), O, 0xDDDD);
  }
  return O;
}

extern "C" __global__ void __launch_bounds__(768, 3)
gm_fused(const char* __restrict__ x, const char* __restrict__ wqkv, const char* __restrict__ wphi, const char* __restrict__ wq,
         const char* __restrict__ wk, const char* __restrict__ wv, const char* __restrict__ gw, const char* __restrict__ awo,
         const char* __restrict__ bwo, char* ws, char* __restrict__ out) {
  extern __shared__ __attribute__((aligned(16))) char smem[];
  ldsp lds = (ldsp)smem;
  const u32 lds0 = (u32)(uintptr_t)lds;
  const int w = __builtin_amdgcn_readfirstlane(threadIdx.x >> 6);
  const int bid = blockIdx.x, xcd = bid & 7, jb = bid >> 3;
  const rsrc_t rW = __builtin_amdgcn_make_buffer_rsrc(ws, 0, WS_END, 0x00020000);
  const f32x16 zero16 = {0.f, 0.f, 0.f, 0.f, 0.f, 0.f, 0.f, 0.f, 0.f, 0.f, 0.f, 0.f, 0.f, 0.f, 0.f, 0.f};
  asm volatile("" :: "s"(x), "s"(wqkv), "s"(wphi), "s"(wq), "s"(wk), "s"(wv), "s"(gw), "s"(awo), "s"(bwo), "s"(ws), "s"(out));
  const int type = (xcd & 1) ^ 1, tt = ((xcd >> 1) & 1) * 8 + (jb & 7), hg = (xcd >> 2) * 4 + (jb >> 3);
  const u32x4 rsWS = mkrsrc(ws, WS_END);
  u32 vim[9];
  {
    if (w < 4) {
      const int cg = w >> 1, th = w & 1;
      u32x16 pa, pb; u32x4 pc, pd;
      {
        const int lane = lane_id(), g = lane >> 4, i16 = lane & 15;
        u32 aW = lds0 + K1_W + (u32)cg * 10240u + (u32)(4 * g + (i16 >> 2)) * 160u + (u32)(i16 & 3) * 40u;
        u32 aX0 = lds0 + (u32)(64 * th + i16) * 128u + (u32)((g ^ (i16 >> 1)) * 16), aX1 = aX0 ^ 64u;
        u32 cnt, inc;
        asm volatile(K1_LOOP_ASM
                     : "=&{v[80:95]}"(pa), "=&{v[96:111]}"(pb), "=&{v[112:115]}"(pc), "=&{v[156:159]}"(pd),
                       "+{v152}"(aX0), "+{v153}"(aX1), "+{v154}"(aW), [cnt] "=&s"(cnt), [inc] "=&s"(inc)
                     : [wrap] "s"(0u - 3u * K1_SLOT), [stp] "s"(K1_SLOT)
                     : "memory", "scc", K1_LOOP_CLOB);
      }
      const int lane = lane_id();
      const u32x4 qa[4] = {{pa[0], pa[1], pa[2], pa[3]}, {pa[8], pa[9], pa[10], pa[11]}, {pb[0], pb[1], pb[2], pb[3]}, {pb[8], pb[9], pb[10], pb[11]}};
      const u32x4 qb[4] = {{pa[4], pa[5], pa[6], pa[7]}, {pa[12], pa[13], pa[14], pa[15]}, {pb[4], pb[5], pb[6], pb[7]}, {pb[12], pb[13], pb[14], pb[15]}};
      const u32x2 qc[4] = {{pc[0], pc[1]}, {pc[2], pc[3]}, {pd[0], pd[1]}, {pd[2], pd[3]}};
      if (w == 3 && type == 0) {
        const u32 dst = WS_QB + (u32)(tt * 8 + hg) * 10240u;
#pragma unroll
        for (int n = 0; n < 4; ++n) {
          st128(rW, dst + n * 2560 + lane * 16, qa[n]); st128(rW, dst + n * 2560 + 1024 + lane * 16, qb[n]); st64(rW, dst + n * 2560 + 2048 + lane * 8, qc[n]);
        }
      } else {
        ldsp xw = lds + (cg ? K1_LGT : K1_XCH) + (u32)th * 10240u;
#pragma unroll
        for (int n = 0; n < 4; ++n) {
          *(LDSAS u32x4*)(xw + n * 2560 + lane * 16) = qa[n]; *(LDSAS u32x4*)(xw + n * 2560 + 1024 + lane * 16) = qb[n];
          *(LDSAS u32x2*)(xw + n * 2560 + 2048 + lane * 8) = qc[n];
        }
      }
      LBAR();
      if (w == 3) {
        const u32x2 tag = ld64(lds + F_TAG);
        const u32 fq = WS_FQ + (u32)(tt * 8 + hg) * 128u;
        if (type == 0) { DRAIN(); st64(rW, lane == 0 ? fq : OOB, tag); }
        else { flags_wait<1>(rW, fq, 1, tag); lds_st(lds + F_QOK, 1u); }
      }
    } else {
      const int lane = lane_id(), g = lane >> 4, i16 = lane & 15;
      if (w >= 8) {
        __builtin_amdgcn_s_setprio(1);
        const int lw = w - 8, g1 = lw >> 1, hb = lw & 1;
        const u32x4 rsX = mkrsrc(x - 4096, SEQ * DM * 2 + 4096);
        const bool useq = g1 && !type;
        const u32x4 rsW = useq ? mkrsrc(wq - 4096, DM * DM * 2 + 4096) : mkrsrc(wqkv - 4096, DM * 3 * DM * 2 + 4096);
        const u32 pitch = useq ? 1280u : 3840u;
        const u32 col = (type ? (g1 ? 1280u : 640u) : 0u) + 80u * hg;
        u32 vx[4], vw[5];
        const u32 vx0 = 4096u + (u32)(lane >> 3) * 1280u + (u32)(((lane & 7) ^ (lane >> 4)) * 16);
#pragma unroll
        for (int i = 0; i < 4; ++i) vx[i] = (vx0 ^ (u32)((i & 1) * 64)) + (u32)i * 9216u;
#pragma unroll
        for (int j = 0; j < 5; ++j) {
          const u32 c = 64u * j + lane, rho = (c * 205u) >> 11, cc = c - rho * 10u;
          const u32 k = 8u * ((rho >> 2) & 3u) + 4u * (rho >> 4) + (rho & 3u);
          vw[j] = 4096u + k * pitch + cc * 16u - (u32)(j & 3) * 1024u;
        }
        const u32 sx0 = (u32)(128 * tt + 32 * lw) * 1280u, sw0 = 32u * hb * pitch + col * 2u;
        int ks = K1_ROT;
        auto issue = [&](int s) {
          const u32 base = lds0 + (u32)(s & 3) * K1_SLOT;
          u32 keep;
          asm volatile(
              "s_mov_b32 %0, m0\n\ts_mov_b32 m0, %[mx]\n\ts_nop 2\n\t"
              "buffer_load_dwordx4 %[x0], %[rx], %[sx] offen lds\n\t"
              "buffer_load_dwordx4 %[x1], %[rx], %[sx] offen offset:1024 lds\n\t"
              "buffer_load_dwordx4 %[x2], %[rx], %[sx] offen offset:2048 lds\n\t"
              "buffer_load_dwordx4 %[x3], %[rx], %[sx] offen offset:3072 lds\n\t"
              "s_mov_b32 m0, %[mw]\n\ts_nop 0\n\t"
              "buffer_load_dwordx4 %[w0], %[rw], %[sw] offen lds\n\t"
              "buffer_load_dwordx4 %[w1], %[rw], %[sw] offen offset:1024 lds\n\t"
              "buffer_load_dwordx4 %[w2], %[rw], %[sw] offen offset:2048 lds\n\t"
              "buffer_load_dwordx4 %[w3], %[rw], %[sw] offen offset:3072 lds\n\t"
              "s_add_u32 m0, %[mw], 4096\n\ts_nop 0\n\t"
              "buffer_load_dwordx4 %[w4], %[rw], %[sw] offen lds\n\t"
              "s_mov_b32 m0, %0"
              : "=&s"(keep)
              : [mx] "s"(base + (u32)lw * 4096u), [mw] "s"(base + K1_W + (u32)lw * 5120u),
                [rx] "s"(rsX), [rw] "s"(rsW), [sx] "s"(sx0 + (u32)ks * 128u), [sw] "s"(sw0 + (u32)ks * 64u * pitch),
                [x0] "v"(vx[0]), [x1] "v"(vx[1]), [x2] "v"(vx[2]), [x3] "v"(vx[3]), [w0] "v"(vw[0]), [w1] "v"(vw[1]), [w2] "v"(vw[2]), [w3] "v"(vw[3]),
                [w4] "v"(vw[4])
              : "memory", "scc");
          ks = ks == 9 ? 0 : ks + 1;
        };
        __builtin_amdgcn_s_sleep(2);
        issue(0);
        issue(1);
        WAIT_BAR(9);
#pragma unroll 1
        for (int s = 1; s < 9; ++s) {
          issue(s + 1);
          WAIT_BAR(9);
        }
        auto prefill = [&]() {
          const u32 o = (u32)lw * 8192u, vl = (u32)lane * 16u;
          dma4(rsWS, vl, WS_KVIM + o, lds0 + L_RING + o); dma4(rsWS, vl, WS_KVIM + o + 4096u, lds0 + L_RING + o + 4096u);
        };
        WAIT_BAR(0);
        lds_wait_ge<1>(lds + F_KVOK, 2);
        prefill();
      } else {
        const int v4 = w - 4, tok0 = 128 * tt + 16 * hg;
        __builtin_amdgcn_s_setprio(3);
        {
          const u32x4 rsE = mkrsrc((type ? wv : wk) + v4 * 6400, 6400);
          const u32 la = lds0 + K1_EX + (u32)v4 * 7168u;
#pragma unroll 1
          for (u32 c = 0; c < 7; ++c) dma1(rsE, (u32)lane * 16u, c * 1024u, la + c * 1024u);
          u32x4 xf[5], gf[5];
#pragma unroll
          for (int s = 0; s < 5; ++s) {
            xf[s] = *(const u32x4*)(x + (size_t)(tok0 + i16) * 1280 + v4 * 320 + s * 64 + g * 16);
            gf[s] = *(const u32x4*)(gw + v4 * 320 + s * 64 + g * 16);
          }
          __builtin_amdgcn_s_setprio(0);
#pragma unroll
          for (int i = 0; i < 9; ++i) {
            const u32 c = 64u * (u32)(v4 + 3 * i) + lane, rho = (c * 6554u) >> 16, cc = c - rho * 10u;
            const u32 U = 4u * (rho >> 5) + ((rho >> 2) & 3u), u = (rho >> 4) & 1u, q = rho & 3u;
            const u32 hp = (U * 205u) >> 10, un = U - hp * 5u;
            const u32 row = 40u * hp + q + ((un < 4u) ? (un >> 1) * 20u + 4u * (un & 1u) + 8u * u : u * 20u + 16u);
            vim[i] = row * 1280u + cc * 16u;
            PIN(vim[i]);
          }
          __builtin_amdgcn_s_setprio(3);
          DRAIN();
          ldsp pe = lds + K1_EX + (u32)v4 * 7168u + (u32)(8 * g + (i16 >> 2)) * 40u + (u32)(i16 & 3) * 8u;
          f32x4 c0 = {0.f, 0.f, 0.f, 0.f}, c1 = c0, c2 = c0;
          u32x4 w0[5], w1[5];
#pragma unroll
          for (int s = 0; s < 5; ++s) { w0[s] = trd2(pe + s * 1280, pe + s * 1280 + 160); w1[s] = trd2(pe + s * 1280 + 32, pe + s * 1280 + 192); }
#pragma unroll
          for (int s = 0; s < 5; ++s) c2 = mfma16(gf[s], xf[s], c2);
          if (type) {
#pragma unroll
            for (int s = 0; s < 5; ++s) { c0 = mfma16(xf[s], w0[s], c0); c1 = mfma16(xf[s], w1[s], c1); }
          } else {
#pragma unroll
            for (int s = 0; s < 5; ++s) { c0 = mfma16(w0[s], xf[s], c0); c1 = mfma16(w1[s], xf[s], c1); }
          }
          ldsp pp = lds + K1_EXP + (u32)v4 * 1024u + lane * 16;
          *(LDSAS f32x4*)(pp) = c0; *(LDSAS f32x4*)(pp + 4096) = c1; *(LDSAS f32x4*)(pp + 8192) = c2;
        }
        LBAR();
        ldsp pp = lds + K1_EXP + (u32)(v4 < 3 ? v4 : 0) * 4096u + lane * 16;
        f32x4 e0 = (*(LDSAS f32x4*)(pp) + *(LDSAS f32x4*)(pp + 1024)) + (*(LDSAS f32x4*)(pp + 2048) + *(LDSAS f32x4*)(pp + 3072));
        u32x2 ep;
        asm volatile("buffer_load_dwordx2 %0, off, %1, %2 sc1" : "=v"(ep) : "s"(rsWS), "s"(WS_EPOCH) : "memory");
        PIN(e0);
        LBAR();
        {
          const float ks_ = type ? 1.f : QSCALE;
          u32x2 ev = {pk2(e0[0] * ks_, e0[1] * ks_), pk2(e0[2] * ks_, e0[3] * ks_)};
          const int tile = tok0 >> 5, k0 = tok0 & 31;
          const u32 rec = WS_KVIM + (u32)((tile + 63) & 63) * REC;
          const int d = 16 * v4 + i16, k32 = k0 + 4 * g;
          const u32 oK = v4 == 0 ? rec + (g >> 1) * 512 + (k0 + i16) * 16 + (g & 1) * 8 : OOB;
          const u32 oV = d <= 21 ? WS_KVIM + (u32)tile * REC + 1280 + (k32 >> 3) * 352 + d * 16 + (k32 & 7) * 2 : OOB;
          const u32 o2 = (type || g || v4 != 1) ? OOB : rec + 1024 + (k0 + i16) * 4;
          const u32 oG = (type || g || v4 != 2) ? o2 : WS_GATE + (u32)(tok0 + i16) * 4u;
          const u32 gv = __float_as_uint(__builtin_amdgcn_rcpf(1.f + __builtin_amdgcn_exp2f(-e0[0] * LOG2E)));
          st32(rW, oG, v4 == 2 ? gv : ev[0]);
          st32(rW, o2 + 128, ev[1]);
          if (d >= 20) ev = (u32x2){d == 20 ? 0x3F803F80u : 0u, d == 20 ? 0x3F803F80u : 0u};
          st64(rW, type ? oV : oK, ev);
        }
        {
          const u32 be = (u32)(v4 * 64 + lane) * 16u;
          dma1(mkrsrc(wphi, NH * 400 * 2), be < 3200u ? be : OOB, (u32)hg * 3200u, lds0 + K1_WPHI + (u32)v4 * 1024u);
        }
        LBAR();
        LBAR();
        WAIT_BAR(0);
        PIN(ep);
        if (w == 7) {
          const u64 e = ((u64)ep[0] | ((u64)ep[1] << 32)) + 0x9E3779B97F4A7C15ull;
          const u32x2 tag = {(u32)e, (u32)(e >> 32)};
          lds_st(lds + L_FLG + lane * 4, lane == 34 ? tag[0] : (lane == 35 ? tag[1] : 0u));
          st64(rW, lane < 8 ? WS_F1 + (u32)lane * 2048u + (u32)bid * 8u : OOB, tag);
          LBAR(); LBAR(); LBAR(); LBAR();
          const u32 fo = WS_F1 + (u32)xcd * 2048u;
          u32x4 fa = gl128(rW, lane * 16, fo), fb = gl128(rW, lane * 16, fo + 1024);
          LBAR();
          PIN(fa); PIN(fb);
          const u32 dd = (fa[0] ^ tag[0]) | (fa[1] ^ tag[1]) | (fa[2] ^ tag[0]) | (fa[3] ^ tag[1]) | (fb[0] ^ tag[0]) | (fb[1] ^ tag[1]) | (fb[2] ^ tag[0]) | (fb[3] ^ tag[1]);
          if (!__all(dd == 0u)) flags_wait256<2>(rW, fo, tag);
          asm volatile("" ::: "memory");
          const u32x4 ka = gl128(rW, lane * 16, WS_KVIM + 63 * REC);
          const u32 kc = gl32(rW, lane * 4, WS_KVIM + 63 * REC + 1024);
          const u32 gtv = gl32(rW, lane * 4, WS_GATE + (u32)(2 * tt + type) * 256u);
          u32 wrm = gl32(rW, lane < 34 ? (u32)(jb * 34 + lane) * 128u : OOB, WS_KVIM + 32768);
          asm volatile("" ::: "memory");
          lds_st(lds + F_KVOK, 2u);
          if (bid == 0 && lane == 0)
            __hip_atomic_store((gu64*)(ws + WS_EPOCH), (u64)tag[0] | ((u64)tag[1] << 32), __ATOMIC_RELAXED, __HIP_MEMORY_SCOPE_AGENT);
          *(LDSAS u32x4*)(lds + L_K01 + lane * 16) = ka; *(LDSAS u32*)(lds + L_K01 + 1024 + lane * 4) = kc;
          *(LDSAS u32*)(lds + L_GT + lane * 4) = gtv;
          PIN(wrm);
        } else {
          LBAR(); LBAR(); LBAR(); LBAR();
          LBAR();
        }
      }
      __builtin_amdgcn_s_setprio(2);
      const int j8 = w - 4;
      const u32 aP = K1_WPHI + (((i16 & 3) == g) ? (u32)g * 800u + (u32)(i16 >> 2) * 40u : 3200u);
      u32x4 a0[5], a1[5]; u32x2 a2[5];
#pragma unroll
      for (int T = 0; T < 5; ++T) {
        a0[T] = trd2(lds + aP + T * 8, lds + aP + T * 8 + 160);
        a1[T] = trd2(lds + aP + T * 8 + 320, lds + aP + T * 8 + 480);
        a2[T] = trd(lds + aP + T * 8 + 640);
      }
      LBAR();
      ldsp xr = lds + K1_XCH + (u32)j8 * 2560u;
      const u32x4 bq0 = ld128(xr + lane * 16), bq1 = ld128(xr + 1024 + lane * 16);
      const u32x2 bq2 = ld64(xr + 2048 + lane * 8);
      if (w >= 8) {
        DRAIN();
        if (lds_arrive(lds + F_K01) == 3 && lane < 8) lds_st(lds + L_FLG + 32 + lane * 4, 3);
      }
      f32x4 pq[5];
#pragma unroll
      for (int T = 0; T < 5; ++T) pq[T] = mfma16(a0[T], bq0, (f32x4){0.f, 0.f, 0.f, 0.f});
#pragma unroll
      for (int T = 0; T < 5; ++T) pq[T] = mfma16(a1[T], bq1, pq[T]);
#pragma unroll
      for (int T = 0; T < 5; ++T) pq[T] = mfma16((u32x4){a2[T][0], a2[T][1], 0u, 0u}, (u32x4){bq2[0], bq2[1], 0u, 0u}, pq[T]);
      float mx = fmaxf(pq[0][0], pq[0][1]), mn = fminf(pq[0][0], pq[0][1]);
      mx = fmaxf(fmaxf(mx, pq[0][2]), pq[0][3]); mn = fminf(fminf(mn, pq[0][2]), pq[0][3]);
#pragma unroll
      for (int T = 1; T < 5; ++T) {
        mx = fmaxf(fmaxf(mx, pq[T][0]), pq[T][1]); mx = fmaxf(fmaxf(mx, pq[T][2]), pq[T][3]);
        mn = fminf(fminf(mn, pq[T][0]), pq[T][1]); mn = fminf(fminf(mn, pq[T][2]), pq[T][3]);
      }
      const float cp = -mx * LOG2E, cn = mn * LOG2E;
      f32x4 ep[5], en[5];
#pragma unroll
      for (int T = 0; T < 5; ++T) { ep[T] = pq[T] * LOG2E + cp; en[T] = pq[T] * (-LOG2E) + cn; PIN(ep[T]); PIN(en[T]); }
#pragma unroll
      for (int T = 0; T < 5; ++T)
#pragma unroll
        for (int i = 0; i < 4; ++i) { ep[T][i] = __builtin_amdgcn_exp2f(ep[T][i]); en[T][i] = __builtin_amdgcn_exp2f(en[T][i]); }
      const f32x4 sp = (ep[0] + ep[1]) + (ep[2] + ep[3]) + ep[4], sn = (en[0] + en[1]) + (en[2] + en[3]) + en[4];
      const float rp = __builtin_amdgcn_rcpf((sp[0] + sp[1]) + (sp[2] + sp[3])), rn = __builtin_amdgcn_rcpf((sn[0] + sn[1]) + (sn[2] + sn[3]));
      u32 ph[20];
#pragma unroll
      for (int T = 0; T < 5; ++T) {
        const f32x4 a = ep[T] * rp, b = en[T] * rn;
        ph[2 * T] = pk2(a[0], a[1]); ph[2 * T + 1] = pk2(a[2], a[3]);
        ph[10 + 2 * T] = pk2(b[0], b[1]); ph[11 + 2 * T] = pk2(b[2], b[3]);
      }
      const int tl = 16 * j8 + i16;
      if (type == 0) {
        ldsp dst = lds + K1_PHQ + (u32)(4 * g + (tl >> 5)) * 2560u + (tl & 31) * 16;
#pragma unroll
        for (int c = 0; c < 5; ++c) *(LDSAS u32x4*)(dst + c * 512) = (u32x4){ph[4 * c], ph[4 * c + 1], ph[4 * c + 2], ph[4 * c + 3]};
      } else {
        ldsp dst = lds + K1_PHK + (u32)tl * K1_PHK_P + (u32)g * 80u;
#pragma unroll
        for (int c = 0; c < 5; ++c) *(LDSAS u32x4*)(dst + 16 * c) = (u32x4){ph[4 * c], ph[4 * c + 1], ph[4 * c + 2], ph[4 * c + 3]};
        lds_arrive(lds + F_PHB);
        lds_wait_ge<1>(lds + F_PHB, 8);
        const int hl = j8 >> 1, dt = j8 & 1;
        const u32 trow = (u32)(8 * g + (i16 >> 2));
        ldsp pa = lds + K1_PHK + trow * K1_PHK_P + (u32)hl * 80u + (u32)(i16 & 3) * 8u;
        const u32 vrow = (u32)(hl * 16) + (trow & 15u), vq = (u32)(i16 & 3);
        ldsp pb = lds + K1_LGT + (u32)(g >> 1) * 2560u + (dt ? 2048u + vrow * 8u : (vq >> 1) * 1024u + vrow * 16u + (vq & 1u) * 8u);
        const u32 vstep = dt ? 32u : 64u;
        f32x4 kv[3];
#pragma unroll
        for (int ft = 0; ft < 3; ++ft) kv[ft] = (f32x4){0.f, 0.f, 0.f, 0.f};
#pragma unroll
        for (int ks = 0; ks < 4; ++ks) {
          u32x4 bv = trd2(pb + ks * 5120, pb + ks * 5120 + vstep);
          if (dt && i16 == 4) bv = (u32x4){0x3F803F80u, 0x3F803F80u, 0x3F803F80u, 0x3F803F80u};
#pragma unroll
          for (int ft = 0; ft < 3; ++ft) {
            u32x4 av = trd2(pa + ks * 32 * K1_PHK_P + ft * 32, pa + (ks * 32 + 4) * K1_PHK_P + ft * 32);
            kv[ft] = mfma16(av, bv, kv[ft]);
          }
        }
        const u32 kb = WS_KVP + (u32)((hg * 16 + tt) * 4 + hl) * 3360u;
        const u32 ko = dt ? (i16 < 5 ? kb + 2560u + (u32)(g * 5 + i16) * 16u : OOB) : kb + (u32)lane * 16u, kst = dt ? 320u : 1024u;
        st128(rW, ko, __builtin_bit_cast(u32x4, kv[0])); st128(rW, ko + kst, __builtin_bit_cast(u32x4, kv[1]));
        st128(rW, g < 2 ? ko + 2 * kst : OOB, __builtin_bit_cast(u32x4, kv[2]));
      }
      lds_arrive(lds + F_P2);
      if (type) {
        const u32x2 tag = ld64(lds + F_TAG);
        DRAIN();
        if (lds_arrive(lds + F_P1) == 7) { st64(rW, lane == 0 ? WS_F2 + (u32)(hg * 16 + tt) * 8u : OOB, tag); }
      }
    }
  }

  if (w >= 8) {
    __builtin_amdgcn_s_setprio(3);
    const int hw = w - 8, kh = hw >> 1, t64 = xcd * 4 + (jb & 3), cgp = jb >> 2;
    const u32x2 tag = ld64(lds + F_TAG);
    lds_wait_ge<2>(lds + F_P2, 8);
    {
      const int lane = lane_id(), g = lane >> 4, i16 = lane & 15, r32 = lane & 31, hh = lane >> 5;
      u32x4 p0[4], p1[4], p2[4]; u32 gt[4];
      if (type) {
        const u32 it = (u32)(tt * 53 + hw * 14 + i16);
        const bool ok = i16 < 14 && hw * 14 + i16 < 53 && it < 840u;
        const u32 hl = (it * 4994u) >> 20, e = it - hl * 210u;
        const u32 e1 = e - 160u, ft1 = (e1 * 205u) >> 12, r1 = e1 - ft1 * 20u, g1 = (r1 * 205u) >> 10;
        const u32 d = e < 160u ? (e & 15u) : 16u + r1 - g1 * 5u, f = e < 160u ? (e >> 6) * 16u + ((e >> 4) & 3u) * 4u : ft1 * 16u + g1 * 4u;
        const u32 vo = ok ? it * 16u + (u32)g * 4u * 13440u : OOB, pb = WS_KVP + (u32)hg * 16u * 13440u;
        u32 dst = (ok && g == 0) ? WS_KVT + (u32)(4 * hg + hl) * 1680u + d * 80u + f * 2u : OOB;
        PIN(dst);
        if (hw == 0) { flags_wait<1>(rW, WS_F2 + (u32)hg * 128u, 16, tag); lds_st(lds + F_GBAR, 1u); }
        else lds_wait_ge<2>(lds + F_GBAR, 1);
        f32x4 a = (__builtin_bit_cast(f32x4, gl128(rW, vo, pb)) + __builtin_bit_cast(f32x4, gl128(rW, vo, pb + 13440))) +
                  (__builtin_bit_cast(f32x4, gl128(rW, vo, pb + 2 * 13440)) + __builtin_bit_cast(f32x4, gl128(rW, vo, pb + 3 * 13440)));
#pragma unroll
        for (int i = 0; i < 4; ++i) { a[i] += __shfl_xor(a[i], 16); a[i] += __shfl_xor(a[i], 32); }
        st64(rW, dst, (u32x2){pk2(a[0], a[1]), pk2(a[2], a[3])});
        DRAIN();
        if (lds_arrive(lds + F_APUB) == 3) { st64(rW, lane == 0 ? WS_F3 + (u32)(hg * 16 + tt) * 8u : OOB, tag); }
      } else {
#pragma unroll
        for (int t4 = 0; t4 < 4; ++t4) {
          ldsp pk = lds + K1_PHQ + (u32)(4 * hw + t4) * 2560u;
          p0[t4] = ld128(pk + lane * 16); p1[t4] = ld128(pk + 1024 + lane * 16); p2[t4] = ld128(pk + 2048 + r32 * 16);
          gt[t4] = gl32(rW, r32 * 4, WS_GATE + (u32)(128 * tt + 32 * t4) * 4u);
          PIN(p0[t4]); PIN(p1[t4]); PIN(p2[t4]);
        }
        lds_arrive(lds + F_P3);
        if (hw == 0) { lds_wait_ge<4>(lds + F_QOK, 4); lds_arrive(lds + F_P3); }
      }
      if (type == 0) {
        if (hw == 0) { flags_wait<1>(rW, WS_F3 + (u32)hg * 128u, 16, tag); lds_st(lds + F_GBAR, 1u); }
        lds_wait_ge<2>(lds + F_GBAR, 1);
        const u32 ao = (u32)(r32 < 20 ? r32 : 20) * 80u + (u32)hh * 16u, ab = WS_KVT + (u32)(4 * hg + hw) * 1680u;
        const u32x4 a0 = gl128(rW, ao, ab), a1 = gl128(rW, ao, ab + 32), a2 = gl128(rW, hh ? OOB : ao, ab + 64);
        const u32 vo0 = WS_OFR + (u32)(8 * tt + (r32 >> 4)) * 20480u + (r32 & 15) * 16 + (u32)(2 * hg + (hw >> 1)) * 1280u;
        const u32 vo1 = vo0 + hh * 256 + (hw & 1) * 512, vo2 = hh ? OOB : vo0 + 1024 + (hw & 1) * 8;
        f32x16 N[4];
#pragma unroll
        for (int t4 = 0; t4 < 4; ++t4) N[t4] = mfma32(a0, p0[t4], zero16);
#pragma unroll
        for (int t4 = 0; t4 < 4; ++t4) N[t4] = mfma32(a1, p1[t4], N[t4]);
#pragma unroll
        for (int t4 = 0; t4 < 4; ++t4) N[t4] = mfma32(a2, p2[t4], N[t4]);
#pragma unroll
        for (int t4 = 0; t4 < 4; ++t4) {
          const float sa = __uint_as_float(gt[t4]) * __builtin_amdgcn_rcpf(N[t4][12] + 1e-6f);
          st128(rW, vo1 + t4 * 40960, (u32x4){pk2(N[t4][0] * sa, N[t4][1] * sa), pk2(N[t4][2] * sa, N[t4][3] * sa), pk2(N[t4][4] * sa, N[t4][5] * sa), pk2(N[t4][6] * sa, N[t4][7] * sa)});
          st64(rW, vo2 + t4 * 40960, (u32x2){pk2(N[t4][8] * sa, N[t4][9] * sa), pk2(N[t4][10] * sa, N[t4][11] * sa)});
        }
        DRAIN();
        if (lds_arrive(lds + F_APUB) == 3) { st64(rW, lane == 0 ? WS_FLGA + (u32)(tt * 16 + hg) * 8u : OOB, tag); }
      }
      if (hw == 0) { flags_wait<1>(rW, WS_FLGA + (u32)(t64 >> 1) * 128u, 8, tag); lds_arrive(lds + F_AOK); }
    }
    f32x16 ac0 = zero16, ac1 = zero16; f32x8 ac2 = {0.f, 0.f, 0.f, 0.f, 0.f, 0.f, 0.f, 0.f};
#pragma unroll 1
    for (int part = 0; part < 2; ++part) {
      if (part == 0) {
        lds_wait_ge<2>(lds + F_AOK, 4u);
      } else {
        lds_arrive(lds + F_AFREE + 4);
        lds_wait_ge<8>(lds + F_BIMG, 3u);
        lds_wait_ge<4>(lds + F_FIN, 4u);
        if (hw == 0) { flags_wait<1>(rW, WS_FLGB + (u32)t64 * 256u, 32, tag); lds_st(lds + F_FIN, 5u); }
        else lds_wait_ge<0>(lds + F_FIN, 5u);
      }
      const int ln = lane_id();
      u32 s0 = WS_OFR + (u32)(part * 2621440 + (4 * t64 + 2 * (hw & 1) + kh) * 20480 + kh * 10240), s1 = s0 + (kh ? 0u - 20480u : 20480u);
      asm volatile(TILE_GEMM_ASM
                   : "+{v[0:15]}"(ac0), "+{v[16:31]}"(ac1), "+{v[32:39]}"(ac2), [s0] "+s"(s0), [s1] "+s"(s1)
                   : "{v140}"((u32)ln * 16u), "{v141}"(lds0 + L_IMG + (u32)(4 * (ln >> 4) + ((ln & 15) >> 2)) * 160u + (u32)(ln & 3) * 40u + (u32)kh * 51200u), "{v142}"(lds0 + F_AFREE), "{v143}"(1u), [rs] "s"(rsWS)
                   : "memory", "scc", TILE_GEMM_CLOB);
    }
    {
      const int ln = lane_id();
      f32x4 acc[5][2];
#pragma unroll
      for (int n = 0; n < 2; ++n) {
        acc[0][n] = (f32x4){ac0[4 * n], ac0[4 * n + 1], ac0[4 * n + 2], ac0[4 * n + 3]}; acc[1][n] = (f32x4){ac0[8 + 4 * n], ac0[9 + 4 * n], ac0[10 + 4 * n], ac0[11 + 4 * n]};
        acc[2][n] = (f32x4){ac1[4 * n], ac1[4 * n + 1], ac1[4 * n + 2], ac1[4 * n + 3]}; acc[3][n] = (f32x4){ac1[8 + 4 * n], ac1[9 + 4 * n], ac1[10 + 4 * n], ac1[11 + 4 * n]};
        acc[4][n] = (f32x4){ac2[4 * n], ac2[4 * n + 1], ac2[4 * n + 2], ac2[4 * n + 3]};
      }
      ldsp xb = lds + L_RING + 3 * SL;
      ldsp xw = xb + (u32)hw * 5120u + ln * 16, xr = xb + (u32)(hw ^ 2) * 5120u + ln * 16;
#pragma unroll
      for (int T = 0; T < 5; ++T) *(LDSAS f32x4*)(xw + T * 1024) = acc[T][1];
      asm volatile("" ::: "memory");
      lds_st(lds + F_XF + hw * 4, 1u);
      f32x4 pv[5];
#pragma unroll 1
      for (u32 it = 0; it < SPIN_CAP; ++it) {
        asm volatile("" ::: "memory");
        const u32 f = lds_ld(lds + F_XF + (hw ^ 2) * 4);
        asm volatile("" ::: "memory");
#pragma unroll
        for (int T = 0; T < 5; ++T) pv[T] = *(LDSAS f32x4*)(xr + T * 1024);
        if (__builtin_amdgcn_readfirstlane(f)) break;
      }
      u32 o[10];
#pragma unroll
      for (int T = 0; T < 5; ++T) {
        const f32x4 v = acc[T][0] + pv[T];
        o[2 * T] = pk2(v[0], v[1]); o[2 * T + 1] = pk2(v[2], v[3]);
      }
      const rsrc_t rOut = __builtin_amdgcn_make_buffer_rsrc(out, 0, SEQ * DM * 2, 0x00020000);
      const int dst = (64 * t64 + 32 * (hw & 1) + 16 * kh + (ln & 15)) * 1280 + cgp * 160 + (ln >> 4) * 40;
      __builtin_amdgcn_raw_buffer_store_b128((u32x4){o[0], o[1], o[2], o[3]}, rOut, dst, 0, 2);
      __builtin_amdgcn_raw_buffer_store_b128((u32x4){o[4], o[5], o[6], o[7]}, rOut, dst + 16, 0, 2);
      __builtin_amdgcn_raw_buffer_store_b64((u32x2){o[8], o[9]}, rOut, dst + 32, 0, 2);
    }
    return;
  }

  if (w >= 4) {
    __builtin_amdgcn_s_setprio(3);
    if (w < 7) {
      const int v4 = w - 4, cgp = jb >> 2;
#pragma unroll 1
      for (int part = 0; part < 2; ++part) {
        if (part == 0) lds_wait_ge<4>(lds + (type ? F_P2 : F_P3), type ? 8u : 5u);
        const u32x4 rsO = mkrsrc(part ? bwo : awo, DM * DM * 2);
#pragma unroll 1
        for (u32 pi = 0; pi < 4; ++pi) {
          const u32 per = (pi >> 1) | ((pi & 1) << 1);
          if (part && !(pi & 1)) lds_wait_ge<2>(lds + F_AFREE + (pi >> 1) * 4, 4u);
          const u32 so = per * 204800u + (u32)cgp * 160u, la = lds0 + L_IMG + (per * 25u + (u32)v4) * 1024u;
#pragma unroll
          for (int i = 0; i < 8; ++i) dma1(rsO, vim[i], so, la + i * 3072);
          if (v4 == 0) dma1(rsO, vim[8], so, la + 24576);
        }
        DRAIN();
        lds_arrive(lds + (part ? F_BIMG : F_AOK));
        if (part == 0) {
          const int ln = lane_id();
          const u32 l0 = (u32)jb * 200u + (u32)v4 * 64u + ln;
          u32 dummy;
          asm volatile("s_nop 4\n\tbuffer_load_dword %0, %1, %2, 0 offen\n\tbuffer_load_dword %0, %3, %2, 0 offen\n\ts_waitcnt vmcnt(0)"
                       : "=&v"(dummy) : "v"(l0 * 128u), "s"(mkrsrc(bwo, DM * DM * 2)), "v"((v4 == 0 && ln < 8) ? (l0 + 192u) * 128u : OOB) : "memory");
        }
      }
    } else {
      const int ln = lane_id();
      const u32 vl = (u32)ln * 16u;
      u32 s = 3, slot = 3, ps = 3;
      lds_wait_ge<2>(lds + L_FLG + 32, 3);
#pragma unroll 1
      for (u32 it = 0; it < SPIN_CAP && ps < 16; ++it) {
        const u32 fl = lds_ld(lds + L_FLG + (ln & 3) * 4);
        const u32 ns = s;
        if (s < 16 && __all(fl + 4 >= s)) {
          const u32 so = WS_KVIM + s * SL, la = lds0 + L_RING + slot * SL;
          dma4(rsWS, vl, so, la); dma4(rsWS, vl, so + 4096u, la + 4096u); dma2h(rsWS, vl, so + 8192u, la + 8192u);
          asm volatile("s_waitcnt vmcnt(11)" ::: "memory");
          ++s; slot = slot == 4 ? 0 : slot + 1;
        } else {
          DRAIN();
          if (ps == ns) __builtin_amdgcn_s_sleep(14);
        }
        if (ps != ns) { ps = ns; if (ln < 4) lds_st(lds + L_FLG + 32 + ln * 4, ps); }
      }
    }
    return;
  }
  const int head = 4 * hg + w, t64 = 2 * tt + type;
  u32x4 q1[2]; u32x2 q2[2];
  f32x16 O0, O1;
  const int lane = lane_id(), r32 = lane & 31, hh = lane >> 5;
  float og[2]; u32x2 tag; u32 vo1, vo2;
  __builtin_amdgcn_s_setprio(3);
  {
    const u32 qo1 = (u32)(r32 >> 4) * 2560u + (u32)hh * 1024u + (u32)(w * 16 + (r32 & 15)) * 16u;
    const u32 qo2 = (u32)(r32 >> 4) * 2560u + 2048u + (u32)(w * 16 + (r32 & 15)) * 8u + (u32)hh * 4u;
    if (type) {
      lds_wait_ge<1>(lds + F_QOK, 1);
#pragma unroll
      for (int j = 0; j < 2; ++j) {
        const u32 qrow = WS_QB + (u32)(tt * 8 + hg) * 10240u + (u32)j * 5120u;
        q1[j] = gl128(rW, qo1, qrow);
        q2[j] = (u32x2){gl32(rW, qo2, qrow), 0u};
      }
    } else {
#pragma unroll
      for (int j = 0; j < 2; ++j) {
        q1[j] = ld128(lds + K1_LGT + j * 5120 + qo1);
        q2[j] = (u32x2){*(LDSAS u32*)(lds + K1_LGT + j * 5120 + qo2), 0u};
      }
      PIN(q1[0]); PIN(q1[1]); PIN(q2[0]); PIN(q2[1]);
      lds_arrive(lds + F_QOK);
    }
    u32x4 k1 = ld128(lds + L_K01 + lane * 16);
    u32x2 k2 = {*(LDSAS u32*)(lds + L_K01 + 1024 + lane * 4), hh ? 0u : 0x00003F80u};
    f32x16 S0, S1, S2, S3;
#pragma unroll
    for (int j = 0; j < 2; ++j) {
      f32x16 t = mfma32(k1, q1[j], zero16);
      t = mfma32k8(k2, q2[j], t);
      const float m = xhalf_max(max16(t));
      q2[j][1] = hh ? 0u : (pk2(-(m + 48.f), 0.f) & 0xffffu);
    }
    S0 = mfma32(k1, q1[0], zero16);
    S0 = mfma32k8(k2, q2[0], S0);
    u32x4 vlo, vhi;
    const u32 rb = lds0 + L_RING;
    u32 aK1 = rb + (u32)lane * 16u, aK2 = rb + 1024u + (u32)lane * 4u;
    u32 aV = rb + 1280u + (u32)hh * 352u + (u32)(r32 <= 20 ? r32 : 21) * 16u;
    u32 aF = lds0 + L_FLG + (u32)w * 4u, tmp;
    og[0] = 1.f - *(LDSAS float*)(lds + L_GT + r32 * 4); og[1] = 1.f - *(LDSAS float*)(lds + L_GT + 128 + r32 * 4);
    tag = ld64(lds + F_TAG);
    const u32 vo = WS_OFR + 2621440u + (u32)(4 * t64 + (r32 >> 4)) * 20480u + (r32 & 15) * 16 + (head >> 1) * 1280;
    vo1 = vo + hh * 256 + (head & 1) * 512; vo2 = hh ? OOB : vo + 1024 + (head & 1) * 8;
    PIN(og[0]); PIN(og[1]); PIN(tag); PIN(vo1); PIN(vo2);
    u32 cnt, inc, slot, rdy;
    asm volatile(ATTN_LOOP_ASM
                 : "=&{v[0:15]}"(O0), "=&{v[16:31]}"(O1), "+{v[32:47]}"(S0), "=&{v[48:63]}"(S1), "=&{v[64:79]}"(S2), "=&{v[80:95]}"(S3),
                   "+{v[108:111]}"(k1), "+{v[112:113]}"(k2), "=&{v[116:119]}"(vlo), "=&{v[120:123]}"(vhi),
                   "+{v124}"(aK1), "+{v125}"(aK2), "+{v126}"(aV), "=&{v115}"(tmp), [cnt] "=&s"(cnt), [inc] "=&s"(inc), [slot] "=&s"(slot), [rdy] "=&s"(rdy)
                 : "{v[96:99]}"(q1[0]), "{v[100:103]}"(q1[1]), "{v[104:105]}"(q2[0]), "{v[106:107]}"(q2[1]), "{v114}"(0xDDDD), "{v127}"(aF),
                   [wrap] "s"(0u - 4u * SL), [stg] "s"(SL)
                 : "memory", "scc");
  }
  auto finish = [&](const f32x16& O, int j) -> bool {
    const float l = from_hi(O[8]), sb = og[j] * __builtin_amdgcn_rcpf(l);
    const f32x4 y0 = (f32x4){O[0], O[1], O[2], O[3]} * sb, y1 = (f32x4){O[4], O[5], O[6], O[7]} * sb, y2 = (f32x4){O[8], O[9], O[10], O[11]} * sb;
    st128(rW, vo1 + j * 40960, (u32x4){pk2(y0[0], y0[1]), pk2(y0[2], y0[3]), pk2(y1[0], y1[1]), pk2(y1[2], y1[3])});
    st64(rW, vo2 + j * 40960, (u32x2){pk2(y2[0], y2[1]), pk2(y2[2], y2[3])});
    const f32x4 ys = (y0 + y1) + y2;
    const float yy = (ys[0] + ys[1]) + (ys[2] + ys[3]);
    return !(__builtin_fabsf(yy) < 3.0e38f) || !(l < 1.0e37f) || !(l > 1.0e-25f);
  };
  const bool bad0 = finish(O0, 0), bad1 = finish(O1, 1);
  if (__builtin_expect(__any(bad0 || bad1), 0)) {
    DRAIN();
#pragma unroll 1
    for (int j = 0; j < 2; ++j) {
      if (!__any(j ? bad1 : bad0)) continue;
      const f32x16 O = attn_exact(rW, j ? q1[1] : q1[0], j ? q2[1][0] : q2[0][0], lane);
      const float sb = (j ? og[1] : og[0]) * __builtin_amdgcn_rcpf(from_hi(O[8]));
      st128(rW, vo1 + j * 40960, (u32x4){pk2(O[0] * sb, O[1] * sb), pk2(O[2] * sb, O[3] * sb), pk2(O[4] * sb, O[5] * sb), pk2(O[6] * sb, O[7] * sb)});
      st64(rW, vo2 + j * 40960, (u32x2){pk2(O[8] * sb, O[9] * sb), pk2(O[10] * sb, O[11] * sb)});
    }
  }
  DRAIN();
  st64(rW, lane == 0 ? WS_FLGB + (u32)(t64 * 32 + head) * 8u : OOB, tag);
  lds_arrive(lds + F_FIN);
}

extern "C" void kernel_launch(void* const* d_in, const int* in_sizes, int n_in, void* d_out, int out_size, void* d_ws,
                              size_t ws_size, hipStream_t stream) {
  (void)in_sizes; (void)n_in; (void)out_size; (void)ws_size;
  (void)hipFuncSetAttribute((const void*)gm_fused, hipFuncAttributeMaxDynamicSharedMemorySize, LDS_BYTES);
  gm_fused<<<256, 768, LDS_BYTES, stream>>>((const char*)d_in[0], (const char*)d_in[1], (const char*)d_in[2], (const char*)d_in[5], (const char*)d_in[6],
      (const char*)d_in[7], (const char*)d_in[9], (const char*)d_in[3], (const char*)d_in[8], (char*)d_ws, (char*)d_out);
}
```
